```python
import torch

def _softmax(x, axis=-1):
    return torch.softmax(x, dim=axis)
def _gelu(x):
    return torch.nn.functional.gelu(x, approximate="tanh")
def _silu(x):
    return torch.nn.functional.silu(x)
def _sigmoid(x):
    return torch.sigmoid(x)
def _layernorm(x, axis=-1, eps=1e-5):
    m = x.mean(dim=axis, keepdim=True)
    v = x.var(dim=axis, keepdim=True, unbiased=False)
    return (x - m) / torch.sqrt(v + eps)
def _scatter_add(tgt, idx, src, axis=0):
    return tgt.clone().index_add_(axis, idx, src)
def _segment_sum(src, seg, n):
    out = torch.zeros((n,) + src.shape[1:], dtype=src.dtype)
    return out.index_add_(0, seg, src)
def _pad(x, pw):
    flat = tuple(v for pair in reversed(pw) for v in pair)
    return torch.nn.functional.pad(x, flat)

kernel_name = 'interleave_layers__spectral_gfnet__glu_gated_reglu__prompt'

def setup_inputs():

    _w = (lambda a: (lambda t: t.to(torch.bfloat16) if t.is_floating_point() else t)(torch.as_tensor(a)))
    def _A_setup():
        B, S, D = 1, 2048, 512
        Df = D // 2 + 1
        return {
            "x": _w((lambda *s: torch.randn(*s, dtype=torch.bfloat16) * 0.05)(B, S, D)),
            "w_r": _w((lambda *s: torch.randn(*s, dtype=torch.bfloat16) * 0.4)(S, Df) * 0.02),
            "w_i": _w((lambda *s: torch.randn(*s, dtype=torch.bfloat16) * 0.4)(S, Df) * 0.02),
        }
    _A_in = _A_setup()

    def _B_setup():
        B, S, D, F = 1, 2048, 512, 1792
        return {
            "x": _w((lambda *s: torch.randn(*s, dtype=torch.bfloat16) * 0.05)(B, S, D)),
            "W_gate": _w((lambda *s: torch.randn(*s, dtype=torch.bfloat16) * 0.2)(D, F) * (2.0 / D) ** 0.5),
            "W_up": _w((lambda *s: torch.randn(*s, dtype=torch.bfloat16) * 0.2)(D, F) * (2.0 / D) ** 0.5),
            "W_down": _w((lambda *s: torch.randn(*s, dtype=torch.bfloat16) * 0.2)(F, D) * (1.0 / F) ** 0.5),
        }
    _B_in = _B_setup()

    return {
        "A_x": _A_in["x"],
        "A_w_r": _A_in["w_r"],
        "A_w_i": _A_in["w_i"],
        "B_x": _B_in["x"],
        "B_W_gate": _B_in["W_gate"],
        "B_W_up": _B_in["W_up"],
        "B_W_down": _B_in["W_down"],
    }

def reference(A_x, A_w_r, A_w_i, B_x, B_W_gate, B_W_up, B_W_down):
    def _A_ref(x, w_r, w_i):
        B, S, D = x.shape
        X = (lambda x, axes=(-2,-1): torch.fft.rfft2(x.float(), dim=axes))(x, axes=(-2, -1))
        W = w_r + 1j * w_i
        X = X * W
        y = (lambda x, s, axes=(-2,-1): torch.fft.irfft2(x, s=s, dim=axes).to(torch.bfloat16))(X, (S, D), axes=(-2, -1))
        return y

    def _B_ref(x, W_gate, W_up, W_down):
        gate = torch.einsum("bsd,df->bsf", x, W_gate)
        up = torch.einsum("bsd,df->bsf", x, W_up)
        gate = gate * (lambda x, dt: x.to(dt))(gate > 0, gate.dtype)
        h = gate * up
        return torch.einsum("bsf,fd->bsd", h, W_down)

    x = A_x
    x = x + _A_ref(_layernorm(x, axis=-1), A_w_r, A_w_i)
    x = x + _B_ref(_layernorm(x, axis=-1), B_W_gate, B_W_up, B_W_down)
    x = x + _A_ref(_layernorm(x, axis=-1), A_w_r, A_w_i)
    x = x + _B_ref(_layernorm(x, axis=-1), B_W_gate, B_W_up, B_W_down)
    return x
```

```cpp
#include <hip/hip_runtime.h>
#include <hip/hip_bf16.h>
#include <stdint.h>

typedef unsigned char u8;
typedef unsigned short u16;
typedef unsigned int u32;
typedef u32 u32x2 __attribute__((ext_vector_type(2)));
typedef u32 u32x4 __attribute__((ext_vector_type(4)));
typedef float f32x2 __attribute__((ext_vector_type(2)));
typedef float f32x4 __attribute__((ext_vector_type(4)));
typedef float f32x16 __attribute__((ext_vector_type(16)));
typedef _Float16 f16x2 __attribute__((ext_vector_type(2)));
typedef _Float16 f16x8 __attribute__((ext_vector_type(8)));
typedef __bf16 bf16x2 __attribute__((ext_vector_type(2)));
typedef __bf16 bf16x8 __attribute__((ext_vector_type(8)));
#define DI __device__ __forceinline__

constexpr size_t MB2 = 2u << 20;
constexpr size_t OFF_Z = 0;
constexpr size_t OFF_U = OFF_Z + MB2;
constexpr size_t OFF_X1 = OFF_U + MB2;
constexpr size_t OFF_XN = OFF_X1 + MB2;
constexpr size_t OFF_X2 = OFF_XN + MB2;
constexpr size_t OFF_WC = OFF_X2 + MB2;
constexpr size_t OFF_H = OFF_WC + MB2;
constexpr size_t OFF_W1 = OFF_H + 7340032;
constexpr size_t OFF_W2 = OFF_W1 + 3670016;
constexpr size_t OFF_WAB = OFF_W2 + 1835008;

#define Z_READER_CONTIG 1
#define U_READER_CONTIG 1
DI u32 zoff(u32 phi, u32 pos) { return Z_READER_CONTIG ? phi * 8192u + pos * 32u : pos * 8192u + phi * 32u; }
DI u32 uoff(u32 phi, u32 pos) { return U_READER_CONTIG ? pos * 8192u + phi * 32u : phi * 8192u + pos * 32u; }

DI u32 pkh(float a, float b) { f32x2 v = {a, b}; return __builtin_bit_cast(u32, __builtin_convertvector(v, f16x2)); }
DI u32 pkb(float a, float b) { f32x2 v = {a, b}; return __builtin_bit_cast(u32, __builtin_convertvector(v, bf16x2)); }
DI float blo(u32 u) { return __builtin_bit_cast(float, u << 16); }
DI float bhi(u32 u) { return __builtin_bit_cast(float, u & 0xffff0000u); }
DI float bfr(float a) { return blo(pkb(a, a)); }
DI f32x4 mfma16h(u32x4 a, u32x4 b, f32x4 c) {
  return __builtin_amdgcn_mfma_f32_16x16x32_f16(__builtin_bit_cast(f16x8, a), __builtin_bit_cast(f16x8, b), c, 0, 0, 0);
}
DI f32x4 mfma16b(u32x4 a, u32x4 b, f32x4 c) {
  return __builtin_amdgcn_mfma_f32_16x16x32_bf16(__builtin_bit_cast(bf16x8, a), __builtin_bit_cast(bf16x8, b), c, 0, 0, 0);
}
DI f32x16 mfma32b(u32x4 a, u32x4 b, f32x16 c) {
  return __builtin_amdgcn_mfma_f32_32x32x16_bf16(__builtin_bit_cast(bf16x8, a), __builtin_bit_cast(bf16x8, b), c, 0, 0, 0);
}
DI void sincos_rev(int n, int N, float& c, float& s) {
  float a = (float)(n & (N - 1)) * (1.0f / (float)N);
  c = __builtin_amdgcn_cosf(a);
  s = __builtin_amdgcn_sinf(a);
}
template <int CTRL> DI float dppf(float x) {
  return __builtin_bit_cast(float, __builtin_amdgcn_mov_dpp(__builtin_bit_cast(int, x), CTRL, 0xf, 0xf, true));
}
DI float wave_sum(float x) {
  x += dppf<0xB1>(x);
  x += dppf<0x4E>(x);
  x += dppf<0x141>(x);
  x += dppf<0x140>(x);
  auto s = __builtin_amdgcn_permlane16_swap(__builtin_bit_cast(u32, x), __builtin_bit_cast(u32, x), false, false);
  x = __uint_as_float(s[0]) + __uint_as_float(s[1]);
  auto t = __builtin_amdgcn_permlane32_swap(__builtin_bit_cast(u32, x), __builtin_bit_cast(u32, x), false, false);
  return __uint_as_float(t[0]) + __uint_as_float(t[1]);
}
DI void ln2(float (&a)[8], float (&b)[8]) {
  float sa = 0.f, sb = 0.f;
#pragma unroll
  for (int i = 0; i < 8; ++i) { sa += a[i]; sb += b[i]; }
  sa = wave_sum(sa) * (1.0f / 512.0f);
  sb = wave_sum(sb) * (1.0f / 512.0f);
  float qa = 0.f, qb = 0.f;
#pragma unroll
  for (int i = 0; i < 8; ++i) { float da = a[i] - sa, db = b[i] - sb; qa += da * da; qb += db * db; }
  qa = wave_sum(qa) * (1.0f / 512.0f);
  qb = wave_sum(qb) * (1.0f / 512.0f);
  u32 mm = pkb(sa, sb), vv = pkb(qa, qb);
  u32 ve = pkb(blo(vv) + 1e-5f, bhi(vv) + 1e-5f);
  u32 dd = pkb(__builtin_amdgcn_sqrtf(blo(ve)), __builtin_amdgcn_sqrtf(bhi(ve)));
  float ra = __builtin_amdgcn_rcpf(blo(dd)), rb = __builtin_amdgcn_rcpf(bhi(dd));
  float ma = blo(mm), mb = bhi(mm);
#pragma unroll
  for (int i = 0; i < 8; ++i) {
    u32 t = pkb(a[i] - ma, b[i] - mb);
    u32 o = pkb(blo(t) * ra, bhi(t) * rb);
    a[i] = blo(o); b[i] = bhi(o);
  }
}
DI int row_of_pos(int pos, int j) { return 512 * ((pos >> 6) & 1) + 128 * ((pos >> 4) & 3) + 32 * j + 16 * (pos >> 7) + (pos & 15); }
DI int xcd_map(int b) { return (b & 7) * 32 + (b >> 3); }

DI void prep_wc(const u16* __restrict__ wr, const u16* __restrict__ wi, u8* __restrict__ wc, int bi) {
  const int phi = threadIdx.x;
  const int kq = bi & 3, g = (bi >> 2) & 3, t = (bi >> 4) & 1, w = bi >> 5;
  u32x4 o[4];
#pragma unroll
  for (int c = 0; c < 4; ++c) {
#pragma unroll
    for (int r = 0; r < 4; ++r) {
      int k = 2 * (16 * (w & 1) + 4 * kq + c) + (w >> 1) + 64 * (16 * t + 4 * g + r);
      o[c][r] = (u32)wr[k * 257 + phi] | ((u32)wi[k * 257 + phi] << 16);
    }
  }
  u32x4* dst = (u32x4*)(wc + phi * 8192 + (w * 2 + t) * 1024 + (g * 16 + 4 * kq) * 16);
#pragma unroll
  for (int c = 0; c < 4; ++c) dst[c] = o[c];
}
DI void prep_wab(const u16* __restrict__ wr, const u16* __restrict__ wi, f32x4* __restrict__ wab, int bi) {
  const int idx = bi * 256 + threadIdx.x;
  const int w = idx >> 9, t = (idx >> 8) & 1, lane = (idx >> 2) & 63, r = idx & 3;
  const int k = 2 * (16 * (w & 1) + (lane & 15)) + (w >> 1) + 64 * (16 * t + 4 * (lane >> 4) + r);
  const int km = (2048 - k) & 2047;
  float ar = 0.5f * (blo(wr[k * 257]) + blo(wr[km * 257])), ai = 0.5f * (blo(wi[k * 257]) - blo(wi[km * 257]));
  float br = 0.5f * (blo(wr[k * 257 + 256]) + blo(wr[km * 257 + 256])), bi2 = 0.5f * (blo(wi[k * 257 + 256]) - blo(wi[km * 257 + 256]));
  f32x4 o = {0.5f * (ar + br), 0.5f * (ai + bi2), 0.5f * (ar - br), 0.5f * (ai - bi2)};
  wab[idx] = o;
}
DI void transpose_store(const u32x2 (&v)[8], u32x4* __restrict__ dst) {
  u32x4 o0, o1, o2, o3;
#pragma unroll
  for (int jj = 0; jj < 4; ++jj) {
    o0[jj] = (v[2 * jj].x & 0xffffu) | (v[2 * jj + 1].x << 16);
    o1[jj] = (v[2 * jj].x >> 16) | (v[2 * jj + 1].x & 0xffff0000u);
    o2[jj] = (v[2 * jj].y & 0xffffu) | (v[2 * jj + 1].y << 16);
    o3[jj] = (v[2 * jj].y >> 16) | (v[2 * jj + 1].y & 0xffff0000u);
  }
  dst[0] = o0; dst[1] = o1; dst[2] = o2; dst[3] = o3;
}
DI void prep_w1(const u16* __restrict__ wg, const u16* __restrict__ wu, u8* __restrict__ w1t, int bi) {
  const int idx = bi * 256 + threadIdx.x;
  const int cq = idx % 448, rest = idx / 448, piece = rest & 63, mat = rest >> 6;
  const u16* src = (mat ? wu : wg) + 4 * cq;
  const int d0 = 16 * (piece >> 2) + 4 * (piece & 3);
  u32x2 v[8];
#pragma unroll
  for (int j = 0; j < 8; ++j) v[j] = *(const u32x2*)(src + (d0 + (j & 3) + 256 * (j >> 2)) * 1792);
  const int col = 4 * cq, nt = col / 112, T = (col % 112) >> 4, c16 = col & 15;
  transpose_store(v, (u32x4*)(w1t + ((nt * 32 + (piece >> 1)) * 7 + T) * 1024 + ((piece & 1) * 32 + mat * 16 + c16) * 16));
}
DI void prep_w2(const u16* __restrict__ wd, u8* __restrict__ w2t, int bi) {
  const int idx = bi * 256 + threadIdx.x;
  const int cq = idx & 127, piece = idx >> 7, KS = piece >> 1, h = piece & 1;
  const u16* src = wd + 4 * cq;
  u32x2 v[8];
#pragma unroll
  for (int j = 0; j < 8; ++j) v[j] = *(const u32x2*)(src + (16 * KS + (j & 3) + 8 * (j >> 2) + 4 * h) * 512);
  const int col = 4 * cq, ct = col >> 6, c2 = (col >> 5) & 1, hp = (col >> 4) & 1, q = col & 15;
  transpose_store(v, (u32x4*)(w2t + ((ct * 112 + KS) * 2 + c2) * 1024 + (h * 32 + 8 * (q >> 2) + 4 * hp) * 16));
}

DI void rfwd_body(const u16* __restrict__ x, u8* __restrict__ zb, int pos, float* img) {
  const int tid = threadIdx.x, lane = tid & 63, w = __builtin_amdgcn_readfirstlane(tid >> 6);
  const int m = lane & 15, g = lane >> 4;
  const u32* plo = (const u32*)(x + row_of_pos(pos, w) * 512) + 64 * g + m;
  const u32* phi_ = plo + 1024 * 256;
  u32 lo[4], hi[4];
#pragma unroll
  for (int i = 0; i < 4; ++i) { lo[i] = plo[16 * i]; hi[i] = phi_[16 * i]; }
  u32x4 b1re, b1im, b2re[2][2], b2im[2][2];
  float twc[2][4], tws[2][4];
#pragma unroll
  for (int i = 0; i < 4; ++i) {
    float c, s;
    sincos_rev((4 * g + i) * m, 16, c, s);
    b1re[i] = pkh(c, s); b1im[i] = pkh(-s, c);
#pragma unroll
    for (int e = 0; e < 2; ++e) {
      const int n2 = 2 * (4 * g + i) + e;
      sincos_rev(n2 * m, 512, c, s);
      twc[e][i] = c * (1.0f / 16.0f); tws[e][i] = s * (1.0f / 16.0f);
#pragma unroll
      for (int t = 0; t < 2; ++t) {
        sincos_rev(n2 * (16 * t + m), 32, c, s);
        b2re[t][e][i] = pkh(c, s); b2im[t][e][i] = pkh(-s, c);
      }
    }
  }
  float a[8], b[8];
#pragma unroll
  for (int i = 0; i < 4; ++i) { a[2 * i] = blo(lo[i]); a[2 * i + 1] = bhi(lo[i]); b[2 * i] = blo(hi[i]); b[2 * i + 1] = bhi(hi[i]); }
  ln2(a, b);
  const f32x4 z4 = {0.f, 0.f, 0.f, 0.f};
  u32x4 a2[2];
#pragma unroll
  for (int e = 0; e < 2; ++e) {
    u32x4 a1;
#pragma unroll
    for (int i = 0; i < 4; ++i) a1[i] = pkh(a[2 * i + e], b[2 * i + e]);
    f32x4 dr = mfma16h(a1, b1re, z4), di = mfma16h(a1, b1im, z4);
#pragma unroll
    for (int r = 0; r < 4; ++r)
      a2[e][r] = pkh(dr[r] * twc[e][r] + di[r] * tws[e][r], di[r] * twc[e][r] - dr[r] * tws[e][r]);
  }
#pragma unroll
  for (int t = 0; t < 2; ++t) {
    f32x4 dr = mfma16h(a2[1], b2re[t][1], mfma16h(a2[0], b2re[t][0], z4));
    f32x4 di = mfma16h(a2[1], b2im[t][1], mfma16h(a2[0], b2im[t][0], z4));
    *(f32x4*)(img + (w * 2 + 0) * 640 + 4 * g + 20 * m + 320 * t) = dr;
    *(f32x4*)(img + (w * 2 + 1) * 640 + 4 * g + 20 * m + 320 * t) = di;
  }
  __syncthreads();
  const int phi = tid, fq = phi ? 512 - phi : 256;
  const int ip = phi + (phi >> 4) * 4, iq = fq + (fq >> 4) * 4;
  u32x4 oe, oo;
#pragma unroll
  for (int j = 0; j < 4; ++j) {
    float pr = img[(j * 2) * 640 + ip], pi = img[(j * 2 + 1) * 640 + ip];
    float qr = img[(j * 2) * 640 + iq], qi = img[(j * 2 + 1) * 640 + iq];
    float sa = pr + pi, sb = pr - pi, sc = qr + qi, sd = qr - qi;
    float er = phi ? 0.25f * (sa + sc) : 0.5f * sa, ei = phi ? 0.25f * (sd - sb) : 0.5f * sc;
    float orr = phi ? 0.25f * (sb + sd) : 0.5f * sb, oi = phi ? 0.25f * (sa - sc) : 0.5f * sd;
    oe[j] = pkh(er, ei); oo[j] = pkh(orr, oi);
  }
  u32x4* dst = (u32x4*)(zb + zoff(phi, pos));
  dst[0] = oe; dst[1] = oo;
}

DI void col_body(const u8* __restrict__ zb, const u8* __restrict__ wc, const f32x4* __restrict__ wab, u8* __restrict__ ub, int phi,
                 u8* lds) {
  const int tid = threadIdx.x, lane = tid & 63, w = __builtin_amdgcn_readfirstlane(tid >> 6);
  const int l = lane & 15, g = lane >> 4;
  const int pi_ = w >> 1, kt = w & 1;
  u32x4 a1[2][2], wv[2];
#pragma unroll
  for (int tau = 0; tau < 2; ++tau)
#pragma unroll
    for (int ks = 0; ks < 2; ++ks) a1[tau][ks] = *(const u32x4*)(zb + zoff(phi, tau * 128 + ks * 64 + lane) + pi_ * 16);
#pragma unroll
  for (int t = 0; t < 2; ++t) wv[t] = *(const u32x4*)(wc + phi * 8192 + (w * 2 + t) * 1024 + lane * 16);
  const int k1l = 2 * (16 * kt + l) + pi_;
  u32x4 b1re[2], b1im[2], a2re[2][2], a2im[2][2], b3re[2][2], b3im[2][2], a4re[2][2], a4im[2][2];
  float t1c[2][4], t1s[2][4], t2c[2][4], t2s[2][4];
#pragma unroll
  for (int r = 0; r < 4; ++r) {
    float c, s;
#pragma unroll
    for (int u = 0; u < 2; ++u) {
      sincos_rev((16 * u + 4 * g + r) * k1l, 64, c, s);
      b1re[u][r] = pkh(c, s); b1im[u][r] = pkh(-s, c);
      sincos_rev((16 * u + 4 * g + r) * k1l, 2048, c, s);
      t1c[u][r] = c * (1.0f / 32.0f); t1s[u][r] = s * (1.0f / 32.0f);
      sincos_rev((16 * u + l) * (2 * (16 * kt + 4 * g + r) + pi_), 2048, c, s);
      t2c[u][r] = c * (1.0f / 32.0f); t2s[u][r] = s * (1.0f / 32.0f);
#pragma unroll
      for (int v = 0; v < 2; ++v) {
        sincos_rev((16 * u + 4 * g + r) * (16 * v + l), 32, c, s);
        a2re[v][u][r] = pkh(c, s); a2im[v][u][r] = pkh(-s, c);
        b3re[v][u][r] = pkb(c, -s); b3im[v][u][r] = pkb(s, c);
        sincos_rev((16 * (w >> 1) + l) * (2 * (16 * v + 4 * g + r) + u), 64, c, s);
        a4re[u][v][r] = pkb(c, -s); a4im[u][v][r] = pkb(s, c);
      }
    }
  }
  const f32x4 z4 = {0.f, 0.f, 0.f, 0.f};
  u32x4 b2[2];
#pragma unroll
  for (int tau = 0; tau < 2; ++tau) {
    f32x4 dr = mfma16h(a1[tau][1], b1re[1], mfma16h(a1[tau][0], b1re[0], z4));
    f32x4 di = mfma16h(a1[tau][1], b1im[1], mfma16h(a1[tau][0], b1im[0], z4));
#pragma unroll
    for (int r = 0; r < 4; ++r)
      b2[tau][r] = pkh(dr[r] * t1c[tau][r] + di[r] * t1s[tau][r], di[r] * t1c[tau][r] - dr[r] * t1s[tau][r]);
  }
  f32x4 pr[2], pi[2];
#pragma unroll
  for (int t = 0; t < 2; ++t) {
    pr[t] = mfma16h(a2re[t][1], b2[1], mfma16h(a2re[t][0], b2[0], z4));
    pi[t] = mfma16h(a2im[t][1], b2[1], mfma16h(a2im[t][0], b2[0], z4));
  }
  u32x4 a3[2];
  if (phi != 0) {
#pragma unroll
    for (int t = 0; t < 2; ++t)
#pragma unroll
      for (int r = 0; r < 4; ++r) {
        float wr = blo(wv[t][r]), wi = bhi(wv[t][r]);
        a3[t][r] = pkb(pr[t][r] * wr - pi[t][r] * wi, pr[t][r] * wi + pi[t][r] * wr);
      }
  } else {
    f32x2* pl = (f32x2*)(lds + 8192);
#pragma unroll
    for (int t = 0; t < 2; ++t)
#pragma unroll
      for (int r = 0; r < 4; ++r) { f32x2 v = {pr[t][r], pi[t][r]}; pl[k1l + 64 * (16 * t + 4 * g + r)] = v; }
    __syncthreads();
#pragma unroll
    for (int t = 0; t < 2; ++t)
#pragma unroll
      for (int r = 0; r < 4; ++r) {
        f32x2 q = pl[(2048 - (k1l + 64 * (16 * t + 4 * g + r))) & 2047];
        f32x4 ab = wab[((w * 2 + t) * 64 + lane) * 4 + r];
        a3[t][r] = pkb(pr[t][r] * ab[0] - pi[t][r] * ab[1] + q[0] * ab[2] + q[1] * ab[3],
                       pr[t][r] * ab[1] + pi[t][r] * ab[0] + q[0] * ab[3] - q[1] * ab[2]);
      }
  }
#pragma unroll
  for (int tau = 0; tau < 2; ++tau) {
    f32x4 dr = mfma16b(a3[1], b3re[tau][1], mfma16b(a3[0], b3re[tau][0], z4));
    f32x4 di = mfma16b(a3[1], b3im[tau][1], mfma16b(a3[0], b3im[tau][0], z4));
    u32x4 o;
#pragma unroll
    for (int r = 0; r < 4; ++r)
      o[r] = pkb(dr[r] * t2c[tau][r] - di[r] * t2s[tau][r], di[r] * t2c[tau][r] + dr[r] * t2s[tau][r]);
    *(u32x4*)(lds + (tau * 4 + w) * 1024 + lane * 16) = o;
  }
  __syncthreads();
  const int tau = w & 1, nu = w >> 1;
  f32x4 er[2], ei[2];
#pragma unroll
  for (int p = 0; p < 2; ++p) {
    u32x4 f0 = *(const u32x4*)(lds + (tau * 4 + p * 2 + 0) * 1024 + lane * 16);
    u32x4 f1 = *(const u32x4*)(lds + (tau * 4 + p * 2 + 1) * 1024 + lane * 16);
    er[p] = mfma16b(a4re[p][1], f1, mfma16b(a4re[p][0], f0, z4));
    ei[p] = mfma16b(a4im[p][1], f1, mfma16b(a4im[p][0], f0, z4));
  }
  u32x4 o0, o1;
#pragma unroll
  for (int r = 0; r < 4; ++r) {
    float lr = 0.5f * (er[0][r] + er[1][r]), li = 0.5f * (ei[0][r] + ei[1][r]);
    float hr = 0.5f * (er[0][r] - er[1][r]), hi = 0.5f * (ei[0][r] - ei[1][r]);
    u32 p = phi ? pkb(lr - hi, li + hr) : pkb(lr, hr);
    u32 q = phi ? pkb(lr + hi, hr - li) : pkb(li, hi);
    if (r < 2) { o0[2 * r] = p; o0[2 * r + 1] = q; } else { o1[2 * r - 4] = p; o1[2 * r - 3] = q; }
  }
  u32x4* dst = (u32x4*)(ub + uoff(phi, tau * 128 + nu * 64 + lane));
  dst[0] = o0; dst[1] = o1;
}

DI void rinv_body(const u8* __restrict__ ub, const u16* __restrict__ x, u16* __restrict__ x1, u8* __restrict__ xn, int pos, u32* img) {
  const int tid = threadIdx.x, lane = tid & 63, w = __builtin_amdgcn_readfirstlane(tid >> 6);
  const int m = lane & 15, g = lane >> 4;
  const u32x4* src = (const u32x4*)(ub + uoff(tid, pos));
  u32x4 c0 = src[0], c1 = src[1];
  const int row = row_of_pos(pos, w);
  u32x2 xl[2], xh[2];
#pragma unroll
  for (int t = 0; t < 2; ++t) {
    xl[t] = *(const u32x2*)(x + row * 512 + 16 * m + 4 * g + 256 * t);
    xh[t] = *(const u32x2*)(x + (row + 1024) * 512 + 16 * m + 4 * g + 256 * t);
  }
  u32x4 b1re, b1im, b2re[2][2], b2im[2][2];
  float twc[2][4], tws[2][4];
#pragma unroll
  for (int i = 0; i < 4; ++i) {
    float c, s;
    sincos_rev((4 * g + i) * m, 16, c, s);
    b1re[i] = pkb(c, -s); b1im[i] = pkb(s, c);
#pragma unroll
    for (int e = 0; e < 2; ++e) {
      const int n2 = 2 * (4 * g + i) + e;
      sincos_rev(n2 * m, 512, c, s);
      twc[e][i] = c * (1.0f / 16.0f); tws[e][i] = s * (1.0f / 16.0f);
#pragma unroll
      for (int t = 0; t < 2; ++t) {
        sincos_rev(n2 * (16 * t + m), 32, c, s);
        b2re[t][e][i] = pkb(c, -s); b2im[t][e][i] = pkb(s, c);
      }
    }
  }
  {
    const int phi = tid, fq = phi ? 512 - phi : 256;
    const int ip = phi + (phi >> 7) * 32, iq = fq + (fq >> 7) * 32;
    img[0 * 640 + ip] = c0[0]; img[0 * 640 + iq] = c0[1];
    img[1 * 640 + ip] = c0[2]; img[1 * 640 + iq] = c0[3];
    img[2 * 640 + ip] = c1[0]; img[2 * 640 + iq] = c1[1];
    img[3 * 640 + ip] = c1[2]; img[3 * 640 + iq] = c1[3];
  }
  __syncthreads();
  u32x4 a1[2];
#pragma unroll
  for (int i = 0; i < 4; ++i) {
    u32x2 v = *(const u32x2*)(img + w * 640 + 160 * g + 32 * i + 2 * m);
    a1[0][i] = v.x; a1[1][i] = v.y;
  }
  const f32x4 z4 = {0.f, 0.f, 0.f, 0.f};
  u32x4 a2[2];
#pragma unroll
  for (int e = 0; e < 2; ++e) {
    f32x4 dr = mfma16b(a1[e], b1re, z4), di = mfma16b(a1[e], b1im, z4);
#pragma unroll
    for (int r = 0; r < 4; ++r)
      a2[e][r] = pkb(dr[r] * twc[e][r] - di[r] * tws[e][r], di[r] * twc[e][r] + dr[r] * tws[e][r]);
  }
  float a[8], b[8];
#pragma unroll
  for (int t = 0; t < 2; ++t) {
    f32x4 dr = mfma16b(a2[1], b2re[t][1], mfma16b(a2[0], b2re[t][0], z4));
    f32x4 di = mfma16b(a2[1], b2im[t][1], mfma16b(a2[0], b2im[t][0], z4));
    u32x2 ol, oh;
#pragma unroll
    for (int p = 0; p < 2; ++p) {
      u32 yl = pkb(dr[2 * p], dr[2 * p + 1]), yh = pkb(di[2 * p], di[2 * p + 1]);
      ol[p] = pkb(blo(xl[t][p]) + blo(yl), bhi(xl[t][p]) + bhi(yl));
      oh[p] = pkb(blo(xh[t][p]) + blo(yh), bhi(xh[t][p]) + bhi(yh));
      a[4 * t + 2 * p] = blo(ol[p]); a[4 * t + 2 * p + 1] = bhi(ol[p]);
      b[4 * t + 2 * p] = blo(oh[p]); b[4 * t + 2 * p + 1] = bhi(oh[p]);
    }
    *(u32x2*)(x1 + row * 512 + 16 * m + 4 * g + 256 * t) = ol;
    *(u32x2*)(x1 + (row + 1024) * 512 + 16 * m + 4 * g + 256 * t) = oh;
  }
  ln2(a, b);
  u32x4 na, nb;
#pragma unroll
  for (int p = 0; p < 4; ++p) {
    na[p] = (__builtin_bit_cast(u32, a[2 * p]) >> 16) | (__builtin_bit_cast(u32, a[2 * p + 1]) & 0xffff0000u);
    nb[p] = (__builtin_bit_cast(u32, b[2 * p]) >> 16) | (__builtin_bit_cast(u32, b[2 * p + 1]) & 0xffff0000u);
  }
  const int tok = 8 * pos + 2 * w;
  u32x4* dst = (u32x4*)(xn + (tok >> 5) * 32768 + (4 * m + g) * 512 + (tok & 31) * 16);
  dst[0] = na; dst[1] = nb;
}

__global__ __launch_bounds__(256) void k_rfwd_first(const u16* __restrict__ x, const u16* __restrict__ wr, const u16* __restrict__ wi, u8* __restrict__ ws) {
  __shared__ __attribute__((aligned(16))) float img[8 * 640];
  const int b = blockIdx.x;
  if (b < 256) rfwd_body(x, ws + OFF_Z, xcd_map(b), img);
  else if (b < 384) prep_wc(wr, wi, ws + OFF_WC, b - 256);
  else prep_wab(wr, wi, (f32x4*)(ws + OFF_WAB), b - 384);
}
__global__ __launch_bounds__(256) void k_rfwd(const u16* __restrict__ x, u8* __restrict__ ws) {
  __shared__ __attribute__((aligned(16))) float img[8 * 640];
  rfwd_body(x, ws + OFF_Z, xcd_map(blockIdx.x), img);
}
__global__ __launch_bounds__(256) void k_col(const u16* __restrict__ wg, const u16* __restrict__ wu, u8* __restrict__ ws) {
  __shared__ __attribute__((aligned(16))) u8 lds[8192 + 16384];
  const int b = blockIdx.x;
  if (b < 256) col_body(ws + OFF_Z, ws + OFF_WC, (const f32x4*)(ws + OFF_WAB), ws + OFF_U, xcd_map(b), lds);
  else prep_w1(wg, wu, ws + OFF_W1, b - 256);
}
__global__ __launch_bounds__(256) void k_rinv(const u16* __restrict__ x, const u16* __restrict__ wd, u8* __restrict__ ws) {
  __shared__ __attribute__((aligned(16))) u32 img[4 * 640];
  const int b = blockIdx.x;
  if (b < 256) rinv_body(ws + OFF_U, x, (u16*)(ws + OFF_X1), ws + OFF_XN, xcd_map(b), img);
  else prep_w2(wd, ws + OFF_W2, b - 256);
}

constexpr int G1_STAGE = 28672, G1_SLOTS = 5, G1_LDS = G1_STAGE * G1_SLOTS;
#define G1_ISSUE(s)                                                                                                          \
  do {                                                                                                                        \
    const u8* tp_ = tokbase + (s) * 4096;                                                                                     \
    asm volatile("global_load_dwordx4 %0, %4, %5\n\tglobal_load_dwordx4 %1, %4, %5 offset:1024\n\t"                           \
                 "global_load_dwordx4 %2, %4, %5 offset:2048\n\tglobal_load_dwordx4 %3, %4, %5 offset:3072"                   \
                 : "=&v"(bt[4 * (s)]), "=&v"(bt[4 * (s) + 1]), "=&v"(bt[4 * (s) + 2]), "=&v"(bt[4 * (s) + 3])                 \
                 : "v"(voff), "s"(tp_));                                                                                      \
    const u8* wp_ = wbase + (s) * G1_STAGE;                                                                                   \
    const u8* wq_ = wp_ + 4096;                                                                                               \
    const u32 la_ = ldsw + ((s) % G1_SLOTS) * G1_STAGE, lb_ = la_ + 4096;                                                    \
    u32 keep_;                                                                                                                \
    asm volatile("s_mov_b32 %0, m0\n\ts_mov_b32 m0, %4\n\ts_nop 0\n\t"                                                        \
                 "global_load_lds_dwordx4 %1, %2\n\tglobal_load_lds_dwordx4 %1, %2 offset:1024\n\t"                           \
                 "global_load_lds_dwordx4 %1, %2 offset:2048\n\tglobal_load_lds_dwordx4 %1, %2 offset:3072\n\t"               \
                 "s_mov_b32 m0, %5\n\ts_nop 0\n\t"                                                                            \
                 "global_load_lds_dwordx4 %1, %3\n\tglobal_load_lds_dwordx4 %1, %3 offset:1024\n\t"                           \
                 "global_load_lds_dwordx4 %1, %3 offset:2048\n\ts_mov_b32 m0, %0"                                             \
                 : "=&s"(keep_)                                                                                               \
                 : "v"(voff), "s"(wp_), "s"(wq_), "s"(la_), "s"(lb_)                                                          \
                 : "memory");                                                                                                 \
  } while (0)
#define G1_WAIT(s, N)                                                                                  \
  asm volatile("s_waitcnt vmcnt(" #N ") lgkmcnt(0)\n\ts_barrier"                                       \
               : "+v"(bt[4 * (s)]), "+v"(bt[4 * (s) + 1]), "+v"(bt[4 * (s) + 2]), "+v"(bt[4 * (s) + 3]) \
               :                                                                                        \
               : "memory")

__global__ __launch_bounds__(256, 1) void k_g1(u8* __restrict__ ws) {
  extern __shared__ __attribute__((aligned(16))) u8 dlds[];
  const int tid = threadIdx.x, lane = tid & 63, w = __builtin_amdgcn_readfirstlane(tid >> 6);
  const int b = blockIdx.x, xcd = b & 7, i = b >> 3;
  const int mt = (xcd >> 2) * 8 + (i & 7), nt = (xcd & 3) * 4 + (i >> 3);
  const u8* tokbase = ws + OFF_XN + (size_t)(4 * mt + w) * 32768;
  const u8* wbase = ws + OFF_W1 + (size_t)nt * (8 * G1_STAGE) + w * 7168;
  const u32 voff = lane * 16;
  const u32 ldsw = (u32)(size_t)(__attribute__((address_space(3))) u8*)dlds + w * 7168;
  u32x4 bt[32];
  G1_ISSUE(0); G1_ISSUE(1); G1_ISSUE(2); G1_ISSUE(3); G1_ISSUE(4);
  f32x16 acc[7];
#pragma unroll
  for (int T = 0; T < 7; ++T)
#pragma unroll
    for (int q = 0; q < 16; ++q) acc[T][q] = 0.f;
  u32x4 af[2][7];
  const u8* lrd = dlds + lane * 16;
  G1_WAIT(0, 44);
#pragma unroll
  for (int T = 0; T < 7; ++T) af[0][T] = *(const u32x4*)(lrd + T * 1024);
#pragma unroll
  for (int ks = 0; ks < 32; ++ks) {
    const int nx = ks + 1;
    if (nx == 4) { G1_WAIT(1, 33); G1_ISSUE(5); }
    if (nx == 8) { G1_WAIT(2, 33); G1_ISSUE(6); }
    if (nx == 12) { G1_WAIT(3, 33); G1_ISSUE(7); }
    if (nx == 16) { G1_WAIT(4, 33); }
    if (nx == 20) { G1_WAIT(5, 22); }
    if (nx == 24) { G1_WAIT(6, 11); }
    if (nx == 28) { G1_WAIT(7, 0); }
#pragma unroll
    for (int T = 0; T < 7; ++T) {
      if (nx < 32) af[nx & 1][T] = *(const u32x4*)(lrd + ((nx >> 2) % G1_SLOTS) * G1_STAGE + ((nx & 3) * 7 + T) * 1024);
      __builtin_amdgcn_sched_barrier(0);
      acc[T] = mfma32b(af[ks & 1][T], bt[ks], acc[T]);
      __builtin_amdgcn_sched_barrier(0);
    }
  }
  u8* hp = ws + OFF_H + (size_t)(4 * mt + w) * 114688 + (7 * nt) * 1024 + lane * 16;
#pragma unroll
  for (int T = 0; T < 7; ++T) {
    u32x4 o;
#pragma unroll
    for (int p = 0; p < 4; ++p) {
      u32 gg = pkb(acc[T][2 * p], acc[T][2 * p + 1]), uu = pkb(acc[T][2 * p + 8], acc[T][2 * p + 9]);
      o[p] = pkb(fmaxf(blo(gg), 0.f) * blo(uu), fmaxf(bhi(gg), 0.f) * bhi(uu));
    }
    *(u32x4*)(hp + T * 1024) = o;
  }
}

constexpr int G2_WIN = 8;
__global__ __launch_bounds__(256, 1) void k_g2(const u8* __restrict__ ws, const u16* __restrict__ xres, u16* __restrict__ out) {
  __shared__ __attribute__((aligned(16))) f32x4 red[4 * 3 * 4 * 64];
  const int tid = threadIdx.x, lane = tid & 63, w = __builtin_amdgcn_readfirstlane(tid >> 6);
  const int b = blockIdx.x, xcd = b & 7, i = b >> 3;
  const int tt = (xcd >> 1) * 8 + (i & 7), ct = (xcd & 1) * 4 + (i >> 3);
  const int c2 = w >> 1, u = w & 1;
  const u8* hp0 = ws + OFF_H + (size_t)(2 * tt + u) * 114688 + (28 * w) * 1024 + lane * 16;
  const u8* hp1 = ws + OFF_H + (size_t)(2 * tt + (u ^ 1)) * 114688 + (28 * w) * 1024 + lane * 16;
  const u8* wp0 = ws + OFF_W2 + (size_t)(ct * 112 + 28 * w) * 2048 + c2 * 1024 + lane * 16;
  const u8* wp1 = ws + OFF_W2 + (size_t)(ct * 112 + 28 * w) * 2048 + (c2 ^ 1) * 1024 + lane * 16;
  u32x4 a0[G2_WIN], a1[G2_WIN], b0[G2_WIN], b1[G2_WIN];
#pragma unroll
  for (int k = 0; k < G2_WIN; ++k) {
    a0[k] = *(const u32x4*)(wp0 + k * 2048); b0[k] = *(const u32x4*)(hp0 + k * 1024);
    a1[k] = *(const u32x4*)(wp1 + k * 2048); b1[k] = *(const u32x4*)(hp1 + k * 1024);
    __builtin_amdgcn_sched_barrier(0);
  }
  const int tok = 64 * tt + 32 * u + (lane & 31);
  const int row = row_of_pos(tok >> 3, (tok >> 1) & 3) + 1024 * (tok & 1);
  const int eoff = row * 512 + 64 * ct + 32 * c2 + 16 * (lane >> 5);
  u32x4 xr0 = *(const u32x4*)(xres + eoff), xr1 = *(const u32x4*)(xres + eoff + 8);
  __builtin_amdgcn_sched_barrier(0);
  f32x16 acc[2][2];
#pragma unroll
  for (int q = 0; q < 16; ++q) { acc[0][0][q] = 0.f; acc[0][1][q] = 0.f; acc[1][0][q] = 0.f; acc[1][1][q] = 0.f; }
#pragma unroll
  for (int k = 0; k < 28; ++k) {
    const int s = k % G2_WIN;
    acc[0][0] = mfma32b(a0[s], b0[s], acc[0][0]);
    acc[0][1] = mfma32b(a0[s], b1[s], acc[0][1]);
    acc[1][0] = mfma32b(a1[s], b0[s], acc[1][0]);
    acc[1][1] = mfma32b(a1[s], b1[s], acc[1][1]);
    __builtin_amdgcn_sched_barrier(0);
    if (k + G2_WIN < 28) {
      const int kn = k + G2_WIN;
      a0[s] = *(const u32x4*)(wp0 + kn * 2048); b0[s] = *(const u32x4*)(hp0 + kn * 1024);
      a1[s] = *(const u32x4*)(wp1 + kn * 2048); b1[s] = *(const u32x4*)(hp1 + kn * 1024);
      __builtin_amdgcn_sched_barrier(0);
    }
  }
#pragma unroll
  for (int x = 1; x < 4; ++x) {
    f32x4* dst = red + (((w ^ x) * 3 + (x - 1)) * 4) * 64 + lane;
#pragma unroll
    for (int p = 0; p < 4; ++p) {
      f32x4 v = {acc[x >> 1][x & 1][4 * p], acc[x >> 1][x & 1][4 * p + 1], acc[x >> 1][x & 1][4 * p + 2], acc[x >> 1][x & 1][4 * p + 3]};
      dst[p * 64] = v;
    }
  }
  __syncthreads();
  float o[16];
#pragma unroll
  for (int p = 0; p < 4; ++p) {
    f32x4 v0 = red[((w * 3 + 0) * 4 + p) * 64 + lane], v1 = red[((w * 3 + 1) * 4 + p) * 64 + lane], v2 = red[((w * 3 + 2) * 4 + p) * 64 + lane];
#pragma unroll
    for (int q = 0; q < 4; ++q) o[4 * p + q] = (acc[0][0][4 * p + q] + v0[q]) + (v1[q] + v2[q]);
  }
  u32x4 r0, r1;
#pragma unroll
  for (int p = 0; p < 4; ++p) {
    u32 y0 = pkb(o[2 * p], o[2 * p + 1]), y1 = pkb(o[8 + 2 * p], o[8 + 2 * p + 1]);
    r0[p] = pkb(blo(xr0[p]) + blo(y0), bhi(xr0[p]) + bhi(y0));
    r1[p] = pkb(blo(xr1[p]) + blo(y1), bhi(xr1[p]) + bhi(y1));
  }
  *(u32x4*)(out + eoff) = r0;
  *(u32x4*)(out + eoff + 8) = r1;
}

extern "C" void kernel_launch(void* const* d_in, const int* in_sizes, int n_in, void* d_out, int out_size, void* d_ws, size_t ws_size,
                              hipStream_t stream) {
  const u16* x = (const u16*)d_in[0];
  const u16* wr = (const u16*)d_in[1];
  const u16* wi = (const u16*)d_in[2];
  const u16* wg = (const u16*)d_in[4];
  const u16* wu = (const u16*)d_in[5];
  const u16* wd = (const u16*)d_in[6];
  u8* ws = (u8*)d_ws;
  u16* out = (u16*)d_out;
  (void)hipFuncSetAttribute((const void*)k_g1, hipFuncAttributeMaxDynamicSharedMemorySize, G1_LDS);
  k_rfwd_first<<<392, 256, 0, stream>>>(x, wr, wi, ws);
  k_col<<<256 + 224, 256, 0, stream>>>(wg, wu, ws);
  k_rinv<<<256 + 112, 256, 0, stream>>>(x, wd, ws);
  k_g1<<<256, 256, G1_LDS, stream>>>(ws);
  k_g2<<<256, 256, 0, stream>>>(ws, (const u16*)(ws + OFF_X1), (u16*)(ws + OFF_X2));
  k_rfwd<<<256, 256, 0, stream>>>((const u16*)(ws + OFF_X2), ws);
  k_col<<<256, 256, 0, stream>>>(wg, wu, ws);
  k_rinv<<<256, 256, 0, stream>>>((const u16*)(ws + OFF_X2), wd, ws);
  k_g1<<<256, 256, G1_LDS, stream>>>(ws);
  k_g2<<<256, 256, 0, stream>>>(ws, (const u16*)(ws + OFF_X1), out);
}
```

```cpp
#include <hip/hip_runtime.h>
#include <hip/hip_bf16.h>
#include <stdint.h>
#include <type_traits>
#include <utility>

typedef unsigned char u8;
typedef unsigned short u16;
typedef unsigned int u32;
typedef unsigned long long u64;
typedef u32 u32x2 __attribute__((ext_vector_type(2)));
typedef u32 u32x4 __attribute__((ext_vector_type(4)));
typedef float f32x2 __attribute__((ext_vector_type(2)));
typedef float f32x4 __attribute__((ext_vector_type(4)));
typedef _Float16 f16x2 __attribute__((ext_vector_type(2)));
typedef __bf16 bf16x2 __attribute__((ext_vector_type(2)));
typedef __bf16 bf16x8 __attribute__((ext_vector_type(8)));
#define DI __device__ __forceinline__
#define SB() __builtin_amdgcn_sched_barrier(0)
#define MEMFENCE() asm volatile("" ::: "memory")

constexpr u32 MB2 = 2u << 20;
constexpr u32 OFF_E = 0, OFF_FLAG = 4096, OFF_WNY = 65536;
constexpr u32 OFF_Z = MB2;
constexpr u32 OFF_U = OFF_Z + 2 * MB2;
constexpr u32 OFF_XN = OFF_U + 2 * MB2;
constexpr u32 OFF_WX = OFF_XN + 2 * MB2;
constexpr u32 OFF_DL = OFF_WX + MB2;
constexpr u32 OFF_X3 = OFF_DL + MB2;
constexpr u32 H_BYTES = 7340032;
constexpr u32 OFF_H = OFF_X3 + MB2;
constexpr u32 OFF_W1 = OFF_H + 2 * H_BYTES;
constexpr u32 OFF_W2 = OFF_W1 + 3670016;
constexpr u32 OFF_WP = OFF_W2 + 1835008;
constexpr u32 WS_USED = OFF_WP + MB2;
constexpr u32 SENT = 0xFFFFFFFFu;
constexpr int SPIN_CAP = 1 << 15;

constexpr int LDS_SCR = 131072, LDS_STASH = LDS_SCR + 24576, LDS_TOTAL = 163840;

struct Params {
  const u16* x; const u16* wr; const u16* wi; const u16* wg; const u16* wu; const u16* wd; u16* out; u8* ws;
};

#define ST(i)
#define ST_DECL
#define CNT(i)
#define ST_OUT()

DI u32 pkh(float a, float b) { f32x2 v = {a, b}; return __builtin_bit_cast(u32, __builtin_convertvector(v, f16x2)); }
DI u32 pkb(float a, float b) { f32x2 v = {a, b}; return __builtin_bit_cast(u32, __builtin_convertvector(v, bf16x2)); }
template <bool BF> DI u32 pkc(float a, float b) { return BF ? pkb(a, b) : pkh(a, b); }
DI u32 pkh_n0(float a, float b, u32 after) { u32 r; asm("v_cvt_pk_f16_f32 %0, -%1, %2" : "=v"(r) : "v"(a), "v"(b), "v"(after)); return r; }
DI u32 pkb_n1(float a, float b, u32 after) { u32 r; asm("v_cvt_pk_bf16_f32 %0, %1, -%2" : "=v"(r) : "v"(a), "v"(b), "v"(after)); return r; }
DI float blo(u32 u) { return __uint_as_float(u << 16); }
DI float bhi(u32 u) { return __uint_as_float(u & 0xffff0000u); }
DI float bfr(float a) { return blo(pkb(a, a)); }
DI float relu(float a) { const int i = __float_as_int(a); return __int_as_float(i > 0 ? i : 0); }
DI u32 umax(u32 a, u32 b) { return a > b ? a : b; }
DI u32 max3(u32 a, u32 b, u32 c) { return umax(umax(a, b), c); }
DI u32 max4(u32 acc, u32x4 v) { return max3(max3(acc, v.x, v.y), v.z, v.w); }
DI f32x2 lo2(f32x4 v) { return __builtin_shufflevector(v, v, 0, 1); }
DI f32x2 hi2(f32x4 v) { return __builtin_shufflevector(v, v, 2, 3); }
DI f32x2 swp(f32x2 v) { return __builtin_shufflevector(v, v, 1, 0); }
DI f32x4 mfma_b(u32x4 a, u32x4 b, f32x4 c) {
  return __builtin_amdgcn_mfma_f32_16x16x32_bf16(__builtin_bit_cast(bf16x8, a), __builtin_bit_cast(bf16x8, b), c, 0, 0, 0);
}
template <class T> DI void PIN(T& x) { asm volatile("" : "+v"(x)); }
template <class T> DI void PINS(T& x) { asm volatile("" : "+s"(x)); }
template <class T> DI void PINA(T& x) { asm volatile("" : "+a"(x)); }
template <class F, int... I> DI void static_for_(F&& f, std::integer_sequence<int, I...>) { (f(std::integral_constant<int, I>{}), ...); }
template <int N, class F> DI void static_for(F&& f) { static_for_(f, std::make_integer_sequence<int, N>{}); }

#define MM_(T) "v_mfma_f32_16x16x32_" T
template <bool BF> DI void mm4(f32x4& d0, f32x4& d1, f32x4& d2, f32x4& d3, u32x4 a0, u32x4 a1, u32x4 b0, u32x4 b1) {
#define MM4_(T) asm volatile("s_nop 1\n\t" MM_(T) " %0, %4, %6, 0\n\t" MM_(T) " %1, %4, %7, 0\n\t" MM_(T) " %2, %5, %6, 0\n\t" MM_(T) " %3, %5, %7, 0\n\ts_nop 5" \
               : "=&v"(d0), "=&v"(d1), "=&v"(d2), "=&v"(d3) : "v"(a0), "v"(a1), "v"(b0), "v"(b1))
  if constexpr (BF) MM4_("bf16"); else MM4_("f16");
}
template <bool BF, int TAIL, int AG = 0> DI void mm8(f32x4& d0, f32x4& d1, f32x4& d2, f32x4& d3, u32x4 a00, u32x4 b00, u32x4 a01, u32x4 b01, u32x4 a10,
                               u32x4 b10, u32x4 a11, u32x4 b11, u32x4 a20, u32x4 b20, u32x4 a21, u32x4 b21, u32x4 a30, u32x4 b30, u32x4 a31, u32x4 b31) {
#define MM8_(T, A, B) asm volatile("s_nop 1\n\t" MM_(T) " %0, %4, %5, 0\n\t" MM_(T) " %1, %8, %9, 0\n\t" MM_(T) " %0, %6, %7, %0\n\t" MM_(T) " %1, %10, %11, %1\n\t" \
               MM_(T) " %2, %12, %13, 0\n\t" MM_(T) " %3, %16, %17, 0\n\t" MM_(T) " %2, %14, %15, %2\n\t" MM_(T) " %3, %18, %19, %3\n\ts_nop %20"         \
               : "=&v"(d0), "=&v"(d1), "=&v"(d2), "=&v"(d3)                                                                                              \
               : A(a00), B(b00), A(a01), B(b01), A(a10), B(b10), A(a11), B(b11), A(a20), B(b20), A(a21), B(b21), A(a30), B(b30), A(a31), B(b31), "i"(TAIL))
  if constexpr (!BF) MM8_("f16", "v", "v");
  else if constexpr (AG == 0) MM8_("bf16", "v", "v");
  else if constexpr (AG == 1) MM8_("bf16", "a", "v");
  else MM8_("bf16", "v", "a");
}
template <bool BF> DI u32x4 twiddle(f32x4 r, f32x4 i, const f32x2 (&c)[2], const f32x2 (&s)[2]) {
  const f32x2 re0 = lo2(r) * c[0] + lo2(i) * s[0], im0 = lo2(i) * c[0] - lo2(r) * s[0];
  const f32x2 re1 = hi2(r) * c[1] + hi2(i) * s[1], im1 = hi2(i) * c[1] - hi2(r) * s[1];
  return u32x4{pkc<BF>(re0.x, im0.x), pkc<BF>(re0.y, im0.y), pkc<BF>(re1.x, im1.x), pkc<BF>(re1.y, im1.y)};
}
DI u32x4 cmul_b(f32x4 r, f32x4 i, const f32x2 (&c)[2], const f32x2 (&s)[2]) {
  const f32x2 re0 = lo2(r) * c[0] - lo2(i) * s[0], im0 = lo2(i) * c[0] + lo2(r) * s[0];
  const f32x2 re1 = hi2(r) * c[1] - hi2(i) * s[1], im1 = hi2(i) * c[1] + hi2(r) * s[1];
  return u32x4{pkb(re0.x, im0.x), pkb(re0.y, im0.y), pkb(re1.x, im1.x), pkb(re1.y, im1.y)};
}
DI float cosr(float a) { return __builtin_amdgcn_cosf(a); }
DI float sinr(float a) { return __builtin_amdgcn_sinf(a); }
template <int CTRL> DI float dppf(float x) {
  return __builtin_bit_cast(float, __builtin_amdgcn_mov_dpp(__builtin_bit_cast(int, x), CTRL, 0xf, 0xf, true));
}
DI float reduce2(float a, float b) {
  auto t = __builtin_amdgcn_permlane32_swap(__float_as_uint(a), __float_as_uint(b), false, false);
  float x = __uint_as_float(t[0]) + __uint_as_float(t[1]);
  x += dppf<0xB1>(x);
  x += dppf<0x4E>(x);
  x += dppf<0x141>(x);
  x += dppf<0x140>(x);
  auto s = __builtin_amdgcn_permlane16_swap(__float_as_uint(x), __float_as_uint(x), false, false);
  return __uint_as_float(s[0]) + __uint_as_float(s[1]);
}
DI f32x2 bcast2(float x) {
  auto t = __builtin_amdgcn_permlane32_swap(__float_as_uint(x), __float_as_uint(x), false, false);
  return f32x2{__uint_as_float(t[0]), __uint_as_float(t[1])};
}
DI void ln_finish(float mean, float q, f32x2& R, f32x2& O) {
  const float r = __builtin_amdgcn_rcpf(bfr(__builtin_amdgcn_sqrtf(bfr(bfr(q * (1.0f / 512.0f)) + 1e-5f))));
  const float o = (mean - bfr(mean)) * r;
  R = bcast2(r); O = bcast2(o);
}
DI int row_of_pos(int pos, int j) {
  const int c = 8 * (pos >> 7) + 2 * ((pos >> 3) & 3) + ((pos >> 5) & 1), q = 8 * ((pos >> 6) & 1) + (pos & 7);
  return 32 * (2 * c + (j & 1)) + 2 * q + (j >> 1);
}

template <int I> DI void ld4(u32x4& d, u32 v, const u8* s) { asm volatile("global_load_dwordx4 %0, %1, %2 offset:%3 sc1" : "=v"(d) : "v"(v), "s"(s), "i"(I) : "memory"); }
template <int I> DI void ld4a(u32x4& d, u32 v, const u8* s) { asm volatile("global_load_dwordx4 %0, %1, %2 offset:%3 sc1" : "=a"(d) : "v"(v), "s"(s), "i"(I) : "memory"); }
template <int I> DI void ld4r(u32x4& d, u32 v, const u8* s) { asm volatile("global_load_dwordx4 %0, %1, %2 offset:%3 sc1" : "+v"(d) : "v"(v), "s"(s), "i"(I) : "memory"); }
template <int I> DI void ld2(u32x2& d, u32 v, const u8* s) { asm volatile("global_load_dwordx2 %0, %1, %2 offset:%3 sc1" : "=v"(d) : "v"(v), "s"(s), "i"(I) : "memory"); }
template <int I> DI void ld2r(u32x2& d, u32 v, const u8* s) { asm volatile("global_load_dwordx2 %0, %1, %2 offset:%3 sc1" : "+v"(d) : "v"(v), "s"(s), "i"(I) : "memory"); }
template <int I> DI void ld2p(u32x2& d, u32 v, const u8* s) { asm volatile("global_load_dwordx2 %0, %1, %2 offset:%3" : "=v"(d) : "v"(v), "s"(s), "i"(I) : "memory"); }
DI void touch(u32& d, u32 v, const u8* s) { asm volatile("global_load_dword %0, %1, %2 sc1" : "+v"(d) : "v"(v), "s"(s) : "memory"); }
DI void ld1s(u32& d, u32 v, const u8* s) { asm volatile("global_load_ushort %0, %1, %2" : "=v"(d) : "v"(v), "s"(s) : "memory"); }
template <int N> DI void vmwait() { static_assert(N >= 0 && N < 64, ""); asm volatile("s_waitcnt vmcnt(%0)" ::"i"(N) : "memory"); }
DI void set_m0(u32 ldsaddr) { asm volatile("s_mov_b32 m0, %0" ::"s"(ldsaddr) : "memory"); }
template <int I> DI void dma1(u32 voff, const u8* src) {
  asm volatile("global_load_lds_dwordx4 %0, %1 offset:%2 sc1" ::"v"(voff), "s"(src), "i"(I) : "memory");
}
DI void dma4(u32 voff, const u8* src, u32 ldsaddr) {
  asm volatile("s_mov_b32 m0, %2\n\ts_nop 0\n\tglobal_load_lds_dwordx4 %0, %1 sc1\n\tglobal_load_lds_dwordx4 %0, %1 offset:1024 sc1\n\t"
               "global_load_lds_dwordx4 %0, %1 offset:2048 sc1\n\tglobal_load_lds_dwordx4 %0, %1 offset:3072 sc1" ::"v"(voff), "s"(src), "s"(ldsaddr) : "memory");
}
#define RETRY(COND, ...)                                                      \
  do {                                                                        \
    bool bad_ = (COND);                                                       \
    if (__builtin_expect(__any(bad_), 0)) {                                   \
      _Pragma("nounroll") for (int sp_ = 0; sp_ < SPIN_CAP; ++sp_) {          \
        __VA_ARGS__                                                           \
        bad_ = (COND);                                                        \
        if (!__any(bad_)) break;                                              \
      }                                                                       \
    }                                                                         \
  } while (0)
DI void bar() { asm volatile("s_waitcnt lgkmcnt(0)\n\ts_barrier" ::: "memory"); }
DI void bar_nw() { asm volatile("s_barrier" ::: "memory"); }

struct RowC { u32x4 b1re, b1im, b2re[2][2], b2im[2][2]; f32x2 twc[2][2], tws[2][2]; };
template <bool INV> DI void pair_cs(float c, float s, u32& re, u32& im) {
  if constexpr (INV) { im = pkb(s, c); re = pkb_n1(c, s, im); } else { re = pkh(c, s); im = pkh_n0(s, c, re); }
}
template <bool AG = false> DI void sign4(u32x4 base, u32 lm, u32x4& f00, u32x4& f01, u32x4& f10, u32x4& f11) {
  f00 = base;
  f01 = base ^ lm;
  f10 = base ^ u32x4{0u, 0x80008000u, 0u, 0x80008000u};
  f11 = f10 ^ lm;
  if constexpr (AG) { PINA(f00); PINA(f01); PINA(f10); PINA(f11); } else { PIN(f00); PIN(f01); PIN(f10); PIN(f11); }
}
template <bool INV, int PART> DI void row_consts(RowC& k, int m, int g) {
  const float fm = (float)m, fg = (float)(4 * g);
  if constexpr (PART == 0) {
    u32x4 br, bi;
#pragma unroll
    for (int i = 0; i < 4; ++i) {
      const float a = (fg + (float)i) * (fm * (1.0f / 16.0f));
      u32 x, y;
      pair_cs<INV>(cosr(a), sinr(a), x, y); k.b1re[i] = x; k.b1im[i] = y;
      const float a2 = (fg + (float)i) * (fm * (1.0f / 32.0f));
      pair_cs<INV>(cosr(a2), sinr(a2), x, y); br[i] = x; bi[i] = y;
    }
    PIN(k.b1re); PIN(k.b1im);
    const u32 lm = (m & 1) ? 0x80008000u : 0u;
    sign4(br, lm, k.b2re[0][0], k.b2re[0][1], k.b2re[1][0], k.b2re[1][1]);
    sign4(bi, lm, k.b2im[0][0], k.b2im[0][1], k.b2im[1][0], k.b2im[1][1]);
  } else {
#pragma unroll
    for (int e = 0; e < 2; ++e)
#pragma unroll
      for (int i = 0; i < 4; ++i) {
        const float a = (fg + (float)(16 * e + i)) * (fm * (INV ? -1.0f / 512.0f : 1.0f / 512.0f));
        k.twc[e][i >> 1][i & 1] = cosr(a); k.tws[e][i >> 1][i & 1] = sinr(a);
      }
#pragma unroll
    for (int e = 0; e < 2; ++e) { PIN(k.twc[e][0]); PIN(k.twc[e][1]); PIN(k.tws[e][0]); PIN(k.tws[e][1]); }
  }
}
template <bool INV, int TAIL> DI void row_fft(const RowC& k, u32x4 a0, u32x4 a1, f32x4& dr0, f32x4& di0, f32x4& dr1, f32x4& di1) {
  f32x4 tr0, ti0, tr1, ti1;
  mm4<INV>(tr0, ti0, tr1, ti1, a0, a1, k.b1re, k.b1im);
  const u32x4 p0 = twiddle<INV>(tr0, ti0, k.twc[0], k.tws[0]);
  SB();
  const u32x4 p1 = twiddle<INV>(tr1, ti1, k.twc[1], k.tws[1]);
  mm8<INV, TAIL>(dr0, di0, dr1, di1, p0, k.b2re[0][0], p1, k.b2re[0][1], p0, k.b2im[0][0], p1, k.b2im[0][1], p0, k.b2re[1][0], p1, k.b2re[1][1], p0,
           k.b2im[1][0], p1, k.b2im[1][1]);
}

DI void transpose8x4(const u32x2 (&v)[8], u32x4 (&o)[4]) {
#pragma unroll
  for (int jj = 0; jj < 4; ++jj) {
    o[0][jj] = __builtin_amdgcn_perm(v[2 * jj + 1].x, v[2 * jj].x, 0x05040100u);
    o[1][jj] = __builtin_amdgcn_perm(v[2 * jj + 1].x, v[2 * jj].x, 0x07060302u);
    o[2][jj] = __builtin_amdgcn_perm(v[2 * jj + 1].y, v[2 * jj].y, 0x05040100u);
    o[3][jj] = __builtin_amdgcn_perm(v[2 * jj + 1].y, v[2 * jj].y, 0x07060302u);
  }
}
DI void prep_load(const Params& P, int wt, int lane, u32x2 (&v)[8]) {
  const u32 vo = lane * 8;
  if (wt < 896) {
    const int rest = wt / 7, piece = rest & 63;
    const u8* src = (const u8*)((rest >> 6) ? P.wu : P.wg) + (wt % 7) * 512 + (16 * (piece >> 2) + 4 * (piece & 3)) * 3584;
    const u32 vo2 = vo + 7168;
    static_for<8>([&](auto J) { constexpr int j = decltype(J)::value; ld2p<(j & 1) * 3584>(v[j], j & 2 ? vo2 : vo, src + (j >> 2) * (256 * 3584)); });
  } else {
    const int piece = (wt - 896) >> 1, g = piece & 3, kq = piece >> 2, wv = kq / 14, ks = kq % 14;
    int f0, f1;
    if (ks < 4) { f0 = 112 * (4 * wv + ks); f1 = f0 + 16; }
    else if (ks < 6) { f0 = 112 * (4 * wv + 2 * (ks - 4)) + 32; f1 = f0 + 112; }
    else if (ks < 10) { f0 = 112 * (4 * wv + ks - 6) + 48; f1 = f0 + 16; }
    else { f0 = 112 * (4 * wv + ks - 10) + 80; f1 = f0 + 16; }
    const u8* s0 = (const u8*)P.wd + (wt & 1) * 512 + (4 * g + f0) * 1024;
    const u8* s1 = (const u8*)P.wd + (wt & 1) * 512 + (4 * g + f1) * 1024;
    static_for<8>([&](auto J) { constexpr int j = decltype(J)::value; ld2p<(j & 3) * 1024>(v[j], vo, j < 4 ? s0 : s1); });
  }
}
DI u32 prep_dst(int wt, int tile) {
  if (wt < 896) {
    const int rest = wt / 7, piece = rest & 63, mat = rest >> 6, tl = (wt % 7) * 16 + tile;
    const int nt = tl / 7, p = tl % 7, ks = piece >> 2;
    const u32 frag = (p < 3 ? ks * 6 + 2 * p : p < 5 ? 96 + ks * 4 + 2 * (p - 3) : 160 + ks * 4 + 2 * (p - 5)) + mat;
    return OFF_W1 + (nt * 224 + frag) * 1024 + (piece & 3) * 256;
  } else {
    const int piece = (wt - 896) >> 1, tl = (wt & 1) * 16 + tile;
    return OFF_W2 + (((tl >> 2) * 56 + (piece >> 2)) * 4 + (tl & 3)) * 1024 + (piece & 3) * 256;
  }
}

constexpr int NTOK0 = 16, NRES = 40, NEARLY = 40, NFILL = 15;
constexpr int g1_job(int L, int x) {
  if (x < 2 * (32 - NTOK0) && !(x & 1)) return 1;
  if (((x >= 34 && x < 96) && (x & 3) == 2) || (x >= 98 && x < 130 && (x & 3) == 0)) return 2;
  if (x >= 152) return (x < 184 ? (x & 1) == 0 : x < 208 ? x % 3 != 2 : (x & 1) == 0) ? 4 : 0;
  if (x >= 142 && x < 150 && !(x & 1)) return 5;
  if (L == 0 && ((x < 32 && x % 6 == 1) || (x >= 36 && x < 98 && (x & 3) == 0) || (x >= 98 && x < 130 && (x & 3) == 2) || (x >= 130 && x < 142 && !(x & 1)) ||
                 (x >= 49 && x < 130 && (x & 15) == 1)))
    return 3;
  if (L == 0 && x >= 4 && x < 128 && x % 6 == 5) return 6;
  return 0;
}
constexpr int NEPI1 = 112, NEPI2 = 74;
struct G1Sched {
  int job[224], idx[224], epi[225], nst[224], cum[225], pos[7][64], cnt[7];
  constexpr G1Sched(int L) : job{}, idx{}, epi{}, nst{}, cum{}, pos{}, cnt{} {
    cnt[1] = NTOK0;
    for (int i = 0; i < NTOK0; ++i) pos[1][i] = i;
    cum[0] = NTOK0;
    for (int x = 0; x < 224; ++x) {
      int j = g1_job(L, x);
      if ((j == 3 && cnt[3] >= NRES) || (j == 4 && cnt[4] >= NEARLY) || (j == 6 && cnt[6] >= NFILL)) j = 0;
      job[x] = j; idx[x] = cnt[j];
      if (x == 161) epi[x] = NEPI1;
      const bool on = (x >= 97 && x < 160 && epi[x] < NEPI1) || x >= 161;
      epi[x + 1] = on ? epi[x] + (j ? 2 : 3) : epi[x];
      if (x < 160 && epi[x + 1] > NEPI1) epi[x + 1] = NEPI1;
      for (int n = epi[x]; n < epi[x + 1]; ++n)
        nst[x] += n < NEPI1 ? (n == 72 || n == 73 || n == 110 || n == 111) : (n < NEPI1 + NEPI2 && (n - NEPI1) % 37 == 36);
      if (j) pos[j][cnt[j]++] = cum[x] + nst[x];
      cum[x + 1] = cum[x] + nst[x] + (j ? 1 : 0);
    }
  }
};
constexpr G1Sched G1S[2] = {G1Sched(0), G1Sched(1)};
static_assert(G1S[0].cnt[1] == 32 && G1S[0].cnt[2] == 24 && G1S[0].cnt[3] == NRES && G1S[0].cnt[4] == NEARLY && G1S[0].cnt[5] == 4, "");
static_assert(G1S[0].idx[66] == 8 && G1S[0].idx[100] == 16 && G1S[0].cnt[6] == NFILL && G1S[0].pos[6][1] < G1S[0].pos[2][7], "");
static_assert(G1S[0].epi[150] == NEPI1 && G1S[0].epi[204] >= NEPI1 + NEPI2, "");
constexpr int clamp63(int n) { return n > 63 ? 63 : n; }
constexpr int g1_wait(int L, int x, int job, int idx) { return clamp63(G1S[L].cum[x] - G1S[L].pos[job][idx] - 1); }

#define FRESH()                                                                                                   \
  int tid = threadIdx.x, b = blockIdx.x, w = w_;                                                                   \
  u8* ws = P.ws;                                                                                                   \
  asm volatile("" : "+v"(tid), "+s"(b), "+s"(w), "+s"(ws));                                                        \
  const int lane = tid & 63, m = lane & 15, g = lane >> 4, half2 = b & 1, ct = b >> 5;                             \
  const int G = ((b >> 1) & 3) * 4 + ((b >> 3) & 3), member = half2 * 8 + ct, pos = G * 16 + member;               \
  const u32 l16 = lane * 16, t16 = tid * 16, xo = 32 * m + 8 * g;                                                  \
  auto fill_xn = [&](u32 base) {                                                                                   \
    const u32 o = base + (pos >> 1) * 16384 + (tid >> 3) * 256 + (pos & 1) * 128 + (tid & 7) * 16;                 \
    st4(sent4, o); st4(sent4, o + 8192);                                                                           \
  };                                                                                                               \
  auto fill_h = [&](u32 base, int q) {                                                                             \
    const int i = q * 64 + lane;                                                                                   \
    st4(sent4, base + (8 * G + 2 * w + (i >= 224)) * 57344 + member * 3584 + (i >= 224 ? i - 224 : i) * 16);       \
  };                                                                                                               \
  auto fill_tile = [&](u32 base) {                                                                                 \
    const u32 o = base + (128 * G + 64 * half2 + (tid >> 3)) * 1024 + ct * 128 + (tid & 7) * 16;                   \
    st4(sent4, o); st4(sent4, o + 32768);                                                                          \
  };                                                                                                               \
  auto fill_rows = [&](u32 base) { st4(sent4, base + pos * 8192 + t16); st4(sent4, base + pos * 8192 + 4096 + t16); }; \
  auto ring4 = [&](int q) {                                   \
    const u32 o = (q >> 1) * 32768 + w * 8192 + (q & 1) * 4096;                                                    \
    dma4(l16, ws + OFF_W1 + member * 229376 + o, ldsbase + o);                                                     \
  };                                                                                                               \
  (void)m; (void)g; (void)half2; (void)ct; (void)l16; (void)t16; (void)xo; (void)fill_xn; (void)fill_h; (void)fill_tile; (void)fill_rows; (void)ring4

__global__ __launch_bounds__(256, 1) void k_all(Params P) {
  extern __shared__ __attribute__((aligned(16))) u8 lds[];
  ST_DECL;
  const int w_ = __builtin_amdgcn_readfirstlane(threadIdx.x >> 6);
  const __amdgpu_buffer_rsrc_t rs = __builtin_amdgcn_make_buffer_rsrc(P.ws, 0, WS_USED, 0x00020000);
  const u32 ldsbase = (u32)(size_t)(__attribute__((address_space(3))) u8*)lds;
  const u32x4 sent4 = {SENT, SENT, SENT, SENT};
  auto st4 = [&](u32x4 v, u32 off) { __builtin_amdgcn_raw_buffer_store_b128(v, rs, off, 0, 16); };
  auto st2 = [&](u32x2 v, u32 off) { __builtin_amdgcn_raw_buffer_store_b64(v, rs, off, 0, 16); };
  u32x2 xl[2], xh[2], pv0[8], pv1[8];
  u32 wfr[8], wfi[8], wn[8];
  f32x2 xv[8];
  u32x2 ep;
  RowC rc;
  ST(0);
  {
    FRESH();
    const u8* xp = (const u8*)P.x + row_of_pos(pos, w) * 1024;
    ld2p<0>(xl[0], xo, xp); ld2p<512>(xl[1], xo, xp); ld2p<0>(xh[0], xo, xp + 1024 * 1024); ld2p<512>(xh[1], xo, xp + 1024 * 1024);
    { const u32 z = 0; ld2<0>(ep, z, ws); }
    fill_rows(OFF_Z); fill_rows(OFF_WX); fill_rows(OFF_U);
    if (tid < 8) st4(sent4, OFF_WNY + tid * 4096 + pos * 16);
    SB();
    const int kb = 2 * (16 * ((pos >> 6) & 1) + (pos & 15)) + (pos >> 7) + 256 * ((pos >> 4) & 3);
    static_for<8>([&](auto J) {
      constexpr int j = decltype(J)::value;
      const u32 o = tid * 2 + (kb + 64 * (16 * (j >> 2) + (j & 3))) * 514;
      ld1s(wfr[j], o, (const u8*)P.wr); ld1s(wfi[j], o, (const u8*)P.wi);
    });
    {
      const int k = kb + 64 * (16 * ((lane >> 2) & 1) + (lane & 3)), km = (2048 - k) & 2047;
      const u32 o = k * 514, om = km * 514;
      ld1s(wn[0], o, (const u8*)P.wr); ld1s(wn[1], o, (const u8*)P.wi); ld1s(wn[2], om, (const u8*)P.wr); ld1s(wn[3], om, (const u8*)P.wi);
      ld1s(wn[4], o, (const u8*)P.wr + 512); ld1s(wn[5], o, (const u8*)P.wi + 512); ld1s(wn[6], om, (const u8*)P.wr + 512); ld1s(wn[7], om, (const u8*)P.wi + 512);
    }
    SB();
    row_consts<false, 0>(rc, m, g);
    SB();
    prep_load(P, b * 4 + w, lane, pv0);
    SB();
    row_consts<false, 1>(rc, m, g);
    SB();
    if (b < 80) prep_load(P, b * 4 + w + 1024, lane, pv1);
    fill_xn(OFF_XN); fill_rows(OFF_Z + MB2);
#pragma unroll
    for (int q = 0; q < 7; ++q) fill_h(OFF_H, q);
    if (b < 80) vmwait<24 + 16 + 11>(); else vmwait<24 + 8 + 11>();
    bar_nw();
    PIN(ep); PIN(xl[0]); PIN(xl[1]); PIN(xh[0]); PIN(xh[1]);
    { const u64 e = (((u64)(u32)__builtin_amdgcn_readfirstlane(ep.y) << 32) | (u32)__builtin_amdgcn_readfirstlane(ep.x)) + 1; ep = u32x2{(u32)e, (u32)(e >> 32)}; }
    if (tid == 0) st2(ep, OFF_FLAG + pos * 8);
#pragma unroll
    for (int t = 0; t < 2; ++t)
#pragma unroll
      for (int p = 0; p < 2; ++p) {
        xv[4 * t + 2 * p] = f32x2{blo(xl[t][p]), blo(xh[t][p])};
        xv[4 * t + 2 * p + 1] = f32x2{bhi(xl[t][p]), bhi(xh[t][p])};
      }
  }
  ST(1);

  u32x4 w2a[10][4];

  auto layer = [&](auto LC) {
    constexpr int L = decltype(LC)::value;
    constexpr int S0 = 2 + 16 * L;
    {
      FRESH();
      const f32x2 s2 = ((xv[0] + xv[1]) + (xv[2] + xv[3])) + ((xv[4] + xv[5]) + (xv[6] + xv[7]));
      const float mean = reduce2(s2.x, s2.y) * (1.0f / 512.0f);
      const f32x2 M = bcast2(mean);
      f32x2 d[8];
#pragma unroll
      for (int i = 0; i < 8; ++i) d[i] = xv[i] - M;
      f32x2 q0 = d[0] * d[0], q1 = d[1] * d[1];
#pragma unroll
      for (int i = 2; i < 8; i += 2) { q0 += d[i] * d[i]; q1 += d[i + 1] * d[i + 1]; }
      q0 += q1;
      f32x2 R, O;
      ln_finish(mean, reduce2(q0.x, q0.y), R, O);
      float* img = (float*)(lds + LDS_SCR);
      u32* inp = (u32*)(img + w * 1280);
#pragma unroll
      for (int i = 0; i < 8; ++i) {
        const f32x2 v = d[i] * R + O;
        inp[(16 * (m & 1) + 4 * g + (i & 3)) * 20 + (m >> 1) + 8 * (i >> 2)] = pkh(v.x, v.y);
      }
      const u32x4 a0 = *(const u32x4*)(inp + m * 20 + 4 * g), a1 = *(const u32x4*)(inp + (16 + m) * 20 + 4 * g);
      const int ph = tid >> 1, fq = ph ? 512 - ph : 256, fq1 = 384 - ph;
      u32 ap0 = (ph + (ph >> 4) * 4) * 4, ap1 = ap0 + 640, ar0 = (fq + (fq >> 4) * 4) * 4, ar1 = (fq1 + (fq1 >> 4) * 4) * 4;
      PIN(ap0); PIN(ap1); PIN(ar0); PIN(ar1);
      const float* p0 = (const float*)((const u8*)img + ap0);
      const float* p1 = (const float*)((const u8*)img + ap1);
      const float* r0 = (const float*)((const u8*)img + ar0);
      const float* r1 = (const float*)((const u8*)img + ar1);
      const float sc = 1.0f / 64.0f, sg = (tid & 1) ? -sc : sc;
      f32x2 cA = {sc, sc}, cB = {sg, -sg}, cC = {sc, -sc}, cD = {sg, sg};
      f32x2 eA = ph ? cA : f32x2{2 * sc, 0.f}, eB = ph ? cB : f32x2{2 * sg, 0.f}, eC = ph ? cC : f32x2{0.f, 2 * sg}, eD = ph ? cD : f32x2{0.f, 2 * sc};
      u32 zo = OFF_Z + L * MB2 + pos * 8192 + t16;
      PIN(cB); PIN(cD); PIN(eA); PIN(eB); PIN(eC); PIN(eD); PIN(zo);
      f32x4 dr0, di0, dr1, di1;
      row_fft<false, 5>(rc, a0, a1, dr0, di0, dr1, di1);
      float* op = img + w * 1280 + 4 * g + 20 * m;
      *(f32x4*)(op) = dr0; *(f32x4*)(op + 640) = di0;
      SB();
      *(f32x4*)(op + 320) = dr1; *(f32x4*)(op + 960) = di1;
      bar();
      f32x2 pa[4], qa[4], pb[4], qb[4];
#pragma unroll
      for (int j = 0; j < 4; ++j) {
        pa[j] = f32x2{p0[j * 1280], p0[j * 1280 + 640]}; qa[j] = f32x2{r0[j * 1280], r0[j * 1280 + 640]};
      }
#pragma unroll
      for (int j = 0; j < 4; ++j) {
        pb[j] = f32x2{p1[j * 1280], p1[j * 1280 + 640]}; qb[j] = f32x2{r1[j * 1280], r1[j * 1280 + 640]};
      }
      SB();
      u32x4 o;
#pragma unroll
      for (int j = 0; j < 4; ++j) {
        const f32x2 v = eD * swp(qa[j]) + (eC * qa[j] + (eB * swp(pa[j]) + eA * pa[j]));
        o[j] = pkh(v.x, v.y);
      }
      st4(o, zo);
#pragma unroll
      for (int j = 0; j < 4; ++j) {
        const f32x2 v = cD * swp(qb[j]) + (cC * qb[j] + (cB * swp(pb[j]) + cA * pb[j]));
        o[j] = pkh(v.x, v.y);
      }
      st4(o, zo + 4096);
      SB();
      if constexpr (L == 1) {
#pragma unroll
        for (int t = 0; t < 2; ++t)
#pragma unroll
          for (int p = 0; p < 2; ++p) {
            xl[t][p] = pkb(xv[4 * t + 2 * p].x, xv[4 * t + 2 * p + 1].x); xh[t][p] = pkb(xv[4 * t + 2 * p].y, xv[4 * t + 2 * p + 1].y);
          }
        PIN(xl[0]); PIN(xl[1]); PIN(xh[0]); PIN(xh[1]);
      }
    }
    if constexpr (L == 0) {
      FRESH();
      if (b < 80) vmwait<3 + 16 + 11>(); else vmwait<3 + 8 + 11>();
#pragma unroll
      for (int j = 0; j < 8; ++j) { PIN(wfr[j]); PIN(wfi[j]); PIN(wn[j]); }
      u8* bw = lds + w * 2048;
      u32x4 v0, v1;
#pragma unroll
      for (int j = 0; j < 4; ++j) { v0[j] = wfr[j] | (wfi[j] << 16); v1[j] = wfr[4 + j] | (wfi[4 + j] << 16); }
      *(u32x4*)(bw + lane * 32) = v0; *(u32x4*)(bw + lane * 32 + 16) = v1;
      st4(*(const u32x4*)(bw + l16), OFF_WX + pos * 8192 + w * 2048 + l16);
      st4(*(const u32x4*)(bw + 1024 + l16), OFF_WX + pos * 8192 + w * 2048 + 1024 + l16);
      const float hr = blo(wn[0]) + blo(wn[2]), hi = blo(wn[1]) - blo(wn[3]), nr = blo(wn[4]) + blo(wn[6]), ni = blo(wn[5]) - blo(wn[7]);
      const f32x4 ab = {0.25f * (hr + nr), 0.25f * (hi + ni), 0.25f * (hr - nr), 0.25f * (hi - ni)};
      if (tid < 8) st4(__builtin_bit_cast(u32x4, ab), OFF_WNY + tid * 4096 + pos * 16);
    }
    ST(S0);
    {
      FRESH();
      const int phi = pos, pi_ = w >> 1, kt = w & 1;
      const int k1l = 2 * (16 * kt + m) + pi_;
      u32x4 b1re[2], b1im[2], a2re[2][2], a2im[2][2], b3re[2][2], b3im[2][2], a4re[2][2], a4im[2][2];
      f32x2 t1c[2][2], t1s[2][2], t2c[2][2], t2s[2][2];
      const float fm = (float)m, fg = (float)(4 * g);
      const u32 lm = (m & 1) ? 0x80008000u : 0u;
      auto consts = [&](auto PC) {
        constexpr int part = decltype(PC)::value;
        if constexpr (part == 0) {
          const float fk = (float)k1l;
#pragma unroll
          for (int u = 0; u < 2; ++u) {
#pragma unroll
            for (int r = 0; r < 4; ++r) {
              const float n = fg + (float)(16 * u + r), a = n * (fk * (1.0f / 64.0f)), a2 = n * (fk * (1.0f / 2048.0f));
              u32 x, y;
              pair_cs<false>(cosr(a), sinr(a), x, y); b1re[u][r] = x; b1im[u][r] = y;
              t1c[u][r >> 1][r & 1] = cosr(a2); t1s[u][r >> 1][r & 1] = sinr(a2);
            }
            PIN(b1re[u]); PIN(b1im[u]); PIN(t1c[u][0]); PIN(t1c[u][1]); PIN(t1s[u][0]); PIN(t1s[u][1]);
          }
        } else if constexpr (part == 1) {
          u32x4 fr, fi, ir, ii;
#pragma unroll
          for (int r = 0; r < 4; ++r) {
            const float a = (fg + (float)r) * (fm * (1.0f / 32.0f)), c = cosr(a), s = sinr(a);
            u32 x, y;
            pair_cs<false>(c, s, x, y); fr[r] = x; fi[r] = y;
            pair_cs<true>(c, s, x, y); ir[r] = x; ii[r] = y;
          }
          sign4(fr, lm, a2re[0][0], a2re[0][1], a2re[1][0], a2re[1][1]);
          sign4(fi, lm, a2im[0][0], a2im[0][1], a2im[1][0], a2im[1][1]);
          sign4<true>(ir, lm, b3re[0][0], b3re[0][1], b3re[1][0], b3re[1][1]);
          sign4<true>(ii, lm, b3im[0][0], b3im[0][1], b3im[1][0], b3im[1][1]);
        } else if constexpr (part == 2) {
          const float kb = (float)(32 * kt + pi_) + 2.0f * fg;
#pragma unroll
          for (int u = 0; u < 2; ++u) {
#pragma unroll
            for (int r = 0; r < 4; ++r) {
              const float a = (kb + (float)(2 * r)) * ((fm + (float)(16 * u)) * (-1.0f / 2048.0f));
              t2c[u][r >> 1][r & 1] = cosr(a); t2s[u][r >> 1][r & 1] = sinr(a);
            }
            PIN(t2c[u][0]); PIN(t2c[u][1]); PIN(t2s[u][0]); PIN(t2s[u][1]);
          }
        } else {
          const float fn = (fm + (float)(16 * (w >> 1))) * (1.0f / 64.0f);
#pragma unroll
          for (int u = 0; u < 2; ++u) {
#pragma unroll
            for (int r = 0; r < 4; ++r) {
              const float a = (2.0f * fg + (float)(2 * r + u)) * fn;
              u32 x, y;
              pair_cs<true>(cosr(a), sinr(a), x, y); a4re[u][0][r] = x; a4im[u][0][r] = y;
            }
            a4re[u][1] = a4re[u][0] ^ lm; a4im[u][1] = a4im[u][0] ^ lm;
            PINA(a4re[u][0]); PINA(a4re[u][1]); PINA(a4im[u][0]); PINA(a4im[u][1]);
          }
        }
        SB();
      };
      using I0 = std::integral_constant<int, 0>; using I1 = std::integral_constant<int, 1>;
      using I2 = std::integral_constant<int, 2>; using I3 = std::integral_constant<int, 3>;
      u8* const bw = lds + 16384 + w * 8192;
      if constexpr (L == 0) {
        u32x4 fl0, fl1;
        const u32 o = lane * 32;
        ld4<0>(fl0, o, ws + OFF_FLAG); ld4<16>(fl1, o, ws + OFF_FLAG);
        vmwait<2 + 4 + 11>();
        {
          u32x4 ot[4];
#pragma unroll
          for (int j = 0; j < 8; ++j) PIN(pv0[j]);
          transpose8x4(pv0, ot);
#pragma unroll
          for (int x = 0; x < 4; ++x) *(u32x4*)(bw + lane * 64 + ((x + lane) & 3) * 16) = ot[x];
          if (b < 80) {
#pragma unroll
            for (int j = 0; j < 8; ++j) PIN(pv1[j]);
            transpose8x4(pv1, ot);
#pragma unroll
            for (int x = 0; x < 4; ++x) *(u32x4*)(bw + 4096 + lane * 64 + ((x + lane) & 3) * 16) = ot[x];
          }
        }
        vmwait<0>(); PIN(fl0); PIN(fl1);
        RETRY(!(fl0.x == ep.x && fl0.y == ep.y && fl0.z == ep.x && fl0.w == ep.y && fl1.x == ep.x && fl1.y == ep.y && fl1.z == ep.x && fl1.w == ep.y), {
          CNT(0);
          ld4r<0>(fl0, o, ws + OFF_FLAG); ld4r<16>(fl1, o, ws + OFF_FLAG);
          vmwait<0>(); PIN(fl0); PIN(fl1);
        });
      } else {
        SB();
        consts(I0{});
        consts(I1{});
      }
      u32x4 z0, z1, wv[2];
      const u32 zo = (tid >> 1) * 8192 + (tid & 1) * 16;
      const u8* zb0 = ws + OFF_Z + L * MB2 + phi * 32;
      const u8* zb1 = zb0 + 128 * 8192;
      const u8* wb0 = L == 0 ? ws + OFF_WX + phi * 32 : ws + OFF_WP + phi * 8192;
      const u8* wb1 = L == 0 ? wb0 + 128 * 8192 : wb0 + 4096;
      const u32 wo = L == 0 ? zo : t16;
      ld4<0>(z0, zo, zb0); ld4<0>(z1, zo, zb1);
      ld4<0>(wv[0], wo, wb0); ld4<0>(wv[1], wo, wb1);
      SB();
      if constexpr (L == 0) consts(I0{}); else consts(I2{});
      u32x4 wa[8];
      if (phi == 0) static_for<8>([&](auto J) { constexpr int j = decltype(J)::value; ld4<0>(wa[j], t16, ws + OFF_WNY + j * 4096); });
      if constexpr (L == 0) {
        auto put = [&](int rep) {
#pragma unroll
          for (int x = 0; x < 4; ++x) {
            const int src = x * 16 + (lane >> 2);
            st4(*(const u32x4*)(bw + rep * 4096 + src * 64 + (((lane & 3) + src) & 3) * 16), prep_dst(b * 4 + w + 1024 * rep, 4 * x + g) + m * 16);
          }
        };
        put(0);
        SB();
        consts(I1{});
        if (b < 80) put(1);
        SB();
        consts(I2{});
        consts(I3{});
      } else {
        ring4(2);
        SB();
        consts(I3{});
      }
      u8* const zi = lds + LDS_SCR;
      u32 zw;
      {
        const int pp = tid >> 1;
        zw = (tid & 1) * 4096 + ((pp >> 6) & 1) * 2048 + (((pp >> 3) & 3) * 16 + 2 * (pp & 7)) * 16 + ((pp >> 5) & 1) * 8;
        PIN(zw);
      }
      u32 uo = OFF_U + L * MB2 + phi * 8192 + w * 2048 + l16;
      PIN(uo);
      SB();
      bar_nw();
      ST(S0 + 1);
      vmwait<4>();
      PIN(z0); PIN(z1); PIN(wv[0]); PIN(wv[1]);
      if constexpr (L == 0) {
        RETRY(max4(max4(max4(max4(0, z0), z1), wv[0]), wv[1]) == SENT, {
          CNT(1);
          if (bad_) { ld4r<0>(z0, zo, zb0); ld4r<0>(z1, zo, zb1); ld4r<0>(wv[0], wo, wb0); ld4r<0>(wv[1], wo, wb1); }
          vmwait<0>(); PIN(z0); PIN(z1); PIN(wv[0]); PIN(wv[1]);
        });
      } else {
        RETRY(max4(max4(0, z0), z1) == SENT, {
          CNT(2);
          if (bad_) { ld4r<0>(z0, zo, zb0); ld4r<0>(z1, zo, zb1); }
          vmwait<0>(); PIN(z0); PIN(z1);
        });
      }
      ST(S0 + 2);
      *(u32x2*)(zi + zw) = u32x2{z0.x, z0.y}; *(u32x2*)(zi + zw + 16) = u32x2{z0.z, z0.w};
      *(u32x2*)(zi + zw + 1024) = u32x2{z1.x, z1.y}; *(u32x2*)(zi + zw + 1024 + 16) = u32x2{z1.z, z1.w};
      if constexpr (L == 0) { *(u32x4*)(zi + 8192 + t16) = wv[0]; *(u32x4*)(zi + 12288 + t16) = wv[1]; }
      bar();
      const u8* zr = zi + pi_ * 4096 + l16;
      if constexpr (L == 1) ring4(3);
      const u32x4 f00 = *(const u32x4*)(zr), f01 = *(const u32x4*)(zr + 1024), f10 = *(const u32x4*)(zr + 2048), f11 = *(const u32x4*)(zr + 3072);
      if constexpr (L == 0) { wv[0] = *(const u32x4*)(zi + 8192 + tid * 32); wv[1] = *(const u32x4*)(zi + 8192 + tid * 32 + 16); }
      f32x4 d0, d1, d2, d3;
      mm8<false, 3>(d0, d1, d2, d3, f00, b1re[0], f01, b1re[1], f00, b1im[0], f01, b1im[1], f10, b1re[0], f11, b1re[1], f10, b1im[0], f11, b1im[1]);
      const u32x4 b20 = twiddle<false>(d0, d1, t1c[0], t1s[0]);
      SB();
      const u32x4 b21 = twiddle<false>(d2, d3, t1c[1], t1s[1]);
      f32x2 wr[2][2], wi[2][2];
      auto unpack_w = [&]() {
#pragma unroll
        for (int t = 0; t < 2; ++t)
#pragma unroll
          for (int h = 0; h < 2; ++h) {
            wr[t][h] = f32x2{blo(wv[t][2 * h]), blo(wv[t][2 * h + 1])}; wi[t][h] = f32x2{bhi(wv[t][2 * h]), bhi(wv[t][2 * h + 1])};
            PIN(wr[t][h]); PIN(wi[t][h]);
          }
      };
      mm8<false, 0>(d0, d1, d2, d3, a2re[0][0], b20, a2re[0][1], b21, a2im[0][0], b20, a2im[0][1], b21, a2re[1][0], b20, a2re[1][1], b21, a2im[1][0],
                    b20, a2im[1][1], b21);
      SB();
      unpack_w();
      SB();
      u32x4 a30, a31;
      if (phi != 0) {
        a30 = cmul_b(d0, d1, wr[0], wi[0]);
        a31 = cmul_b(d2, d3, wr[1], wi[1]);
      } else {
        u32 mx = 0;
#pragma unroll
        for (int j = 0; j < 8; ++j) { PIN(wa[j]); mx = max4(mx, wa[j]); }
        _Pragma("nounroll") for (int sp = 0; sp < SPIN_CAP && __any(mx == SENT); ++sp) {
          static_for<8>([&](auto J) { constexpr int j = decltype(J)::value; ld4r<0>(wa[j], t16, ws + OFF_WNY + j * 4096); });
          vmwait<0>();
          mx = 0;
#pragma unroll
          for (int j = 0; j < 8; ++j) { PIN(wa[j]); mx = max4(mx, wa[j]); }
        }
        float* pl = (float*)zi;
        bar_nw();
        float* pw = pl + k1l * 36 + 4 * g;
        *(f32x4*)(pw) = d0; *(f32x4*)(pw + 16) = d2; *(f32x4*)(pw + 2340) = d1; *(f32x4*)(pw + 2340 + 16) = d3;
        if (k1l == 0) {
#pragma unroll
          for (int r = 0; r < 4; ++r) {
            pl[64 * 36 + ((4 * g + r - 1) & 31)] = d0[r]; pl[64 * 36 + ((16 + 4 * g + r - 1) & 31)] = d2[r];
            pl[2340 + 64 * 36 + ((4 * g + r - 1) & 31)] = d1[r]; pl[2340 + 64 * 36 + ((16 + 4 * g + r - 1) & 31)] = d3[r];
          }
        }
        bar();
        const float* pr_ = pl + (64 - k1l) * 36 + 28 - 4 * g;
        const f32x4 qr0 = *(const f32x4*)(pr_), qr1 = *(const f32x4*)(pr_ - 16), qi0 = *(const f32x4*)(pr_ + 2340), qi1 = *(const f32x4*)(pr_ + 2340 - 16);
#pragma unroll
        for (int r = 0; r < 4; ++r) {
          const f32x4 c0 = __builtin_bit_cast(f32x4, wa[r]), c1 = __builtin_bit_cast(f32x4, wa[4 + r]);
          a30[r] = pkb(d0[r] * c0[0] - d1[r] * c0[1] + qr0[3 - r] * c0[2] + qi0[3 - r] * c0[3],
                       d0[r] * c0[1] + d1[r] * c0[0] + qr0[3 - r] * c0[3] - qi0[3 - r] * c0[2]);
          a31[r] = pkb(d2[r] * c1[0] - d3[r] * c1[1] + qr1[3 - r] * c1[2] + qi1[3 - r] * c1[3],
                       d2[r] * c1[1] + d3[r] * c1[0] + qr1[3 - r] * c1[3] - qi1[3 - r] * c1[2]);
        }
      }
      u8* ex = lds + LDS_SCR + 20480;
      mm8<true, 3, 2>(d0, d1, d2, d3, a30, b3re[0][0], a31, b3re[0][1], a30, b3im[0][0], a31, b3im[0][1], a30, b3re[1][0], a31, b3re[1][1], a30, b3im[1][0],
                   a31, b3im[1][1]);
      *(u32x4*)(ex + w * 1024 + l16) = twiddle<true>(d0, d1, t2c[0], t2s[0]);
      SB();
      *(u32x4*)(ex + (4 + w) * 1024 + l16) = twiddle<true>(d2, d3, t2c[1], t2s[1]);
      if constexpr (L == 0) vmwait<0>();
      bar();
      const u8* er = ex + (w & 1) * 4096 + l16;
      const u32x4 g0 = *(const u32x4*)(er), g1 = *(const u32x4*)(er + 1024), g2 = *(const u32x4*)(er + 2048), g3 = *(const u32x4*)(er + 3072);
      mm8<true, 7, 1>(d0, d1, d2, d3, a4re[0][0], g0, a4re[0][1], g1, a4im[0][0], g0, a4im[0][1], g1, a4re[1][0], g2, a4re[1][1], g3, a4im[1][0], g2,
                   a4im[1][1], g3);
      {
        const f32x2 lr0 = lo2(d0) + lo2(d2), li0 = lo2(d1) + lo2(d3), hr0 = lo2(d0) - lo2(d2), hi0 = lo2(d1) - lo2(d3);
        const f32x2 lr1 = hi2(d0) + hi2(d2), li1 = hi2(d1) + hi2(d3), hr1 = hi2(d0) - hi2(d2), hi1 = hi2(d1) - hi2(d3);
        if (phi != 0) {
          const f32x2 pr0 = lr0 - hi0, pi0 = li0 + hr0, qr0 = lr0 + hi0, qi0 = hr0 - li0;
          st4(u32x4{pkb(pr0.x, pi0.x), pkb(qr0.x, qi0.x), pkb(pr0.y, pi0.y), pkb(qr0.y, qi0.y)}, uo);
          const f32x2 pr1 = lr1 - hi1, pi1 = li1 + hr1, qr1 = lr1 + hi1, qi1 = hr1 - li1;
          st4(u32x4{pkb(pr1.x, pi1.x), pkb(qr1.x, qi1.x), pkb(pr1.y, pi1.y), pkb(qr1.y, qi1.y)}, uo + 1024);
          MEMFENCE();
        } else {
          st4(u32x4{pkb(lr0.x, hr0.x), pkb(li0.x, hi0.x), pkb(lr0.y, hr0.y), pkb(li0.y, hi0.y)}, uo);
          st4(u32x4{pkb(lr1.x, hr1.x), pkb(li1.x, hi1.x), pkb(lr1.y, hr1.y), pkb(li1.y, hi1.y)}, uo + 1024);
        }
      }
      if constexpr (L == 0) { st4(wv[0], OFF_WP + phi * 8192 + t16); st4(wv[1], OFF_WP + phi * 8192 + 4096 + t16); }
    }
    ST(S0 + 3);
    {
      FRESH();
      SB();
      row_consts<true, 0>(rc, m, g);
      f32x2 xa[4], xb[4];
#pragma unroll
      for (int i = 0; i < 4; ++i) {
        xa[i] = f32x2{blo(xl[i >> 1][i & 1]), bhi(xl[i >> 1][i & 1])}; xb[i] = f32x2{blo(xh[i >> 1][i & 1]), bhi(xh[i >> 1][i & 1])};
        PIN(xa[i]); PIN(xb[i]);
      }
      SB();
      u32x4 c0, c1;
      const u32 uo = (tid >> 1) * 8192 + (tid & 1) * 16;
      const u8* ub0 = ws + OFF_U + L * MB2 + pos * 32;
      const u8* ub1 = ub0 + 128 * 8192;
      ld4<0>(c0, uo, ub0); ld4<0>(c1, uo, ub1);
      SB();
      row_consts<true, 1>(rc, m, g);
      SB();
      if constexpr (L == 1) ring4(4);
      u32* img = (u32*)(lds + LDS_SCR);
      u32 ip0, iq0, iq1, xno, tw0, tw1, trd;
      {
        const int ph = tid >> 1, fq0 = ph ? 512 - ph : 256, fq1 = 384 - ph;
        ip0 = ((tid & 1) * 1280 + (ph & 31) * 20 + (ph >> 5)) * 4;
        iq0 = ((tid & 1) * 1280 + (fq0 & 31) * 20 + (fq0 >> 5)) * 4; iq1 = ((tid & 1) * 1280 + (fq1 & 31) * 20 + (fq1 >> 5)) * 4;
        xno = OFF_XN + L * MB2 + (pos >> 1) * 16384 + (tid >> 3) * 256 + (pos & 1) * 128 + (tid & 7) * 16;
        tw0 = (4 * m + g) * 128 + ((2 * w) ^ (m & 7)) * 16; tw1 = (4 * m + g) * 128 + ((2 * w + 1) ^ (m & 7)) * 16;
        trd = (tid >> 3) * 128 + ((tid & 7) ^ ((tid >> 5) & 7)) * 16;
        PIN(ip0); PIN(iq0); PIN(iq1); PIN(xno); PIN(tw0); PIN(tw1); PIN(trd);
      }
      SB();
      ST(S0 + 4);
      vmwait<(L == 0 ? 0 : 4)>(); PIN(c0); PIN(c1);
      {
        RETRY(max4(max4(0, c0), c1) == SENT, {
          CNT(3);
          if (bad_) { ld4r<0>(c0, uo, ub0); ld4r<0>(c1, uo, ub1); }
          vmwait<0>(); PIN(c0); PIN(c1);
        });
      }
      ST(S0 + 5);
      {
        u8* d = (u8*)img;
        *(u32*)(d + ip0) = c0.x; *(u32*)(d + iq0) = c0.y; *(u32*)(d + 2560 + ip0) = c0.z; *(u32*)(d + 2560 + iq0) = c0.w;
        *(u32*)(d + ip0 + 16) = c1.x; *(u32*)(d + iq1) = c1.y; *(u32*)(d + 2560 + ip0 + 16) = c1.z; *(u32*)(d + 2560 + iq1) = c1.w;
      }
      bar();
      const u32x4 a0 = *(const u32x4*)(img + w * 640 + m * 20 + 4 * g), a1 = *(const u32x4*)(img + w * 640 + (16 + m) * 20 + 4 * g);
      if constexpr (L == 1) ring4(5);
      if constexpr (L == 0) ring4(0);
      f32x4 dr0, di0, dr1, di1;
      row_fft<true, 7>(rc, a0, a1, dr0, di0, dr1, di1);
      if constexpr (L == 0) ring4(1);
      const float ys = 1.0f / 32768.0f;
      xa[0] += lo2(dr0) * ys; xa[1] += hi2(dr0) * ys; xa[2] += lo2(dr1) * ys; xa[3] += hi2(dr1) * ys;
      xb[0] += lo2(di0) * ys; xb[1] += hi2(di0) * ys; xb[2] += lo2(di1) * ys; xb[3] += hi2(di1) * ys;
      const f32x2 sa = (xa[0] + xa[1]) + (xa[2] + xa[3]), sb = (xb[0] + xb[1]) + (xb[2] + xb[3]);
      const float mean = reduce2(sa.x + sa.y, sb.x + sb.y) * (1.0f / 512.0f);
      const f32x2 M = bcast2(mean);
      f32x2 da[4], db[4];
#pragma unroll
      for (int i = 0; i < 4; ++i) { da[i] = xa[i] - M.x; db[i] = xb[i] - M.y; }
      f32x2 qa = da[0] * da[0], qb = db[0] * db[0];
#pragma unroll
      for (int i = 1; i < 4; ++i) { qa += da[i] * da[i]; qb += db[i] * db[i]; }
      if constexpr (L == 0) { SB(); ring4(2); SB(); }
      f32x2 R, O;
      ln_finish(mean, reduce2(qa.x + qa.y, qb.x + qb.y), R, O);
      u32x4 na, nb;
#pragma unroll
      for (int i = 0; i < 4; ++i) {
        const f32x2 va = da[i] * R.x + O.x, vb = db[i] * R.y + O.y;
        na[i] = pkb(va.x, va.y); nb[i] = pkb(vb.x, vb.y);
      }
      u8* tr = lds + LDS_SCR + 12288;
      *(u32x4*)(tr + tw0) = na; *(u32x4*)(tr + tw1) = nb;
      bar();
      st4(*(const u32x4*)(tr + trd), xno);
      st4(*(const u32x4*)(tr + 4096 + trd), xno + 8192);
      SB();
      const u32x4 rl = {pkb(xa[0].x, xa[0].y), pkb(xa[1].x, xa[1].y), pkb(xa[2].x, xa[2].y), pkb(xa[3].x, xa[3].y)};
      const u32x4 rh = {pkb(xb[0].x, xb[0].y), pkb(xb[1].x, xb[1].y), pkb(xb[2].x, xb[2].y), pkb(xb[3].x, xb[3].y)};
      if constexpr (L == 0) {
        *(u32x4*)(lds + LDS_STASH + t16) = rl; *(u32x4*)(lds + LDS_STASH + 4096 + t16) = rh;
        ring4(3); ring4(4); ring4(5); ring4(6); ring4(7);
      } else {
        const u32 o = OFF_X3 + (8 * pos + 2 * w) * 1024 + xo;
        st2(u32x2{rl.x, rl.y}, o); st2(u32x2{rl.z, rl.w}, o + 512);
        st2(u32x2{rh.x, rh.y}, o + 1024); st2(u32x2{rh.z, rh.w}, o + 1536);
        __builtin_amdgcn_s_sleep(6);
      }
    }
    ST(S0 + 6);
    {
      FRESH();
      u32x4 tk[32], he[32], hl[16], w2t[4], w2v[3][4];
      u32x2 hh[16];
      f32x4 ac1[6][2], ac2[4][2], ac3[4][2];
      const u8* hs0 = ws + OFF_H + L * H_BYTES + (8 * G + 4 * half2) * 57344 + (4 * w) * 3584 + 3584;
      const u8* hs2 = hs0 + 7168;
      const u8* w2src = ws + OFF_W2 + (ct * 4 + w) * 57344;
      PINS(hs0); PINS(hs2);
      u32 hv[4] = {l16, l16 + 57344, l16 + 2 * 57344, l16 + 3 * 57344}, h8[4];
#pragma unroll
      for (int u = 0; u < 4; ++u) { h8[u] = hv[u] - lane * 8; PIN(hv[u]); PIN(h8[u]); }
      auto hload = [&](auto RE, auto KC, auto UC) {
        constexpr int k = decltype(KC)::value, u = decltype(UC)::value;
        constexpr bool re = decltype(RE)::value;
        if constexpr (k == 4 || k == 5) {
          constexpr int o = (u >> 2) * 3584 + 3072 - 3584;
          if constexpr (re) ld2r<o>(hh[8 * (k - 4) + u], h8[u & 3], k == 5 ? hs2 : hs0); else ld2<o>(hh[8 * (k - 4) + u], h8[u & 3], k == 5 ? hs2 : hs0);
        } else {
          constexpr int j = k < 4 ? k : k < 10 ? k - 6 : k - 10, o = (j & 1) * 3584 + (k < 4 ? 0 : k < 10 ? 1024 : 2048) - 3584;
          u32x4& d = k < 10 ? he[(k < 4 ? 0 : 16) + 4 * j + u] : hl[4 * j + u];
          if constexpr (re) ld4r<o>(d, hv[u], j >> 1 ? hs2 : hs0); else ld4<o>(d, hv[u], j >> 1 ? hs2 : hs0);
        }
      };
      auto hseq = [&](auto IC) {
        constexpr int i = decltype(IC)::value, k = i < 16 ? i >> 2 : i < 32 ? 4 + ((i - 16) >> 3) : 6 + ((i - 32) >> 2);
        hload(std::false_type{}, std::integral_constant<int, k>{}, std::integral_constant<int, (i < 16 || i >= 32) ? (i & 3) : (i & 7)>{});
      };
      {
        const u8* w1b = ws + OFF_W1 + member * 229376 + 131072 + w * 8192;
        const u8* tk0 = ws + OFF_XN + L * MB2 + (8 * G + 2 * w) * 16384 + 4096;
        const u8* tk1 = tk0 + 8192;
        u32 tv1 = l16 + 16384, lhi = l16 + 65536;
        u32 hd0 = OFF_H + L * H_BYTES + (8 * G + 2 * w) * 57344 + member * 3584 + l16, hd1 = hd0 + 57344;
        u32 hd2 = hd0 + 3072 - lane * 8, hd3 = hd2 + 57344;
        PIN(tv1); PIN(lhi); PIN(hd0); PIN(hd1); PIN(hd2); PIN(hd3); PINS(tk0); PINS(tk1);
        u32 fo_ = pos * 8192 + t16, fdl = OFF_DL + (128 * G + 64 * half2 + (tid >> 3)) * 1024 + ct * 128 + (tid & 7) * 16;
        u32 fxn = OFF_XN + MB2 + (pos >> 1) * 16384 + (tid >> 3) * 256 + (pos & 1) * 128 + (tid & 7) * 16;
        u32 fhh = hd0 - l16 + H_BYTES + 3072 + (lane >> 5) * 57344 + (lane & 31) * 16;
        if constexpr (L == 0) { PIN(fo_); PIN(fdl); PIN(fxn); PIN(fhh); }
        auto tok = [&](auto RE, auto QC) {
          constexpr int q = decltype(QC)::value, ks = q >> 1;
          if constexpr (decltype(RE)::value) ld4r<(ks & 7) * 1024 - 4096>(tk[q], q & 1 ? tv1 : l16, ks >> 3 ? tk1 : tk0);
          else ld4<(ks & 7) * 1024 - 4096>(tk[q], q & 1 ? tv1 : l16, ks >> 3 ? tk1 : tk0);
        };
        u32 mx0 = 0, mx1 = 0;
        auto fix = [&](auto QC, u64 bad) {
          constexpr int q0 = decltype(QC)::value;
          if (__builtin_expect(bad != 0, 0)) {
            _Pragma("nounroll") for (int sp = 0; sp < SPIN_CAP; ++sp) {
              CNT(4);
              static_for<4>([&](auto I) { tok(std::true_type{}, std::integral_constant<int, q0 + decltype(I)::value>{}); });
              vmwait<0>();
              u32 m2 = 0;
#pragma unroll
              for (int q = q0; q < q0 + 4; ++q) { PIN(tk[q]); m2 = max4(m2, tk[q]); }
              if (!__any(m2 == SENT)) break;
            }
          }
        };
        static_for<NTOK0>([&](auto Q) { tok(std::false_type{}, Q); });
        constexpr int PD = 8;
        u32x4 af[PD];
        auto frag = [&](int x) {
          const int o = (x & 127) * 1024;
          return o < 65536 ? *(const u32x4*)(lds + l16 + o) : *(const u32x4*)(lds + lhi + (o - 65536));
        };
        vmwait<NTOK0>();
        bar_nw();
#pragma unroll
        for (int x = 0; x < PD - 1; ++x) af[x] = frag(x);
        vmwait<NTOK0 - 4>();
        ST(S0 + 7);
#pragma unroll
        for (int q = 0; q < 4; ++q) PIN(tk[q]);
        mx0 = max4(max4(0, tk[0]), tk[1]); mx1 = max4(max4(0, tk[2]), tk[3]);
        fix(std::integral_constant<int, 0>{}, __ballot(umax(mx0, mx1) == SENT));
        SB();
        ST(S0 + 8);
        float eg[2][2], eu[2][2];
        u64 bm = 0;
        const u32 fo = fo_;
        u32x4 eo[2];
        auto epi = [&](auto NC) {
          constexpr int n = decltype(NC)::value;
          if constexpr (n < NEPI1 + NEPI2) {
            constexpr bool p1 = n < NEPI1;
            constexpr int i = p1 ? (n < 72 ? n : n - 2) : (n - NEPI1) - (n - NEPI1) / 37;
            if constexpr (p1 && (n == 72 || n == 73)) st4(eo[n - 72], n == 72 ? hd0 : hd1);
            else if constexpr (p1 && n >= 110) st2(u32x2{eo[n - 110].x, eo[n - 110].y}, n == 110 ? hd2 : hd3);
            else if constexpr (!p1 && (n - NEPI1) % 37 == 36) st4(eo[0], ((n - NEPI1) / 37 ? hd1 : hd0) + 1024);
            else {
              constexpr int s = i & 1, st = (i % 18) >> 1, blk = i / 18;
              constexpr int u = p1 ? (blk < 4 ? blk >> 1 : blk - 4) : blk >> 1, pr = p1 ? (blk < 4 ? blk & 1 : 2) : blk & 1;
              constexpr int ei = p1 ? u : 0, d = 2 * (pr & 1) + s;
              const f32x4& ga = p1 ? ac1[2 * pr][u] : ac2[2 * (pr & 1)][u];
              const f32x4& up = p1 ? ac1[2 * pr + 1][u] : ac2[2 * (pr & 1) + 1][u];
              if constexpr (st < 2) { eg[s][st] = ga[2 * s + st]; PIN(eg[s][st]); }
              else if constexpr (st < 4) { eu[s][st - 2] = up[2 * s + st - 2]; PIN(eu[s][st - 2]); }
              else if constexpr (st < 6) { eg[s][st - 4] = relu(eg[s][st - 4]); PIN(eg[s][st - 4]); }
              else if constexpr (st < 8) { eg[s][st - 6] *= eu[s][st - 6]; PIN(eg[s][st - 6]); }
              else { u32 v = pkb(eg[s][0], eg[s][1]); PIN(v); eo[ei][d] = v; }
            }
            SB();
          }
        };
        static_for<224>([&](auto XC) {
          constexpr int x = decltype(XC)::value, job = G1S[L].job[x], ji = G1S[L].idx[x], e0 = G1S[L].epi[x], e1 = G1S[L].epi[x + 1];
          if constexpr (x == 32 || x == 64) bar_nw();
          if constexpr (x == 97) { vmwait<g1_wait(L, x, 2, 7)>(); bar_nw(); }
          if constexpr (x == 150) { vmwait<g1_wait(L, x, 2, 23)>(); bar_nw(); }
          constexpr int cj = (x + 12) / 12, co = x + 12 - 12 * cj;
          constexpr bool chk = x < 84 && co >= 5;
          constexpr int ks = x < 96 ? x / 6 : x < 160 ? (x - 96) >> 2 : (x - 160) >> 2, t = x < 96 ? x % 6 : x & 3;
          auto mm = [&](int u) {
            f32x4& a = x < 96 ? ac1[x < 96 ? t : 0][u] : x < 160 ? ac2[x < 96 ? 0 : t][u] : ac3[x < 96 ? 0 : t][u];
            if constexpr (ks == 0) a = mfma_b(af[x % PD], tk[u], f32x4{0.f, 0.f, 0.f, 0.f}); else a = mfma_b(af[x % PD], tk[2 * ks + u], a);
          };
          SB();
          mm(0);
          SB();
          if constexpr (x < 223) __builtin_amdgcn_s_waitcnt(0xC07F | ((x + PD - 2 < 224 ? PD - 3 : 222 - x) << 8));
          if constexpr (chk && co == 5) {
            vmwait<g1_wait(L, x, 1, 4 * cj + 3)>();
            PIN(tk[4 * cj]); PIN(tk[4 * cj + 1]); PIN(tk[4 * cj + 2]); PIN(tk[4 * cj + 3]);
          }
          SB();
          if constexpr (chk && co >= 6 && co < 10) {
            const u32x4 v = tk[4 * cj + co - 6];
            mx0 = co == 6 ? umax(v.x, v.y) : max3(mx0, v.x, v.y); PIN(mx0); SB();
            mx1 = co == 6 ? umax(v.z, v.w) : max3(mx1, v.z, v.w); PIN(mx1);
          }
          if constexpr (chk && co == 10) { mx0 = umax(mx0, mx1); PIN(mx0); SB(); bm = __ballot(mx0 == SENT); PINS(bm); }
          if constexpr (e0 < e1) epi(std::integral_constant<int, e0>{});
          if constexpr (e0 + 1 < e1) epi(std::integral_constant<int, e0 + 1>{});
          SB();
          mm(1);
          SB();
          if constexpr (chk && co == 11) fix(std::integral_constant<int, 4 * cj>{}, bm);
          if constexpr (job == 2 && (ji & 3) == 0) set_m0(ldsbase + (ji >> 3) * 32768 + w * 8192 + (ji & 4) * 1024);
          SB();
          if constexpr (x + PD - 1 < 224) af[(x + PD - 1) % PD] = frag(x + PD - 1);
          SB();
          if constexpr (job == 1) tok(std::false_type{}, std::integral_constant<int, ji>{});
          if constexpr (job == 2) dma1<(ji & 3) * 1024>(l16, w1b + (ji >> 3) * 32768 + (ji & 4) * 1024);
          if constexpr (job == 3) ld4a<(ji & 7) * 1024 - 4096>(w2a[ji >> 2][ji & 3], l16, w2src + (ji >> 3) * 8192 + 4096);
          if constexpr (job == 5) ld4a<ji * 1024>(w2t[ji], l16, w2src + 40960);
          if constexpr (job == 6) {
            if constexpr (ji < 2) st4(sent4, fdl + ji * 32768);
            else if constexpr (ji < 4) st4(sent4, fo + OFF_U + MB2 + (ji - 2) * 4096);
            else if constexpr (ji < 6) st4(sent4, fo + OFF_X3 + (ji - 4) * 4096);
            else if constexpr (ji < 8) st4(sent4, fxn + (ji - 6) * 8192);
            else if constexpr (ji < 11) st4(sent4, hd0 + H_BYTES + (ji - 8) * 1024);
            else if constexpr (ji < 14) st4(sent4, hd1 + H_BYTES + (ji - 11) * 1024);
            else st4(sent4, fhh);
          }
          if constexpr (job == 4) hseq(std::integral_constant<int, ji>{});
          if constexpr (e0 + 2 < e1) epi(std::integral_constant<int, e0 + 2>{});
          SB();
        });
        ST(S0 + 9);
#pragma unroll
        for (int u = 0; u < 2; ++u) {
          u32x4 o;
#pragma unroll
          for (int p = 0; p < 2; ++p) {
            const f32x4 ga = ac3[2 * p][u], up = ac3[2 * p + 1][u];
            const f32x2 a = f32x2{relu(ga[0]), relu(ga[1])} * lo2(up), c = f32x2{relu(ga[2]), relu(ga[3])} * hi2(up);
            o[2 * p] = pkb(a.x, a.y); o[2 * p + 1] = pkb(c.x, c.y);
          }
          st4(o, (u ? hd1 : hd0) + 2048);
        }
        SB();
      }
      ST(S0 + 10);
      {
        f32x4 acc[4][4];
        u32x4 x3a, x3b;
        const int tokl = 16 * w + (lane >> 3);
        u32 eo = (128 * G + 64 * half2 + tokl) * 1024 + ct * 128 + (lane & 7) * 16;
        PIN(eo);
        const u8* w2x = w2src + 45056 + 4096;
        const u8* x3s = ws + OFF_X3;
        u32 lx4 = l16 + 4096, eo2 = eo + 8192;
        u64 bm = 0;
        PINS(w2x); PINS(x3s); PIN(lx4); PIN(eo2);
        constexpr int NX = L == 1 ? 1 : 0, LB = 16;
        auto cum = [](int n) constexpr { int c = 0; for (int i = 0; i < n; ++i) c += i < 36 ? 1 : i < 38 ? NX : 0; return c; };
        auto young = [cum](int k, int n) constexpr {
          if (k < 8) return cum(n) + 2 + G1S[L].cum[224] - G1S[L].pos[4][k < 4 ? 4 * k + 3 : k < 6 ? 16 + 8 * (k - 4) + 7 : 32 + 4 * (k - 6) + 3] - 1;
          if (k < 10) return cum(n) - cum(4 * (k - 8) + 4);
          return cum(n) - cum(k == 13 ? 36 : LB + 4 * (k - 10) + 4);
        };
        u32 mx0 = 0, mx1 = 0;
        auto pin_k = [&](auto KC) {
          constexpr int k = decltype(KC)::value;
#pragma unroll
          for (int u = 0; u < 4; ++u) {
            if constexpr (k < 4) PIN(he[4 * k + u]);
            else if constexpr (k < 6) { PIN(hh[8 * (k - 4) + u]); PIN(hh[8 * (k - 4) + 4 + u]); }
            else if constexpr (k < 10) PIN(he[16 + 4 * (k - 6) + u]);
            else { PIN(hl[4 * (k - 10) + u]); if constexpr (k > 10) PIN(w2v[k - 11][u]); }
          }
        };
        auto opnd = [&](auto KC, int u) {
          constexpr int k = decltype(KC)::value;
          if constexpr (k < 4) return he[4 * k + u];
          else if constexpr (k < 6) return u32x4{hh[8 * (k - 4) + u].x, hh[8 * (k - 4) + u].y, hh[8 * (k - 4) + 4 + u].x, hh[8 * (k - 4) + 4 + u].y};
          else if constexpr (k < 10) return he[16 + 4 * (k - 6) + u];
          else return hl[4 * (k - 10) + u];
        };
        auto fixk = [&](auto KC, u64 bad) {
          constexpr int k = decltype(KC)::value;
          if (__builtin_expect(bad != 0, 0)) {
            _Pragma("nounroll") for (int sp = 0; sp < SPIN_CAP; ++sp) {
              CNT(k < 10 ? 5 : 6);
              static_for<(k == 4 || k == 5) ? 8 : 4>([&](auto U) { hload(std::true_type{}, KC, U); });
              vmwait<0>();
              pin_k(KC);
              if (!__any(max4(max4(max4(max4(0, opnd(KC, 0)), opnd(KC, 1)), opnd(KC, 2)), opnd(KC, 3)) == SENT)) break;
            }
          }
        };
        u32 rw[4];
#pragma unroll
        for (int c = 0; c < 4; ++c) { rw[c] = w * 16384 + c * 4096 + (g * 16 + (m ^ (2 * (2 * c + (g >> 1))))) * 16; PIN(rw[c]); }
        u32 oo[2] = {0, 0};
        if constexpr (L == 1) {
#pragma unroll
          for (int a = 0; a < 2; ++a) {
            const int tok = 128 * G + 64 * half2 + tokl + 8 * a;
            oo[a] = (row_of_pos(tok >> 3, (tok >> 1) & 3) + 1024 * (tok & 1)) * 1024 + ct * 128 + (lane & 7) * 16;
            PIN(oo[a]);
          }
        }
        vmwait<clamp63(young(0, 0))>();
        if constexpr (L == 0) {
#pragma unroll
          for (int k = 0; k < 10; ++k) { PINA(w2a[k][0]); PINA(w2a[k][1]); PINA(w2a[k][2]); PINA(w2a[k][3]); }
        }
        PINA(w2t[0]); PINA(w2t[1]); PINA(w2t[2]); PINA(w2t[3]);
        pin_k(std::integral_constant<int, 0>{});
        mx0 = max4(max4(0, he[0]), he[1]); mx1 = max4(max4(0, he[2]), he[3]);
        fixk(std::integral_constant<int, 0>{}, __ballot(umax(mx0, mx1) == SENT));
        ST(S0 + 11);
        auto wfr = [&](auto KC, int c) {
          constexpr int k = decltype(KC)::value;
          if constexpr (k < 10) return w2a[k][c]; else if constexpr (k == 10) return w2t[c]; else return w2v[k - 11][c];
        };
        static_for<48>([&](auto NC) {
          constexpr int n = decltype(NC)::value, k = n >> 2, c = n & 3;
          using KC = std::integral_constant<int, k>;
          using KN = std::integral_constant<int, k + 1>;
          static_for<4>([&](auto UC) {
            constexpr int u = decltype(UC)::value;
            SB();
            if constexpr (k == 0) acc[c][u] = mfma_b(wfr(KC{}, c), opnd(KC{}, u), f32x4{0.f, 0.f, 0.f, 0.f});
            else acc[c][u] = mfma_b(wfr(KC{}, c), opnd(KC{}, u), acc[c][u]);
            SB();
            if constexpr (c == 1 && u == 0) {
              vmwait<clamp63(young(k == 11 ? 13 : k + 1, n))>(); pin_k(KN{});
              if constexpr (k == 11) pin_k(std::integral_constant<int, 13>{});
            }
            if constexpr ((c == 1 && (u == 1 || u == 2)) || (c == 2 && u < (k == 11 ? 4 : 2)) || (k == 11 && c == 3 && u < 2)) {
              constexpr int j = c == 1 ? u - 1 : c == 2 ? 2 + u : 6 + u;
              const u32x4 v = j < 4 ? opnd(KN{}, j & 3) : opnd(std::integral_constant<int, 13>{}, j & 3);
              mx0 = j == 0 ? umax(v.x, v.y) : max3(mx0, v.x, v.y); PIN(mx0); SB();
              mx1 = j == 0 ? umax(v.z, v.w) : max3(mx1, v.z, v.w); PIN(mx1);
            }
            if constexpr (c == 3 && u == (k == 11 ? 2 : 0)) { mx0 = umax(mx0, mx1); PIN(mx0); SB(); bm = __ballot(mx0 == SENT); PINS(bm); }
            if constexpr (u == 3) {
              if constexpr (n < 8) hseq(std::integral_constant<int, NEARLY + n>{});
              else if constexpr (n < 16) ld4<(n - 8) * 1024 - 4096>(w2v[(n - 8) >> 2][n & 3], l16, w2x);
              else if constexpr (n < 32) hload(std::false_type{}, std::integral_constant<int, 10 + ((n - LB) >> 2)>{}, std::integral_constant<int, (n - LB) & 3>{});
              else if constexpr (n < 36) ld4<(n - 32) * 1024>(w2v[2][n & 3], lx4, w2x);
              else if constexpr (n < 38) { if constexpr (L == 1) ld4<0>(n == 36 ? x3a : x3b, n == 36 ? eo : eo2, x3s); }
              if constexpr (c == 3) { fixk(KN{}, bm); if constexpr (k == 11) fixk(std::integral_constant<int, 13>{}, bm); }
            }
          });
        });
        bar_nw();
        static_for<20>([&](auto TC) {
          constexpr int t = decltype(TC)::value;
          if constexpr (t < 16) {
            SB();
            acc[t >> 2][t & 3] = mfma_b(w2v[1][t >> 2], hl[8 + (t & 3)], acc[t >> 2][t & 3]);
            acc[t >> 2][t & 3] = mfma_b(w2v[2][t >> 2], hl[12 + (t & 3)], acc[t >> 2][t & 3]);
            SB();
          }
          if constexpr (t >= 4) *(f32x4*)(lds + rw[(t - 4) >> 2] + ((t - 4) & 3) * 1024) = acc[(t - 4) >> 2][(t - 4) & 3];
        });
        SB();
        ST(S0 + 12);
        const int o8 = lane & 7;
        const u8* rp0 = lds + ((o8 >> 1) * 4 + w) * 1024 + (2 * (o8 & 1) * 16 + ((lane >> 3) ^ (2 * o8))) * 16;
        const u8* rp1 = lds + ((o8 >> 1) * 4 + w) * 1024 + (2 * (o8 & 1) * 16 + ((8 + (lane >> 3)) ^ (2 * o8))) * 16;
        bar();
        f32x4 s[2][2][4];
#pragma unroll
        for (int p = 0; p < 4; ++p) { s[0][0][p] = *(const f32x4*)(rp0 + p * 16384); s[0][1][p] = *(const f32x4*)(rp0 + p * 16384 + 256); }
#pragma unroll
        for (int p = 0; p < 4; ++p) { s[1][0][p] = *(const f32x4*)(rp1 + p * 16384); s[1][1][p] = *(const f32x4*)(rp1 + p * 16384 + 256); }
        SB();
        if constexpr (L == 1) {
          vmwait<0>(); PIN(x3a); PIN(x3b);
          RETRY(max4(max4(0, x3a), x3b) == SENT, {
            ld4r<0>(x3a, eo, ws + OFF_X3); ld4r<0>(x3b, eo, ws + OFF_X3 + 8192);
            vmwait<0>(); PIN(x3a); PIN(x3b);
          });
        }
#pragma unroll
        for (int a = 0; a < 2; ++a) {
          const f32x4 s0 = (s[a][0][0] + s[a][0][1]) + (s[a][0][2] + s[a][0][3]), s1 = (s[a][1][0] + s[a][1][1]) + (s[a][1][2] + s[a][1][3]);
          const u32x4 y = {pkb(s0[0], s0[1]), pkb(s0[2], s0[3]), pkb(s1[0], s1[1]), pkb(s1[2], s1[3])};
          if constexpr (L == 0) {
            st4(y, OFF_DL + eo + a * 8192);
          } else {
            const u32x4 xr = a ? x3b : x3a;
            u32x4 o;
#pragma unroll
            for (int p = 0; p < 4; ++p) o[p] = pkb(blo(xr[p]) + blo(y[p]), bhi(xr[p]) + bhi(y[p]));
            *(u32x4*)((u8*)P.out + oo[a]) = o;
          }
        }
      }
    }
    ST(S0 + 13);
    if constexpr (L == 0) {
      FRESH();
      SB();
      row_consts<false, 0>(rc, m, g);
      SB();
      u32x2 d0, d1, d2, d3;
      const u8* db = ws + OFF_DL + (8 * pos + 2 * w) * 1024;
      const u32x4 sl = *(const u32x4*)(lds + LDS_STASH + t16), sh = *(const u32x4*)(lds + LDS_STASH + 4096 + t16);
#pragma unroll
      for (int i = 0; i < 4; ++i) {
        xv[2 * i] = f32x2{blo(sl[i]), blo(sh[i])}; xv[2 * i + 1] = f32x2{bhi(sl[i]), bhi(sh[i])};
        PIN(xv[2 * i]); PIN(xv[2 * i + 1]);
      }
      SB();
      bar_nw();
      ld2<0>(d0, xo, db); ld2<512>(d1, xo, db); ld2<1024>(d2, xo, db); ld2<1536>(d3, xo, db);
      ring4(0);
      SB();
      row_consts<false, 1>(rc, m, g);
      SB();
      ring4(1);
      ST(S0 + 14);
      vmwait<8>(); PIN(d0); PIN(d1); PIN(d2); PIN(d3);
      RETRY(max3(max3(max3(max3(0, d0.x, d0.y), d1.x, d1.y), d2.x, d2.y), d3.x, d3.y) == SENT, {
        CNT(7);
        if (bad_) { ld2r<0>(d0, xo, db); ld2r<512>(d1, xo, db); ld2r<1024>(d2, xo, db); ld2r<1536>(d3, xo, db); }
        vmwait<0>(); PIN(d0); PIN(d1); PIN(d2); PIN(d3);
      });
      xv[0] += f32x2{blo(d0.x), blo(d2.x)}; xv[1] += f32x2{bhi(d0.x), bhi(d2.x)}; xv[2] += f32x2{blo(d0.y), blo(d2.y)}; xv[3] += f32x2{bhi(d0.y), bhi(d2.y)};
      xv[4] += f32x2{blo(d1.x), blo(d3.x)}; xv[5] += f32x2{bhi(d1.x), bhi(d3.x)}; xv[6] += f32x2{blo(d1.y), blo(d3.y)}; xv[7] += f32x2{bhi(d1.y), bhi(d3.y)};
      ST(S0 + 15);
    }
  };
  layer(std::integral_constant<int, 0>{});
  layer(std::integral_constant<int, 1>{});
  if (blockIdx.x == 0 && threadIdx.x == 0) st2(ep, OFF_E);
  ST_OUT();
}

extern "C" void kernel_launch(void* const* d_in, const int* in_sizes, int n_in, void* d_out, int out_size, void* d_ws, size_t ws_size,
                              hipStream_t stream) {
  Params p;
  p.x = (const u16*)d_in[0]; p.wr = (const u16*)d_in[1]; p.wi = (const u16*)d_in[2];
  p.wg = (const u16*)d_in[4]; p.wu = (const u16*)d_in[5]; p.wd = (const u16*)d_in[6];
  p.out = (u16*)d_out; p.ws = (u8*)d_ws;
  (void)hipFuncSetAttribute((const void*)k_all, hipFuncAttributeMaxDynamicSharedMemorySize, LDS_TOTAL);
  k_all<<<256, 256, LDS_TOTAL, stream>>>(p);
}
```

```cpp
#include <hip/hip_runtime.h>
#include <hip/hip_bf16.h>
#include <stdint.h>
#include <type_traits>
#include <utility>

typedef unsigned char u8;
typedef unsigned short u16;
typedef unsigned int u32;
typedef unsigned long long u64;
typedef u32 u32x2 __attribute__((ext_vector_type(2)));
typedef u32 u32x4 __attribute__((ext_vector_type(4)));
typedef float f32x2 __attribute__((ext_vector_type(2)));
typedef float f32x4 __attribute__((ext_vector_type(4)));
typedef _Float16 f16x2 __attribute__((ext_vector_type(2)));
typedef __bf16 bf16x2 __attribute__((ext_vector_type(2)));
typedef __bf16 bf16x8 __attribute__((ext_vector_type(8)));
#define DI __device__ __forceinline__
#define SB() __builtin_amdgcn_sched_barrier(0)
#define MEMFENCE() asm volatile("" ::: "memory")

constexpr u32 MB2 = 2u << 20;
constexpr u32 OFF_E = 0, OFF_FLAG = 4096, OFF_WNY = 65536;
constexpr u32 OFF_Z = MB2;
constexpr u32 OFF_U = OFF_Z + 2 * MB2;
constexpr u32 OFF_XN = OFF_U + 2 * MB2;
constexpr u32 OFF_WX = OFF_XN + 2 * MB2;
constexpr u32 OFF_DL = OFF_WX + MB2;
constexpr u32 OFF_X3 = OFF_DL + MB2;
constexpr u32 H_BYTES = 7340032;
constexpr u32 OFF_H = OFF_X3 + MB2;
constexpr u32 OFF_W1 = OFF_H + 2 * H_BYTES;
constexpr u32 OFF_W2 = OFF_W1 + 3670016;
constexpr u32 OFF_WP = OFF_W2 + 1835008;
constexpr u32 WS_USED = OFF_WP + MB2;
constexpr u32 SENT = 0xFFFFFFFFu;
constexpr int SPIN_CAP = 1 << 15;
constexpr u32 CODE_BYTES = 64 * 1024;

constexpr int LDS_SCR = 131072, LDS_STASH = LDS_SCR + 24576, LDS_TOTAL = 163840;

struct Params {
  const u16* x; const u16* wr; const u16* wi; const u16* wg; const u16* wu; const u16* wd; u16* out; u8* ws;
};

#define ST(i)
#define ST_DECL
#define CNT(i)
#define ST_OUT()

DI u32 pkh(float a, float b) { f32x2 v = {a, b}; return __builtin_bit_cast(u32, __builtin_convertvector(v, f16x2)); }
DI u32 pkb(float a, float b) { f32x2 v = {a, b}; return __builtin_bit_cast(u32, __builtin_convertvector(v, bf16x2)); }
template <bool BF> DI u32 pkc(float a, float b) { return BF ? pkb(a, b) : pkh(a, b); }
DI u32 pkh_n0(float a, float b, u32 after) { u32 r; asm("v_cvt_pk_f16_f32 %0, -%1, %2" : "=v"(r) : "v"(a), "v"(b), "v"(after)); return r; }
DI u32 pkb_n1(float a, float b, u32 after) { u32 r; asm("v_cvt_pk_bf16_f32 %0, %1, -%2" : "=v"(r) : "v"(a), "v"(b), "v"(after)); return r; }
DI float blo(u32 u) { return __uint_as_float(u << 16); }
DI float bhi(u32 u) { return __uint_as_float(u & 0xffff0000u); }
DI float bfr(float a) { return blo(pkb(a, a)); }
DI float relu(float a) { const int i = __float_as_int(a); return __int_as_float(i > 0 ? i : 0); }
DI u32 umax(u32 a, u32 b) { return a > b ? a : b; }
DI u32 max3(u32 a, u32 b, u32 c) { return umax(umax(a, b), c); }
DI u32 max4(u32 acc, u32x4 v) { return max3(max3(acc, v.x, v.y), v.z, v.w); }
DI f32x2 lo2(f32x4 v) { return __builtin_shufflevector(v, v, 0, 1); }
DI f32x2 hi2(f32x4 v) { return __builtin_shufflevector(v, v, 2, 3); }
DI f32x2 swp(f32x2 v) { return __builtin_shufflevector(v, v, 1, 0); }
DI f32x4 mfma_b(u32x4 a, u32x4 b, f32x4 c) {
  return __builtin_amdgcn_mfma_f32_16x16x32_bf16(__builtin_bit_cast(bf16x8, a), __builtin_bit_cast(bf16x8, b), c, 0, 0, 0);
}
template <class T> DI void PIN(T& x) { asm volatile("" : "+v"(x)); }
template <class T> DI void PINS(T& x) { asm volatile("" : "+s"(x)); }
template <class T> DI void PINA(T& x) { asm volatile("" : "+a"(x)); }
template <class F, int... I> DI void static_for_(F&& f, std::integer_sequence<int, I...>) { (f(std::integral_constant<int, I>{}), ...); }
template <int N, class F> DI void static_for(F&& f) { static_for_(f, std::make_integer_sequence<int, N>{}); }

#define MM_(T) "v_mfma_f32_16x16x32_" T
template <bool BF> DI void mm4(f32x4& d0, f32x4& d1, f32x4& d2, f32x4& d3, u32x4 a0, u32x4 a1, u32x4 b0, u32x4 b1) {
#define MM4_(T) asm volatile("s_nop 1\n\t" MM_(T) " %0, %4, %6, 0\n\t" MM_(T) " %1, %4, %7, 0\n\t" MM_(T) " %2, %5, %6, 0\n\t" MM_(T) " %3, %5, %7, 0\n\ts_nop 5" \
               : "=&v"(d0), "=&v"(d1), "=&v"(d2), "=&v"(d3) : "v"(a0), "v"(a1), "v"(b0), "v"(b1))
  if constexpr (BF) MM4_("bf16"); else MM4_("f16");
}
template <bool BF, int TAIL, int AG = 0> DI void mm8(f32x4& d0, f32x4& d1, f32x4& d2, f32x4& d3, u32x4 a00, u32x4 b00, u32x4 a01, u32x4 b01, u32x4 a10,
                               u32x4 b10, u32x4 a11, u32x4 b11, u32x4 a20, u32x4 b20, u32x4 a21, u32x4 b21, u32x4 a30, u32x4 b30, u32x4 a31, u32x4 b31) {
#define MM8_(T, A, B) asm volatile("s_nop 1\n\t" MM_(T) " %0, %4, %5, 0\n\t" MM_(T) " %1, %8, %9, 0\n\t" MM_(T) " %0, %6, %7, %0\n\t" MM_(T) " %1, %10, %11, %1\n\t" \
               MM_(T) " %2, %12, %13, 0\n\t" MM_(T) " %3, %16, %17, 0\n\t" MM_(T) " %2, %14, %15, %2\n\t" MM_(T) " %3, %18, %19, %3\n\ts_nop %20"         \
               : "=&v"(d0), "=&v"(d1), "=&v"(d2), "=&v"(d3)                                                                                              \
               : A(a00), B(b00), A(a01), B(b01), A(a10), B(b10), A(a11), B(b11), A(a20), B(b20), A(a21), B(b21), A(a30), B(b30), A(a31), B(b31), "i"(TAIL))
  if constexpr (!BF) MM8_("f16", "v", "v");
  else if constexpr (AG == 0) MM8_("bf16", "v", "v");
  else if constexpr (AG == 1) MM8_("bf16", "a", "v");
  else MM8_("bf16", "v", "a");
}
template <bool BF> DI u32x4 twiddle(f32x4 r, f32x4 i, const f32x2 (&c)[2], const f32x2 (&s)[2]) {
  const f32x2 re0 = lo2(r) * c[0] + lo2(i) * s[0], im0 = lo2(i) * c[0] - lo2(r) * s[0];
  const f32x2 re1 = hi2(r) * c[1] + hi2(i) * s[1], im1 = hi2(i) * c[1] - hi2(r) * s[1];
  return u32x4{pkc<BF>(re0.x, im0.x), pkc<BF>(re0.y, im0.y), pkc<BF>(re1.x, im1.x), pkc<BF>(re1.y, im1.y)};
}
DI u32x4 cmul_b(f32x4 r, f32x4 i, const f32x2 (&c)[2], const f32x2 (&s)[2]) {
  const f32x2 re0 = lo2(r) * c[0] - lo2(i) * s[0], im0 = lo2(i) * c[0] + lo2(r) * s[0];
  const f32x2 re1 = hi2(r) * c[1] - hi2(i) * s[1], im1 = hi2(i) * c[1] + hi2(r) * s[1];
  return u32x4{pkb(re0.x, im0.x), pkb(re0.y, im0.y), pkb(re1.x, im1.x), pkb(re1.y, im1.y)};
}
DI float cosr(float a) { return __builtin_amdgcn_cosf(a); }
DI float sinr(float a) { return __builtin_amdgcn_sinf(a); }
template <int CTRL> DI float dppf(float x) {
  return __builtin_bit_cast(float, __builtin_amdgcn_mov_dpp(__builtin_bit_cast(int, x), CTRL, 0xf, 0xf, true));
}
DI float reduce2(float a, float b) {
  auto t = __builtin_amdgcn_permlane32_swap(__float_as_uint(a), __float_as_uint(b), false, false);
  float x = __uint_as_float(t[0]) + __uint_as_float(t[1]);
  x += dppf<0xB1>(x);
  x += dppf<0x4E>(x);
  x += dppf<0x141>(x);
  x += dppf<0x140>(x);
  auto s = __builtin_amdgcn_permlane16_swap(__float_as_uint(x), __float_as_uint(x), false, false);
  return __uint_as_float(s[0]) + __uint_as_float(s[1]);
}
DI f32x2 bcast2(float x) {
  auto t = __builtin_amdgcn_permlane32_swap(__float_as_uint(x), __float_as_uint(x), false, false);
  return f32x2{__uint_as_float(t[0]), __uint_as_float(t[1])};
}
DI void ln_finish(float mean, float q, f32x2& R, f32x2& O) {
  const float r = __builtin_amdgcn_rcpf(bfr(__builtin_amdgcn_sqrtf(bfr(bfr(q * (1.0f / 512.0f)) + 1e-5f))));
  const float o = (mean - bfr(mean)) * r;
  R = bcast2(r); O = bcast2(o);
}
DI int row_of_pos(int pos, int j) {
  const int c = 8 * (pos >> 7) + 2 * ((pos >> 3) & 3) + ((pos >> 5) & 1), q = 8 * ((pos >> 6) & 1) + (pos & 7);
  return 32 * (2 * c + (j & 1)) + 2 * q + (j >> 1);
}

template <int I> DI void ld4(u32x4& d, u32 v, const u8* s) { asm volatile("global_load_dwordx4 %0, %1, %2 offset:%3 sc1" : "=v"(d) : "v"(v), "s"(s), "i"(I) : "memory"); }
template <int I> DI void ld4a(u32x4& d, u32 v, const u8* s) { asm volatile("global_load_dwordx4 %0, %1, %2 offset:%3 sc1" : "=a"(d) : "v"(v), "s"(s), "i"(I) : "memory"); }
template <int I> DI void ld4r(u32x4& d, u32 v, const u8* s) { asm volatile("global_load_dwordx4 %0, %1, %2 offset:%3 sc1" : "+v"(d) : "v"(v), "s"(s), "i"(I) : "memory"); }
template <int I> DI void ld2(u32x2& d, u32 v, const u8* s) { asm volatile("global_load_dwordx2 %0, %1, %2 offset:%3 sc1" : "=v"(d) : "v"(v), "s"(s), "i"(I) : "memory"); }
template <int I> DI void ld2r(u32x2& d, u32 v, const u8* s) { asm volatile("global_load_dwordx2 %0, %1, %2 offset:%3 sc1" : "+v"(d) : "v"(v), "s"(s), "i"(I) : "memory"); }
template <int I> DI void ld2p(u32x2& d, u32 v, const u8* s) { asm volatile("global_load_dwordx2 %0, %1, %2 offset:%3" : "=v"(d) : "v"(v), "s"(s), "i"(I) : "memory"); }
DI void touch(u32& d, u32 v, const u8* s) { asm volatile("global_load_dword %0, %1, %2 sc1" : "+v"(d) : "v"(v), "s"(s) : "memory"); }
DI void ld1s(u32& d, u32 v, const u8* s) { asm volatile("global_load_ushort %0, %1, %2" : "=v"(d) : "v"(v), "s"(s) : "memory"); }
template <int N> DI void vmwait() { static_assert(N >= 0 && N < 64, ""); asm volatile("s_waitcnt vmcnt(%0)" ::"i"(N) : "memory"); }
DI void set_m0(u32 ldsaddr) { asm volatile("s_mov_b32 m0, %0" ::"s"(ldsaddr) : "memory"); }
template <int I> DI void dma1(u32 voff, const u8* src) {
  asm volatile("global_load_lds_dwordx4 %0, %1 offset:%2 sc1" ::"v"(voff), "s"(src), "i"(I) : "memory");
}
DI void dma4(u32 voff, const u8* src, u32 ldsaddr) {
  asm volatile("s_mov_b32 m0, %2\n\ts_nop 0\n\tglobal_load_lds_dwordx4 %0, %1 sc1\n\tglobal_load_lds_dwordx4 %0, %1 offset:1024 sc1\n\t"
               "global_load_lds_dwordx4 %0, %1 offset:2048 sc1\n\tglobal_load_lds_dwordx4 %0, %1 offset:3072 sc1" ::"v"(voff), "s"(src), "s"(ldsaddr) : "memory");
}
#define RETRY(COND, ...)                                                      \
  do {                                                                        \
    bool bad_ = (COND);                                                       \
    if (__builtin_expect(__any(bad_), 0)) {                                   \
      _Pragma("nounroll") for (int sp_ = 0; sp_ < SPIN_CAP; ++sp_) {          \
        __VA_ARGS__                                                           \
        bad_ = (COND);                                                        \
        if (!__any(bad_)) break;                                              \
      }                                                                       \
    }                                                                         \
  } while (0)
DI void bar() { asm volatile("s_waitcnt lgkmcnt(0)\n\ts_barrier" ::: "memory"); }
DI void bar_nw() { asm volatile("s_barrier" ::: "memory"); }

struct RowC { u32x4 b1re, b1im, b2re[2][2], b2im[2][2]; f32x2 twc[2][2], tws[2][2]; };
template <bool INV> DI void pair_cs(float c, float s, u32& re, u32& im) {
  if constexpr (INV) { im = pkb(s, c); re = pkb_n1(c, s, im); } else { re = pkh(c, s); im = pkh_n0(s, c, re); }
}
template <bool AG = false> DI void sign4(u32x4 base, u32 lm, u32x4& f00, u32x4& f01, u32x4& f10, u32x4& f11) {
  f00 = base;
  f01 = base ^ lm;
  f10 = base ^ u32x4{0u, 0x80008000u, 0u, 0x80008000u};
  f11 = f10 ^ lm;
  if constexpr (AG) { PINA(f00); PINA(f01); PINA(f10); PINA(f11); } else { PIN(f00); PIN(f01); PIN(f10); PIN(f11); }
}
template <bool INV, int PART> DI void row_consts(RowC& k, int m, int g) {
  const float fm = (float)m, fg = (float)(4 * g);
  if constexpr (PART == 0) {
    u32x4 br, bi;
#pragma unroll
    for (int i = 0; i < 4; ++i) {
      const float a = (fg + (float)i) * (fm * (1.0f / 16.0f));
      u32 x, y;
      pair_cs<INV>(cosr(a), sinr(a), x, y); k.b1re[i] = x; k.b1im[i] = y;
      const float a2 = (fg + (float)i) * (fm * (1.0f / 32.0f));
      pair_cs<INV>(cosr(a2), sinr(a2), x, y); br[i] = x; bi[i] = y;
    }
    PIN(k.b1re); PIN(k.b1im);
    const u32 lm = (m & 1) ? 0x80008000u : 0u;
    sign4(br, lm, k.b2re[0][0], k.b2re[0][1], k.b2re[1][0], k.b2re[1][1]);
    sign4(bi, lm, k.b2im[0][0], k.b2im[0][1], k.b2im[1][0], k.b2im[1][1]);
  } else {
#pragma unroll
    for (int e = 0; e < 2; ++e)
#pragma unroll
      for (int i = 0; i < 4; ++i) {
        const float a = (fg + (float)(16 * e + i)) * (fm * (INV ? -1.0f / 512.0f : 1.0f / 512.0f));
        k.twc[e][i >> 1][i & 1] = cosr(a); k.tws[e][i >> 1][i & 1] = sinr(a);
      }
#pragma unroll
    for (int e = 0; e < 2; ++e) { PIN(k.twc[e][0]); PIN(k.twc[e][1]); PIN(k.tws[e][0]); PIN(k.tws[e][1]); }
  }
}
template <bool INV, int TAIL> DI void row_fft(const RowC& k, u32x4 a0, u32x4 a1, f32x4& dr0, f32x4& di0, f32x4& dr1, f32x4& di1) {
  f32x4 tr0, ti0, tr1, ti1;
  mm4<INV>(tr0, ti0, tr1, ti1, a0, a1, k.b1re, k.b1im);
  const u32x4 p0 = twiddle<INV>(tr0, ti0, k.twc[0], k.tws[0]);
  SB();
  const u32x4 p1 = twiddle<INV>(tr1, ti1, k.twc[1], k.tws[1]);
  mm8<INV, TAIL>(dr0, di0, dr1, di1, p0, k.b2re[0][0], p1, k.b2re[0][1], p0, k.b2im[0][0], p1, k.b2im[0][1], p0, k.b2re[1][0], p1, k.b2re[1][1], p0,
           k.b2im[1][0], p1, k.b2im[1][1]);
}

DI void transpose8x4(const u32x2 (&v)[8], u32x4 (&o)[4]) {
#pragma unroll
  for (int jj = 0; jj < 4; ++jj) {
    o[0][jj] = __builtin_amdgcn_perm(v[2 * jj + 1].x, v[2 * jj].x, 0x05040100u);
    o[1][jj] = __builtin_amdgcn_perm(v[2 * jj + 1].x, v[2 * jj].x, 0x07060302u);
    o[2][jj] = __builtin_amdgcn_perm(v[2 * jj + 1].y, v[2 * jj].y, 0x05040100u);
    o[3][jj] = __builtin_amdgcn_perm(v[2 * jj + 1].y, v[2 * jj].y, 0x07060302u);
  }
}
DI void prep_load(const Params& P, int wt, int lane, u32x2 (&v)[8]) {
  const u32 vo = lane * 8;
  if (wt < 896) {
    const int rest = wt / 7, piece = rest & 63;
    const u8* src = (const u8*)((rest >> 6) ? P.wu : P.wg) + (wt % 7) * 512 + (16 * (piece >> 2) + 4 * (piece & 3)) * 3584;
    const u32 vo2 = vo + 7168;
    static_for<8>([&](auto J) { constexpr int j = decltype(J)::value; ld2p<(j & 1) * 3584>(v[j], j & 2 ? vo2 : vo, src + (j >> 2) * (256 * 3584)); });
  } else {
    const int piece = (wt - 896) >> 1, g = piece & 3, kq = piece >> 2, wv = kq / 14, ks = kq % 14;
    int f0, f1;
    if (ks < 4) { f0 = 112 * (4 * wv + ks); f1 = f0 + 16; }
    else if (ks < 6) { f0 = 112 * (4 * wv + 2 * (ks - 4)) + 32; f1 = f0 + 112; }
    else if (ks < 10) { f0 = 112 * (4 * wv + ks - 6) + 48; f1 = f0 + 16; }
    else { f0 = 112 * (4 * wv + ks - 10) + 80; f1 = f0 + 16; }
    const u8* s0 = (const u8*)P.wd + (wt & 1) * 512 + (4 * g + f0) * 1024;
    const u8* s1 = (const u8*)P.wd + (wt & 1) * 512 + (4 * g + f1) * 1024;
    static_for<8>([&](auto J) { constexpr int j = decltype(J)::value; ld2p<(j & 3) * 1024>(v[j], vo, j < 4 ? s0 : s1); });
  }
}
DI u32 prep_dst(int wt, int tile) {
  if (wt < 896) {
    const int rest = wt / 7, piece = rest & 63, mat = rest >> 6, tl = (wt % 7) * 16 + tile;
    const int nt = tl / 7, p = tl % 7, ks = piece >> 2;
    const u32 frag = (p < 3 ? ks * 6 + 2 * p : p < 5 ? 96 + ks * 4 + 2 * (p - 3) : 160 + ks * 4 + 2 * (p - 5)) + mat;
    return OFF_W1 + (nt * 224 + frag) * 1024 + (piece & 3) * 256;
  } else {
    const int piece = (wt - 896) >> 1, tl = (wt & 1) * 16 + tile;
    return OFF_W2 + (((tl >> 2) * 56 + (piece >> 2)) * 4 + (tl & 3)) * 1024 + (piece & 3) * 256;
  }
}

constexpr int NTOK0 = 16, NRES = 40, NEARLY = 40, XE = 160;
constexpr int g1_job(int L, int x) {
  if (x < 3 * (32 - NTOK0) && x % 3 == 0) return 1;
  if (x >= 65 && x < 97 && (x & 1)) return 2;
  if (x >= 101 && x < 117 && (x & 1)) return 2;
  if (x >= XE) return (x < 192 ? (x & 1) == 0 : x < 208 ? 1 : (x & 1) == 0) ? 4 : 0;
  if (x >= 150 && x < 158 && !(x & 1)) return 5;
  if (L == 0 && ((x < 48 && x % 6 == 1) || (x >= 48 && x < 64 && (x & 1)) || (x >= 118 && x < 150 && x % 4 != 3))) return 3;
  return 0;
}
constexpr int NEPI1 = 100, NEPI2 = 66;
struct G1Sched {
  int job[224], idx[224], epi[225], nst[224], cum[225], pos[6][64], cnt[6];
  constexpr G1Sched(int L) : job{}, idx{}, epi{}, nst{}, cum{}, pos{}, cnt{} {
    cnt[1] = NTOK0;
    for (int i = 0; i < NTOK0; ++i) pos[1][i] = i;
    cum[0] = NTOK0;
    for (int x = 0; x < 224; ++x) {
      int j = g1_job(L, x);
      if ((j == 3 && cnt[3] >= NRES) || (j == 4 && cnt[4] >= NEARLY)) j = 0;
      job[x] = j; idx[x] = cnt[j];
      if (x == 161) epi[x] = NEPI1;
      const bool on = (x >= 97 && x < 160 && epi[x] < NEPI1) || x >= 161;
      epi[x + 1] = on ? epi[x] + (j ? 2 : 3) : epi[x];
      if (x < 160 && epi[x + 1] > NEPI1) epi[x + 1] = NEPI1;
      for (int n = epi[x]; n < epi[x + 1]; ++n)
        nst[x] += n < NEPI1 ? (n == 64 || n == 65 || n == 98 || n == 99) : (n < NEPI1 + NEPI2 && (n - NEPI1) % 33 == 32);
      if (j) pos[j][cnt[j]++] = cum[x] + nst[x];
      cum[x + 1] = cum[x] + nst[x] + (j ? 1 : 0);
    }
  }
};
constexpr G1Sched G1S[2] = {G1Sched(0), G1Sched(1)};
static_assert(G1S[0].cnt[1] == 32 && G1S[0].cnt[2] == 24 && G1S[0].cnt[3] == NRES && G1S[0].cnt[4] == NEARLY && G1S[0].cnt[5] == 4 && G1S[0].idx[101] == 16, "");
static_assert(G1S[0].epi[150] == NEPI1 && G1S[0].epi[200] >= NEPI1 + NEPI2 && G1S[0].epi[117] <= 64 && G1S[1].epi[117] <= 64, "");
constexpr int clamp63(int n) { return n > 63 ? 63 : n; }
constexpr int g1_wait(int L, int x, int job, int idx) { return clamp63(G1S[L].cum[x] - G1S[L].pos[job][idx] - 1); }

#define FRESH()                                                                                                   \
  int tid = threadIdx.x, b = blockIdx.x, w = w_;                                                                   \
  u8* ws = P.ws;                                                                                                   \
  asm volatile("" : "+v"(tid), "+s"(b), "+s"(w), "+s"(ws));                                                        \
  const int lane = tid & 63, m = lane & 15, g = lane >> 4, half2 = b & 1, ct = b >> 5;                             \
  const int G = ((b >> 1) & 3) * 4 + ((b >> 3) & 3), member = half2 * 8 + ct, pos = G * 16 + member;               \
  const u32 l16 = lane * 16, t16 = tid * 16, xo = 32 * m + 8 * g;                                                  \
  auto fill_xn = [&](u32 base) {                                                                                   \
    const u32 o = base + (pos >> 1) * 16384 + (tid >> 3) * 256 + (pos & 1) * 128 + (tid & 7) * 16;                 \
    st4(sent4, o); st4(sent4, o + 8192);                                                                           \
  };                                                                                                               \
  auto fill_h = [&](u32 base, int q) {                                                                             \
    const int i = q * 64 + lane;                                                                                   \
    st4(sent4, base + (8 * G + 2 * w + (i >= 224)) * 57344 + member * 3584 + (i >= 224 ? i - 224 : i) * 16);       \
  };                                                                                                               \
  auto fill_tile = [&](u32 base) {                                                                                 \
    const u32 o = base + (128 * G + 64 * half2 + (tid >> 3)) * 1024 + ct * 128 + (tid & 7) * 16;                   \
    st4(sent4, o); st4(sent4, o + 32768);                                                                          \
  };                                                                                                               \
  auto fill_rows = [&](u32 base) { st4(sent4, base + pos * 8192 + t16); st4(sent4, base + pos * 8192 + 4096 + t16); }; \
  auto ring4 = [&](int q) {                                   \
    const u32 o = (q >> 1) * 32768 + w * 8192 + (q & 1) * 4096;                                                    \
    dma4(l16, ws + OFF_W1 + member * 229376 + o, ldsbase + o);                                                     \
  };                                                                                                               \
  (void)m; (void)g; (void)half2; (void)ct; (void)l16; (void)t16; (void)xo; (void)fill_xn; (void)fill_h; (void)fill_tile; (void)fill_rows; (void)ring4

__global__ __launch_bounds__(256, 1) void k_all(Params P) {
  extern __shared__ __attribute__((aligned(16))) u8 lds[];
  ST_DECL;
  const int w_ = __builtin_amdgcn_readfirstlane(threadIdx.x >> 6);
  const __amdgpu_buffer_rsrc_t rs = __builtin_amdgcn_make_buffer_rsrc(P.ws, 0, WS_USED, 0x00020000);
  const u32 ldsbase = (u32)(size_t)(__attribute__((address_space(3))) u8*)lds;
  const u32x4 sent4 = {SENT, SENT, SENT, SENT};
  auto st4 = [&](u32x4 v, u32 off) { __builtin_amdgcn_raw_buffer_store_b128(v, rs, off, 0, 16); };
  auto st2 = [&](u32x2 v, u32 off) { __builtin_amdgcn_raw_buffer_store_b64(v, rs, off, 0, 16); };
  u32x2 xl[2], xh[2], pv0[8], pv1[8];
  u32 wfr[8], wfi[8], wn[8];
  f32x2 xv[8];
  u32x2 ep;
  u32 cpf = 0;
  RowC rc;
  ST(0);
  {
    FRESH();
    const u8* xp = (const u8*)P.x + row_of_pos(pos, w) * 1024;
    ld2p<0>(xl[0], xo, xp); ld2p<512>(xl[1], xo, xp); ld2p<0>(xh[0], xo, xp + 1024 * 1024); ld2p<512>(xh[1], xo, xp + 1024 * 1024);
    { const u32 z = 0; ld2<0>(ep, z, ws); }
    {
      const u8* pc;
      asm volatile("s_getpc_b64 %0" : "=s"(pc));
      const u32 o = (((b >> 3) * 4 + w) * 8 + (lane & 7)) * 128;
      touch(cpf, o < CODE_BYTES ? o : 0, pc);
    }
    fill_rows(OFF_Z); fill_rows(OFF_WX); fill_rows(OFF_U);
    if (tid < 8) st4(sent4, OFF_WNY + tid * 4096 + pos * 16);
    SB();
    const int kb = 2 * (16 * ((pos >> 6) & 1) + (pos & 15)) + (pos >> 7) + 256 * ((pos >> 4) & 3);
    static_for<8>([&](auto J) {
      constexpr int j = decltype(J)::value;
      const u32 o = tid * 2 + (kb + 64 * (16 * (j >> 2) + (j & 3))) * 514;
      ld1s(wfr[j], o, (const u8*)P.wr); ld1s(wfi[j], o, (const u8*)P.wi);
    });
    {
      const int k = kb + 64 * (16 * ((lane >> 2) & 1) + (lane & 3)), km = (2048 - k) & 2047;
      const u32 o = k * 514, om = km * 514;
      ld1s(wn[0], o, (const u8*)P.wr); ld1s(wn[1], o, (const u8*)P.wi); ld1s(wn[2], om, (const u8*)P.wr); ld1s(wn[3], om, (const u8*)P.wi);
      ld1s(wn[4], o, (const u8*)P.wr + 512); ld1s(wn[5], o, (const u8*)P.wi + 512); ld1s(wn[6], om, (const u8*)P.wr + 512); ld1s(wn[7], om, (const u8*)P.wi + 512);
    }
    SB();
    row_consts<false, 0>(rc, m, g);
    SB();
    prep_load(P, b * 4 + w, lane, pv0);
    SB();
    row_consts<false, 1>(rc, m, g);
    SB();
    if (b < 80) prep_load(P, b * 4 + w + 1024, lane, pv1);
    if (b < 80) vmwait<24 + 16>(); else vmwait<24 + 8>();
    bar_nw();
    PIN(cpf); PIN(ep); PIN(xl[0]); PIN(xl[1]); PIN(xh[0]); PIN(xh[1]);
    { const u64 e = (((u64)(u32)__builtin_amdgcn_readfirstlane(ep.y) << 32) | (u32)__builtin_amdgcn_readfirstlane(ep.x)) + 1; ep = u32x2{(u32)e, (u32)(e >> 32)}; }
    if (tid == 0) st2(ep, OFF_FLAG + pos * 8);
#pragma unroll
    for (int t = 0; t < 2; ++t)
#pragma unroll
      for (int p = 0; p < 2; ++p) {
        xv[4 * t + 2 * p] = f32x2{blo(xl[t][p]), blo(xh[t][p])};
        xv[4 * t + 2 * p + 1] = f32x2{bhi(xl[t][p]), bhi(xh[t][p])};
      }
  }
  ST(1);

  u32x4 w2a[10][4];

  auto layer = [&](auto LC) {
    constexpr int L = decltype(LC)::value;
    constexpr int S0 = 2 + 16 * L;
    {
      FRESH();
      const f32x2 s2 = ((xv[0] + xv[1]) + (xv[2] + xv[3])) + ((xv[4] + xv[5]) + (xv[6] + xv[7]));
      const float mean = reduce2(s2.x, s2.y) * (1.0f / 512.0f);
      const f32x2 M = bcast2(mean);
      f32x2 d[8];
#pragma unroll
      for (int i = 0; i < 8; ++i) d[i] = xv[i] - M;
      f32x2 q0 = d[0] * d[0], q1 = d[1] * d[1];
#pragma unroll
      for (int i = 2; i < 8; i += 2) { q0 += d[i] * d[i]; q1 += d[i + 1] * d[i + 1]; }
      q0 += q1;
      f32x2 R, O;
      ln_finish(mean, reduce2(q0.x, q0.y), R, O);
      float* img = (float*)(lds + LDS_SCR);
      u32* inp = (u32*)(img + w * 1280);
#pragma unroll
      for (int i = 0; i < 8; ++i) {
        const f32x2 v = d[i] * R + O;
        inp[(16 * (m & 1) + 4 * g + (i & 3)) * 20 + (m >> 1) + 8 * (i >> 2)] = pkh(v.x, v.y);
      }
      const u32x4 a0 = *(const u32x4*)(inp + m * 20 + 4 * g), a1 = *(const u32x4*)(inp + (16 + m) * 20 + 4 * g);
      const int ph = tid >> 1, fq = ph ? 512 - ph : 256, fq1 = 384 - ph;
      u32 ap0 = (ph + (ph >> 4) * 4) * 4, ap1 = ap0 + 640, ar0 = (fq + (fq >> 4) * 4) * 4, ar1 = (fq1 + (fq1 >> 4) * 4) * 4;
      PIN(ap0); PIN(ap1); PIN(ar0); PIN(ar1);
      const float* p0 = (const float*)((const u8*)img + ap0);
      const float* p1 = (const float*)((const u8*)img + ap1);
      const float* r0 = (const float*)((const u8*)img + ar0);
      const float* r1 = (const float*)((const u8*)img + ar1);
      const float sc = 1.0f / 64.0f, sg = (tid & 1) ? -sc : sc;
      f32x2 cA = {sc, sc}, cB = {sg, -sg}, cC = {sc, -sc}, cD = {sg, sg};
      f32x2 eA = ph ? cA : f32x2{2 * sc, 0.f}, eB = ph ? cB : f32x2{2 * sg, 0.f}, eC = ph ? cC : f32x2{0.f, 2 * sg}, eD = ph ? cD : f32x2{0.f, 2 * sc};
      u32 zo = OFF_Z + L * MB2 + pos * 8192 + t16;
      PIN(cB); PIN(cD); PIN(eA); PIN(eB); PIN(eC); PIN(eD); PIN(zo);
      f32x4 dr0, di0, dr1, di1;
      row_fft<false, 5>(rc, a0, a1, dr0, di0, dr1, di1);
      float* op = img + w * 1280 + 4 * g + 20 * m;
      *(f32x4*)(op) = dr0; *(f32x4*)(op + 640) = di0;
      SB();
      *(f32x4*)(op + 320) = dr1; *(f32x4*)(op + 960) = di1;
      if constexpr (L == 1) vmwait<8>();
      bar();
      f32x2 pa[4], qa[4], pb[4], qb[4];
#pragma unroll
      for (int j = 0; j < 4; ++j) {
        pa[j] = f32x2{p0[j * 1280], p0[j * 1280 + 640]}; qa[j] = f32x2{r0[j * 1280], r0[j * 1280 + 640]};
      }
#pragma unroll
      for (int j = 0; j < 4; ++j) {
        pb[j] = f32x2{p1[j * 1280], p1[j * 1280 + 640]}; qb[j] = f32x2{r1[j * 1280], r1[j * 1280 + 640]};
      }
      SB();
      u32x4 o;
#pragma unroll
      for (int j = 0; j < 4; ++j) {
        const f32x2 v = eD * swp(qa[j]) + (eC * qa[j] + (eB * swp(pa[j]) + eA * pa[j]));
        o[j] = pkh(v.x, v.y);
      }
      st4(o, zo);
#pragma unroll
      for (int j = 0; j < 4; ++j) {
        const f32x2 v = cD * swp(qb[j]) + (cC * qb[j] + (cB * swp(pb[j]) + cA * pb[j]));
        o[j] = pkh(v.x, v.y);
      }
      st4(o, zo + 4096);
      SB();
      if constexpr (L == 1) {
#pragma unroll
        for (int t = 0; t < 2; ++t)
#pragma unroll
          for (int p = 0; p < 2; ++p) {
            xl[t][p] = pkb(xv[4 * t + 2 * p].x, xv[4 * t + 2 * p + 1].x); xh[t][p] = pkb(xv[4 * t + 2 * p].y, xv[4 * t + 2 * p + 1].y);
          }
        PIN(xl[0]); PIN(xl[1]); PIN(xh[0]); PIN(xh[1]);
      }
    }
    if constexpr (L == 0) {
      FRESH();
      if (b < 80) vmwait<3 + 16>(); else vmwait<3 + 8>();
#pragma unroll
      for (int j = 0; j < 8; ++j) { PIN(wfr[j]); PIN(wfi[j]); PIN(wn[j]); }
      u8* bw = lds + w * 2048;
      u32x4 v0, v1;
#pragma unroll
      for (int j = 0; j < 4; ++j) { v0[j] = wfr[j] | (wfi[j] << 16); v1[j] = wfr[4 + j] | (wfi[4 + j] << 16); }
      *(u32x4*)(bw + lane * 32) = v0; *(u32x4*)(bw + lane * 32 + 16) = v1;
      st4(*(const u32x4*)(bw + l16), OFF_WX + pos * 8192 + w * 2048 + l16);
      st4(*(const u32x4*)(bw + 1024 + l16), OFF_WX + pos * 8192 + w * 2048 + 1024 + l16);
      const float hr = blo(wn[0]) + blo(wn[2]), hi = blo(wn[1]) - blo(wn[3]), nr = blo(wn[4]) + blo(wn[6]), ni = blo(wn[5]) - blo(wn[7]);
      const f32x4 ab = {0.25f * (hr + nr), 0.25f * (hi + ni), 0.25f * (hr - nr), 0.25f * (hi - ni)};
      if (tid < 8) st4(__builtin_bit_cast(u32x4, ab), OFF_WNY + tid * 4096 + pos * 16);
    }
    ST(S0);
    {
      FRESH();
      const int phi = pos, pi_ = w >> 1, kt = w & 1;
      const int k1l = 2 * (16 * kt + m) + pi_;
      u32x4 b1re[2], b1im[2], a2re[2][2], a2im[2][2], b3re[2][2], b3im[2][2], a4re[2][2], a4im[2][2];
      f32x2 t1c[2][2], t1s[2][2], t2c[2][2], t2s[2][2];
      const float fm = (float)m, fg = (float)(4 * g);
      const u32 lm = (m & 1) ? 0x80008000u : 0u;
      auto consts = [&](auto PC) {
        constexpr int part = decltype(PC)::value;
        if constexpr (part == 0) {
          const float fk = (float)k1l;
#pragma unroll
          for (int u = 0; u < 2; ++u) {
#pragma unroll
            for (int r = 0; r < 4; ++r) {
              const float n = fg + (float)(16 * u + r), a = n * (fk * (1.0f / 64.0f)), a2 = n * (fk * (1.0f / 2048.0f));
              u32 x, y;
              pair_cs<false>(cosr(a), sinr(a), x, y); b1re[u][r] = x; b1im[u][r] = y;
              t1c[u][r >> 1][r & 1] = cosr(a2); t1s[u][r >> 1][r & 1] = sinr(a2);
            }
            PIN(b1re[u]); PIN(b1im[u]); PIN(t1c[u][0]); PIN(t1c[u][1]); PIN(t1s[u][0]); PIN(t1s[u][1]);
          }
        } else if constexpr (part == 1) {
          u32x4 fr, fi, ir, ii;
#pragma unroll
          for (int r = 0; r < 4; ++r) {
            const float a = (fg + (float)r) * (fm * (1.0f / 32.0f)), c = cosr(a), s = sinr(a);
            u32 x, y;
            pair_cs<false>(c, s, x, y); fr[r] = x; fi[r] = y;
            pair_cs<true>(c, s, x, y); ir[r] = x; ii[r] = y;
          }
          sign4(fr, lm, a2re[0][0], a2re[0][1], a2re[1][0], a2re[1][1]);
          sign4(fi, lm, a2im[0][0], a2im[0][1], a2im[1][0], a2im[1][1]);
          sign4<true>(ir, lm, b3re[0][0], b3re[0][1], b3re[1][0], b3re[1][1]);
          sign4<true>(ii, lm, b3im[0][0], b3im[0][1], b3im[1][0], b3im[1][1]);
        } else if constexpr (part == 2) {
          const float kb = (float)(32 * kt + pi_) + 2.0f * fg;
#pragma unroll
          for (int u = 0; u < 2; ++u) {
#pragma unroll
            for (int r = 0; r < 4; ++r) {
              const float a = (kb + (float)(2 * r)) * ((fm + (float)(16 * u)) * (-1.0f / 2048.0f));
              t2c[u][r >> 1][r & 1] = cosr(a); t2s[u][r >> 1][r & 1] = sinr(a);
            }
            PIN(t2c[u][0]); PIN(t2c[u][1]); PIN(t2s[u][0]); PIN(t2s[u][1]);
          }
        } else {
          const float fn = (fm + (float)(16 * (w >> 1))) * (1.0f / 64.0f);
#pragma unroll
          for (int u = 0; u < 2; ++u) {
#pragma unroll
            for (int r = 0; r < 4; ++r) {
              const float a = (2.0f * fg + (float)(2 * r + u)) * fn;
              u32 x, y;
              pair_cs<true>(cosr(a), sinr(a), x, y); a4re[u][0][r] = x; a4im[u][0][r] = y;
            }
            a4re[u][1] = a4re[u][0] ^ lm; a4im[u][1] = a4im[u][0] ^ lm;
            PINA(a4re[u][0]); PINA(a4re[u][1]); PINA(a4im[u][0]); PINA(a4im[u][1]);
          }
        }
        SB();
      };
      using I0 = std::integral_constant<int, 0>; using I1 = std::integral_constant<int, 1>;
      using I2 = std::integral_constant<int, 2>; using I3 = std::integral_constant<int, 3>;
      u8* const bw = lds + 16384 + w * 8192;
      if constexpr (L == 0) {
        u32x4 fl0, fl1;
        const u32 o = lane * 32;
        ld4<0>(fl0, o, ws + OFF_FLAG); ld4<16>(fl1, o, ws + OFF_FLAG);
        vmwait<2 + 4>();
        {
          u32x4 ot[4];
#pragma unroll
          for (int j = 0; j < 8; ++j) PIN(pv0[j]);
          transpose8x4(pv0, ot);
#pragma unroll
          for (int x = 0; x < 4; ++x) *(u32x4*)(bw + lane * 64 + ((x + lane) & 3) * 16) = ot[x];
          if (b < 80) {
#pragma unroll
            for (int j = 0; j < 8; ++j) PIN(pv1[j]);
            transpose8x4(pv1, ot);
#pragma unroll
            for (int x = 0; x < 4; ++x) *(u32x4*)(bw + 4096 + lane * 64 + ((x + lane) & 3) * 16) = ot[x];
          }
        }
        vmwait<0>(); PIN(fl0); PIN(fl1);
        RETRY(!(fl0.x == ep.x && fl0.y == ep.y && fl0.z == ep.x && fl0.w == ep.y && fl1.x == ep.x && fl1.y == ep.y && fl1.z == ep.x && fl1.w == ep.y), {
          CNT(0);
          ld4r<0>(fl0, o, ws + OFF_FLAG); ld4r<16>(fl1, o, ws + OFF_FLAG);
          vmwait<0>(); PIN(fl0); PIN(fl1);
        });
      } else {
        SB();
        consts(I0{});
        consts(I1{});
      }
      u32x4 z0, z1, wv[2];
      const u32 zo = (tid >> 1) * 8192 + (tid & 1) * 16;
      const u8* zb0 = ws + OFF_Z + L * MB2 + phi * 32;
      const u8* zb1 = zb0 + 128 * 8192;
      const u8* wb0 = L == 0 ? ws + OFF_WX + phi * 32 : ws + OFF_WP + phi * 8192;
      const u8* wb1 = L == 0 ? wb0 + 128 * 8192 : wb0 + 4096;
      const u32 wo = L == 0 ? zo : t16;
      ld4<0>(z0, zo, zb0); ld4<0>(z1, zo, zb1);
      ld4<0>(wv[0], wo, wb0); ld4<0>(wv[1], wo, wb1);
      SB();
      if constexpr (L == 0) consts(I0{}); else consts(I2{});
      u32x4 wa[8];
      if (phi == 0) static_for<8>([&](auto J) { constexpr int j = decltype(J)::value; ld4<0>(wa[j], t16, ws + OFF_WNY + j * 4096); });
      if constexpr (L == 0) {
        auto put = [&](int rep) {
#pragma unroll
          for (int x = 0; x < 4; ++x) {
            const int src = x * 16 + (lane >> 2);
            st4(*(const u32x4*)(bw + rep * 4096 + src * 64 + (((lane & 3) + src) & 3) * 16), prep_dst(b * 4 + w + 1024 * rep, 4 * x + g) + m * 16);
          }
        };
        put(0);
        SB();
        consts(I1{});
        if (b < 80) put(1);
        SB();
        consts(I2{});
        fill_xn(OFF_XN); fill_rows(OFF_Z + MB2);
        SB();
        consts(I3{});
      } else {
        fill_h(OFF_H + H_BYTES, 0); fill_h(OFF_H + H_BYTES, 1); fill_h(OFF_H + H_BYTES, 2); fill_h(OFF_H + H_BYTES, 3);
        SB();
        consts(I3{});
        fill_xn(OFF_XN + MB2); fill_rows(OFF_X3);
        ring4(2);
      }
      u8* const zi = lds + LDS_SCR;
      u32 zw;
      {
        const int pp = tid >> 1;
        zw = (tid & 1) * 4096 + ((pp >> 6) & 1) * 2048 + (((pp >> 3) & 3) * 16 + 2 * (pp & 7)) * 16 + ((pp >> 5) & 1) * 8;
        PIN(zw);
      }
      u32 uo = OFF_U + L * MB2 + phi * 8192 + w * 2048 + l16;
      PIN(uo);
      SB();
      bar_nw();
      ST(S0 + 1);
      vmwait<(L == 0 ? 8 : 12)>();
      PIN(z0); PIN(z1); PIN(wv[0]); PIN(wv[1]);
      if constexpr (L == 0) {
        RETRY(max4(max4(max4(max4(0, z0), z1), wv[0]), wv[1]) == SENT, {
          CNT(1);
          if (bad_) { ld4r<0>(z0, zo, zb0); ld4r<0>(z1, zo, zb1); ld4r<0>(wv[0], wo, wb0); ld4r<0>(wv[1], wo, wb1); }
          vmwait<0>(); PIN(z0); PIN(z1); PIN(wv[0]); PIN(wv[1]);
        });
      } else {
        RETRY(max4(max4(0, z0), z1) == SENT, {
          CNT(2);
          if (bad_) { ld4r<0>(z0, zo, zb0); ld4r<0>(z1, zo, zb1); }
          vmwait<0>(); PIN(z0); PIN(z1);
        });
      }
      ST(S0 + 2);
      *(u32x2*)(zi + zw) = u32x2{z0.x, z0.y}; *(u32x2*)(zi + zw + 16) = u32x2{z0.z, z0.w};
      *(u32x2*)(zi + zw + 1024) = u32x2{z1.x, z1.y}; *(u32x2*)(zi + zw + 1024 + 16) = u32x2{z1.z, z1.w};
      if constexpr (L == 0) { *(u32x4*)(zi + 8192 + t16) = wv[0]; *(u32x4*)(zi + 12288 + t16) = wv[1]; }
      bar();
      const u8* zr = zi + pi_ * 4096 + l16;
      if constexpr (L == 1) ring4(3);
      const u32x4 f00 = *(const u32x4*)(zr), f01 = *(const u32x4*)(zr + 1024), f10 = *(const u32x4*)(zr + 2048), f11 = *(const u32x4*)(zr + 3072);
      if constexpr (L == 0) { wv[0] = *(const u32x4*)(zi + 8192 + tid * 32); wv[1] = *(const u32x4*)(zi + 8192 + tid * 32 + 16); }
      f32x4 d0, d1, d2, d3;
      mm8<false, 3>(d0, d1, d2, d3, f00, b1re[0], f01, b1re[1], f00, b1im[0], f01, b1im[1], f10, b1re[0], f11, b1re[1], f10, b1im[0], f11, b1im[1]);
      const u32x4 b20 = twiddle<false>(d0, d1, t1c[0], t1s[0]);
      SB();
      const u32x4 b21 = twiddle<false>(d2, d3, t1c[1], t1s[1]);
      f32x2 wr[2][2], wi[2][2];
      auto unpack_w = [&]() {
#pragma unroll
        for (int t = 0; t < 2; ++t)
#pragma unroll
          for (int h = 0; h < 2; ++h) {
            wr[t][h] = f32x2{blo(wv[t][2 * h]), blo(wv[t][2 * h + 1])}; wi[t][h] = f32x2{bhi(wv[t][2 * h]), bhi(wv[t][2 * h + 1])};
            PIN(wr[t][h]); PIN(wi[t][h]);
          }
      };
      mm8<false, 0>(d0, d1, d2, d3, a2re[0][0], b20, a2re[0][1], b21, a2im[0][0], b20, a2im[0][1], b21, a2re[1][0], b20, a2re[1][1], b21, a2im[1][0],
                    b20, a2im[1][1], b21);
      SB();
      unpack_w();
      SB();
      u32x4 a30, a31;
      if (phi != 0) {
        a30 = cmul_b(d0, d1, wr[0], wi[0]);
        a31 = cmul_b(d2, d3, wr[1], wi[1]);
      } else {
        u32 mx = 0;
#pragma unroll
        for (int j = 0; j < 8; ++j) { PIN(wa[j]); mx = max4(mx, wa[j]); }
        _Pragma("nounroll") for (int sp = 0; sp < SPIN_CAP && __any(mx == SENT); ++sp) {
          static_for<8>([&](auto J) { constexpr int j = decltype(J)::value; ld4r<0>(wa[j], t16, ws + OFF_WNY + j * 4096); });
          vmwait<0>();
          mx = 0;
#pragma unroll
          for (int j = 0; j < 8; ++j) { PIN(wa[j]); mx = max4(mx, wa[j]); }
        }
        float* pl = (float*)zi;
        bar_nw();
        float* pw = pl + k1l * 36 + 4 * g;
        *(f32x4*)(pw) = d0; *(f32x4*)(pw + 16) = d2; *(f32x4*)(pw + 2340) = d1; *(f32x4*)(pw + 2340 + 16) = d3;
        if (k1l == 0) {
#pragma unroll
          for (int r = 0; r < 4; ++r) {
            pl[64 * 36 + ((4 * g + r - 1) & 31)] = d0[r]; pl[64 * 36 + ((16 + 4 * g + r - 1) & 31)] = d2[r];
            pl[2340 + 64 * 36 + ((4 * g + r - 1) & 31)] = d1[r]; pl[2340 + 64 * 36 + ((16 + 4 * g + r - 1) & 31)] = d3[r];
          }
        }
        bar();
        const float* pr_ = pl + (64 - k1l) * 36 + 28 - 4 * g;
        const f32x4 qr0 = *(const f32x4*)(pr_), qr1 = *(const f32x4*)(pr_ - 16), qi0 = *(const f32x4*)(pr_ + 2340), qi1 = *(const f32x4*)(pr_ + 2340 - 16);
#pragma unroll
        for (int r = 0; r < 4; ++r) {
          const f32x4 c0 = __builtin_bit_cast(f32x4, wa[r]), c1 = __builtin_bit_cast(f32x4, wa[4 + r]);
          a30[r] = pkb(d0[r] * c0[0] - d1[r] * c0[1] + qr0[3 - r] * c0[2] + qi0[3 - r] * c0[3],
                       d0[r] * c0[1] + d1[r] * c0[0] + qr0[3 - r] * c0[3] - qi0[3 - r] * c0[2]);
          a31[r] = pkb(d2[r] * c1[0] - d3[r] * c1[1] + qr1[3 - r] * c1[2] + qi1[3 - r] * c1[3],
                       d2[r] * c1[1] + d3[r] * c1[0] + qr1[3 - r] * c1[3] - qi1[3 - r] * c1[2]);
        }
      }
      u8* ex = lds + LDS_SCR + 20480;
      mm8<true, 3, 2>(d0, d1, d2, d3, a30, b3re[0][0], a31, b3re[0][1], a30, b3im[0][0], a31, b3im[0][1], a30, b3re[1][0], a31, b3re[1][1], a30, b3im[1][0],
                   a31, b3im[1][1]);
      *(u32x4*)(ex + w * 1024 + l16) = twiddle<true>(d0, d1, t2c[0], t2s[0]);
      SB();
      *(u32x4*)(ex + (4 + w) * 1024 + l16) = twiddle<true>(d2, d3, t2c[1], t2s[1]);
      vmwait<(L == 0 ? 0 : 8)>();
      bar();
      const u8* er = ex + (w & 1) * 4096 + l16;
      const u32x4 g0 = *(const u32x4*)(er), g1 = *(const u32x4*)(er + 1024), g2 = *(const u32x4*)(er + 2048), g3 = *(const u32x4*)(er + 3072);
      mm8<true, 7, 1>(d0, d1, d2, d3, a4re[0][0], g0, a4re[0][1], g1, a4im[0][0], g0, a4im[0][1], g1, a4re[1][0], g2, a4re[1][1], g3, a4im[1][0], g2,
                   a4im[1][1], g3);
      {
        const f32x2 lr0 = lo2(d0) + lo2(d2), li0 = lo2(d1) + lo2(d3), hr0 = lo2(d0) - lo2(d2), hi0 = lo2(d1) - lo2(d3);
        const f32x2 lr1 = hi2(d0) + hi2(d2), li1 = hi2(d1) + hi2(d3), hr1 = hi2(d0) - hi2(d2), hi1 = hi2(d1) - hi2(d3);
        if (phi != 0) {
          const f32x2 pr0 = lr0 - hi0, pi0 = li0 + hr0, qr0 = lr0 + hi0, qi0 = hr0 - li0;
          st4(u32x4{pkb(pr0.x, pi0.x), pkb(qr0.x, qi0.x), pkb(pr0.y, pi0.y), pkb(qr0.y, qi0.y)}, uo);
          const f32x2 pr1 = lr1 - hi1, pi1 = li1 + hr1, qr1 = lr1 + hi1, qi1 = hr1 - li1;
          st4(u32x4{pkb(pr1.x, pi1.x), pkb(qr1.x, qi1.x), pkb(pr1.y, pi1.y), pkb(qr1.y, qi1.y)}, uo + 1024);
          MEMFENCE();
        } else {
          st4(u32x4{pkb(lr0.x, hr0.x), pkb(li0.x, hi0.x), pkb(lr0.y, hr0.y), pkb(li0.y, hi0.y)}, uo);
          st4(u32x4{pkb(lr1.x, hr1.x), pkb(li1.x, hi1.x), pkb(lr1.y, hr1.y), pkb(li1.y, hi1.y)}, uo + 1024);
        }
      }
      if constexpr (L == 0) { st4(wv[0], OFF_WP + phi * 8192 + t16); st4(wv[1], OFF_WP + phi * 8192 + 4096 + t16); }
    }
    ST(S0 + 3);
    {
      FRESH();
      SB();
      row_consts<true, 0>(rc, m, g);
      f32x2 xa[4], xb[4];
#pragma unroll
      for (int i = 0; i < 4; ++i) {
        xa[i] = f32x2{blo(xl[i >> 1][i & 1]), bhi(xl[i >> 1][i & 1])}; xb[i] = f32x2{blo(xh[i >> 1][i & 1]), bhi(xh[i >> 1][i & 1])};
        PIN(xa[i]); PIN(xb[i]);
      }
      SB();
      u32x4 c0, c1;
      const u32 uo = (tid >> 1) * 8192 + (tid & 1) * 16;
      const u8* ub0 = ws + OFF_U + L * MB2 + pos * 32;
      const u8* ub1 = ub0 + 128 * 8192;
      ld4<0>(c0, uo, ub0); ld4<0>(c1, uo, ub1);
      if constexpr (L == 0) { fill_h(OFF_H, 0); fill_h(OFF_H, 1); fill_h(OFF_H, 2); fill_h(OFF_H, 3); }
      else { fill_h(OFF_H + H_BYTES, 4); fill_h(OFF_H + H_BYTES, 5); fill_h(OFF_H + H_BYTES, 6); }
      SB();
      row_consts<true, 1>(rc, m, g);
      SB();
      if constexpr (L == 0) {
        fill_h(OFF_H, 4); fill_h(OFF_H, 5); fill_h(OFF_H, 6); fill_tile(OFF_DL);
      } else {
        ring4(4);
      }
      u32* img = (u32*)(lds + LDS_SCR);
      u32 ip0, iq0, iq1, xno, tw0, tw1, trd;
      {
        const int ph = tid >> 1, fq0 = ph ? 512 - ph : 256, fq1 = 384 - ph;
        ip0 = ((tid & 1) * 1280 + (ph & 31) * 20 + (ph >> 5)) * 4;
        iq0 = ((tid & 1) * 1280 + (fq0 & 31) * 20 + (fq0 >> 5)) * 4; iq1 = ((tid & 1) * 1280 + (fq1 & 31) * 20 + (fq1 >> 5)) * 4;
        xno = OFF_XN + L * MB2 + (pos >> 1) * 16384 + (tid >> 3) * 256 + (pos & 1) * 128 + (tid & 7) * 16;
        tw0 = (4 * m + g) * 128 + ((2 * w) ^ (m & 7)) * 16; tw1 = (4 * m + g) * 128 + ((2 * w + 1) ^ (m & 7)) * 16;
        trd = (tid >> 3) * 128 + ((tid & 7) ^ ((tid >> 5) & 7)) * 16;
        PIN(ip0); PIN(iq0); PIN(iq1); PIN(xno); PIN(tw0); PIN(tw1); PIN(trd);
      }
      SB();
      ST(S0 + 4);
      vmwait<(L == 0 ? 9 : 7)>(); PIN(c0); PIN(c1);
      {
        RETRY(max4(max4(0, c0), c1) == SENT, {
          CNT(3);
          if (bad_) { ld4r<0>(c0, uo, ub0); ld4r<0>(c1, uo, ub1); }
          vmwait<0>(); PIN(c0); PIN(c1);
        });
      }
      ST(S0 + 5);
      {
        u8* d = (u8*)img;
        *(u32*)(d + ip0) = c0.x; *(u32*)(d + iq0) = c0.y; *(u32*)(d + 2560 + ip0) = c0.z; *(u32*)(d + 2560 + iq0) = c0.w;
        *(u32*)(d + ip0 + 16) = c1.x; *(u32*)(d + iq1) = c1.y; *(u32*)(d + 2560 + ip0 + 16) = c1.z; *(u32*)(d + 2560 + iq1) = c1.w;
      }
      bar();
      const u32x4 a0 = *(const u32x4*)(img + w * 640 + m * 20 + 4 * g), a1 = *(const u32x4*)(img + w * 640 + (16 + m) * 20 + 4 * g);
      if constexpr (L == 1) ring4(5);
      if constexpr (L == 0) ring4(0);
      f32x4 dr0, di0, dr1, di1;
      row_fft<true, 7>(rc, a0, a1, dr0, di0, dr1, di1);
      if constexpr (L == 0) ring4(1);
      const float ys = 1.0f / 32768.0f;
      xa[0] += lo2(dr0) * ys; xa[1] += hi2(dr0) * ys; xa[2] += lo2(dr1) * ys; xa[3] += hi2(dr1) * ys;
      xb[0] += lo2(di0) * ys; xb[1] += hi2(di0) * ys; xb[2] += lo2(di1) * ys; xb[3] += hi2(di1) * ys;
      const f32x2 sa = (xa[0] + xa[1]) + (xa[2] + xa[3]), sb = (xb[0] + xb[1]) + (xb[2] + xb[3]);
      const float mean = reduce2(sa.x + sa.y, sb.x + sb.y) * (1.0f / 512.0f);
      const f32x2 M = bcast2(mean);
      f32x2 da[4], db[4];
#pragma unroll
      for (int i = 0; i < 4; ++i) { da[i] = xa[i] - M.x; db[i] = xb[i] - M.y; }
      f32x2 qa = da[0] * da[0], qb = db[0] * db[0];
#pragma unroll
      for (int i = 1; i < 4; ++i) { qa += da[i] * da[i]; qb += db[i] * db[i]; }
      if constexpr (L == 0) { SB(); ring4(2); SB(); }
      f32x2 R, O;
      ln_finish(mean, reduce2(qa.x + qa.y, qb.x + qb.y), R, O);
      u32x4 na, nb;
#pragma unroll
      for (int i = 0; i < 4; ++i) {
        const f32x2 va = da[i] * R.x + O.x, vb = db[i] * R.y + O.y;
        na[i] = pkb(va.x, va.y); nb[i] = pkb(vb.x, vb.y);
      }
      u8* tr = lds + LDS_SCR + 12288;
      *(u32x4*)(tr + tw0) = na; *(u32x4*)(tr + tw1) = nb;
      vmwait<(L == 0 ? 12 : 8)>();
      bar();
      st4(*(const u32x4*)(tr + trd), xno);
      st4(*(const u32x4*)(tr + 4096 + trd), xno + 8192);
      SB();
      const u32x4 rl = {pkb(xa[0].x, xa[0].y), pkb(xa[1].x, xa[1].y), pkb(xa[2].x, xa[2].y), pkb(xa[3].x, xa[3].y)};
      const u32x4 rh = {pkb(xb[0].x, xb[0].y), pkb(xb[1].x, xb[1].y), pkb(xb[2].x, xb[2].y), pkb(xb[3].x, xb[3].y)};
      if constexpr (L == 0) {
        *(u32x4*)(lds + LDS_STASH + t16) = rl; *(u32x4*)(lds + LDS_STASH + 4096 + t16) = rh;
        ring4(3); ring4(4); ring4(5); ring4(6); ring4(7);
      } else {
        const u32 o = OFF_X3 + (8 * pos + 2 * w) * 1024 + xo;
        st2(u32x2{rl.x, rl.y}, o); st2(u32x2{rl.z, rl.w}, o + 512);
        st2(u32x2{rh.x, rh.y}, o + 1024); st2(u32x2{rh.z, rh.w}, o + 1536);
        __builtin_amdgcn_s_sleep(6);
      }
    }
    ST(S0 + 6);
    {
      FRESH();
      u32x4 tk[32], he[32], hl[16], w2t[4], w2v[3][4];
      u32x2 hh[16];
      f32x4 ac1[6][2], ac2[4][2], ac3[4][2];
      const u8* hs0 = ws + OFF_H + L * H_BYTES + (8 * G + 4 * half2) * 57344 + (4 * w) * 3584 + 3584;
      const u8* hs2 = hs0 + 7168;
      const u8* w2src = ws + OFF_W2 + (ct * 4 + w) * 57344;
      PINS(hs0); PINS(hs2);
      u32 hv[4] = {l16, l16 + 57344, l16 + 2 * 57344, l16 + 3 * 57344}, h8[4];
#pragma unroll
      for (int u = 0; u < 4; ++u) { h8[u] = hv[u] - lane * 8; PIN(hv[u]); PIN(h8[u]); }
      auto hload = [&](auto RE, auto KC, auto UC) {
        constexpr int k = decltype(KC)::value, u = decltype(UC)::value;
        constexpr bool re = decltype(RE)::value;
        if constexpr (k == 4 || k == 5) {
          constexpr int o = (u >> 2) * 3584 + 3072 - 3584;
          if constexpr (re) ld2r<o>(hh[8 * (k - 4) + u], h8[u & 3], k == 5 ? hs2 : hs0); else ld2<o>(hh[8 * (k - 4) + u], h8[u & 3], k == 5 ? hs2 : hs0);
        } else {
          constexpr int j = k < 4 ? k : k < 10 ? k - 6 : k - 10, o = (j & 1) * 3584 + (k < 4 ? 0 : k < 10 ? 1024 : 2048) - 3584;
          u32x4& d = k < 10 ? he[(k < 4 ? 0 : 16) + 4 * j + u] : hl[4 * j + u];
          if constexpr (re) ld4r<o>(d, hv[u], j >> 1 ? hs2 : hs0); else ld4<o>(d, hv[u], j >> 1 ? hs2 : hs0);
        }
      };
      auto hseq = [&](auto IC) {
        constexpr int i = decltype(IC)::value, k = i < 16 ? i >> 2 : i < 32 ? 4 + ((i - 16) >> 3) : 6 + ((i - 32) >> 2);
        hload(std::false_type{}, std::integral_constant<int, k>{}, std::integral_constant<int, (i < 16 || i >= 32) ? (i & 3) : (i & 7)>{});
      };
      {
        const u8* w1b = ws + OFF_W1 + member * 229376 + 131072 + w * 8192;
        const u8* tk0 = ws + OFF_XN + L * MB2 + (8 * G + 2 * w) * 16384 + 4096;
        const u8* tk1 = tk0 + 8192;
        u32 tv1 = l16 + 16384, lhi = l16 + 65536;
        u32 hd0 = OFF_H + L * H_BYTES + (8 * G + 2 * w) * 57344 + member * 3584 + l16, hd1 = hd0 + 57344;
        u32 hd2 = hd0 + 3072 - lane * 8, hd3 = hd2 + 57344;
        PIN(tv1); PIN(lhi); PIN(hd0); PIN(hd1); PIN(hd2); PIN(hd3); PINS(tk0); PINS(tk1);
        auto tok = [&](auto RE, auto QC) {
          constexpr int q = decltype(QC)::value, ks = q >> 1;
          if constexpr (decltype(RE)::value) ld4r<(ks & 7) * 1024 - 4096>(tk[q], q & 1 ? tv1 : l16, ks >> 3 ? tk1 : tk0);
          else ld4<(ks & 7) * 1024 - 4096>(tk[q], q & 1 ? tv1 : l16, ks >> 3 ? tk1 : tk0);
        };
        u32 mx0 = 0, mx1 = 0;
        auto fix = [&](auto QC) {
          constexpr int q0 = decltype(QC)::value;
          if (__builtin_expect(__any(umax(mx0, mx1) == SENT), 0)) {
            _Pragma("nounroll") for (int sp = 0; sp < SPIN_CAP; ++sp) {
              CNT(4);
              static_for<4>([&](auto I) { tok(std::true_type{}, std::integral_constant<int, q0 + decltype(I)::value>{}); });
              vmwait<0>();
              u32 m2 = 0;
#pragma unroll
              for (int q = q0; q < q0 + 4; ++q) { PIN(tk[q]); m2 = max4(m2, tk[q]); }
              if (!__any(m2 == SENT)) break;
            }
          }
        };
        static_for<NTOK0>([&](auto Q) { tok(std::false_type{}, Q); });
        constexpr int PD = 8;
        u32x4 af[PD];
        auto frag = [&](int x) {
          const int o = (x & 127) * 1024;
          return o < 65536 ? *(const u32x4*)(lds + l16 + o) : *(const u32x4*)(lds + lhi + (o - 65536));
        };
        vmwait<NTOK0>();
        bar_nw();
#pragma unroll
        for (int x = 0; x < PD - 1; ++x) af[x] = frag(x);
        vmwait<NTOK0 - 4>();
        ST(S0 + 7);
#pragma unroll
        for (int q = 0; q < 4; ++q) PIN(tk[q]);
        mx0 = max4(max4(0, tk[0]), tk[1]); mx1 = max4(max4(0, tk[2]), tk[3]);
        fix(std::integral_constant<int, 0>{});
        SB();
        ST(S0 + 8);
        f32x2 eg[2], eu[2];
        u32x4 eo[2];
        auto epi = [&](auto NC) {
          constexpr int n = decltype(NC)::value;
          if constexpr (n < NEPI1 + NEPI2) {
            constexpr bool p1 = n < NEPI1;
            constexpr int i = p1 ? (n < 64 ? n : n - 2) : (n - NEPI1) - (n - NEPI1) / 33;
            if constexpr (p1 && (n == 64 || n == 65)) st4(eo[n - 64], n == 64 ? hd0 : hd1);
            else if constexpr (p1 && n >= 98) st2(u32x2{eo[n - 98].x, eo[n - 98].y}, n == 98 ? hd2 : hd3);
            else if constexpr (!p1 && (n - NEPI1) % 33 == 32) st4(eo[0], ((n - NEPI1) / 33 ? hd1 : hd0) + 1024);
            else {
              constexpr int s = i & 1, st = (i & 15) >> 1, blk = i >> 4;
              constexpr int u = p1 ? (blk < 4 ? blk >> 1 : blk - 4) : blk >> 1, pr = p1 ? (blk < 4 ? blk & 1 : 2) : blk & 1;
              constexpr int ei = p1 ? u : 0, d = 2 * (pr & 1) + s;
              const f32x4& ga = p1 ? ac1[2 * pr][u] : ac2[2 * (pr & 1)][u];
              const f32x4& up = p1 ? ac1[2 * pr + 1][u] : ac2[2 * (pr & 1) + 1][u];
              if constexpr (st < 2) { float v = ga[2 * s + st]; PIN(v); eg[s][st] = v; }
              else if constexpr (st < 4) { float v = up[2 * s + st - 2]; PIN(v); eu[s][st - 2] = v; }
              else if constexpr (st < 6) { float v = relu(eg[s][st - 4]); PIN(v); eg[s][st - 4] = v; }
              else if constexpr (st == 6) { eg[s] *= eu[s]; PIN(eg[s]); }
              else { u32 v = pkb(eg[s].x, eg[s].y); PIN(v); eo[ei][d] = v; }
            }
            SB();
          }
        };
        static_for<224>([&](auto XC) {
          constexpr int x = decltype(XC)::value, job = G1S[L].job[x], ji = G1S[L].idx[x], e0 = G1S[L].epi[x], e1 = G1S[L].epi[x + 1];
          if constexpr (x == 64) bar_nw();
          if constexpr (x == 100) { vmwait<g1_wait(L, x, 2, 7)>(); bar_nw(); }
          if constexpr (x == 150) { vmwait<g1_wait(L, x, 2, 23)>(); bar_nw(); }
          constexpr int cj = (x + 12) / 12, co = x + 12 - 12 * cj;
          constexpr bool chk = x < 84 && co >= 5;
          constexpr int ks = x < 96 ? x / 6 : x < 160 ? (x - 96) >> 2 : (x - 160) >> 2, t = x < 96 ? x % 6 : x & 3;
          auto mm = [&](int u) {
            f32x4& a = x < 96 ? ac1[x < 96 ? t : 0][u] : x < 160 ? ac2[x < 96 ? 0 : t][u] : ac3[x < 96 ? 0 : t][u];
            if constexpr (ks == 0) a = mfma_b(af[x % PD], tk[u], f32x4{0.f, 0.f, 0.f, 0.f}); else a = mfma_b(af[x % PD], tk[2 * ks + u], a);
          };
          SB();
          mm(0);
          SB();
          if constexpr (x < 223) __builtin_amdgcn_s_waitcnt(0xC07F | ((x + PD - 2 < 224 ? PD - 3 : 222 - x) << 8));
          if constexpr (chk && co == 5) {
            vmwait<g1_wait(L, x, 1, 4 * cj + 3)>();
            PIN(tk[4 * cj]); PIN(tk[4 * cj + 1]); PIN(tk[4 * cj + 2]); PIN(tk[4 * cj + 3]);
          }
          SB();
          if constexpr (chk && co >= 6 && co < 10) {
            const u32x4 v = tk[4 * cj + co - 6];
            mx0 = co == 6 ? umax(v.x, v.y) : max3(mx0, v.x, v.y); PIN(mx0); SB();
            mx1 = co == 6 ? umax(v.z, v.w) : max3(mx1, v.z, v.w); PIN(mx1);
          }
          if constexpr (e0 < e1) epi(std::integral_constant<int, e0>{});
          if constexpr (e0 + 1 < e1) epi(std::integral_constant<int, e0 + 1>{});
          SB();
          mm(1);
          SB();
          if constexpr (job == 2 && (ji & 3) == 0) set_m0(ldsbase + (ji >> 3) * 32768 + w * 8192 + (ji & 4) * 1024);
          SB();
          if constexpr (x + PD - 1 < 224) af[(x + PD - 1) % PD] = frag(x + PD - 1);
          SB();
          if constexpr (job == 1) tok(std::false_type{}, std::integral_constant<int, ji>{});
          if constexpr (job == 2) dma1<(ji & 3) * 1024>(l16, w1b + (ji >> 3) * 32768 + (ji & 4) * 1024);
          if constexpr (job == 3) ld4a<(ji & 7) * 1024 - 4096>(w2a[ji >> 2][ji & 3], l16, w2src + (ji >> 3) * 8192 + 4096);
          if constexpr (job == 5) ld4a<ji * 1024>(w2t[ji], l16, w2src + 40960);
          if constexpr (job == 4) hseq(std::integral_constant<int, ji>{});
          if constexpr (e0 + 2 < e1) epi(std::integral_constant<int, e0 + 2>{});
          if constexpr (chk && co == 11) fix(std::integral_constant<int, 4 * cj>{});
          SB();
        });
        ST(S0 + 9);
#pragma unroll
        for (int u = 0; u < 2; ++u) {
          u32x4 o;
#pragma unroll
          for (int p = 0; p < 2; ++p) {
            const f32x4 ga = ac3[2 * p][u], up = ac3[2 * p + 1][u];
            const f32x2 a = f32x2{relu(ga[0]), relu(ga[1])} * lo2(up), c = f32x2{relu(ga[2]), relu(ga[3])} * hi2(up);
            o[2 * p] = pkb(a.x, a.y); o[2 * p + 1] = pkb(c.x, c.y);
          }
          st4(o, (u ? hd1 : hd0) + 2048);
        }
        SB();
      }
      ST(S0 + 10);
      {
        f32x4 acc[4][4];
        u32x4 x3a, x3b;
        const int tokl = 16 * w + (lane >> 3);
        u32 eo = (128 * G + 64 * half2 + tokl) * 1024 + ct * 128 + (lane & 7) * 16;
        PIN(eo);
        constexpr int NX = L == 1 ? 1 : 0, LB = 22;
        auto cum = [](int n) constexpr { int c = 0; for (int i = 0; i < n; ++i) c += i < 20 ? 1 : i < 22 ? NX : i < LB + 16 ? 1 : 0; return c; };
        auto young = [cum](int k, int n) constexpr {
          if (k < 8) return cum(n) + 2 + G1S[L].cum[224] - G1S[L].pos[4][k < 4 ? 4 * k + 3 : k < 6 ? 16 + 8 * (k - 4) + 7 : 32 + 4 * (k - 6) + 3] - 1;
          if (k < 10) return cum(n) - cum(4 * (k - 8) + 4);
          return cum(n) - cum(LB + 4 * (k - 10) + 4);
        };
        u32 mx0 = 0, mx1 = 0;
        auto pin_k = [&](auto KC) {
          constexpr int k = decltype(KC)::value;
#pragma unroll
          for (int u = 0; u < 4; ++u) {
            if constexpr (k < 4) PIN(he[4 * k + u]);
            else if constexpr (k < 6) { PIN(hh[8 * (k - 4) + u]); PIN(hh[8 * (k - 4) + 4 + u]); }
            else if constexpr (k < 10) PIN(he[16 + 4 * (k - 6) + u]);
            else { PIN(hl[4 * (k - 10) + u]); if constexpr (k > 10) PIN(w2v[k - 11][u]); }
          }
        };
        auto opnd = [&](auto KC, int u) {
          constexpr int k = decltype(KC)::value;
          if constexpr (k < 4) return he[4 * k + u];
          else if constexpr (k < 6) return u32x4{hh[8 * (k - 4) + u].x, hh[8 * (k - 4) + u].y, hh[8 * (k - 4) + 4 + u].x, hh[8 * (k - 4) + 4 + u].y};
          else if constexpr (k < 10) return he[16 + 4 * (k - 6) + u];
          else return hl[4 * (k - 10) + u];
        };
        auto fixk = [&](auto KC) {
          constexpr int k = decltype(KC)::value;
          if (__builtin_expect(__any(umax(mx0, mx1) == SENT), 0)) {
            _Pragma("nounroll") for (int sp = 0; sp < SPIN_CAP; ++sp) {
              CNT(k < 10 ? 5 : 6);
              static_for<(k == 4 || k == 5) ? 8 : 4>([&](auto U) { hload(std::true_type{}, KC, U); });
              vmwait<0>();
              pin_k(KC);
              if (!__any(max4(max4(max4(max4(0, opnd(KC, 0)), opnd(KC, 1)), opnd(KC, 2)), opnd(KC, 3)) == SENT)) break;
            }
          }
        };
        u32 rw[4];
#pragma unroll
        for (int c = 0; c < 4; ++c) { rw[c] = w * 16384 + c * 4096 + (g * 16 + (m ^ (2 * (2 * c + (g >> 1))))) * 16; PIN(rw[c]); }
        u32 oo[2] = {0, 0};
        if constexpr (L == 1) {
#pragma unroll
          for (int a = 0; a < 2; ++a) {
            const int tok = 128 * G + 64 * half2 + tokl + 8 * a;
            oo[a] = (row_of_pos(tok >> 3, (tok >> 1) & 3) + 1024 * (tok & 1)) * 1024 + ct * 128 + (lane & 7) * 16;
            PIN(oo[a]);
          }
        }
        vmwait<clamp63(young(0, 0))>();
        if constexpr (L == 0) {
#pragma unroll
          for (int k = 0; k < 10; ++k) { PINA(w2a[k][0]); PINA(w2a[k][1]); PINA(w2a[k][2]); PINA(w2a[k][3]); }
        }
        PINA(w2t[0]); PINA(w2t[1]); PINA(w2t[2]); PINA(w2t[3]);
        pin_k(std::integral_constant<int, 0>{});
        mx0 = max4(max4(0, he[0]), he[1]); mx1 = max4(max4(0, he[2]), he[3]);
        fixk(std::integral_constant<int, 0>{});
        ST(S0 + 11);
        auto wfr = [&](auto KC, int c) {
          constexpr int k = decltype(KC)::value;
          if constexpr (k < 10) return w2a[k][c]; else if constexpr (k == 10) return w2t[c]; else return w2v[k - 11][c];
        };
        static_for<48>([&](auto NC) {
          constexpr int n = decltype(NC)::value, k = n >> 2, c = n & 3;
          using KC = std::integral_constant<int, k>;
          using KN = std::integral_constant<int, k + 1>;
          static_for<4>([&](auto UC) {
            constexpr int u = decltype(UC)::value;
            SB();
            if constexpr (k == 0) acc[c][u] = mfma_b(wfr(KC{}, c), opnd(KC{}, u), f32x4{0.f, 0.f, 0.f, 0.f});
            else acc[c][u] = mfma_b(wfr(KC{}, c), opnd(KC{}, u), acc[c][u]);
            SB();
            if constexpr (c == 1 && u == 0) {
              vmwait<clamp63(young(k == 11 ? 13 : k + 1, n))>(); pin_k(KN{});
              if constexpr (k == 11) pin_k(std::integral_constant<int, 13>{});
            }
            if constexpr ((c == 1 && (u == 1 || u == 2)) || (c == 2 && u < (k == 11 ? 3 : 2)) || (k == 11 && c == 3 && u < 3)) {
              constexpr int j = c == 1 ? u - 1 : c == 2 ? 2 + u : 5 + u;
              const u32x4 v = j < 4 ? opnd(KN{}, j & 3) : opnd(std::integral_constant<int, 13>{}, j & 3);
              mx0 = j == 0 ? umax(v.x, v.y) : max3(mx0, v.x, v.y); PIN(mx0); SB();
              mx1 = j == 0 ? umax(v.z, v.w) : max3(mx1, v.z, v.w); PIN(mx1);
            }
            if constexpr (u == 3) {
              if constexpr (n < 8) hseq(std::integral_constant<int, NEARLY + n>{});
              else if constexpr (n < 20) ld4<((n - 8) & 3) * 1024>(w2v[(n - 8) >> 2][n & 3], l16, w2src + 45056 + ((n - 8) >> 2) * 4096);
              else if constexpr (n < 22) { if constexpr (L == 1) ld4<0>(n == 20 ? x3a : x3b, eo, ws + OFF_X3 + (n - 20) * 8192); }
              else if constexpr (n < LB + 16) hload(std::false_type{}, std::integral_constant<int, 10 + ((n - LB) >> 2)>{}, std::integral_constant<int, (n - LB) & 3>{});
              if constexpr (c == 3) { fixk(KN{}); if constexpr (k == 11) fixk(std::integral_constant<int, 13>{}); }
            }
          });
        });
        bar_nw();
        static_for<20>([&](auto TC) {
          constexpr int t = decltype(TC)::value;
          if constexpr (t < 16) {
            SB();
            acc[t >> 2][t & 3] = mfma_b(w2v[1][t >> 2], hl[8 + (t & 3)], acc[t >> 2][t & 3]);
            acc[t >> 2][t & 3] = mfma_b(w2v[2][t >> 2], hl[12 + (t & 3)], acc[t >> 2][t & 3]);
            SB();
          }
          if constexpr (t >= 4) *(f32x4*)(lds + rw[(t - 4) >> 2] + ((t - 4) & 3) * 1024) = acc[(t - 4) >> 2][(t - 4) & 3];
        });
        SB();
        ST(S0 + 12);
        const int o8 = lane & 7;
        const u8* rp0 = lds + ((o8 >> 1) * 4 + w) * 1024 + (2 * (o8 & 1) * 16 + ((lane >> 3) ^ (2 * o8))) * 16;
        const u8* rp1 = lds + ((o8 >> 1) * 4 + w) * 1024 + (2 * (o8 & 1) * 16 + ((8 + (lane >> 3)) ^ (2 * o8))) * 16;
        bar();
        f32x4 s[2][2][4];
#pragma unroll
        for (int p = 0; p < 4; ++p) { s[0][0][p] = *(const f32x4*)(rp0 + p * 16384); s[0][1][p] = *(const f32x4*)(rp0 + p * 16384 + 256); }
#pragma unroll
        for (int p = 0; p < 4; ++p) { s[1][0][p] = *(const f32x4*)(rp1 + p * 16384); s[1][1][p] = *(const f32x4*)(rp1 + p * 16384 + 256); }
        SB();
        if constexpr (L == 1) {
          vmwait<0>(); PIN(x3a); PIN(x3b);
          RETRY(max4(max4(0, x3a), x3b) == SENT, {
            ld4r<0>(x3a, eo, ws + OFF_X3); ld4r<0>(x3b, eo, ws + OFF_X3 + 8192);
            vmwait<0>(); PIN(x3a); PIN(x3b);
          });
        }
#pragma unroll
        for (int a = 0; a < 2; ++a) {
          const f32x4 s0 = (s[a][0][0] + s[a][0][1]) + (s[a][0][2] + s[a][0][3]), s1 = (s[a][1][0] + s[a][1][1]) + (s[a][1][2] + s[a][1][3]);
          const u32x4 y = {pkb(s0[0], s0[1]), pkb(s0[2], s0[3]), pkb(s1[0], s1[1]), pkb(s1[2], s1[3])};
          if constexpr (L == 0) {
            st4(y, OFF_DL + eo + a * 8192);
          } else {
            const u32x4 xr = a ? x3b : x3a;
            u32x4 o;
#pragma unroll
            for (int p = 0; p < 4; ++p) o[p] = pkb(blo(xr[p]) + blo(y[p]), bhi(xr[p]) + bhi(y[p]));
            *(u32x4*)((u8*)P.out + oo[a]) = o;
          }
        }
      }
    }
    ST(S0 + 13);
    if constexpr (L == 0) {
      FRESH();
      SB();
      row_consts<false, 0>(rc, m, g);
      SB();
      u32x2 d0, d1, d2, d3;
      const u8* db = ws + OFF_DL + (8 * pos + 2 * w) * 1024;
      const u32x4 sl = *(const u32x4*)(lds + LDS_STASH + t16), sh = *(const u32x4*)(lds + LDS_STASH + 4096 + t16);
#pragma unroll
      for (int i = 0; i < 4; ++i) {
        xv[2 * i] = f32x2{blo(sl[i]), blo(sh[i])}; xv[2 * i + 1] = f32x2{bhi(sl[i]), bhi(sh[i])};
        PIN(xv[2 * i]); PIN(xv[2 * i + 1]);
      }
      SB();
      bar_nw();
      ld2<0>(d0, xo, db); ld2<512>(d1, xo, db); ld2<1024>(d2, xo, db); ld2<1536>(d3, xo, db);
      fill_rows(OFF_U + MB2);
      ring4(0);
      SB();
      row_consts<false, 1>(rc, m, g);
      SB();
      ring4(1);
      ST(S0 + 14);
      vmwait<10>(); PIN(d0); PIN(d1); PIN(d2); PIN(d3);
      RETRY(max3(max3(max3(max3(0, d0.x, d0.y), d1.x, d1.y), d2.x, d2.y), d3.x, d3.y) == SENT, {
        CNT(7);
        if (bad_) { ld2r<0>(d0, xo, db); ld2r<512>(d1, xo, db); ld2r<1024>(d2, xo, db); ld2r<1536>(d3, xo, db); }
        vmwait<0>(); PIN(d0); PIN(d1); PIN(d2); PIN(d3);
      });
      xv[0] += f32x2{blo(d0.x), blo(d2.x)}; xv[1] += f32x2{bhi(d0.x), bhi(d2.x)}; xv[2] += f32x2{blo(d0.y), blo(d2.y)}; xv[3] += f32x2{bhi(d0.y), bhi(d2.y)};
      xv[4] += f32x2{blo(d1.x), blo(d3.x)}; xv[5] += f32x2{bhi(d1.x), bhi(d3.x)}; xv[6] += f32x2{blo(d1.y), blo(d3.y)}; xv[7] += f32x2{bhi(d1.y), bhi(d3.y)};
      ST(S0 + 15);
    }
  };
  layer(std::integral_constant<int, 0>{});
  layer(std::integral_constant<int, 1>{});
  if (blockIdx.x == 0 && threadIdx.x == 0) st2(ep, OFF_E);
  ST_OUT();
}

extern "C" void kernel_launch(void* const* d_in, const int* in_sizes, int n_in, void* d_out, int out_size, void* d_ws, size_t ws_size,
                              hipStream_t stream) {
  Params p;
  p.x = (const u16*)d_in[0]; p.wr = (const u16*)d_in[1]; p.wi = (const u16*)d_in[2];
  p.wg = (const u16*)d_in[4]; p.wu = (const u16*)d_in[5]; p.wd = (const u16*)d_in[6];
  p.out = (u16*)d_out; p.ws = (u8*)d_ws;
  (void)hipFuncSetAttribute((const void*)k_all, hipFuncAttributeMaxDynamicSharedMemorySize, LDS_TOTAL);
  k_all<<<256, 256, LDS_TOTAL, stream>>>(p);
}
```

```cpp
#include <hip/hip_runtime.h>
#include <hip/hip_bf16.h>
#include <stdint.h>
#include <type_traits>
#include <utility>

typedef unsigned char u8;
typedef unsigned short u16;
typedef unsigned int u32;
typedef unsigned long long u64;
typedef u32 u32x2 __attribute__((ext_vector_type(2)));
typedef u32 u32x4 __attribute__((ext_vector_type(4)));
typedef float f32x2 __attribute__((ext_vector_type(2)));
typedef float f32x4 __attribute__((ext_vector_type(4)));
typedef _Float16 f16x2 __attribute__((ext_vector_type(2)));
typedef __bf16 bf16x2 __attribute__((ext_vector_type(2)));
typedef __bf16 bf16x8 __attribute__((ext_vector_type(8)));
#define DI __device__ __forceinline__
#define SB() __builtin_amdgcn_sched_barrier(0)
#define MEMFENCE() asm volatile("" ::: "memory")

constexpr u32 MB2 = 2u << 20;
constexpr u32 OFF_E = 0, OFF_FLAG = 4096, OFF_FLAG2 = 8192, OFF_WNY = 65536;
constexpr u32 OFF_Z = MB2;
constexpr u32 OFF_U = OFF_Z + 2 * MB2;
constexpr u32 OFF_XN = OFF_U + 2 * MB2;
constexpr u32 OFF_WX = OFF_XN + 2 * MB2;
constexpr u32 OFF_DL = OFF_WX + MB2;
constexpr u32 OFF_X3 = OFF_DL + MB2;
constexpr u32 H_BYTES = 7340032;
constexpr u32 OFF_H = OFF_X3 + MB2;
constexpr u32 OFF_W1 = OFF_H + 2 * H_BYTES;
constexpr u32 OFF_W2 = OFF_W1 + 3670016;
constexpr u32 WS_USED = OFF_W2 + 1835008;
constexpr u32 SENT = 0xFFFFFFFFu;
constexpr int SPIN_CAP = 1 << 15;
constexpr u32 CODE_BYTES = 66 * 1024;

constexpr int LDS_SCR = 131072, LDS_STASH = LDS_SCR + 24576, LDS_TOTAL = 163840;

struct Params {
  const u16* x; const u16* wr; const u16* wi; const u16* wg; const u16* wu; const u16* wd; u16* out; u8* ws;
};

#define ST(i)
#define ST_DECL
#define CNT(i)
#define ST_OUT()

#define XS(i)
DI u32 pkh(float a, float b) { f32x2 v = {a, b}; return __builtin_bit_cast(u32, __builtin_convertvector(v, f16x2)); }
DI u32 pkb(float a, float b) { f32x2 v = {a, b}; return __builtin_bit_cast(u32, __builtin_convertvector(v, bf16x2)); }
template <bool BF> DI u32 pkc(float a, float b) { return BF ? pkb(a, b) : pkh(a, b); }
DI u32 pkh_n0(float a, float b, u32 after) { u32 r; asm("v_cvt_pk_f16_f32 %0, -%1, %2" : "=v"(r) : "v"(a), "v"(b), "v"(after)); return r; }
DI u32 pkb_n1(float a, float b, u32 after) { u32 r; asm("v_cvt_pk_bf16_f32 %0, %1, -%2" : "=v"(r) : "v"(a), "v"(b), "v"(after)); return r; }
DI float blo(u32 u) { return __uint_as_float(u << 16); }
DI float bhi(u32 u) { return __uint_as_float(u & 0xffff0000u); }
DI float bfr(float a) { return blo(pkb(a, a)); }
DI float relu(float a) { const int i = __float_as_int(a); return __int_as_float(i > 0 ? i : 0); }
DI u32 umax(u32 a, u32 b) { return a > b ? a : b; }
DI u32 max3(u32 a, u32 b, u32 c) { return umax(umax(a, b), c); }
DI u32 max4(u32 acc, u32x4 v) { return max3(max3(acc, v.x, v.y), v.z, v.w); }
DI f32x2 lo2(f32x4 v) { return __builtin_shufflevector(v, v, 0, 1); }
DI f32x2 hi2(f32x4 v) { return __builtin_shufflevector(v, v, 2, 3); }
DI f32x2 swp(f32x2 v) { return __builtin_shufflevector(v, v, 1, 0); }
DI f32x4 mfma_b(u32x4 a, u32x4 b, f32x4 c) {
  return __builtin_amdgcn_mfma_f32_16x16x32_bf16(__builtin_bit_cast(bf16x8, a), __builtin_bit_cast(bf16x8, b), c, 0, 0, 0);
}
template <class T> DI void PIN(T& x) { asm volatile("" : "+v"(x)); }
template <class T> DI void PINS(T& x) { asm volatile("" : "+s"(x)); }
template <class T> DI void PINA(T& x) { asm volatile("" : "+a"(x)); }
template <class F, int... I> DI void static_for_(F&& f, std::integer_sequence<int, I...>) { (f(std::integral_constant<int, I>{}), ...); }
template <int N, class F> DI void static_for(F&& f) { static_for_(f, std::make_integer_sequence<int, N>{}); }

#define MM_(T) "v_mfma_f32_16x16x32_" T
template <bool BF> DI void mm4(f32x4& d0, f32x4& d1, f32x4& d2, f32x4& d3, u32x4 a0, u32x4 a1, u32x4 b0, u32x4 b1) {
#define MM4_(T) asm volatile("s_nop 1\n\t" MM_(T) " %0, %4, %6, 0\n\t" MM_(T) " %1, %4, %7, 0\n\t" MM_(T) " %2, %5, %6, 0\n\t" MM_(T) " %3, %5, %7, 0\n\ts_nop 5" \
               : "=&v"(d0), "=&v"(d1), "=&v"(d2), "=&v"(d3) : "v"(a0), "v"(a1), "v"(b0), "v"(b1))
  if constexpr (BF) MM4_("bf16"); else MM4_("f16");
}
template <bool BF, int TAIL, int AG = 0> DI void mm8(f32x4& d0, f32x4& d1, f32x4& d2, f32x4& d3, u32x4 a00, u32x4 b00, u32x4 a01, u32x4 b01, u32x4 a10,
                               u32x4 b10, u32x4 a11, u32x4 b11, u32x4 a20, u32x4 b20, u32x4 a21, u32x4 b21, u32x4 a30, u32x4 b30, u32x4 a31, u32x4 b31) {
#define MM8_(T, A, B) asm volatile("s_nop 1\n\t" MM_(T) " %0, %4, %5, 0\n\t" MM_(T) " %1, %8, %9, 0\n\t" MM_(T) " %0, %6, %7, %0\n\t" MM_(T) " %1, %10, %11, %1\n\t" \
               MM_(T) " %2, %12, %13, 0\n\t" MM_(T) " %3, %16, %17, 0\n\t" MM_(T) " %2, %14, %15, %2\n\t" MM_(T) " %3, %18, %19, %3\n\ts_nop %20"         \
               : "=&v"(d0), "=&v"(d1), "=&v"(d2), "=&v"(d3)                                                                                              \
               : A(a00), B(b00), A(a01), B(b01), A(a10), B(b10), A(a11), B(b11), A(a20), B(b20), A(a21), B(b21), A(a30), B(b30), A(a31), B(b31), "i"(TAIL))
  if constexpr (!BF) MM8_("f16", "v", "v");
  else if constexpr (AG == 0) MM8_("bf16", "v", "v");
  else if constexpr (AG == 1) MM8_("bf16", "a", "v");
  else MM8_("bf16", "v", "a");
}
template <bool BF> DI u32x4 twiddle(f32x4 r, f32x4 i, const f32x2 (&c)[2], const f32x2 (&s)[2]) {
  const f32x2 re0 = lo2(r) * c[0] + lo2(i) * s[0], im0 = lo2(i) * c[0] - lo2(r) * s[0];
  const f32x2 re1 = hi2(r) * c[1] + hi2(i) * s[1], im1 = hi2(i) * c[1] - hi2(r) * s[1];
  return u32x4{pkc<BF>(re0.x, im0.x), pkc<BF>(re0.y, im0.y), pkc<BF>(re1.x, im1.x), pkc<BF>(re1.y, im1.y)};
}
DI u32x4 cmul_b(f32x4 r, f32x4 i, const f32x2 (&c)[2], const f32x2 (&s)[2]) {
  const f32x2 re0 = lo2(r) * c[0] - lo2(i) * s[0], im0 = lo2(i) * c[0] + lo2(r) * s[0];
  const f32x2 re1 = hi2(r) * c[1] - hi2(i) * s[1], im1 = hi2(i) * c[1] + hi2(r) * s[1];
  return u32x4{pkb(re0.x, im0.x), pkb(re0.y, im0.y), pkb(re1.x, im1.x), pkb(re1.y, im1.y)};
}
DI float cosr(float a) { return __builtin_amdgcn_cosf(a); }
DI float sinr(float a) { return __builtin_amdgcn_sinf(a); }
template <int CTRL> DI float dppf(float x) {
  return __builtin_bit_cast(float, __builtin_amdgcn_mov_dpp(__builtin_bit_cast(int, x), CTRL, 0xf, 0xf, true));
}
DI float reduce2(float a, float b) {
  auto t = __builtin_amdgcn_permlane32_swap(__float_as_uint(a), __float_as_uint(b), false, false);
  float x = __uint_as_float(t[0]) + __uint_as_float(t[1]);
  x += dppf<0xB1>(x);
  x += dppf<0x4E>(x);
  x += dppf<0x141>(x);
  x += dppf<0x140>(x);
  auto s = __builtin_amdgcn_permlane16_swap(__float_as_uint(x), __float_as_uint(x), false, false);
  return __uint_as_float(s[0]) + __uint_as_float(s[1]);
}
DI f32x2 bcast2(float x) {
  auto t = __builtin_amdgcn_permlane32_swap(__float_as_uint(x), __float_as_uint(x), false, false);
  return f32x2{__uint_as_float(t[0]), __uint_as_float(t[1])};
}
DI void ln_finish(float mean, float q, f32x2& R, f32x2& O) {
  const float r = __builtin_amdgcn_rcpf(bfr(__builtin_amdgcn_sqrtf(bfr(bfr(q * (1.0f / 512.0f)) + 1e-5f))));
  const float o = (mean - bfr(mean)) * r;
  R = bcast2(r); O = bcast2(o);
}
DI int row_of_pos(int pos, int j) {
  const int c = 8 * (pos >> 7) + 2 * ((pos >> 3) & 3) + ((pos >> 5) & 1), q = 8 * ((pos >> 6) & 1) + (pos & 7);
  return 32 * (2 * c + (j & 1)) + 2 * q + (j >> 1);
}

template <int I> DI void ld4(u32x4& d, u32 v, const u8* s) { asm volatile("global_load_dwordx4 %0, %1, %2 offset:%3 sc1" : "=v"(d) : "v"(v), "s"(s), "i"(I) : "memory"); }
template <int I> DI void ld4a(u32x4& d, u32 v, const u8* s) { asm volatile("global_load_dwordx4 %0, %1, %2 offset:%3 sc1" : "=a"(d) : "v"(v), "s"(s), "i"(I) : "memory"); }
template <int I> DI void ld4r(u32x4& d, u32 v, const u8* s) { asm volatile("global_load_dwordx4 %0, %1, %2 offset:%3 sc1" : "+v"(d) : "v"(v), "s"(s), "i"(I) : "memory"); }
template <int I> DI void ld2(u32x2& d, u32 v, const u8* s) { asm volatile("global_load_dwordx2 %0, %1, %2 offset:%3 sc1" : "=v"(d) : "v"(v), "s"(s), "i"(I) : "memory"); }
template <int I> DI void ld2r(u32x2& d, u32 v, const u8* s) { asm volatile("global_load_dwordx2 %0, %1, %2 offset:%3 sc1" : "+v"(d) : "v"(v), "s"(s), "i"(I) : "memory"); }
template <int I> DI void ld2p(u32x2& d, u32 v, const u8* s) { asm volatile("global_load_dwordx2 %0, %1, %2 offset:%3" : "=v"(d) : "v"(v), "s"(s), "i"(I) : "memory"); }
DI void touch(u32& d, u32 v, const u8* s) { asm volatile("global_load_dword %0, %1, %2 sc1" : "+v"(d) : "v"(v), "s"(s) : "memory"); }
DI void ld1s(u32& d, u32 v, const u8* s) { asm volatile("global_load_ushort %0, %1, %2" : "=v"(d) : "v"(v), "s"(s) : "memory"); }
template <int N> DI void vmwait() { static_assert(N >= 0 && N < 64, ""); asm volatile("s_waitcnt vmcnt(%0)" ::"i"(N) : "memory"); }
DI void set_m0(u32 ldsaddr) { asm volatile("s_mov_b32 m0, %0" ::"s"(ldsaddr) : "memory"); }
template <int I> DI void dma1(u32 voff, const u8* src) {
  asm volatile("global_load_lds_dwordx4 %0, %1 offset:%2 sc1" ::"v"(voff), "s"(src), "i"(I) : "memory");
}
DI void dma4(u32 voff, const u8* src, u32 ldsaddr) {
  asm volatile("s_mov_b32 m0, %2\n\ts_nop 0\n\tglobal_load_lds_dwordx4 %0, %1 sc1\n\tglobal_load_lds_dwordx4 %0, %1 offset:1024 sc1\n\t"
               "global_load_lds_dwordx4 %0, %1 offset:2048 sc1\n\tglobal_load_lds_dwordx4 %0, %1 offset:3072 sc1" ::"v"(voff), "s"(src), "s"(ldsaddr) : "memory");
}
#define RETRY(COND, ...)                                                      \
  do {                                                                        \
    bool bad_ = (COND);                                                       \
    if (__builtin_expect(__any(bad_), 0)) {                                   \
      _Pragma("nounroll") for (int sp_ = 0; sp_ < SPIN_CAP; ++sp_) {          \
        __VA_ARGS__                                                           \
        bad_ = (COND);                                                        \
        if (!__any(bad_)) break;                                              \
      }                                                                       \
    }                                                                         \
  } while (0)
DI void bar() { asm volatile("s_waitcnt lgkmcnt(0)\n\ts_barrier" ::: "memory"); }
DI void bar_nw() { asm volatile("s_barrier" ::: "memory"); }

struct RowC { u32x4 b1re, b1im, b2re[2][2], b2im[2][2]; f32x2 twc[2][2], tws[2][2]; };
template <bool INV> DI void pair_cs(float c, float s, u32& re, u32& im) {
  if constexpr (INV) { im = pkb(s, c); re = pkb_n1(c, s, im); } else { re = pkh(c, s); im = pkh_n0(s, c, re); }
}
template <bool AG = false> DI void sign4(u32x4 base, u32 lm, u32x4& f00, u32x4& f01, u32x4& f10, u32x4& f11) {
  f00 = base;
  f01 = base ^ lm;
  f10 = base ^ u32x4{0u, 0x80008000u, 0u, 0x80008000u};
  f11 = f10 ^ lm;
  if constexpr (AG) { PINA(f00); PINA(f01); PINA(f10); PINA(f11); } else { PIN(f00); PIN(f01); PIN(f10); PIN(f11); }
}
template <bool INV, int PART> DI void row_consts(RowC& k, int m, int g) {
  const float fm = (float)m, fg = (float)(4 * g);
  if constexpr (PART == 0) {
    u32x4 br, bi;
#pragma unroll
    for (int i = 0; i < 4; ++i) {
      const float a = (fg + (float)i) * (fm * (1.0f / 16.0f));
      u32 x, y;
      pair_cs<INV>(cosr(a), sinr(a), x, y); k.b1re[i] = x; k.b1im[i] = y;
      const float a2 = (fg + (float)i) * (fm * (1.0f / 32.0f));
      pair_cs<INV>(cosr(a2), sinr(a2), x, y); br[i] = x; bi[i] = y;
    }
    PIN(k.b1re); PIN(k.b1im);
    const u32 lm = (m & 1) ? 0x80008000u : 0u;
    sign4(br, lm, k.b2re[0][0], k.b2re[0][1], k.b2re[1][0], k.b2re[1][1]);
    sign4(bi, lm, k.b2im[0][0], k.b2im[0][1], k.b2im[1][0], k.b2im[1][1]);
  } else {
#pragma unroll
    for (int e = 0; e < 2; ++e)
#pragma unroll
      for (int i = 0; i < 4; ++i) {
        const float a = (fg + (float)(16 * e + i)) * (fm * (INV ? -1.0f / 512.0f : 1.0f / 512.0f));
        k.twc[e][i >> 1][i & 1] = cosr(a); k.tws[e][i >> 1][i & 1] = sinr(a);
      }
#pragma unroll
    for (int e = 0; e < 2; ++e) { PIN(k.twc[e][0]); PIN(k.twc[e][1]); PIN(k.tws[e][0]); PIN(k.tws[e][1]); }
  }
}
template <bool INV, int TAIL> DI void row_fft(const RowC& k, u32x4 a0, u32x4 a1, f32x4& dr0, f32x4& di0, f32x4& dr1, f32x4& di1) {
  f32x4 tr0, ti0, tr1, ti1;
  mm4<INV>(tr0, ti0, tr1, ti1, a0, a1, k.b1re, k.b1im);
  const u32x4 p0 = twiddle<INV>(tr0, ti0, k.twc[0], k.tws[0]);
  SB();
  const u32x4 p1 = twiddle<INV>(tr1, ti1, k.twc[1], k.tws[1]);
  mm8<INV, TAIL>(dr0, di0, dr1, di1, p0, k.b2re[0][0], p1, k.b2re[0][1], p0, k.b2im[0][0], p1, k.b2im[0][1], p0, k.b2re[1][0], p1, k.b2re[1][1], p0,
           k.b2im[1][0], p1, k.b2im[1][1]);
}

struct ColC {
  u32x4 b1re[2], b1im[2], a2re[2][2], a2im[2][2], b3re[2][2], b3im[2][2], a4re[2][2], a4im[2][2];
  f32x2 t1c[2][2], t1s[2][2], t2c[2][2], t2s[2][2];
};
template <int PART> DI void col_consts(ColC& k, int m, int g, int w) {
  const int pi_ = w >> 1, kt = w & 1;
  const float fm = (float)m, fg = (float)(4 * g);
  const u32 lm = (m & 1) ? 0x80008000u : 0u;
  if constexpr (PART == 0) {
    const float fk = (float)(2 * (16 * kt + m) + pi_);
#pragma unroll
    for (int u = 0; u < 2; ++u) {
#pragma unroll
      for (int r = 0; r < 4; ++r) {
        const float n = fg + (float)(16 * u + r), a = n * (fk * (1.0f / 64.0f)), a2 = n * (fk * (1.0f / 2048.0f));
        u32 x, y;
        pair_cs<false>(cosr(a), sinr(a), x, y); k.b1re[u][r] = x; k.b1im[u][r] = y;
        k.t1c[u][r >> 1][r & 1] = cosr(a2); k.t1s[u][r >> 1][r & 1] = sinr(a2);
      }
      PIN(k.b1re[u]); PIN(k.b1im[u]); PIN(k.t1c[u][0]); PIN(k.t1c[u][1]); PIN(k.t1s[u][0]); PIN(k.t1s[u][1]);
    }
  } else if constexpr (PART == 1) {
    u32x4 fr, fi, ir, ii;
#pragma unroll
    for (int r = 0; r < 4; ++r) {
      const float a = (fg + (float)r) * (fm * (1.0f / 32.0f)), c = cosr(a), s = sinr(a);
      u32 x, y;
      pair_cs<false>(c, s, x, y); fr[r] = x; fi[r] = y;
      pair_cs<true>(c, s, x, y); ir[r] = x; ii[r] = y;
    }
    sign4(fr, lm, k.a2re[0][0], k.a2re[0][1], k.a2re[1][0], k.a2re[1][1]);
    sign4(fi, lm, k.a2im[0][0], k.a2im[0][1], k.a2im[1][0], k.a2im[1][1]);
    sign4<true>(ir, lm, k.b3re[0][0], k.b3re[0][1], k.b3re[1][0], k.b3re[1][1]);
    sign4<true>(ii, lm, k.b3im[0][0], k.b3im[0][1], k.b3im[1][0], k.b3im[1][1]);
  } else if constexpr (PART == 2) {
    const float kb = (float)(32 * kt + pi_) + 2.0f * fg;
#pragma unroll
    for (int u = 0; u < 2; ++u) {
#pragma unroll
      for (int r = 0; r < 4; ++r) {
        const float a = (kb + (float)(2 * r)) * ((fm + (float)(16 * u)) * (-1.0f / 2048.0f));
        k.t2c[u][r >> 1][r & 1] = cosr(a); k.t2s[u][r >> 1][r & 1] = sinr(a);
      }
      PIN(k.t2c[u][0]); PIN(k.t2c[u][1]); PIN(k.t2s[u][0]); PIN(k.t2s[u][1]);
    }
  } else {
    const float fn = (fm + (float)(16 * (w >> 1))) * (1.0f / 64.0f);
#pragma unroll
    for (int u = 0; u < 2; ++u) {
#pragma unroll
      for (int r = 0; r < 4; ++r) {
        const float a = (2.0f * fg + (float)(2 * r + u)) * fn;
        u32 x, y;
        pair_cs<true>(cosr(a), sinr(a), x, y); k.a4re[u][0][r] = x; k.a4im[u][0][r] = y;
      }
      k.a4re[u][1] = k.a4re[u][0] ^ lm; k.a4im[u][1] = k.a4im[u][0] ^ lm;
      PINA(k.a4re[u][0]); PINA(k.a4re[u][1]); PINA(k.a4im[u][0]); PINA(k.a4im[u][1]);
    }
  }
}

DI void transpose8x4(const u32x2 (&v)[8], u32x4 (&o)[4]) {
#pragma unroll
  for (int jj = 0; jj < 4; ++jj) {
    o[0][jj] = __builtin_amdgcn_perm(v[2 * jj + 1].x, v[2 * jj].x, 0x05040100u);
    o[1][jj] = __builtin_amdgcn_perm(v[2 * jj + 1].x, v[2 * jj].x, 0x07060302u);
    o[2][jj] = __builtin_amdgcn_perm(v[2 * jj + 1].y, v[2 * jj].y, 0x05040100u);
    o[3][jj] = __builtin_amdgcn_perm(v[2 * jj + 1].y, v[2 * jj].y, 0x07060302u);
  }
}
DI void prep_load(const Params& P, int wt, int lane, u32x2 (&v)[8]) {
  const u32 vo = lane * 8;
  if (wt < 896) {
    const int rest = wt / 7, piece = rest & 63;
    const u8* src = (const u8*)((rest >> 6) ? P.wu : P.wg) + (wt % 7) * 512 + (16 * (piece >> 2) + 4 * (piece & 3)) * 3584;
    const u32 vo2 = vo + 7168;
    static_for<8>([&](auto J) { constexpr int j = decltype(J)::value; ld2p<(j & 1) * 3584>(v[j], j & 2 ? vo2 : vo, src + (j >> 2) * (256 * 3584)); });
  } else {
    const int piece = (wt - 896) >> 1, g = piece & 3, kq = piece >> 2, wv = kq / 14, ks = kq % 14;
    int f0, f1;
    if (ks < 4) { f0 = 112 * (4 * wv + ks); f1 = f0 + 16; }
    else if (ks < 6) { f0 = 112 * (4 * wv + 2 * (ks - 4)) + 32; f1 = f0 + 112; }
    else if (ks < 10) { f0 = 112 * (4 * wv + ks - 6) + 48; f1 = f0 + 16; }
    else { f0 = 112 * (4 * wv + ks - 10) + 80; f1 = f0 + 16; }
    const u8* s0 = (const u8*)P.wd + (wt & 1) * 512 + (4 * g + f0) * 1024;
    const u8* s1 = (const u8*)P.wd + (wt & 1) * 512 + (4 * g + f1) * 1024;
    static_for<8>([&](auto J) { constexpr int j = decltype(J)::value; ld2p<(j & 3) * 1024>(v[j], vo, j < 4 ? s0 : s1); });
  }
}
DI u32 prep_dst(int wt, int tile) {
  if (wt < 896) {
    const int rest = wt / 7, piece = rest & 63, mat = rest >> 6, tl = (wt % 7) * 16 + tile;
    const int nt = tl / 7, p = tl % 7, ks = piece >> 2;
    const u32 frag = (p < 3 ? ks * 6 + 2 * p : p < 5 ? 96 + ks * 4 + 2 * (p - 3) : 160 + ks * 4 + 2 * (p - 5)) + mat;
    return OFF_W1 + (nt * 224 + frag) * 1024 + (piece & 3) * 256;
  } else {
    const int piece = (wt - 896) >> 1, tl = (wt & 1) * 16 + tile;
    return OFF_W2 + (((tl >> 2) * 56 + (piece >> 2)) * 4 + (tl & 3)) * 1024 + (piece & 3) * 256;
  }
}

constexpr int NTOK0 = 16, NRES = 40, NEARLY = 37, NFILL = 4;
constexpr int g1_job(int L, int x) {
  if (((x >= 34 && x < 96) && (x & 3) == 2) || (x >= 98 && x < 130 && (x & 3) == 0)) return 2;
  if ((x < 42 && x % 3 == 0) || x == 41 || x == 44) return 1;
  if (x >= 150) return (x & 1) ? 0 : 4;
  if (x >= 132 && x < 148 && (x & 3) == 0) return 5;
  if (L == 0 && (x == 47 || x == 59 || x == 71 || x == 83)) return 6;
  if (L == 0 && ((x < 34 && x % 6 == 1) || (x >= 48 && x < 98 && (x & 3) == 0) || (x >= 98 && x < 150 && (x & 3) == 2) || (x >= 49 && x < 150 && x % 12 == 1)))
    return 3;
  return 0;
}
constexpr int NEPI1 = 112, NEPI2 = 74;
struct G1Sched {
  int job[224], idx[224], epi[225], nst[224], cum[225], pos[7][64], cnt[7];
  constexpr G1Sched(int L) : job{}, idx{}, epi{}, nst{}, cum{}, pos{}, cnt{} {
    cnt[1] = NTOK0;
    for (int i = 0; i < NTOK0; ++i) pos[1][i] = i;
    cum[0] = NTOK0;
    for (int x = 0; x < 224; ++x) {
      int j = g1_job(L, x);
      if ((j == 3 && cnt[3] >= NRES) || (j == 4 && cnt[4] >= NEARLY) || (j == 6 && cnt[6] >= NFILL)) j = 0;
      job[x] = j; idx[x] = cnt[j];
      if (x == 161) epi[x] = NEPI1;
      const bool on = (x >= 97 && x < 160 && epi[x] < NEPI1) || x >= 161;
      epi[x + 1] = on ? epi[x] + (j ? 2 : 3) : epi[x];
      if (x < 160 && epi[x + 1] > NEPI1) epi[x + 1] = NEPI1;
      for (int n = epi[x]; n < epi[x + 1]; ++n)
        nst[x] += n < NEPI1 ? (n == 36 || n == 73 || n == 92 || n == 111) : (n < NEPI1 + NEPI2 && (n - NEPI1) % 37 == 36);
      if (j) pos[j][cnt[j]++] = cum[x] + nst[x];
      cum[x + 1] = cum[x] + nst[x] + (j ? 1 : 0);
    }
  }
};
constexpr G1Sched G1S[2] = {G1Sched(0), G1Sched(1)};
static_assert(G1S[0].cnt[1] == 32 && G1S[0].cnt[2] == 24 && G1S[0].cnt[3] == NRES && G1S[0].cnt[4] == NEARLY && G1S[0].cnt[5] == 4, "");
static_assert(G1S[0].idx[66] == 8 && G1S[0].idx[100] == 16 && G1S[0].cnt[6] == NFILL && G1S[0].pos[6][1] < G1S[0].pos[2][7] && G1S[0].pos[6][NFILL - 1] < G1S[0].pos[2][23], "");
static_assert(G1S[0].epi[150] == NEPI1 && G1S[0].epi[204] >= NEPI1 + NEPI2, "");
constexpr int clamp63(int n) { return n > 63 ? 63 : n; }
constexpr int g1_wait(int L, int x, int job, int idx) { return clamp63(G1S[L].cum[x] - G1S[L].pos[job][idx] - 1); }

#define FRESH()                                                                                                   \
  int tid = threadIdx.x, b = blockIdx.x, w = w_;                                                                   \
  u8* ws = P.ws;                                                                                                   \
  asm volatile("" : "+v"(tid), "+s"(b), "+s"(w), "+s"(ws));                                                        \
  const int lane = tid & 63, m = lane & 15, g = lane >> 4, half2 = b & 1, ct = b >> 5;                             \
  const int G = ((b >> 1) & 3) * 4 + ((b >> 3) & 3), member = half2 * 8 + ct, pos = G * 16 + member;               \
  const u32 l16 = lane * 16, t16 = tid * 16, xo = 32 * m + 8 * g;                                                  \
  auto fill_xn = [&](u32 base) {                                                                                   \
    const u32 o = base + (pos >> 1) * 16384 + (tid >> 3) * 256 + (pos & 1) * 128 + (tid & 7) * 16;                 \
    st4(sent4, o); st4(sent4, o + 8192);                                                                           \
  };                                                                                                               \
  auto fill_h = [&](u32 base, int q) {                                                                             \
    const int i = q * 64 + lane;                                                                                   \
    st4(sent4, base + (8 * G + 2 * w + (i >= 224)) * 57344 + member * 3584 + (i >= 224 ? i - 224 : i) * 16);       \
  };                                                                                                               \
  auto fill_tile = [&](u32 base) {                                                                                 \
    const u32 o = base + (128 * G + 64 * half2 + (tid >> 3)) * 1024 + ct * 128 + (tid & 7) * 16;                   \
    st4(sent4, o); st4(sent4, o + 32768);                                                                          \
  };                                                                                                               \
  auto fill_rows = [&](u32 base) { st4(sent4, base + pos * 8192 + t16); st4(sent4, base + pos * 8192 + 4096 + t16); }; \
  auto ring4 = [&](int q) {                                   \
    const u32 o = (q >> 1) * 32768 + w * 8192 + (q & 1) * 4096;                                                    \
    dma4(l16, ws + OFF_W1 + member * 229376 + o, ldsbase + o);                                                     \
  };                                                                                                               \
  (void)m; (void)g; (void)half2; (void)ct; (void)l16; (void)t16; (void)xo; (void)fill_xn; (void)fill_h; (void)fill_tile; (void)fill_rows; (void)ring4

__global__ __launch_bounds__(256, 1) void k_all(Params P) {
  extern __shared__ __attribute__((aligned(16))) u8 lds[];
  ST_DECL;
  const int w_ = __builtin_amdgcn_readfirstlane(threadIdx.x >> 6);
  const __amdgpu_buffer_rsrc_t rs = __builtin_amdgcn_make_buffer_rsrc(P.ws, 0, WS_USED, 0x00020000);
  const u32 ldsbase = (u32)(size_t)(__attribute__((address_space(3))) u8*)lds;
  const u32x4 sent4 = {SENT, SENT, SENT, SENT};
  auto st4 = [&](u32x4 v, u32 off) { __builtin_amdgcn_raw_buffer_store_b128(v, rs, off, 0, 16); };
  auto st2 = [&](u32x2 v, u32 off) { __builtin_amdgcn_raw_buffer_store_b64(v, rs, off, 0, 16); };
  u32x2 xl[2], xh[2], pv0[8], pv1[8];
  u32 wfr[8], wfi[8], wn[8];
  f32x2 xv[8];
  u32x2 ep;
  u32 cpf = 0;
  RowC rc;
  ColC cc;
  ST(0);
  {
    FRESH();
    const u8* xp = (const u8*)P.x + row_of_pos(pos, w) * 1024;
    ld2p<0>(xl[0], xo, xp); ld2p<512>(xl[1], xo, xp); ld2p<0>(xh[0], xo, xp + 1024 * 1024); ld2p<512>(xh[1], xo, xp + 1024 * 1024);
    { const u32 z = 0; ld2<0>(ep, z, ws); }
    {
      const u8* pc;
      asm volatile("s_getpc_b64 %0" : "=s"(pc));
      const u32 o = (((b >> 3) * 4 + w) * 8 + (lane & 7)) * 128;
      touch(cpf, o < CODE_BYTES ? o : 0, pc);
    }
    XS(32);
    fill_rows(OFF_Z); fill_rows(OFF_WX);
    SB();
    row_consts<false, 0>(rc, m, g);
    SB();
    XS(33);
    fill_rows(OFF_U);
    if (tid < 8) st4(sent4, OFF_WNY + tid * 4096 + pos * 16);
    SB();
    row_consts<false, 1>(rc, m, g);
    SB();
    XS(34);
    const int kb = 2 * (16 * ((pos >> 6) & 1) + (pos & 15)) + (pos >> 7) + 256 * ((pos >> 4) & 3);
    static_for<8>([&](auto J) {
      constexpr int j = decltype(J)::value;
      const u32 o = tid * 2 + (kb + 64 * (16 * (j >> 2) + (j & 3))) * 514;
      ld1s(wfr[j], o, (const u8*)P.wr); ld1s(wfi[j], o, (const u8*)P.wi);
    });
    {
      const int k = kb + 64 * (16 * ((lane >> 2) & 1) + (lane & 3)), km = (2048 - k) & 2047;
      const u32 o = k * 514, om = km * 514;
      ld1s(wn[0], o, (const u8*)P.wr); ld1s(wn[1], o, (const u8*)P.wi); ld1s(wn[2], om, (const u8*)P.wr); ld1s(wn[3], om, (const u8*)P.wi);
      ld1s(wn[4], o, (const u8*)P.wr + 512); ld1s(wn[5], o, (const u8*)P.wi + 512); ld1s(wn[6], om, (const u8*)P.wr + 512); ld1s(wn[7], om, (const u8*)P.wi + 512);
    }
    XS(35);
    vmwait<24>();
    XS(36);
    bar_nw();
    PIN(cpf); PIN(ep); PIN(xl[0]); PIN(xl[1]); PIN(xh[0]); PIN(xh[1]);
    { const u64 e = (((u64)(u32)__builtin_amdgcn_readfirstlane(ep.y) << 32) | (u32)__builtin_amdgcn_readfirstlane(ep.x)) + 1; ep = u32x2{(u32)e, (u32)(e >> 32)}; }
    if (tid == 0) st2(ep, OFF_FLAG + pos * 8);
    XS(37);
    prep_load(P, b * 4 + w, lane, pv0);
    SB();
#pragma unroll
    for (int t = 0; t < 2; ++t)
#pragma unroll
      for (int p = 0; p < 2; ++p) {
        xv[4 * t + 2 * p] = f32x2{blo(xl[t][p]), blo(xh[t][p])};
        xv[4 * t + 2 * p + 1] = f32x2{bhi(xl[t][p]), bhi(xh[t][p])};
      }
  }
  ST(1);

  u32x4 w2a[10][4];

  auto layer = [&](auto LC) {
    constexpr int L = decltype(LC)::value;
    constexpr int S0 = 2 + 16 * L;
    {
      FRESH();
      if constexpr (L == 1) { fill_rows(OFF_U + MB2); fill_xn(OFF_XN + MB2); SB(); }
      const f32x2 s2 = ((xv[0] + xv[1]) + (xv[2] + xv[3])) + ((xv[4] + xv[5]) + (xv[6] + xv[7]));
      const float mean = reduce2(s2.x, s2.y) * (1.0f / 512.0f);
      const f32x2 M = bcast2(mean);
      f32x2 d[8];
#pragma unroll
      for (int i = 0; i < 8; ++i) d[i] = xv[i] - M;
      f32x2 q0 = d[0] * d[0], q1 = d[1] * d[1];
#pragma unroll
      for (int i = 2; i < 8; i += 2) { q0 += d[i] * d[i]; q1 += d[i + 1] * d[i + 1]; }
      q0 += q1;
      f32x2 R, O;
      ln_finish(mean, reduce2(q0.x, q0.y), R, O);
      float* img = (float*)(lds + LDS_SCR);
      u32* inp = (u32*)(img + w * 1280);
#pragma unroll
      for (int i = 0; i < 8; ++i) {
        const f32x2 v = d[i] * R + O;
        inp[(16 * (m & 1) + 4 * g + (i & 3)) * 20 + (m >> 1) + 8 * (i >> 2)] = pkh(v.x, v.y);
      }
      const u32x4 a0 = *(const u32x4*)(inp + m * 20 + 4 * g), a1 = *(const u32x4*)(inp + (16 + m) * 20 + 4 * g);
      if constexpr (L == 0) { if (b < 80) prep_load(P, b * 4 + w + 1024, lane, pv1); }
      const int ph = tid >> 1, fq = ph ? 512 - ph : 256, fq1 = 384 - ph;
      u32 ap0 = (ph + (ph >> 4) * 4) * 4, ap1 = ap0 + 640, ar0 = (fq + (fq >> 4) * 4) * 4, ar1 = (fq1 + (fq1 >> 4) * 4) * 4;
      PIN(ap0); PIN(ap1); PIN(ar0); PIN(ar1);
      const float* p0 = (const float*)((const u8*)img + ap0);
      const float* p1 = (const float*)((const u8*)img + ap1);
      const float* r0 = (const float*)((const u8*)img + ar0);
      const float* r1 = (const float*)((const u8*)img + ar1);
      const float sc = 1.0f / 64.0f, sg = (tid & 1) ? -sc : sc;
      f32x2 cA = {sc, sc}, cB = {sg, -sg}, cC = {sc, -sc}, cD = {sg, sg};
      f32x2 eA = ph ? cA : f32x2{2 * sc, 0.f}, eB = ph ? cB : f32x2{2 * sg, 0.f}, eC = ph ? cC : f32x2{0.f, 2 * sg}, eD = ph ? cD : f32x2{0.f, 2 * sc};
      u32 zo = OFF_Z + L * MB2 + pos * 8192 + t16;
      PIN(cB); PIN(cD); PIN(eA); PIN(eB); PIN(eC); PIN(eD); PIN(zo);
      f32x4 dr0, di0, dr1, di1;
      row_fft<false, 5>(rc, a0, a1, dr0, di0, dr1, di1);
      float* op = img + w * 1280 + 4 * g + 20 * m;
      *(f32x4*)(op) = dr0; *(f32x4*)(op + 640) = di0;
      SB();
      *(f32x4*)(op + 320) = dr1; *(f32x4*)(op + 960) = di1;
      if constexpr (L == 1) vmwait<2>();
      bar();
      f32x2 pa[4], qa[4], pb[4], qb[4];
#pragma unroll
      for (int j = 0; j < 4; ++j) {
        pa[j] = f32x2{p0[j * 1280], p0[j * 1280 + 640]}; qa[j] = f32x2{r0[j * 1280], r0[j * 1280 + 640]};
      }
#pragma unroll
      for (int j = 0; j < 4; ++j) {
        pb[j] = f32x2{p1[j * 1280], p1[j * 1280 + 640]}; qb[j] = f32x2{r1[j * 1280], r1[j * 1280 + 640]};
      }
      SB();
      u32x4 o;
#pragma unroll
      for (int j = 0; j < 4; ++j) {
        const f32x2 v = eD * swp(qa[j]) + (eC * qa[j] + (eB * swp(pa[j]) + eA * pa[j]));
        o[j] = pkh(v.x, v.y);
      }
      st4(o, zo);
#pragma unroll
      for (int j = 0; j < 4; ++j) {
        const f32x2 v = cD * swp(qb[j]) + (cC * qb[j] + (cB * swp(pb[j]) + cA * pb[j]));
        o[j] = pkh(v.x, v.y);
      }
      st4(o, zo + 4096);
      SB();
      if constexpr (L == 1) {
#pragma unroll
        for (int t = 0; t < 2; ++t)
#pragma unroll
          for (int p = 0; p < 2; ++p) {
            xl[t][p] = pkb(xv[4 * t + 2 * p].x, xv[4 * t + 2 * p + 1].x); xh[t][p] = pkb(xv[4 * t + 2 * p].y, xv[4 * t + 2 * p + 1].y);
          }
        PIN(xl[0]); PIN(xl[1]); PIN(xh[0]); PIN(xh[1]);
      }
    }
    if constexpr (L == 0) {
      FRESH();
      vmwait<8 + 2>();
#pragma unroll
      for (int j = 0; j < 8; ++j) { PIN(wfr[j]); PIN(wfi[j]); PIN(wn[j]); }
      u8* bw = lds + w * 2048;
      u32x4 v0, v1;
#pragma unroll
      for (int j = 0; j < 4; ++j) { v0[j] = wfr[j] | (wfi[j] << 16); v1[j] = wfr[4 + j] | (wfi[4 + j] << 16); }
      *(u32x4*)(bw + lane * 32) = v0; *(u32x4*)(bw + lane * 32 + 16) = v1;
      st4(*(const u32x4*)(bw + l16), OFF_WX + pos * 8192 + w * 2048 + l16);
      st4(*(const u32x4*)(bw + 1024 + l16), OFF_WX + pos * 8192 + w * 2048 + 1024 + l16);
      const float hr = blo(wn[0]) + blo(wn[2]), hi = blo(wn[1]) - blo(wn[3]), nr = blo(wn[4]) + blo(wn[6]), ni = blo(wn[5]) - blo(wn[7]);
      const f32x4 ab = {0.25f * (hr + nr), 0.25f * (hi + ni), 0.25f * (hr - nr), 0.25f * (hi - ni)};
      if (tid < 8) st4(__builtin_bit_cast(u32x4, ab), OFF_WNY + tid * 4096 + pos * 16);
    }
    ST(S0);
    {
      FRESH();
      const int phi = pos, pi_ = w >> 1, kt = w & 1;
      const int k1l = 2 * (16 * kt + m) + pi_;
      auto consts = [&](auto PC) { col_consts<decltype(PC)::value>(cc, m, g, w); SB(); };
      using I0 = std::integral_constant<int, 0>; using I1 = std::integral_constant<int, 1>;
      using I2 = std::integral_constant<int, 2>; using I3 = std::integral_constant<int, 3>;
      u8* const bw = lds + 16384 + w * 8192;
      const u32 zo = (tid >> 1) * 8192 + (tid & 1) * 16;
      const u8* zb0 = ws + OFF_Z + L * MB2 + phi * 32;
      const u8* zb1 = zb0 + 128 * 8192;
      const u8* wb0 = ws + OFF_WX + phi * 32;
      const u8* wb1 = wb0 + 128 * 8192;
      const u32 wo = zo;
      PINS(zb0); PINS(zb1); PINS(wb0); PINS(wb1);
      {
        u32x4 fl0, fl1;
        const u32 o = lane * 32;
        const u8* fb = ws + (L == 0 ? OFF_FLAG : OFF_FLAG2);
        ld4<0>(fl0, o, fb); ld4<16>(fl1, o, fb);
        SB();
        consts(I0{});
        consts(I1{});
        vmwait<0>(); PIN(fl0); PIN(fl1);
        RETRY(!(fl0.x == ep.x && fl0.y == ep.y && fl0.z == ep.x && fl0.w == ep.y && fl1.x == ep.x && fl1.y == ep.y && fl1.z == ep.x && fl1.w == ep.y), {
          CNT(0);
          ld4r<0>(fl0, o, fb); ld4r<16>(fl1, o, fb);
          vmwait<0>(); PIN(fl0); PIN(fl1);
        });
      }
      u32x4 z0, z1, wv[2];
      ld4<0>(z0, zo, zb0); ld4<0>(z1, zo, zb1);
      ld4<0>(wv[0], wo, wb0); ld4<0>(wv[1], wo, wb1);
      SB();
      u32x4 wa[8];
      if (phi == 0) static_for<8>([&](auto J) { constexpr int j = decltype(J)::value; ld4<0>(wa[j], t16, ws + OFF_WNY + j * 4096); });
      if constexpr (L == 0) {
        auto put = [&](int rep) {
#pragma unroll
          for (int x = 0; x < 4; ++x) {
            const int src = x * 16 + (lane >> 2);
            st4(*(const u32x4*)(bw + rep * 4096 + src * 64 + (((lane & 3) + src) & 3) * 16), prep_dst(b * 4 + w + 1024 * rep, 4 * x + g) + m * 16);
          }
        };
        u32x4 ot[4];
#pragma unroll
        for (int j = 0; j < 8; ++j) PIN(pv0[j]);
        transpose8x4(pv0, ot);
#pragma unroll
        for (int x = 0; x < 4; ++x) *(u32x4*)(bw + lane * 64 + ((x + lane) & 3) * 16) = ot[x];
        put(0);
        SB();
        consts(I2{});
        if (b < 80) {
#pragma unroll
          for (int j = 0; j < 8; ++j) PIN(pv1[j]);
          transpose8x4(pv1, ot);
#pragma unroll
          for (int x = 0; x < 4; ++x) *(u32x4*)(bw + 4096 + lane * 64 + ((x + lane) & 3) * 16) = ot[x];
          put(1);
        }
        SB();
        consts(I3{});
        fill_xn(OFF_XN);
      } else {
        consts(I2{});
        ring4(2);
        SB();
        consts(I3{});
      }
      u8* const zi = lds + LDS_SCR;
      u32 zw;
      {
        const int pp = tid >> 1;
        zw = (tid & 1) * 4096 + ((pp >> 6) & 1) * 2048 + (((pp >> 3) & 3) * 16 + 2 * (pp & 7)) * 16 + ((pp >> 5) & 1) * 8;
        PIN(zw);
      }
      u32 uo = OFF_U + L * MB2 + phi * 8192 + w * 2048 + l16;
      PIN(uo);
      SB();
      bar_nw();
      ST(S0 + 1);
      vmwait<(L == 0 ? 6 : 4)>();
      PIN(z0); PIN(z1); PIN(wv[0]); PIN(wv[1]);
      if constexpr (L == 0) {
        RETRY(max4(max4(max4(max4(0, z0), z1), wv[0]), wv[1]) == SENT, {
          CNT(1);
          if (bad_) { ld4r<0>(z0, zo, zb0); ld4r<0>(z1, zo, zb1); ld4r<0>(wv[0], wo, wb0); ld4r<0>(wv[1], wo, wb1); }
          vmwait<0>(); PIN(z0); PIN(z1); PIN(wv[0]); PIN(wv[1]);
        });
      } else {
        RETRY(max4(max4(0, z0), z1) == SENT, {
          CNT(2);
          if (bad_) { ld4r<0>(z0, zo, zb0); ld4r<0>(z1, zo, zb1); }
          vmwait<0>(); PIN(z0); PIN(z1);
        });
      }
      ST(S0 + 2);
      *(u32x2*)(zi + zw) = u32x2{z0.x, z0.y}; *(u32x2*)(zi + zw + 16) = u32x2{z0.z, z0.w};
      *(u32x2*)(zi + zw + 1024) = u32x2{z1.x, z1.y}; *(u32x2*)(zi + zw + 1024 + 16) = u32x2{z1.z, z1.w};
      *(u32x4*)(zi + 8192 + t16) = wv[0]; *(u32x4*)(zi + 12288 + t16) = wv[1];
      bar();
      const u8* zr = zi + pi_ * 4096 + l16;
      const u32x4 f00 = *(const u32x4*)(zr), f01 = *(const u32x4*)(zr + 1024), f10 = *(const u32x4*)(zr + 2048), f11 = *(const u32x4*)(zr + 3072);
      wv[0] = *(const u32x4*)(zi + 8192 + tid * 32); wv[1] = *(const u32x4*)(zi + 8192 + tid * 32 + 16);
      fill_h(OFF_H + L * H_BYTES, 0); fill_h(OFF_H + L * H_BYTES, 1); fill_h(OFF_H + L * H_BYTES, 2); fill_h(OFF_H + L * H_BYTES, 3);
      f32x4 d0, d1, d2, d3;
      mm8<false, 3>(d0, d1, d2, d3, f00, cc.b1re[0], f01, cc.b1re[1], f00, cc.b1im[0], f01, cc.b1im[1], f10, cc.b1re[0], f11, cc.b1re[1], f10, cc.b1im[0], f11, cc.b1im[1]);
      const u32x4 b20 = twiddle<false>(d0, d1, cc.t1c[0], cc.t1s[0]);
      SB();
      const u32x4 b21 = twiddle<false>(d2, d3, cc.t1c[1], cc.t1s[1]);
      f32x2 wr[2][2], wi[2][2];
      auto unpack_w = [&]() {
#pragma unroll
        for (int t = 0; t < 2; ++t)
#pragma unroll
          for (int h = 0; h < 2; ++h) {
            wr[t][h] = f32x2{blo(wv[t][2 * h]), blo(wv[t][2 * h + 1])}; wi[t][h] = f32x2{bhi(wv[t][2 * h]), bhi(wv[t][2 * h + 1])};
            PIN(wr[t][h]); PIN(wi[t][h]);
          }
      };
      mm8<false, 0>(d0, d1, d2, d3, cc.a2re[0][0], b20, cc.a2re[0][1], b21, cc.a2im[0][0], b20, cc.a2im[0][1], b21, cc.a2re[1][0], b20, cc.a2re[1][1], b21, cc.a2im[1][0],
                    b20, cc.a2im[1][1], b21);
      SB();
      unpack_w();
      SB();
      u32x4 a30, a31;
      if (phi != 0) {
        a30 = cmul_b(d0, d1, wr[0], wi[0]);
        a31 = cmul_b(d2, d3, wr[1], wi[1]);
      } else {
        u32 mx = 0;
#pragma unroll
        for (int j = 0; j < 8; ++j) { PIN(wa[j]); mx = max4(mx, wa[j]); }
        _Pragma("nounroll") for (int sp = 0; sp < SPIN_CAP && __any(mx == SENT); ++sp) {
          static_for<8>([&](auto J) { constexpr int j = decltype(J)::value; ld4r<0>(wa[j], t16, ws + OFF_WNY + j * 4096); });
          vmwait<0>();
          mx = 0;
#pragma unroll
          for (int j = 0; j < 8; ++j) { PIN(wa[j]); mx = max4(mx, wa[j]); }
        }
        float* pl = (float*)zi;
        bar_nw();
        float* pw = pl + k1l * 36 + 4 * g;
        *(f32x4*)(pw) = d0; *(f32x4*)(pw + 16) = d2; *(f32x4*)(pw + 2340) = d1; *(f32x4*)(pw + 2340 + 16) = d3;
        if (k1l == 0) {
#pragma unroll
          for (int r = 0; r < 4; ++r) {
            pl[64 * 36 + ((4 * g + r - 1) & 31)] = d0[r]; pl[64 * 36 + ((16 + 4 * g + r - 1) & 31)] = d2[r];
            pl[2340 + 64 * 36 + ((4 * g + r - 1) & 31)] = d1[r]; pl[2340 + 64 * 36 + ((16 + 4 * g + r - 1) & 31)] = d3[r];
          }
        }
        bar();
        const float* pr_ = pl + (64 - k1l) * 36 + 28 - 4 * g;
        const f32x4 qr0 = *(const f32x4*)(pr_), qr1 = *(const f32x4*)(pr_ - 16), qi0 = *(const f32x4*)(pr_ + 2340), qi1 = *(const f32x4*)(pr_ + 2340 - 16);
#pragma unroll
        for (int r = 0; r < 4; ++r) {
          const f32x4 c0 = __builtin_bit_cast(f32x4, wa[r]), c1 = __builtin_bit_cast(f32x4, wa[4 + r]);
          a30[r] = pkb(d0[r] * c0[0] - d1[r] * c0[1] + qr0[3 - r] * c0[2] + qi0[3 - r] * c0[3],
                       d0[r] * c0[1] + d1[r] * c0[0] + qr0[3 - r] * c0[3] - qi0[3 - r] * c0[2]);
          a31[r] = pkb(d2[r] * c1[0] - d3[r] * c1[1] + qr1[3 - r] * c1[2] + qi1[3 - r] * c1[3],
                       d2[r] * c1[1] + d3[r] * c1[0] + qr1[3 - r] * c1[3] - qi1[3 - r] * c1[2]);
        }
      }
      u8* ex = lds + LDS_SCR + 20480;
      mm8<true, 3, 2>(d0, d1, d2, d3, a30, cc.b3re[0][0], a31, cc.b3re[0][1], a30, cc.b3im[0][0], a31, cc.b3im[0][1], a30, cc.b3re[1][0], a31, cc.b3re[1][1], a30, cc.b3im[1][0],
                   a31, cc.b3im[1][1]);
      *(u32x4*)(ex + w * 1024 + l16) = twiddle<true>(d0, d1, cc.t2c[0], cc.t2s[0]);
      SB();
      *(u32x4*)(ex + (4 + w) * 1024 + l16) = twiddle<true>(d2, d3, cc.t2c[1], cc.t2s[1]);
      vmwait<4>();
      bar();
      const u8* er = ex + (w & 1) * 4096 + l16;
      const u32x4 g0 = *(const u32x4*)(er), g1 = *(const u32x4*)(er + 1024), g2 = *(const u32x4*)(er + 2048), g3 = *(const u32x4*)(er + 3072);
      mm8<true, 7, 1>(d0, d1, d2, d3, cc.a4re[0][0], g0, cc.a4re[0][1], g1, cc.a4im[0][0], g0, cc.a4im[0][1], g1, cc.a4re[1][0], g2, cc.a4re[1][1], g3, cc.a4im[1][0], g2,
                   cc.a4im[1][1], g3);
      {
        const f32x2 lr0 = lo2(d0) + lo2(d2), li0 = lo2(d1) + lo2(d3), hr0 = lo2(d0) - lo2(d2), hi0 = lo2(d1) - lo2(d3);
        const f32x2 lr1 = hi2(d0) + hi2(d2), li1 = hi2(d1) + hi2(d3), hr1 = hi2(d0) - hi2(d2), hi1 = hi2(d1) - hi2(d3);
        if (phi != 0) {
          const f32x2 pr0 = lr0 - hi0, pi0 = li0 + hr0, qr0 = lr0 + hi0, qi0 = hr0 - li0;
          st4(u32x4{pkb(pr0.x, pi0.x), pkb(qr0.x, qi0.x), pkb(pr0.y, pi0.y), pkb(qr0.y, qi0.y)}, uo);
          const f32x2 pr1 = lr1 - hi1, pi1 = li1 + hr1, qr1 = lr1 + hi1, qi1 = hr1 - li1;
          st4(u32x4{pkb(pr1.x, pi1.x), pkb(qr1.x, qi1.x), pkb(pr1.y, pi1.y), pkb(qr1.y, qi1.y)}, uo + 1024);
          MEMFENCE();
        } else {
          st4(u32x4{pkb(lr0.x, hr0.x), pkb(li0.x, hi0.x), pkb(lr0.y, hr0.y), pkb(li0.y, hi0.y)}, uo);
          st4(u32x4{pkb(lr1.x, hr1.x), pkb(li1.x, hi1.x), pkb(lr1.y, hr1.y), pkb(li1.y, hi1.y)}, uo + 1024);
        }
      }
    }
    ST(S0 + 3);
    {
      FRESH();
      SB();
      row_consts<true, 0>(rc, m, g);
      f32x2 xa[4], xb[4];
#pragma unroll
      for (int i = 0; i < 4; ++i) {
        xa[i] = f32x2{blo(xl[i >> 1][i & 1]), bhi(xl[i >> 1][i & 1])}; xb[i] = f32x2{blo(xh[i >> 1][i & 1]), bhi(xh[i >> 1][i & 1])};
        PIN(xa[i]); PIN(xb[i]);
      }
      SB();
      u32x4 c0, c1;
      const u32 uo = (tid >> 1) * 8192 + (tid & 1) * 16;
      const u8* ub0 = ws + OFF_U + L * MB2 + pos * 32;
      const u8* ub1 = ub0 + 128 * 8192;
      ld4<0>(c0, uo, ub0); ld4<0>(c1, uo, ub1);
      fill_h(OFF_H + L * H_BYTES, 4); fill_h(OFF_H + L * H_BYTES, 5); fill_h(OFF_H + L * H_BYTES, 6);
      SB();
      row_consts<true, 1>(rc, m, g);
      SB();
      if constexpr (L == 1) ring4(3);
      u32* img = (u32*)(lds + LDS_SCR);
      u32 ip0, iq0, iq1, xno, tw0, tw1, trd;
      {
        const int ph = tid >> 1, fq0 = ph ? 512 - ph : 256, fq1 = 384 - ph;
        ip0 = ((tid & 1) * 1280 + (ph & 31) * 20 + (ph >> 5)) * 4;
        iq0 = ((tid & 1) * 1280 + (fq0 & 31) * 20 + (fq0 >> 5)) * 4; iq1 = ((tid & 1) * 1280 + (fq1 & 31) * 20 + (fq1 >> 5)) * 4;
        xno = OFF_XN + L * MB2 + (pos >> 1) * 16384 + (tid >> 3) * 256 + (pos & 1) * 128 + (tid & 7) * 16;
        tw0 = (4 * m + g) * 128 + ((2 * w) ^ (m & 7)) * 16; tw1 = (4 * m + g) * 128 + ((2 * w + 1) ^ (m & 7)) * 16;
        trd = (tid >> 3) * 128 + ((tid & 7) ^ ((tid >> 5) & 7)) * 16;
        PIN(ip0); PIN(iq0); PIN(iq1); PIN(xno); PIN(tw0); PIN(tw1); PIN(trd);
      }
      SB();
      ST(S0 + 4);
      vmwait<(L == 0 ? 3 : 7)>(); PIN(c0); PIN(c1);
      {
        RETRY(max4(max4(0, c0), c1) == SENT, {
          CNT(3);
          if (bad_) { ld4r<0>(c0, uo, ub0); ld4r<0>(c1, uo, ub1); }
          vmwait<0>(); PIN(c0); PIN(c1);
        });
      }
      ST(S0 + 5);
      {
        u8* d = (u8*)img;
        *(u32*)(d + ip0) = c0.x; *(u32*)(d + iq0) = c0.y; *(u32*)(d + 2560 + ip0) = c0.z; *(u32*)(d + 2560 + iq0) = c0.w;
        *(u32*)(d + ip0 + 16) = c1.x; *(u32*)(d + iq1) = c1.y; *(u32*)(d + 2560 + ip0 + 16) = c1.z; *(u32*)(d + 2560 + iq1) = c1.w;
      }
      bar();
      const u32x4 a0 = *(const u32x4*)(img + w * 640 + m * 20 + 4 * g), a1 = *(const u32x4*)(img + w * 640 + (16 + m) * 20 + 4 * g);
      if constexpr (L == 1) ring4(4);
      if constexpr (L == 0) ring4(0);
      f32x4 dr0, di0, dr1, di1;
      row_fft<true, 7>(rc, a0, a1, dr0, di0, dr1, di1);
      if constexpr (L == 0) ring4(1); else ring4(5);
      const float ys = 1.0f / 32768.0f;
      xa[0] += lo2(dr0) * ys; xa[1] += hi2(dr0) * ys; xa[2] += lo2(dr1) * ys; xa[3] += hi2(dr1) * ys;
      xb[0] += lo2(di0) * ys; xb[1] += hi2(di0) * ys; xb[2] += lo2(di1) * ys; xb[3] += hi2(di1) * ys;
      const f32x2 sa = (xa[0] + xa[1]) + (xa[2] + xa[3]), sb = (xb[0] + xb[1]) + (xb[2] + xb[3]);
      const float mean = reduce2(sa.x + sa.y, sb.x + sb.y) * (1.0f / 512.0f);
      const f32x2 M = bcast2(mean);
      f32x2 da[4], db[4];
#pragma unroll
      for (int i = 0; i < 4; ++i) { da[i] = xa[i] - M.x; db[i] = xb[i] - M.y; }
      f32x2 qa = da[0] * da[0], qb = db[0] * db[0];
#pragma unroll
      for (int i = 1; i < 4; ++i) { qa += da[i] * da[i]; qb += db[i] * db[i]; }
      if constexpr (L == 0) { SB(); ring4(2); SB(); }
      f32x2 R, O;
      ln_finish(mean, reduce2(qa.x + qa.y, qb.x + qb.y), R, O);
      u32x4 na, nb;
#pragma unroll
      for (int i = 0; i < 4; ++i) {
        const f32x2 va = da[i] * R.x + O.x, vb = db[i] * R.y + O.y;
        na[i] = pkb(va.x, va.y); nb[i] = pkb(vb.x, vb.y);
      }
      u8* tr = lds + LDS_SCR + 12288;
      *(u32x4*)(tr + tw0) = na; *(u32x4*)(tr + tw1) = nb;
      vmwait<12>();
      bar();
      st4(*(const u32x4*)(tr + trd), xno);
      st4(*(const u32x4*)(tr + 4096 + trd), xno + 8192);
      SB();
      const u32x4 rl = {pkb(xa[0].x, xa[0].y), pkb(xa[1].x, xa[1].y), pkb(xa[2].x, xa[2].y), pkb(xa[3].x, xa[3].y)};
      const u32x4 rh = {pkb(xb[0].x, xb[0].y), pkb(xb[1].x, xb[1].y), pkb(xb[2].x, xb[2].y), pkb(xb[3].x, xb[3].y)};
      if constexpr (L == 0) {
        *(u32x4*)(lds + LDS_STASH + t16) = rl; *(u32x4*)(lds + LDS_STASH + 4096 + t16) = rh;
        ring4(3); ring4(4); ring4(5); ring4(6); ring4(7);
      } else {
        const u32 o = OFF_X3 + (8 * pos + 2 * w) * 1024 + xo;
        st2(u32x2{rl.x, rl.y}, o); st2(u32x2{rl.z, rl.w}, o + 512);
        st2(u32x2{rh.x, rh.y}, o + 1024); st2(u32x2{rh.z, rh.w}, o + 1536);
        __builtin_amdgcn_s_sleep(6);
      }
    }
    ST(S0 + 6);
    {
      FRESH();
      u32x4 tk[32], he[32], hl[16], w2t[4], w2v[3][4];
      u32x2 hh[16];
      f32x4 ac1[6][2], ac2[4][2], ac3[4][2];
      const u8* hs0 = ws + OFF_H + L * H_BYTES + (8 * G + 4 * half2) * 57344 + (4 * w) * 3584 + 3584;
      const u8* hs2 = hs0 + 7168;
      const u8* w2src = ws + OFF_W2 + (ct * 4 + w) * 57344;
      PINS(hs0); PINS(hs2);
      u32 hv[4] = {l16, l16 + 57344, l16 + 2 * 57344, l16 + 3 * 57344}, h8[4];
#pragma unroll
      for (int u = 0; u < 4; ++u) { h8[u] = hv[u] - lane * 8; PIN(hv[u]); PIN(h8[u]); }
      auto hload = [&](auto RE, auto KC, auto UC) {
        constexpr int k = decltype(KC)::value, u = decltype(UC)::value;
        constexpr bool re = decltype(RE)::value;
        if constexpr (k == 4 || k == 5) {
          constexpr int o = (u >> 2) * 3584 + 3072 - 3584;
          if constexpr (re) ld2r<o>(hh[8 * (k - 4) + u], h8[u & 3], k == 5 ? hs2 : hs0); else ld2<o>(hh[8 * (k - 4) + u], h8[u & 3], k == 5 ? hs2 : hs0);
        } else {
          constexpr int j = k < 4 ? k : k < 10 ? k - 6 : k - 10, o = (j & 1) * 3584 + (k < 4 ? 0 : k < 10 ? 1024 : 2048) - 3584;
          u32x4& d = k < 10 ? he[(k < 4 ? 0 : 16) + 4 * j + u] : hl[4 * j + u];
          if constexpr (re) ld4r<o>(d, hv[u], j >> 1 ? hs2 : hs0); else ld4<o>(d, hv[u], j >> 1 ? hs2 : hs0);
        }
      };
      auto hseq = [&](auto IC) {
        constexpr int i = decltype(IC)::value, k = i < 16 ? i >> 2 : i < 32 ? 4 + ((i - 16) >> 3) : 6 + ((i - 32) >> 2);
        hload(std::false_type{}, std::integral_constant<int, k>{}, std::integral_constant<int, (i < 16 || i >= 32) ? (i & 3) : (i & 7)>{});
      };
      {
        const u8* w1b = ws + OFF_W1 + member * 229376 + 131072 + w * 8192;
        const u8* tk0 = ws + OFF_XN + L * MB2 + (8 * G + 2 * w) * 16384 + 4096;
        const u8* tk1 = tk0 + 8192;
        u32 tv1 = l16 + 16384, lhi = l16 + 65536;
        u32 hd0 = OFF_H + L * H_BYTES + (8 * G + 2 * w) * 57344 + member * 3584 + l16, hd1 = hd0 + 57344;
        u32 hd2 = hd0 + 3072 - lane * 8, hd3 = hd2 + 57344;
        PIN(tv1); PIN(lhi); PIN(hd0); PIN(hd1); PIN(hd2); PIN(hd3); PINS(tk0); PINS(tk1);
        u32 fo_ = pos * 8192 + t16, fdl = OFF_DL + (128 * G + 64 * half2 + (tid >> 3)) * 1024 + ct * 128 + (tid & 7) * 16;
        if constexpr (L == 0) { PIN(fo_); PIN(fdl); }
        auto tok = [&](auto RE, auto QC) {
          constexpr int q = decltype(QC)::value, ks = q >> 1;
          if constexpr (decltype(RE)::value) ld4r<(ks & 7) * 1024 - 4096>(tk[q], q & 1 ? tv1 : l16, ks >> 3 ? tk1 : tk0);
          else ld4<(ks & 7) * 1024 - 4096>(tk[q], q & 1 ? tv1 : l16, ks >> 3 ? tk1 : tk0);
        };
        u32 mx0 = 0, mx1 = 0;
        auto fix = [&](auto QC, u64 bad) {
          constexpr int q0 = decltype(QC)::value;
          if (__builtin_expect(bad != 0, 0)) {
            _Pragma("nounroll") for (int sp = 0; sp < SPIN_CAP; ++sp) {
              CNT(4);
              static_for<4>([&](auto I) { tok(std::true_type{}, std::integral_constant<int, q0 + decltype(I)::value>{}); });
              vmwait<0>();
              u32 m2 = 0;
#pragma unroll
              for (int q = q0; q < q0 + 4; ++q) { PIN(tk[q]); m2 = max4(m2, tk[q]); }
              if (!__any(m2 == SENT)) break;
            }
          }
        };
        static_for<NTOK0>([&](auto Q) { tok(std::false_type{}, Q); });
        constexpr int PD = 8;
        u32x4 af[PD];
        auto frag = [&](int x) {
          const int o = (x & 127) * 1024;
          return o < 65536 ? *(const u32x4*)(lds + l16 + o) : *(const u32x4*)(lds + lhi + (o - 65536));
        };
        vmwait<NTOK0>();
        bar_nw();
#pragma unroll
        for (int x = 0; x < PD - 1; ++x) af[x] = frag(x);
        vmwait<NTOK0 - 4>();
        ST(S0 + 7);
#pragma unroll
        for (int q = 0; q < 4; ++q) PIN(tk[q]);
        mx0 = max4(max4(0, tk[0]), tk[1]); mx1 = max4(max4(0, tk[2]), tk[3]);
        fix(std::integral_constant<int, 0>{}, __ballot(umax(mx0, mx1) == SENT));
        SB();
        ST(S0 + 8);
        float eg[2][2], eu[2][2];
        u64 bm = 0;
        const u32 fo = fo_;
        u32x4 eo[1];
        auto epi = [&](auto NC) {
          constexpr int n = decltype(NC)::value;
          if constexpr (n < NEPI1 + NEPI2) {
            constexpr bool p1 = n < NEPI1;
            constexpr int i = p1 ? n - (n > 36) - (n > 73) - (n > 92) : (n - NEPI1) - (n - NEPI1) / 37;
            if constexpr (p1 && (n == 36 || n == 73)) st4(eo[0], n == 36 ? hd0 : hd1);
            else if constexpr (p1 && (n == 92 || n == 111)) st2(u32x2{eo[0].x, eo[0].y}, n == 92 ? hd2 : hd3);
            else if constexpr (!p1 && (n - NEPI1) % 37 == 36) st4(eo[0], ((n - NEPI1) / 37 ? hd1 : hd0) + 1024);
            else {
              constexpr int s = i & 1, st = (i % 18) >> 1, blk = i / 18;
              constexpr int u = p1 ? (blk < 4 ? blk >> 1 : blk - 4) : blk >> 1, pr = p1 ? (blk < 4 ? blk & 1 : 2) : blk & 1;
              constexpr int ei = 0, d = 2 * (pr & 1) + s;
              const f32x4& ga = p1 ? ac1[2 * pr][u] : ac2[2 * (pr & 1)][u];
              const f32x4& up = p1 ? ac1[2 * pr + 1][u] : ac2[2 * (pr & 1) + 1][u];
              if constexpr (st < 2) { eg[s][st] = ga[2 * s + st]; PIN(eg[s][st]); }
              else if constexpr (st < 4) { eu[s][st - 2] = up[2 * s + st - 2]; PIN(eu[s][st - 2]); }
              else if constexpr (st < 6) { eg[s][st - 4] = relu(eg[s][st - 4]); PIN(eg[s][st - 4]); }
              else if constexpr (st < 8) { eg[s][st - 6] *= eu[s][st - 6]; PIN(eg[s][st - 6]); }
              else { u32 v = pkb(eg[s][0], eg[s][1]); PIN(v); eo[ei][d] = v; }
            }
            SB();
          }
        };
        static_for<224>([&](auto XC) {
          constexpr int x = decltype(XC)::value, job = G1S[L].job[x], ji = G1S[L].idx[x], e0 = G1S[L].epi[x], e1 = G1S[L].epi[x + 1];
          if constexpr (x == 32 || x == 64) bar_nw();
          if constexpr (x == 97) { vmwait<g1_wait(L, x, 2, 7)>(); bar_nw(); }
          if constexpr (x == 150) {
            vmwait<g1_wait(L, x, 2, 23)>(); bar_nw();
            if constexpr (L == 0) { if (tid == 0) st2(ep, OFF_FLAG2 + pos * 8); }
          }
          constexpr int cj = (x + 12) / 12, co = x + 12 - 12 * cj;
          constexpr bool chk = x < 84 && co >= 5;
          constexpr int ks = x < 96 ? x / 6 : x < 160 ? (x - 96) >> 2 : (x - 160) >> 2, t = x < 96 ? x % 6 : x & 3;
          auto mm = [&](int u) {
            f32x4& a = x < 96 ? ac1[x < 96 ? t : 0][u] : x < 160 ? ac2[x < 96 ? 0 : t][u] : ac3[x < 96 ? 0 : t][u];
            if constexpr (ks == 0) a = mfma_b(af[x % PD], tk[u], f32x4{0.f, 0.f, 0.f, 0.f}); else a = mfma_b(af[x % PD], tk[2 * ks + u], a);
          };
          SB();
          mm(0);
          SB();
          if constexpr (x < 223) __builtin_amdgcn_s_waitcnt(0xC07F | ((x + PD - 2 < 224 ? PD - 3 : 222 - x) << 8));
          if constexpr (chk && co == 5) {
            vmwait<g1_wait(L, x, 1, 4 * cj + 3)>();
            PIN(tk[4 * cj]); PIN(tk[4 * cj + 1]); PIN(tk[4 * cj + 2]); PIN(tk[4 * cj + 3]);
          }
          SB();
          if constexpr (chk && co >= 6 && co < 10) {
            const u32x4 v = tk[4 * cj + co - 6];
            mx0 = co == 6 ? umax(v.x, v.y) : max3(mx0, v.x, v.y); PIN(mx0); SB();
            mx1 = co == 6 ? umax(v.z, v.w) : max3(mx1, v.z, v.w); PIN(mx1);
          }
          if constexpr (chk && co == 10) { mx0 = umax(mx0, mx1); PIN(mx0); SB(); bm = __ballot(mx0 == SENT); PINS(bm); }
          if constexpr (e0 < e1) epi(std::integral_constant<int, e0>{});
          if constexpr (e0 + 1 < e1) epi(std::integral_constant<int, e0 + 1>{});
          SB();
          mm(1);
          SB();
          if constexpr (chk && co == 11) fix(std::integral_constant<int, 4 * cj>{}, bm);
          if constexpr (job == 2 && (ji & 3) == 0) set_m0(ldsbase + (ji >> 3) * 32768 + w * 8192 + (ji & 4) * 1024);
          SB();
          if constexpr (x + PD - 1 < 224) af[(x + PD - 1) % PD] = frag(x + PD - 1);
          SB();
          if constexpr (job == 1) tok(std::false_type{}, std::integral_constant<int, ji>{});
          if constexpr (job == 2) dma1<(ji & 3) * 1024>(l16, w1b + (ji >> 3) * 32768 + (ji & 4) * 1024);
          if constexpr (job == 3) ld4a<(ji & 7) * 1024 - 4096>(w2a[ji >> 2][ji & 3], l16, w2src + (ji >> 3) * 8192 + 4096);
          if constexpr (job == 5) ld4a<ji * 1024>(w2t[ji], l16, w2src + 40960);
          if constexpr (job == 6) {
            if constexpr (ji < 2) st4(sent4, fdl + ji * 32768);
            else st4(sent4, fo + OFF_Z + MB2 + (ji - 2) * 4096);
          }
          if constexpr (job == 4) hseq(std::integral_constant<int, ji>{});
          if constexpr (e0 + 2 < e1) epi(std::integral_constant<int, e0 + 2>{});
          SB();
        });
        ST(S0 + 9);
#pragma unroll
        for (int u = 0; u < 2; ++u) {
          u32x4 o;
#pragma unroll
          for (int p = 0; p < 2; ++p) {
            const f32x4 ga = ac3[2 * p][u], up = ac3[2 * p + 1][u];
            const f32x2 a = f32x2{relu(ga[0]), relu(ga[1])} * lo2(up), c = f32x2{relu(ga[2]), relu(ga[3])} * hi2(up);
            o[2 * p] = pkb(a.x, a.y); o[2 * p + 1] = pkb(c.x, c.y);
          }
          st4(o, (u ? hd1 : hd0) + 2048);
        }
        SB();
      }
      ST(S0 + 10);
      {
        f32x4 acc[4][4];
        u32x4 x3a, x3b;
        const int tokl = 16 * w + (lane >> 3);
        u32 eo = (128 * G + 64 * half2 + tokl) * 1024 + ct * 128 + (lane & 7) * 16;
        PIN(eo);
        const u8* w2x = w2src + 45056 + 4096;
        const u8* x3s = ws + OFF_X3;
        u32 lx4 = l16 + 4096, eo2 = eo + 8192;
        u64 bm = 0;
        PINS(w2x); PINS(x3s); PIN(lx4); PIN(eo2);
        constexpr int NE2 = 48 - NEARLY, LB = NE2 + 12;
        auto cum = [](int n) constexpr { int c = 0; for (int i = 0; i < n; ++i) c += i < LB + 16 ? 1 : 0; return c; };
        auto young = [cum](int k, int n) constexpr {
          const int i = k < 4 ? 4 * k + 3 : k < 6 ? 16 + 8 * (k - 4) + 7 : 32 + 4 * (k - 6) + 3;
          if (k < 10 && i < NEARLY) return cum(n) + 2 + G1S[L].cum[224] - G1S[L].pos[4][i] - 1;
          if (k < 10) return cum(n) - cum(i - NEARLY + 1);
          return cum(n) - cum(LB + 4 * (k - 10) + 4);
        };
        u32 mx0 = 0, mx1 = 0;
        auto pin_k = [&](auto KC) {
          constexpr int k = decltype(KC)::value;
#pragma unroll
          for (int u = 0; u < 4; ++u) {
            if constexpr (k < 4) PIN(he[4 * k + u]);
            else if constexpr (k < 6) { PIN(hh[8 * (k - 4) + u]); PIN(hh[8 * (k - 4) + 4 + u]); }
            else if constexpr (k < 10) PIN(he[16 + 4 * (k - 6) + u]);
            else { PIN(hl[4 * (k - 10) + u]); if constexpr (k > 10) PIN(w2v[k - 11][u]); }
          }
        };
        auto opnd = [&](auto KC, int u) {
          constexpr int k = decltype(KC)::value;
          if constexpr (k < 4) return he[4 * k + u];
          else if constexpr (k < 6) return u32x4{hh[8 * (k - 4) + u].x, hh[8 * (k - 4) + u].y, hh[8 * (k - 4) + 4 + u].x, hh[8 * (k - 4) + 4 + u].y};
          else if constexpr (k < 10) return he[16 + 4 * (k - 6) + u];
          else return hl[4 * (k - 10) + u];
        };
        auto fixk = [&](auto KC, u64 bad) {
          constexpr int k = decltype(KC)::value;
          if (__builtin_expect(bad != 0, 0)) {
            _Pragma("nounroll") for (int sp = 0; sp < SPIN_CAP; ++sp) {
              CNT(k < 10 ? 5 : 6);
              static_for<(k == 4 || k == 5) ? 8 : 4>([&](auto U) { hload(std::true_type{}, KC, U); });
              vmwait<0>();
              pin_k(KC);
              if (!__any(max4(max4(max4(max4(0, opnd(KC, 0)), opnd(KC, 1)), opnd(KC, 2)), opnd(KC, 3)) == SENT)) break;
            }
          }
        };
        u32 rw[4];
#pragma unroll
        for (int c = 0; c < 4; ++c) { rw[c] = w * 16384 + c * 4096 + (g * 16 + (m ^ (2 * (2 * c + (g >> 1))))) * 16; PIN(rw[c]); }
        u32 oo[2] = {0, 0};
        if constexpr (L == 1) {
#pragma unroll
          for (int a = 0; a < 2; ++a) {
            const int tok = 128 * G + 64 * half2 + tokl + 8 * a;
            oo[a] = (row_of_pos(tok >> 3, (tok >> 1) & 3) + 1024 * (tok & 1)) * 1024 + ct * 128 + (lane & 7) * 16;
            PIN(oo[a]);
          }
        }
        vmwait<clamp63(young(0, 0))>();
        if constexpr (L == 0) {
#pragma unroll
          for (int k = 0; k < 10; ++k) { PINA(w2a[k][0]); PINA(w2a[k][1]); PINA(w2a[k][2]); PINA(w2a[k][3]); }
        }
        PINA(w2t[0]); PINA(w2t[1]); PINA(w2t[2]); PINA(w2t[3]);
        pin_k(std::integral_constant<int, 0>{});
        mx0 = max4(max4(0, he[0]), he[1]); mx1 = max4(max4(0, he[2]), he[3]);
        fixk(std::integral_constant<int, 0>{}, __ballot(umax(mx0, mx1) == SENT));
        ST(S0 + 11);
        auto wfr = [&](auto KC, int c) {
          constexpr int k = decltype(KC)::value;
          if constexpr (k < 10) return w2a[k][c]; else if constexpr (k == 10) return w2t[c]; else return w2v[k - 11][c];
        };
        static_for<48>([&](auto NC) {
          constexpr int n = decltype(NC)::value, k = n >> 2, c = n & 3;
          using KC = std::integral_constant<int, k>;
          using KN = std::integral_constant<int, k + 1>;
          static_for<4>([&](auto UC) {
            constexpr int u = decltype(UC)::value;
            SB();
            if constexpr (k == 0) acc[c][u] = mfma_b(wfr(KC{}, c), opnd(KC{}, u), f32x4{0.f, 0.f, 0.f, 0.f});
            else acc[c][u] = mfma_b(wfr(KC{}, c), opnd(KC{}, u), acc[c][u]);
            SB();
            if constexpr (c == 1 && u == 0) {
              vmwait<clamp63(young(k == 11 ? 13 : k + 1, n))>(); pin_k(KN{});
              if constexpr (k == 11) pin_k(std::integral_constant<int, 13>{});
            }
            if constexpr ((c == 1 && (u == 1 || u == 2)) || (c == 2 && u < (k == 11 ? 4 : 2)) || (k == 11 && c == 3 && u < 2)) {
              constexpr int j = c == 1 ? u - 1 : c == 2 ? 2 + u : 6 + u;
              const u32x4 v = j < 4 ? opnd(KN{}, j & 3) : opnd(std::integral_constant<int, 13>{}, j & 3);
              mx0 = j == 0 ? umax(v.x, v.y) : max3(mx0, v.x, v.y); PIN(mx0); SB();
              mx1 = j == 0 ? umax(v.z, v.w) : max3(mx1, v.z, v.w); PIN(mx1);
            }
            if constexpr (c == 3 && u == (k == 11 ? 2 : 0)) { mx0 = umax(mx0, mx1); PIN(mx0); SB(); bm = __ballot(mx0 == SENT); PINS(bm); }
            if constexpr (u == 3) {
              if constexpr (n < NE2) hseq(std::integral_constant<int, NEARLY + n>{});
              else if constexpr (n < NE2 + 8) ld4<(n - NE2) * 1024 - 4096>(w2v[(n - NE2) >> 2][(n - NE2) & 3], l16, w2x);
              else if constexpr (n < LB) ld4<(n - NE2 - 8) * 1024>(w2v[2][(n - NE2) & 3], lx4, w2x);
              else if constexpr (n < LB + 16) hload(std::false_type{}, std::integral_constant<int, 10 + ((n - LB) >> 2)>{}, std::integral_constant<int, (n - LB) & 3>{});
              if constexpr (c == 3) { fixk(KN{}, bm); if constexpr (k == 11) fixk(std::integral_constant<int, 13>{}, bm); }
            }
          });
        });
        bar_nw();
        if constexpr (L == 1) { ld4<0>(x3a, eo, x3s); ld4<0>(x3b, eo2, x3s); }
        static_for<20>([&](auto TC) {
          constexpr int t = decltype(TC)::value;
          if constexpr (t < 16) {
            SB();
            acc[t >> 2][t & 3] = mfma_b(w2v[1][t >> 2], hl[8 + (t & 3)], acc[t >> 2][t & 3]);
            acc[t >> 2][t & 3] = mfma_b(w2v[2][t >> 2], hl[12 + (t & 3)], acc[t >> 2][t & 3]);
            SB();
          }
          if constexpr (t >= 4) *(f32x4*)(lds + rw[(t - 4) >> 2] + ((t - 4) & 3) * 1024) = acc[(t - 4) >> 2][(t - 4) & 3];
        });
        SB();
        ST(S0 + 12);
        const int o8 = lane & 7;
        const u8* rp0 = lds + ((o8 >> 1) * 4 + w) * 1024 + (2 * (o8 & 1) * 16 + ((lane >> 3) ^ (2 * o8))) * 16;
        const u8* rp1 = lds + ((o8 >> 1) * 4 + w) * 1024 + (2 * (o8 & 1) * 16 + ((8 + (lane >> 3)) ^ (2 * o8))) * 16;
        bar();
        f32x4 s[2][2][4];
#pragma unroll
        for (int p = 0; p < 4; ++p) { s[0][0][p] = *(const f32x4*)(rp0 + p * 16384); s[0][1][p] = *(const f32x4*)(rp0 + p * 16384 + 256); }
#pragma unroll
        for (int p = 0; p < 4; ++p) { s[1][0][p] = *(const f32x4*)(rp1 + p * 16384); s[1][1][p] = *(const f32x4*)(rp1 + p * 16384 + 256); }
        SB();
        if constexpr (L == 1) {
          vmwait<0>(); PIN(x3a); PIN(x3b);
        }
#pragma unroll
        for (int a = 0; a < 2; ++a) {
          const f32x4 s0 = (s[a][0][0] + s[a][0][1]) + (s[a][0][2] + s[a][0][3]), s1 = (s[a][1][0] + s[a][1][1]) + (s[a][1][2] + s[a][1][3]);
          const u32x4 y = {pkb(s0[0], s0[1]), pkb(s0[2], s0[3]), pkb(s1[0], s1[1]), pkb(s1[2], s1[3])};
          if constexpr (L == 0) {
            st4(y, OFF_DL + eo + a * 8192);
          } else {
            const u32x4 xr = a ? x3b : x3a;
            u32x4 o;
#pragma unroll
            for (int p = 0; p < 4; ++p) o[p] = pkb(blo(xr[p]) + blo(y[p]), bhi(xr[p]) + bhi(y[p]));
            *(u32x4*)((u8*)P.out + oo[a]) = o;
          }
        }
      }
    }
    ST(S0 + 13);
    if constexpr (L == 0) {
      FRESH();
      SB();
      row_consts<false, 0>(rc, m, g);
      SB();
      u32x2 d0, d1, d2, d3;
      const u8* db = ws + OFF_DL + (8 * pos + 2 * w) * 1024;
      const u32x4 sl = *(const u32x4*)(lds + LDS_STASH + t16), sh = *(const u32x4*)(lds + LDS_STASH + 4096 + t16);
#pragma unroll
      for (int i = 0; i < 4; ++i) {
        xv[2 * i] = f32x2{blo(sl[i]), blo(sh[i])}; xv[2 * i + 1] = f32x2{bhi(sl[i]), bhi(sh[i])};
        PIN(xv[2 * i]); PIN(xv[2 * i + 1]);
      }
      SB();
      bar_nw();
      ld2<0>(d0, xo, db); ld2<512>(d1, xo, db); ld2<1024>(d2, xo, db); ld2<1536>(d3, xo, db);
      ring4(0);
      SB();
      row_consts<false, 1>(rc, m, g);
      SB();
      ring4(1);
      ST(S0 + 14);
      vmwait<8>(); PIN(d0); PIN(d1); PIN(d2); PIN(d3);
      RETRY(max3(max3(max3(max3(0, d0.x, d0.y), d1.x, d1.y), d2.x, d2.y), d3.x, d3.y) == SENT, {
        CNT(7);
        if (bad_) { ld2r<0>(d0, xo, db); ld2r<512>(d1, xo, db); ld2r<1024>(d2, xo, db); ld2r<1536>(d3, xo, db); }
        vmwait<0>(); PIN(d0); PIN(d1); PIN(d2); PIN(d3);
      });
      xv[0] += f32x2{blo(d0.x), blo(d2.x)}; xv[1] += f32x2{bhi(d0.x), bhi(d2.x)}; xv[2] += f32x2{blo(d0.y), blo(d2.y)}; xv[3] += f32x2{bhi(d0.y), bhi(d2.y)};
      xv[4] += f32x2{blo(d1.x), blo(d3.x)}; xv[5] += f32x2{bhi(d1.x), bhi(d3.x)}; xv[6] += f32x2{blo(d1.y), blo(d3.y)}; xv[7] += f32x2{bhi(d1.y), bhi(d3.y)};
      ST(S0 + 15);
    }
  };
  layer(std::integral_constant<int, 0>{});
  layer(std::integral_constant<int, 1>{});
  if (blockIdx.x == 0 && threadIdx.x == 0) st2(ep, OFF_E);
  ST_OUT();
}

extern "C" void kernel_launch(void* const* d_in, const int* in_sizes, int n_in, void* d_out, int out_size, void* d_ws, size_t ws_size,
                              hipStream_t stream) {
  Params p;
  p.x = (const u16*)d_in[0]; p.wr = (const u16*)d_in[1]; p.wi = (const u16*)d_in[2];
  p.wg = (const u16*)d_in[4]; p.wu = (const u16*)d_in[5]; p.wd = (const u16*)d_in[6];
  p.out = (u16*)d_out; p.ws = (u8*)d_ws;
  (void)hipFuncSetAttribute((const void*)k_all, hipFuncAttributeMaxDynamicSharedMemorySize, LDS_TOTAL);
  k_all<<<256, 256, LDS_TOTAL, stream>>>(p);
}
```

```cpp
#include <hip/hip_runtime.h>
#include <hip/hip_bf16.h>
#include <stdint.h>
#include <type_traits>
#include <utility>
#define STAMPS 1

typedef unsigned char u8;
typedef unsigned short u16;
typedef unsigned int u32;
typedef unsigned long long u64;
typedef u32 u32x2 __attribute__((ext_vector_type(2)));
typedef u32 u32x4 __attribute__((ext_vector_type(4)));
typedef float f32x2 __attribute__((ext_vector_type(2)));
typedef float f32x4 __attribute__((ext_vector_type(4)));
typedef _Float16 f16x2 __attribute__((ext_vector_type(2)));
typedef __bf16 bf16x2 __attribute__((ext_vector_type(2)));
typedef __bf16 bf16x8 __attribute__((ext_vector_type(8)));
#define DI __device__ __forceinline__
#define SB() __builtin_amdgcn_sched_barrier(0)
#define MEMFENCE() asm volatile("" ::: "memory")

constexpr u32 MB2 = 2u << 20;
constexpr u32 OFF_E = 0, OFF_FLAG = 4096, OFF_WNY = 65536;
constexpr u32 OFF_A = MB2;
constexpr u32 OFF_U = OFF_A + MB2;
constexpr u32 OFF_WX = OFF_U + MB2;
constexpr u32 OFF_X3 = OFF_WX + MB2;
constexpr u32 OFF_H = OFF_X3 + MB2;
constexpr u32 OFF_W1 = OFF_H + 7340032;
constexpr u32 OFF_W2 = OFF_W1 + 3670016;
constexpr u32 WS_USED = OFF_W2 + 1835008;
constexpr u32 SENT = 0xFFFFFFFFu;
constexpr int SPIN_CAP = 1 << 15;
constexpr u32 CODE_BYTES = 66 * 1024;

constexpr int LDS_SCR = 131072, LDS_STASH = LDS_SCR + 24576, LDS_TOTAL = 163840;

struct Params {
  const u16* x; const u16* wr; const u16* wi; const u16* wg; const u16* wu; const u16* wd; u16* out; u8* ws;
};

constexpr size_t OFF_DBG = 64u << 20;
constexpr int NSTAMP = 64;
DI u64 rt_() { u64 t; asm volatile("s_memrealtime %0\n\ts_waitcnt lgkmcnt(0)" : "=s"(t)::"memory"); return t; }
template <int I> DI void wl_(u32& v, u32 x) { asm volatile("v_writelane_b32 %0, %1, %2" : "+v"(v) : "s"(x), "i"(I)); }
DI u64 ct_() { u64 t; asm volatile("s_memtime %0\n\ts_waitcnt lgkmcnt(0)" : "=s"(t)::"memory"); return t; }
#define ST(i) do { SB(); u64 t_ = rt_(); wl_<(i)>(stv_, (u32)t_); t_ = ct_(); wl_<(i)>(stc_, (u32)t_); SB(); } while (0)
DI void qt_(u64& t) { asm volatile("s_memtime %0" : "=s"(t)::"memory"); }
#define ST_DECL u32 stv_ = 0, stc_ = 0, cnt_ = 0
#define CNT(i) do { cnt_ += 1u << (4 * (i)); } while (0)
#define ST_OUT() do { wl_<63>(stv_, __builtin_amdgcn_readfirstlane(cnt_)); if (threadIdx.x < NSTAMP) { ((u32*)(P.ws + OFF_DBG))[blockIdx.x * NSTAMP + threadIdx.x] = stv_; ((u32*)(P.ws + OFF_DBG))[(256 + blockIdx.x) * NSTAMP + threadIdx.x] = stc_; ((u32*)(P.ws + OFF_DBG + (1u << 20)))[((ep.x & 15) * 256 + blockIdx.x) * NSTAMP + threadIdx.x] = stv_; } } while (0)

#define XS(i)
DI u32 pkh(float a, float b) { f32x2 v = {a, b}; return __builtin_bit_cast(u32, __builtin_convertvector(v, f16x2)); }
DI u32 pkb(float a, float b) { f32x2 v = {a, b}; return __builtin_bit_cast(u32, __builtin_convertvector(v, bf16x2)); }
template <bool BF> DI u32 pkc(float a, float b) { return BF ? pkb(a, b) : pkh(a, b); }
DI u32 pkh_n0(float a, float b, u32 after) { u32 r; asm("v_cvt_pk_f16_f32 %0, -%1, %2" : "=v"(r) : "v"(a), "v"(b), "v"(after)); return r; }
DI u32 pkb_n1(float a, float b, u32 after) { u32 r; asm("v_cvt_pk_bf16_f32 %0, %1, -%2" : "=v"(r) : "v"(a), "v"(b), "v"(after)); return r; }
DI float blo(u32 u) { return __uint_as_float(u << 16); }
DI float bhi(u32 u) { return __uint_as_float(u & 0xffff0000u); }
DI float bfr(float a) { return blo(pkb(a, a)); }
DI float relu(float a) { const int i = __float_as_int(a); return __int_as_float(i > 0 ? i : 0); }
DI u32 umax(u32 a, u32 b) { return a > b ? a : b; }
DI u32 max3(u32 a, u32 b, u32 c) { return umax(umax(a, b), c); }
DI u32 max4(u32 acc, u32x4 v) { return max3(max3(acc, v.x, v.y), v.z, v.w); }
template <bool ONE> DI u32 red3(u32 a, u32 b, u32 c) { return ONE ? (a & b & c) : (a | b | c); }
template <bool ONE> DI u32 red2(u32 a, u32 b) { return ONE ? (a & b) : (a | b); }
template <bool ONE> DI u32 red4(u32 acc, u32x4 v) { return red3<ONE>(red3<ONE>(acc, v.x, v.y), v.z, v.w); }
template <bool ONE> DI u32 red8(u32x4 a, u32x4 b) { return red4<ONE>(red3<ONE>(red2<ONE>(a.x, a.y), a.z, a.w), b); }
template <bool ONE, u32 BIT> DI bool stale(u32 r) { return ONE ? (r & BIT) == 0 : (r & BIT) != 0; }
template <u32 CLR, u32 SET> DI u32 mark(u32 v) { return (v & ~CLR) | SET; }
template <u32 CLR, u32 SET> DI u32x4 mark4(u32x4 v) { return u32x4{mark<CLR, SET>(v.x), mark<CLR, SET>(v.y), mark<CLR, SET>(v.z), mark<CLR, SET>(v.w)}; }
DI f32x2 lo2(f32x4 v) { return __builtin_shufflevector(v, v, 0, 1); }
DI f32x2 hi2(f32x4 v) { return __builtin_shufflevector(v, v, 2, 3); }
DI f32x2 swp(f32x2 v) { return __builtin_shufflevector(v, v, 1, 0); }
DI f32x4 mfma_b(u32x4 a, u32x4 b, f32x4 c) {
  return __builtin_amdgcn_mfma_f32_16x16x32_bf16(__builtin_bit_cast(bf16x8, a), __builtin_bit_cast(bf16x8, b), c, 0, 0, 0);
}
template <class T> DI void PIN(T& x) { asm volatile("" : "+v"(x)); }
template <class T> DI void PINS(T& x) { asm volatile("" : "+s"(x)); }
template <class T> DI void PINA(T& x) { asm volatile("" : "+a"(x)); }
template <class F, int... I> DI void static_for_(F&& f, std::integer_sequence<int, I...>) { (f(std::integral_constant<int, I>{}), ...); }
template <int N, class F> DI void static_for(F&& f) { static_for_(f, std::make_integer_sequence<int, N>{}); }

#define MM_(T) "v_mfma_f32_16x16x32_" T
template <bool BF> DI void mm4(f32x4& d0, f32x4& d1, f32x4& d2, f32x4& d3, u32x4 a0, u32x4 a1, u32x4 b0, u32x4 b1) {
#define MM4_(T) asm volatile("s_nop 1\n\t" MM_(T) " %0, %4, %6, 0\n\t" MM_(T) " %1, %4, %7, 0\n\t" MM_(T) " %2, %5, %6, 0\n\t" MM_(T) " %3, %5, %7, 0\n\ts_nop 5" \
               : "=&v"(d0), "=&v"(d1), "=&v"(d2), "=&v"(d3) : "v"(a0), "v"(a1), "v"(b0), "v"(b1))
  if constexpr (BF) MM4_("bf16"); else MM4_("f16");
}
template <bool BF, int TAIL, int AG = 0> DI void mm8(f32x4& d0, f32x4& d1, f32x4& d2, f32x4& d3, u32x4 a00, u32x4 b00, u32x4 a01, u32x4 b01, u32x4 a10,
                               u32x4 b10, u32x4 a11, u32x4 b11, u32x4 a20, u32x4 b20, u32x4 a21, u32x4 b21, u32x4 a30, u32x4 b30, u32x4 a31, u32x4 b31) {
#define MM8_(T, A, B) asm volatile("s_nop 1\n\t" MM_(T) " %0, %4, %5, 0\n\t" MM_(T) " %1, %8, %9, 0\n\t" MM_(T) " %0, %6, %7, %0\n\t" MM_(T) " %1, %10, %11, %1\n\t" \
               MM_(T) " %2, %12, %13, 0\n\t" MM_(T) " %3, %16, %17, 0\n\t" MM_(T) " %2, %14, %15, %2\n\t" MM_(T) " %3, %18, %19, %3\n\ts_nop %20"         \
               : "=&v"(d0), "=&v"(d1), "=&v"(d2), "=&v"(d3)                                                                                              \
               : A(a00), B(b00), A(a01), B(b01), A(a10), B(b10), A(a11), B(b11), A(a20), B(b20), A(a21), B(b21), A(a30), B(b30), A(a31), B(b31), "i"(TAIL))
  if constexpr (!BF) MM8_("f16", "v", "v");
  else if constexpr (AG == 0) MM8_("bf16", "v", "v");
  else if constexpr (AG == 1) MM8_("bf16", "a", "v");
  else MM8_("bf16", "v", "a");
}
template <bool BF> DI u32x4 twiddle(f32x4 r, f32x4 i, const f32x2 (&c)[2], const f32x2 (&s)[2]) {
  const f32x2 re0 = lo2(r) * c[0] + lo2(i) * s[0], im0 = lo2(i) * c[0] - lo2(r) * s[0];
  const f32x2 re1 = hi2(r) * c[1] + hi2(i) * s[1], im1 = hi2(i) * c[1] - hi2(r) * s[1];
  return u32x4{pkc<BF>(re0.x, im0.x), pkc<BF>(re0.y, im0.y), pkc<BF>(re1.x, im1.x), pkc<BF>(re1.y, im1.y)};
}
DI u32x4 cmul_b(f32x4 r, f32x4 i, const f32x2 (&c)[2], const f32x2 (&s)[2]) {
  const f32x2 re0 = lo2(r) * c[0] - lo2(i) * s[0], im0 = lo2(i) * c[0] + lo2(r) * s[0];
  const f32x2 re1 = hi2(r) * c[1] - hi2(i) * s[1], im1 = hi2(i) * c[1] + hi2(r) * s[1];
  return u32x4{pkb(re0.x, im0.x), pkb(re0.y, im0.y), pkb(re1.x, im1.x), pkb(re1.y, im1.y)};
}
DI float cosr(float a) { return __builtin_amdgcn_cosf(a); }
DI float sinr(float a) { return __builtin_amdgcn_sinf(a); }
template <int CTRL> DI float dppf(float x) {
  return __builtin_bit_cast(float, __builtin_amdgcn_mov_dpp(__builtin_bit_cast(int, x), CTRL, 0xf, 0xf, true));
}
DI float reduce2(float a, float b) {
  auto t = __builtin_amdgcn_permlane32_swap(__float_as_uint(a), __float_as_uint(b), false, false);
  float x = __uint_as_float(t[0]) + __uint_as_float(t[1]);
  x += dppf<0xB1>(x);
  x += dppf<0x4E>(x);
  x += dppf<0x141>(x);
  x += dppf<0x140>(x);
  auto s = __builtin_amdgcn_permlane16_swap(__float_as_uint(x), __float_as_uint(x), false, false);
  return __uint_as_float(s[0]) + __uint_as_float(s[1]);
}
DI f32x2 bcast2(float x) {
  auto t = __builtin_amdgcn_permlane32_swap(__float_as_uint(x), __float_as_uint(x), false, false);
  return f32x2{__uint_as_float(t[0]), __uint_as_float(t[1])};
}
DI void ln_finish(float mean, float q, f32x2& R, f32x2& O) {
  const float r = __builtin_amdgcn_rcpf(bfr(__builtin_amdgcn_sqrtf(bfr(bfr(q * (1.0f / 512.0f)) + 1e-5f))));
  const float o = (mean - bfr(mean)) * r;
  R = bcast2(r); O = bcast2(o);
}
DI int row_of_pos(int pos, int j) {
  const int c = 8 * (pos >> 7) + 2 * ((pos >> 3) & 3) + ((pos >> 5) & 1), q = 8 * ((pos >> 6) & 1) + (pos & 7);
  return 32 * (2 * c + (j & 1)) + 2 * q + (j >> 1);
}

template <int I> DI void ld4(u32x4& d, u32 v, const u8* s) { asm volatile("global_load_dwordx4 %0, %1, %2 offset:%3 sc1" : "=v"(d) : "v"(v), "s"(s), "i"(I) : "memory"); }
template <int I> DI void ld4a(u32x4& d, u32 v, const u8* s) { asm volatile("global_load_dwordx4 %0, %1, %2 offset:%3 sc1" : "=a"(d) : "v"(v), "s"(s), "i"(I) : "memory"); }
template <int I> DI void ld4r(u32x4& d, u32 v, const u8* s) { asm volatile("global_load_dwordx4 %0, %1, %2 offset:%3 sc1" : "+v"(d) : "v"(v), "s"(s), "i"(I) : "memory"); }
template <int I> DI void ld2(u32x2& d, u32 v, const u8* s) { asm volatile("global_load_dwordx2 %0, %1, %2 offset:%3 sc1" : "=v"(d) : "v"(v), "s"(s), "i"(I) : "memory"); }
template <int I> DI void ld2r(u32x2& d, u32 v, const u8* s) { asm volatile("global_load_dwordx2 %0, %1, %2 offset:%3 sc1" : "+v"(d) : "v"(v), "s"(s), "i"(I) : "memory"); }
template <int I> DI void ld2p(u32x2& d, u32 v, const u8* s) { asm volatile("global_load_dwordx2 %0, %1, %2 offset:%3" : "=v"(d) : "v"(v), "s"(s), "i"(I) : "memory"); }
DI void touch(u32& d, u32 v, const u8* s) { asm volatile("global_load_dword %0, %1, %2 sc1" : "+v"(d) : "v"(v), "s"(s) : "memory"); }
DI void ld1s(u32& d, u32 v, const u8* s) { asm volatile("global_load_ushort %0, %1, %2" : "=v"(d) : "v"(v), "s"(s) : "memory"); }
template <int N> DI void vmwait() { static_assert(N >= 0 && N < 64, ""); asm volatile("s_waitcnt vmcnt(%0)" ::"i"(N) : "memory"); }
DI void set_m0(u32 ldsaddr) { asm volatile("s_mov_b32 m0, %0" ::"s"(ldsaddr) : "memory"); }
template <int I> DI void dma1(u32 voff, const u8* src) {
  asm volatile("global_load_lds_dwordx4 %0, %1 offset:%2 sc1" ::"v"(voff), "s"(src), "i"(I) : "memory");
}
DI void dma4(u32 voff, const u8* src, u32 ldsaddr) {
  asm volatile("s_mov_b32 m0, %2\n\ts_nop 0\n\tglobal_load_lds_dwordx4 %0, %1 sc1\n\tglobal_load_lds_dwordx4 %0, %1 offset:1024 sc1\n\t"
               "global_load_lds_dwordx4 %0, %1 offset:2048 sc1\n\tglobal_load_lds_dwordx4 %0, %1 offset:3072 sc1" ::"v"(voff), "s"(src), "s"(ldsaddr) : "memory");
}
#define RETRY(COND, ...)                                                      \
  do {                                                                        \
    bool bad_ = (COND);                                                       \
    if (__builtin_expect(__any(bad_), 0)) {                                   \
      _Pragma("nounroll") for (int sp_ = 0; sp_ < SPIN_CAP; ++sp_) {          \
        __VA_ARGS__                                                           \
        bad_ = (COND);                                                        \
        if (!__any(bad_)) break;                                              \
      }                                                                       \
    }                                                                         \
  } while (0)
DI void bar() { asm volatile("s_waitcnt lgkmcnt(0)\n\ts_barrier" ::: "memory"); }
DI void bar_nw() { asm volatile("s_barrier" ::: "memory"); }

struct RowC { u32x4 b1re, b1im, b2re[2][2], b2im[2][2]; f32x2 twc[2][2], tws[2][2]; };
template <bool INV> DI void pair_cs(float c, float s, u32& re, u32& im) {
  if constexpr (INV) { im = pkb(s, c); re = pkb_n1(c, s, im); } else { re = pkh(c, s); im = pkh_n0(s, c, re); }
}
template <bool AG = false> DI void sign4(u32x4 base, u32 lm, u32x4& f00, u32x4& f01, u32x4& f10, u32x4& f11) {
  f00 = base;
  f01 = base ^ lm;
  f10 = base ^ u32x4{0u, 0x80008000u, 0u, 0x80008000u};
  f11 = f10 ^ lm;
  if constexpr (AG) { PINA(f00); PINA(f01); PINA(f10); PINA(f11); } else { PIN(f00); PIN(f01); PIN(f10); PIN(f11); }
}
template <bool INV, int PART> DI void row_consts(RowC& k, int m, int g) {
  const float fm = (float)m, fg = (float)(4 * g);
  if constexpr (PART == 0) {
    u32x4 br, bi;
#pragma unroll
    for (int i = 0; i < 4; ++i) {
      const float a = (fg + (float)i) * (fm * (1.0f / 16.0f));
      u32 x, y;
      pair_cs<INV>(cosr(a), sinr(a), x, y); k.b1re[i] = x; k.b1im[i] = y;
      const float a2 = (fg + (float)i) * (fm * (1.0f / 32.0f));
      pair_cs<INV>(cosr(a2), sinr(a2), x, y); br[i] = x; bi[i] = y;
    }
    PIN(k.b1re); PIN(k.b1im);
    const u32 lm = (m & 1) ? 0x80008000u : 0u;
    sign4(br, lm, k.b2re[0][0], k.b2re[0][1], k.b2re[1][0], k.b2re[1][1]);
    sign4(bi, lm, k.b2im[0][0], k.b2im[0][1], k.b2im[1][0], k.b2im[1][1]);
  } else {
#pragma unroll
    for (int e = 0; e < 2; ++e)
#pragma unroll
      for (int i = 0; i < 4; ++i) {
        const float a = (fg + (float)(16 * e + i)) * (fm * (INV ? -1.0f / 512.0f : 1.0f / 512.0f));
        k.twc[e][i >> 1][i & 1] = cosr(a); k.tws[e][i >> 1][i & 1] = sinr(a);
      }
#pragma unroll
    for (int e = 0; e < 2; ++e) { PIN(k.twc[e][0]); PIN(k.twc[e][1]); PIN(k.tws[e][0]); PIN(k.tws[e][1]); }
  }
}
template <bool INV, int TAIL> DI void row_fft(const RowC& k, u32x4 a0, u32x4 a1, f32x4& dr0, f32x4& di0, f32x4& dr1, f32x4& di1) {
  f32x4 tr0, ti0, tr1, ti1;
  mm4<INV>(tr0, ti0, tr1, ti1, a0, a1, k.b1re, k.b1im);
  const u32x4 p0 = twiddle<INV>(tr0, ti0, k.twc[0], k.tws[0]);
  SB();
  const u32x4 p1 = twiddle<INV>(tr1, ti1, k.twc[1], k.tws[1]);
  mm8<INV, TAIL>(dr0, di0, dr1, di1, p0, k.b2re[0][0], p1, k.b2re[0][1], p0, k.b2im[0][0], p1, k.b2im[0][1], p0, k.b2re[1][0], p1, k.b2re[1][1], p0,
           k.b2im[1][0], p1, k.b2im[1][1]);
}

struct ColC {
  u32x4 b1re[2], b1im[2], a2re[2][2], a2im[2][2], b3re[2][2], b3im[2][2], a4re[2][2], a4im[2][2];
  f32x2 t1c[2][2], t1s[2][2], t2c[2][2], t2s[2][2];
};
template <int PART> DI void col_consts(ColC& k, int m, int g, int w) {
  const int pi_ = w >> 1, kt = w & 1;
  const float fm = (float)m, fg = (float)(4 * g);
  const u32 lm = (m & 1) ? 0x80008000u : 0u;
  if constexpr (PART == 0) {
    const float fk = (float)(2 * (16 * kt + m) + pi_);
#pragma unroll
    for (int u = 0; u < 2; ++u) {
#pragma unroll
      for (int r = 0; r < 4; ++r) {
        const float n = fg + (float)(16 * u + r), a = n * (fk * (1.0f / 64.0f)), a2 = n * (fk * (1.0f / 2048.0f));
        u32 x, y;
        pair_cs<false>(cosr(a), sinr(a), x, y); k.b1re[u][r] = x; k.b1im[u][r] = y;
        k.t1c[u][r >> 1][r & 1] = cosr(a2); k.t1s[u][r >> 1][r & 1] = sinr(a2);
      }
      PIN(k.b1re[u]); PIN(k.b1im[u]); PIN(k.t1c[u][0]); PIN(k.t1c[u][1]); PIN(k.t1s[u][0]); PIN(k.t1s[u][1]);
    }
  } else if constexpr (PART == 1) {
    u32x4 fr, fi, ir, ii;
#pragma unroll
    for (int r = 0; r < 4; ++r) {
      const float a = (fg + (float)r) * (fm * (1.0f / 32.0f)), c = cosr(a), s = sinr(a);
      u32 x, y;
      pair_cs<false>(c, s, x, y); fr[r] = x; fi[r] = y;
      pair_cs<true>(c, s, x, y); ir[r] = x; ii[r] = y;
    }
    sign4(fr, lm, k.a2re[0][0], k.a2re[0][1], k.a2re[1][0], k.a2re[1][1]);
    sign4(fi, lm, k.a2im[0][0], k.a2im[0][1], k.a2im[1][0], k.a2im[1][1]);
    sign4<true>(ir, lm, k.b3re[0][0], k.b3re[0][1], k.b3re[1][0], k.b3re[1][1]);
    sign4<true>(ii, lm, k.b3im[0][0], k.b3im[0][1], k.b3im[1][0], k.b3im[1][1]);
  } else if constexpr (PART == 2) {
    const float kb = (float)(32 * kt + pi_) + 2.0f * fg;
#pragma unroll
    for (int u = 0; u < 2; ++u) {
#pragma unroll
      for (int r = 0; r < 4; ++r) {
        const float a = (kb + (float)(2 * r)) * ((fm + (float)(16 * u)) * (-1.0f / 2048.0f));
        k.t2c[u][r >> 1][r & 1] = cosr(a); k.t2s[u][r >> 1][r & 1] = sinr(a);
      }
      PIN(k.t2c[u][0]); PIN(k.t2c[u][1]); PIN(k.t2s[u][0]); PIN(k.t2s[u][1]);
    }
  } else {
    const float fn = (fm + (float)(16 * (w >> 1))) * (1.0f / 64.0f);
#pragma unroll
    for (int u = 0; u < 2; ++u) {
#pragma unroll
      for (int r = 0; r < 4; ++r) {
        const float a = (2.0f * fg + (float)(2 * r + u)) * fn;
        u32 x, y;
        pair_cs<true>(cosr(a), sinr(a), x, y); k.a4re[u][0][r] = x; k.a4im[u][0][r] = y;
      }
      k.a4re[u][1] = k.a4re[u][0] ^ lm; k.a4im[u][1] = k.a4im[u][0] ^ lm;
      PINA(k.a4re[u][0]); PINA(k.a4re[u][1]); PINA(k.a4im[u][0]); PINA(k.a4im[u][1]);
    }
  }
}

DI void transpose8x4(const u32x2 (&v)[8], u32x4 (&o)[4]) {
#pragma unroll
  for (int jj = 0; jj < 4; ++jj) {
    o[0][jj] = __builtin_amdgcn_perm(v[2 * jj + 1].x, v[2 * jj].x, 0x05040100u);
    o[1][jj] = __builtin_amdgcn_perm(v[2 * jj + 1].x, v[2 * jj].x, 0x07060302u);
    o[2][jj] = __builtin_amdgcn_perm(v[2 * jj + 1].y, v[2 * jj].y, 0x05040100u);
    o[3][jj] = __builtin_amdgcn_perm(v[2 * jj + 1].y, v[2 * jj].y, 0x07060302u);
  }
}
DI void prep_load(const Params& P, int wt, int lane, u32x2 (&v)[8]) {
  const u32 vo = lane * 8;
  if (wt < 896) {
    const int rest = wt / 7, piece = rest & 63;
    const u8* src = (const u8*)((rest >> 6) ? P.wu : P.wg) + (wt % 7) * 512 + (16 * (piece >> 2) + 4 * (piece & 3)) * 3584;
    const u32 vo2 = vo + 7168;
    static_for<8>([&](auto J) { constexpr int j = decltype(J)::value; ld2p<(j & 1) * 3584>(v[j], j & 2 ? vo2 : vo, src + (j >> 2) * (256 * 3584)); });
  } else {
    const int piece = (wt - 896) >> 1, g = piece & 3, kq = piece >> 2, wv = kq / 14, ks = kq % 14;
    int f0, f1;
    if (ks < 4) { f0 = 112 * (4 * wv + ks); f1 = f0 + 16; }
    else if (ks < 6) { f0 = 112 * (4 * wv + 2 * (ks - 4)) + 32; f1 = f0 + 112; }
    else if (ks < 10) { f0 = 112 * (4 * wv + ks - 6) + 48; f1 = f0 + 16; }
    else { f0 = 112 * (4 * wv + ks - 10) + 80; f1 = f0 + 16; }
    const u8* s0 = (const u8*)P.wd + (wt & 1) * 512 + (4 * g + f0) * 1024;
    const u8* s1 = (const u8*)P.wd + (wt & 1) * 512 + (4 * g + f1) * 1024;
    static_for<8>([&](auto J) { constexpr int j = decltype(J)::value; ld2p<(j & 3) * 1024>(v[j], vo, j < 4 ? s0 : s1); });
  }
}
DI u32 prep_dst(int wt, int tile) {
  if (wt < 896) {
    const int rest = wt / 7, piece = rest & 63, mat = rest >> 6, tl = (wt % 7) * 16 + tile;
    const int nt = tl / 7, p = tl % 7, ks = piece >> 2;
    const u32 frag = (p < 3 ? ks * 6 + 2 * p : p < 5 ? 96 + ks * 4 + 2 * (p - 3) : 160 + ks * 4 + 2 * (p - 5)) + mat;
    return OFF_W1 + (nt * 224 + frag) * 1024 + (piece & 3) * 256;
  } else {
    const int piece = (wt - 896) >> 1, tl = (wt & 1) * 16 + tile;
    return OFF_W2 + (((tl >> 2) * 56 + (piece >> 2)) * 4 + (tl & 3)) * 1024 + (piece & 3) * 256;
  }
}

constexpr int NTOK0 = 16, NRES = 40, NEARLY = 37;
constexpr int g1_job(int L, int x) {
  if (((x >= 34 && x < 96) && (x & 3) == 2) || (x >= 98 && x < 130 && (x & 3) == 0)) return 2;
  if ((x < 42 && x % 3 == 0) || x == 41 || x == 44) return 1;
  if (x >= 150) return (x & 1) ? 0 : 4;
  if (x >= 132 && x < 148 && (x & 3) == 0) return 5;
  if (L == 0 && ((x < 34 && x % 6 == 1) || (x >= 48 && x < 98 && (x & 3) == 0) || (x >= 98 && x < 150 && (x & 3) == 2) || (x >= 49 && x < 150 && x % 12 == 1)))
    return 3;
  return 0;
}
constexpr int NEPI1 = 124, NEPI2 = 82;
struct G1Sched {
  int job[224], idx[224], epi[225], nst[224], cum[225], pos[7][64], cnt[7];
  constexpr G1Sched(int L) : job{}, idx{}, epi{}, nst{}, cum{}, pos{}, cnt{} {
    cnt[1] = NTOK0;
    for (int i = 0; i < NTOK0; ++i) pos[1][i] = i;
    cum[0] = NTOK0;
    for (int x = 0; x < 224; ++x) {
      int j = g1_job(L, x);
      if ((j == 3 && cnt[3] >= NRES) || (j == 4 && cnt[4] >= NEARLY)) j = 0;
      job[x] = j; idx[x] = cnt[j];
      if (x == 161) epi[x] = NEPI1;
      const bool on = (x >= 97 && x < 160 && epi[x] < NEPI1) || x >= 161;
      epi[x + 1] = on ? epi[x] + (j ? 2 : 3) : epi[x];
      if (x < 160 && epi[x + 1] > NEPI1) epi[x + 1] = NEPI1;
      for (int n = epi[x]; n < epi[x + 1]; ++n)
        nst[x] += n < NEPI1 ? (n == 40 || n == 81 || n == 102 || n == 123) : (n < NEPI1 + NEPI2 && (n - NEPI1) % 41 == 40);
      if (j) pos[j][cnt[j]++] = cum[x] + nst[x];
      cum[x + 1] = cum[x] + nst[x] + (j ? 1 : 0);
    }
  }
};
constexpr G1Sched G1S[2] = {G1Sched(0), G1Sched(1)};
static_assert(G1S[0].cnt[1] == 32 && G1S[0].cnt[2] == 24 && G1S[0].cnt[3] == NRES && G1S[0].cnt[4] == NEARLY && G1S[0].cnt[5] == 4, "");
static_assert(G1S[0].idx[66] == 8 && G1S[0].idx[100] == 16, "");
static_assert(G1S[0].epi[152] == NEPI1 && G1S[0].epi[204] >= NEPI1 + NEPI2, "");
constexpr int clamp63(int n) { return n > 63 ? 63 : n; }
constexpr int g1_wait(int L, int x, int job, int idx) { return clamp63(G1S[L].cum[x] - G1S[L].pos[job][idx] - 1); }

#define FRESH()                                                                                                   \
  int tid = threadIdx.x, b = blockIdx.x, w = w_;                                                                   \
  u8* ws = P.ws;                                                                                                   \
  asm volatile("" : "+v"(tid), "+s"(b), "+s"(w), "+s"(ws));                                                        \
  const int lane = tid & 63, m = lane & 15, g = lane >> 4, half2 = b & 1, ct = b >> 5;                             \
  const int G = ((b >> 1) & 3) * 4 + ((b >> 3) & 3), member = half2 * 8 + ct, pos = G * 16 + member;               \
  const u32 l16 = lane * 16, t16 = tid * 16, xo = 32 * m + 8 * g;                                                  \
  auto fill_h = [&](u32 base, int q) {                                                                             \
    const int i = q * 64 + lane;                                                                                   \
    st4(sent4, base + (8 * G + 2 * w + (i >= 224)) * 57344 + member * 3584 + (i >= 224 ? i - 224 : i) * 16);       \
  };                                                                                                               \
  auto fill_rows = [&](u32 base) { st4(sent4, base + pos * 8192 + t16); st4(sent4, base + pos * 8192 + 4096 + t16); }; \
  auto ring4 = [&](int q) {                                   \
    const u32 o = (q >> 1) * 32768 + w * 8192 + (q & 1) * 4096;                                                    \
    dma4(l16, ws + OFF_W1 + member * 229376 + o, ldsbase + o);                                                     \
  };                                                                                                               \
  (void)m; (void)g; (void)half2; (void)ct; (void)l16; (void)t16; (void)xo; (void)fill_h; (void)fill_rows; (void)ring4

__global__ __launch_bounds__(256, 1) void k_all(Params P) {
  extern __shared__ __attribute__((aligned(16))) u8 lds[];
  ST_DECL;
  const int w_ = __builtin_amdgcn_readfirstlane(threadIdx.x >> 6);
  const __amdgpu_buffer_rsrc_t rs = __builtin_amdgcn_make_buffer_rsrc(P.ws, 0, WS_USED, 0x00020000);
  const u32 ldsbase = (u32)(size_t)(__attribute__((address_space(3))) u8*)lds;
  const u32x4 sent4 = {SENT, SENT, SENT, SENT};
  auto st4 = [&](u32x4 v, u32 off) { __builtin_amdgcn_raw_buffer_store_b128(v, rs, off, 0, 16); };
  auto st2 = [&](u32x2 v, u32 off) { __builtin_amdgcn_raw_buffer_store_b64(v, rs, off, 0, 16); };
  u32x2 xl[2], xh[2], pv0[8], pv1[8];
  u32 wfr[8], wfi[8], wn[8];
  f32x2 xv[8];
  u32x2 ep;
  u32 cpf = 0;
  RowC rc;
  ColC cc;
  ST(0);
  {
    FRESH();
    const u8* xp = (const u8*)P.x + row_of_pos(pos, w) * 1024;
    ld2p<0>(xl[0], xo, xp); ld2p<512>(xl[1], xo, xp); ld2p<0>(xh[0], xo, xp + 1024 * 1024); ld2p<512>(xh[1], xo, xp + 1024 * 1024);
    { const u32 z = 0; ld2<0>(ep, z, ws); }
    {
      const u8* pc;
      asm volatile("s_getpc_b64 %0" : "=s"(pc));
      const u32 o = (((b >> 3) * 4 + w) * 8 + (lane & 7)) * 128;
      touch(cpf, o < CODE_BYTES ? o : 0, pc);
    }
    XS(32);
    fill_rows(OFF_A); fill_rows(OFF_WX);
    SB();
    row_consts<false, 0>(rc, m, g);
    SB();
    XS(33);
    fill_rows(OFF_U);
    if (tid < 8) st4(sent4, OFF_WNY + tid * 4096 + pos * 16);
    SB();
    row_consts<false, 1>(rc, m, g);
    SB();
    XS(34);
    const int kb = 2 * (16 * ((pos >> 6) & 1) + (pos & 15)) + (pos >> 7) + 256 * ((pos >> 4) & 3);
    static_for<8>([&](auto J) {
      constexpr int j = decltype(J)::value;
      const u32 o = tid * 2 + (kb + 64 * (16 * (j >> 2) + (j & 3))) * 514;
      ld1s(wfr[j], o, (const u8*)P.wr); ld1s(wfi[j], o, (const u8*)P.wi);
    });
    {
      const int k = kb + 64 * (16 * ((lane >> 2) & 1) + (lane & 3)), km = (2048 - k) & 2047;
      const u32 o = k * 514, om = km * 514;
      ld1s(wn[0], o, (const u8*)P.wr); ld1s(wn[1], o, (const u8*)P.wi); ld1s(wn[2], om, (const u8*)P.wr); ld1s(wn[3], om, (const u8*)P.wi);
      ld1s(wn[4], o, (const u8*)P.wr + 512); ld1s(wn[5], o, (const u8*)P.wi + 512); ld1s(wn[6], om, (const u8*)P.wr + 512); ld1s(wn[7], om, (const u8*)P.wi + 512);
    }
    XS(35);
    vmwait<24>();
    XS(36);
    bar_nw();
    PIN(cpf); PIN(ep); PIN(xl[0]); PIN(xl[1]); PIN(xh[0]); PIN(xh[1]);
    { const u64 e = (((u64)(u32)__builtin_amdgcn_readfirstlane(ep.y) << 32) | (u32)__builtin_amdgcn_readfirstlane(ep.x)) + 1; ep = u32x2{(u32)e, (u32)(e >> 32)}; }
    if (tid == 0) st2(ep, OFF_FLAG + pos * 8);
    XS(37);
    prep_load(P, b * 4 + w, lane, pv0);
    SB();
#pragma unroll
    for (int t = 0; t < 2; ++t)
#pragma unroll
      for (int p = 0; p < 2; ++p) {
        xv[4 * t + 2 * p] = f32x2{blo(xl[t][p]), blo(xh[t][p])};
        xv[4 * t + 2 * p + 1] = f32x2{bhi(xl[t][p]), bhi(xh[t][p])};
      }
  }
  ST(1);

  u32x4 w2a[10][4];

  auto layer = [&](auto LC) {
    constexpr int L = decltype(LC)::value;
    constexpr int S0 = 2 + 16 * L;
    {
      FRESH();
      const f32x2 s2 = ((xv[0] + xv[1]) + (xv[2] + xv[3])) + ((xv[4] + xv[5]) + (xv[6] + xv[7]));
      const float mean = reduce2(s2.x, s2.y) * (1.0f / 512.0f);
      const f32x2 M = bcast2(mean);
      f32x2 d[8];
#pragma unroll
      for (int i = 0; i < 8; ++i) d[i] = xv[i] - M;
      f32x2 q0 = d[0] * d[0], q1 = d[1] * d[1];
#pragma unroll
      for (int i = 2; i < 8; i += 2) { q0 += d[i] * d[i]; q1 += d[i + 1] * d[i + 1]; }
      q0 += q1;
      f32x2 R, O;
      ln_finish(mean, reduce2(q0.x, q0.y), R, O);
      float* img = (float*)(lds + LDS_SCR);
      u32* inp = (u32*)(img + w * 1280);
#pragma unroll
      for (int i = 0; i < 8; ++i) {
        const f32x2 v = d[i] * R + O;
        inp[(16 * (m & 1) + 4 * g + (i & 3)) * 20 + (m >> 1) + 8 * (i >> 2)] = pkh(v.x, v.y);
      }
      const u32x4 a0 = *(const u32x4*)(inp + m * 20 + 4 * g), a1 = *(const u32x4*)(inp + (16 + m) * 20 + 4 * g);
      if constexpr (L == 0) { if (b < 80) prep_load(P, b * 4 + w + 1024, lane, pv1); }
      const int ph = tid >> 1, fq = ph ? 512 - ph : 256, fq1 = 384 - ph;
      u32 ap0 = (ph + (ph >> 4) * 4) * 4, ap1 = ap0 + 640, ar0 = (fq + (fq >> 4) * 4) * 4, ar1 = (fq1 + (fq1 >> 4) * 4) * 4;
      PIN(ap0); PIN(ap1); PIN(ar0); PIN(ar1);
      const float* p0 = (const float*)((const u8*)img + ap0);
      const float* p1 = (const float*)((const u8*)img + ap1);
      const float* r0 = (const float*)((const u8*)img + ar0);
      const float* r1 = (const float*)((const u8*)img + ar1);
      const float sc = 1.0f / 64.0f, sg = (tid & 1) ? -sc : sc;
      f32x2 cA = {sc, sc}, cB = {sg, -sg}, cC = {sc, -sc}, cD = {sg, sg};
      f32x2 eA = ph ? cA : f32x2{2 * sc, 0.f}, eB = ph ? cB : f32x2{2 * sg, 0.f}, eC = ph ? cC : f32x2{0.f, 2 * sg}, eD = ph ? cD : f32x2{0.f, 2 * sc};
      u32 zo = OFF_A + pos * 8192 + t16;
      PIN(cB); PIN(cD); PIN(eA); PIN(eB); PIN(eC); PIN(eD); PIN(zo);
      f32x4 dr0, di0, dr1, di1;
      row_fft<false, 5>(rc, a0, a1, dr0, di0, dr1, di1);
      float* op = img + w * 1280 + 4 * g + 20 * m;
      *(f32x4*)(op) = dr0; *(f32x4*)(op + 640) = di0;
      SB();
      *(f32x4*)(op + 320) = dr1; *(f32x4*)(op + 960) = di1;
      bar();
      f32x2 pa[4], qa[4], pb[4], qb[4];
#pragma unroll
      for (int j = 0; j < 4; ++j) {
        pa[j] = f32x2{p0[j * 1280], p0[j * 1280 + 640]}; qa[j] = f32x2{r0[j * 1280], r0[j * 1280 + 640]};
      }
#pragma unroll
      for (int j = 0; j < 4; ++j) {
        pb[j] = f32x2{p1[j * 1280], p1[j * 1280 + 640]}; qb[j] = f32x2{r1[j * 1280], r1[j * 1280 + 640]};
      }
      SB();
      u32x4 o;
#pragma unroll
      for (int j = 0; j < 4; ++j) {
        const f32x2 v = eD * swp(qa[j]) + (eC * qa[j] + (eB * swp(pa[j]) + eA * pa[j]));
        o[j] = mark<(L == 0 ? 0x10001u : 0u), (L == 0 ? 0u : 0x10000u)>(pkh(v.x, v.y));
      }
      st4(o, zo);
#pragma unroll
      for (int j = 0; j < 4; ++j) {
        const f32x2 v = cD * swp(qb[j]) + (cC * qb[j] + (cB * swp(pb[j]) + cA * pb[j]));
        o[j] = mark<(L == 0 ? 0x10001u : 0u), (L == 0 ? 0u : 0x10000u)>(pkh(v.x, v.y));
      }
      st4(o, zo + 4096);
      SB();
      if constexpr (L == 1) {
#pragma unroll
        for (int t = 0; t < 2; ++t)
#pragma unroll
          for (int p = 0; p < 2; ++p) {
            xl[t][p] = pkb(xv[4 * t + 2 * p].x, xv[4 * t + 2 * p + 1].x); xh[t][p] = pkb(xv[4 * t + 2 * p].y, xv[4 * t + 2 * p + 1].y);
          }
        PIN(xl[0]); PIN(xl[1]); PIN(xh[0]); PIN(xh[1]);
      }
    }
    if constexpr (L == 0) {
      FRESH();
      vmwait<8 + 2>();
#pragma unroll
      for (int j = 0; j < 8; ++j) { PIN(wfr[j]); PIN(wfi[j]); PIN(wn[j]); }
      u8* bw = lds + w * 2048;
      u32x4 v0, v1;
#pragma unroll
      for (int j = 0; j < 4; ++j) { v0[j] = wfr[j] | (wfi[j] << 16); v1[j] = wfr[4 + j] | (wfi[4 + j] << 16); }
      *(u32x4*)(bw + lane * 32) = v0; *(u32x4*)(bw + lane * 32 + 16) = v1;
      st4(*(const u32x4*)(bw + l16), OFF_WX + pos * 8192 + w * 2048 + l16);
      st4(*(const u32x4*)(bw + 1024 + l16), OFF_WX + pos * 8192 + w * 2048 + 1024 + l16);
      const float hr = blo(wn[0]) + blo(wn[2]), hi = blo(wn[1]) - blo(wn[3]), nr = blo(wn[4]) + blo(wn[6]), ni = blo(wn[5]) - blo(wn[7]);
      const f32x4 ab = {0.25f * (hr + nr), 0.25f * (hi + ni), 0.25f * (hr - nr), 0.25f * (hi - ni)};
      if (tid < 8) st4(__builtin_bit_cast(u32x4, ab), OFF_WNY + tid * 4096 + pos * 16);
    }
    ST(S0);
    {
      FRESH();
      const int phi = pos, pi_ = w >> 1, kt = w & 1;
      const int k1l = 2 * (16 * kt + m) + pi_;
      auto consts = [&](auto PC) { col_consts<decltype(PC)::value>(cc, m, g, w); SB(); };
      using I0 = std::integral_constant<int, 0>; using I1 = std::integral_constant<int, 1>;
      using I2 = std::integral_constant<int, 2>; using I3 = std::integral_constant<int, 3>;
      u8* const bw = lds + 16384 + w * 8192;
      const u32 zo = (tid >> 1) * 8192 + (tid & 1) * 16;
      const u8* zb0 = ws + OFF_A + phi * 32;
      const u8* zb1 = zb0 + 128 * 8192;
      const u8* wb0 = ws + OFF_WX + phi * 32;
      const u8* wb1 = wb0 + 128 * 8192;
      const u32 wo = zo;
      PINS(zb0); PINS(zb1); PINS(wb0); PINS(wb1);
      if constexpr (L == 0) {
        u32x4 fl0, fl1;
        const u32 o = lane * 32;
        const u8* fb = ws + OFF_FLAG;
        ld4<0>(fl0, o, fb); ld4<16>(fl1, o, fb);
        SB();
        consts(I0{});
        consts(I1{});
        vmwait<0>(); PIN(fl0); PIN(fl1);
        RETRY(!(fl0.x == ep.x && fl0.y == ep.y && fl0.z == ep.x && fl0.w == ep.y && fl1.x == ep.x && fl1.y == ep.y && fl1.z == ep.x && fl1.w == ep.y), {
          CNT(0);
          ld4r<0>(fl0, o, fb); ld4r<16>(fl1, o, fb);
          vmwait<0>(); PIN(fl0); PIN(fl1);
        });
      } else {
        SB();
        consts(I0{});
        consts(I1{});
      }
      u32x4 z0, z1, wv[2];
      ld4<0>(z0, zo, zb0); ld4<0>(z1, zo, zb1);
      ld4<0>(wv[0], wo, wb0); ld4<0>(wv[1], wo, wb1);
      SB();
      u32x4 wa[8];
      if (phi == 0) static_for<8>([&](auto J) { constexpr int j = decltype(J)::value; ld4<0>(wa[j], t16, ws + OFF_WNY + j * 4096); });
      if constexpr (L == 0) {
        auto put = [&](int rep) {
#pragma unroll
          for (int x = 0; x < 4; ++x) {
            const int src = x * 16 + (lane >> 2);
            st4(*(const u32x4*)(bw + rep * 4096 + src * 64 + (((lane & 3) + src) & 3) * 16), prep_dst(b * 4 + w + 1024 * rep, 4 * x + g) + m * 16);
          }
        };
        u32x4 ot[4];
#pragma unroll
        for (int j = 0; j < 8; ++j) PIN(pv0[j]);
        transpose8x4(pv0, ot);
#pragma unroll
        for (int x = 0; x < 4; ++x) *(u32x4*)(bw + lane * 64 + ((x + lane) & 3) * 16) = ot[x];
        put(0);
        SB();
        consts(I2{});
        if (b < 80) {
#pragma unroll
          for (int j = 0; j < 8; ++j) PIN(pv1[j]);
          transpose8x4(pv1, ot);
#pragma unroll
          for (int x = 0; x < 4; ++x) *(u32x4*)(bw + 4096 + lane * 64 + ((x + lane) & 3) * 16) = ot[x];
          put(1);
        }
        SB();
        consts(I3{});
      } else {
        consts(I2{});
        ring4(2);
        SB();
        consts(I3{});
      }
      u8* const zi = lds + LDS_SCR;
      u32 zw;
      {
        const int pp = tid >> 1;
        zw = (tid & 1) * 4096 + ((pp >> 6) & 1) * 2048 + (((pp >> 3) & 3) * 16 + 2 * (pp & 7)) * 16 + ((pp >> 5) & 1) * 8;
        PIN(zw);
      }
      u32 uo = OFF_U + phi * 8192 + w * 2048 + l16;
      PIN(uo);
      SB();
      bar_nw();
      ST(S0 + 1);
      vmwait<4>();
      PIN(z0); PIN(z1); PIN(wv[0]); PIN(wv[1]);
      if constexpr (L == 0) {
        RETRY((stale<false, 1u>(red8<false>(z0, z1)) || max4(max4(0, wv[0]), wv[1]) == SENT), {
          CNT(1);
          if (bad_) { ld4r<0>(z0, zo, zb0); ld4r<0>(z1, zo, zb1); ld4r<0>(wv[0], wo, wb0); ld4r<0>(wv[1], wo, wb1); }
          vmwait<0>(); PIN(z0); PIN(z1); PIN(wv[0]); PIN(wv[1]);
        });
      } else {
        RETRY((stale<true, 0x10000u>(red8<true>(z0, z1))), {
          CNT(2);
          if (bad_) { ld4r<0>(z0, zo, zb0); ld4r<0>(z1, zo, zb1); }
          vmwait<0>(); PIN(z0); PIN(z1);
        });
      }
      ST(S0 + 2);
      *(u32x2*)(zi + zw) = u32x2{z0.x, z0.y}; *(u32x2*)(zi + zw + 16) = u32x2{z0.z, z0.w};
      *(u32x2*)(zi + zw + 1024) = u32x2{z1.x, z1.y}; *(u32x2*)(zi + zw + 1024 + 16) = u32x2{z1.z, z1.w};
      *(u32x4*)(zi + 8192 + t16) = wv[0]; *(u32x4*)(zi + 12288 + t16) = wv[1];
      bar();
      const u8* zr = zi + pi_ * 4096 + l16;
      const u32x4 f00 = *(const u32x4*)(zr), f01 = *(const u32x4*)(zr + 1024), f10 = *(const u32x4*)(zr + 2048), f11 = *(const u32x4*)(zr + 3072);
      wv[0] = *(const u32x4*)(zi + 8192 + tid * 32); wv[1] = *(const u32x4*)(zi + 8192 + tid * 32 + 16);
      if constexpr (L == 0) { fill_h(OFF_H, 0); fill_h(OFF_H, 1); fill_h(OFF_H, 2); fill_h(OFF_H, 3); } else ring4(3);
      f32x4 d0, d1, d2, d3;
      mm8<false, 3>(d0, d1, d2, d3, f00, cc.b1re[0], f01, cc.b1re[1], f00, cc.b1im[0], f01, cc.b1im[1], f10, cc.b1re[0], f11, cc.b1re[1], f10, cc.b1im[0], f11, cc.b1im[1]);
      const u32x4 b20 = twiddle<false>(d0, d1, cc.t1c[0], cc.t1s[0]);
      SB();
      const u32x4 b21 = twiddle<false>(d2, d3, cc.t1c[1], cc.t1s[1]);
      f32x2 wr[2][2], wi[2][2];
      auto unpack_w = [&]() {
#pragma unroll
        for (int t = 0; t < 2; ++t)
#pragma unroll
          for (int h = 0; h < 2; ++h) {
            wr[t][h] = f32x2{blo(wv[t][2 * h]), blo(wv[t][2 * h + 1])}; wi[t][h] = f32x2{bhi(wv[t][2 * h]), bhi(wv[t][2 * h + 1])};
            PIN(wr[t][h]); PIN(wi[t][h]);
          }
      };
      mm8<false, 0>(d0, d1, d2, d3, cc.a2re[0][0], b20, cc.a2re[0][1], b21, cc.a2im[0][0], b20, cc.a2im[0][1], b21, cc.a2re[1][0], b20, cc.a2re[1][1], b21, cc.a2im[1][0],
                    b20, cc.a2im[1][1], b21);
      SB();
      unpack_w();
      SB();
      u32x4 a30, a31;
      if (phi != 0) {
        a30 = cmul_b(d0, d1, wr[0], wi[0]);
        a31 = cmul_b(d2, d3, wr[1], wi[1]);
      } else {
        u32 mx = 0;
#pragma unroll
        for (int j = 0; j < 8; ++j) { PIN(wa[j]); mx = max4(mx, wa[j]); }
        _Pragma("nounroll") for (int sp = 0; sp < SPIN_CAP && __any(mx == SENT); ++sp) {
          static_for<8>([&](auto J) { constexpr int j = decltype(J)::value; ld4r<0>(wa[j], t16, ws + OFF_WNY + j * 4096); });
          vmwait<0>();
          mx = 0;
#pragma unroll
          for (int j = 0; j < 8; ++j) { PIN(wa[j]); mx = max4(mx, wa[j]); }
        }
        float* pl = (float*)zi;
        bar_nw();
        float* pw = pl + k1l * 36 + 4 * g;
        *(f32x4*)(pw) = d0; *(f32x4*)(pw + 16) = d2; *(f32x4*)(pw + 2340) = d1; *(f32x4*)(pw + 2340 + 16) = d3;
        if (k1l == 0) {
#pragma unroll
          for (int r = 0; r < 4; ++r) {
            pl[64 * 36 + ((4 * g + r - 1) & 31)] = d0[r]; pl[64 * 36 + ((16 + 4 * g + r - 1) & 31)] = d2[r];
            pl[2340 + 64 * 36 + ((4 * g + r - 1) & 31)] = d1[r]; pl[2340 + 64 * 36 + ((16 + 4 * g + r - 1) & 31)] = d3[r];
          }
        }
        bar();
        const float* pr_ = pl + (64 - k1l) * 36 + 28 - 4 * g;
        const f32x4 qr0 = *(const f32x4*)(pr_), qr1 = *(const f32x4*)(pr_ - 16), qi0 = *(const f32x4*)(pr_ + 2340), qi1 = *(const f32x4*)(pr_ + 2340 - 16);
#pragma unroll
        for (int r = 0; r < 4; ++r) {
          const f32x4 c0 = __builtin_bit_cast(f32x4, wa[r]), c1 = __builtin_bit_cast(f32x4, wa[4 + r]);
          a30[r] = pkb(d0[r] * c0[0] - d1[r] * c0[1] + qr0[3 - r] * c0[2] + qi0[3 - r] * c0[3],
                       d0[r] * c0[1] + d1[r] * c0[0] + qr0[3 - r] * c0[3] - qi0[3 - r] * c0[2]);
          a31[r] = pkb(d2[r] * c1[0] - d3[r] * c1[1] + qr1[3 - r] * c1[2] + qi1[3 - r] * c1[3],
                       d2[r] * c1[1] + d3[r] * c1[0] + qr1[3 - r] * c1[3] - qi1[3 - r] * c1[2]);
        }
      }
      u8* ex = lds + LDS_SCR + 20480;
      mm8<true, 3, 2>(d0, d1, d2, d3, a30, cc.b3re[0][0], a31, cc.b3re[0][1], a30, cc.b3im[0][0], a31, cc.b3im[0][1], a30, cc.b3re[1][0], a31, cc.b3re[1][1], a30, cc.b3im[1][0],
                   a31, cc.b3im[1][1]);
      *(u32x4*)(ex + w * 1024 + l16) = twiddle<true>(d0, d1, cc.t2c[0], cc.t2s[0]);
      SB();
      *(u32x4*)(ex + (4 + w) * 1024 + l16) = twiddle<true>(d2, d3, cc.t2c[1], cc.t2s[1]);
      if constexpr (L == 0) vmwait<4>();
      bar();
      const u8* er = ex + (w & 1) * 4096 + l16;
      const u32x4 g0 = *(const u32x4*)(er), g1 = *(const u32x4*)(er + 1024), g2 = *(const u32x4*)(er + 2048), g3 = *(const u32x4*)(er + 3072);
      mm8<true, 7, 1>(d0, d1, d2, d3, cc.a4re[0][0], g0, cc.a4re[0][1], g1, cc.a4im[0][0], g0, cc.a4im[0][1], g1, cc.a4re[1][0], g2, cc.a4re[1][1], g3, cc.a4im[1][0], g2,
                   cc.a4im[1][1], g3);
      {
        const f32x2 lr0 = lo2(d0) + lo2(d2), li0 = lo2(d1) + lo2(d3), hr0 = lo2(d0) - lo2(d2), hi0 = lo2(d1) - lo2(d3);
        const f32x2 lr1 = hi2(d0) + hi2(d2), li1 = hi2(d1) + hi2(d3), hr1 = hi2(d0) - hi2(d2), hi1 = hi2(d1) - hi2(d3);
        if (phi != 0) {
          const f32x2 pr0 = lr0 - hi0, pi0 = li0 + hr0, qr0 = lr0 + hi0, qi0 = hr0 - li0;
          st4(mark4<1u - L, L>(u32x4{pkb(pr0.x, pi0.x), pkb(qr0.x, qi0.x), pkb(pr0.y, pi0.y), pkb(qr0.y, qi0.y)}), uo);
          const f32x2 pr1 = lr1 - hi1, pi1 = li1 + hr1, qr1 = lr1 + hi1, qi1 = hr1 - li1;
          st4(mark4<1u - L, L>(u32x4{pkb(pr1.x, pi1.x), pkb(qr1.x, qi1.x), pkb(pr1.y, pi1.y), pkb(qr1.y, qi1.y)}), uo + 1024);
          MEMFENCE();
        } else {
          st4(mark4<1u - L, L>(u32x4{pkb(lr0.x, hr0.x), pkb(li0.x, hi0.x), pkb(lr0.y, hr0.y), pkb(li0.y, hi0.y)}), uo);
          st4(mark4<1u - L, L>(u32x4{pkb(lr1.x, hr1.x), pkb(li1.x, hi1.x), pkb(lr1.y, hr1.y), pkb(li1.y, hi1.y)}), uo + 1024);
        }
      }
    }
    ST(S0 + 3);
    {
      FRESH();
      SB();
      row_consts<true, 0>(rc, m, g);
      f32x2 xa[4], xb[4];
#pragma unroll
      for (int i = 0; i < 4; ++i) {
        xa[i] = f32x2{blo(xl[i >> 1][i & 1]), bhi(xl[i >> 1][i & 1])}; xb[i] = f32x2{blo(xh[i >> 1][i & 1]), bhi(xh[i >> 1][i & 1])};
        PIN(xa[i]); PIN(xb[i]);
      }
      SB();
      u32x4 c0, c1;
      const u32 uo = (tid >> 1) * 8192 + (tid & 1) * 16;
      const u8* ub0 = ws + OFF_U + pos * 32;
      const u8* ub1 = ub0 + 128 * 8192;
      ld4<0>(c0, uo, ub0); ld4<0>(c1, uo, ub1);
      if constexpr (L == 0) { fill_h(OFF_H, 4); fill_h(OFF_H, 5); fill_h(OFF_H, 6); }
      SB();
      row_consts<true, 1>(rc, m, g);
      SB();
      if constexpr (L == 1) ring4(4);
      u32* img = (u32*)(lds + LDS_SCR);
      u32 ip0, iq0, iq1, xno, tw0, tw1, trd;
      {
        const int ph = tid >> 1, fq0 = ph ? 512 - ph : 256, fq1 = 384 - ph;
        ip0 = ((tid & 1) * 1280 + (ph & 31) * 20 + (ph >> 5)) * 4;
        iq0 = ((tid & 1) * 1280 + (fq0 & 31) * 20 + (fq0 >> 5)) * 4; iq1 = ((tid & 1) * 1280 + (fq1 & 31) * 20 + (fq1 >> 5)) * 4;
        xno = OFF_A + (pos >> 1) * 16384 + (tid >> 3) * 256 + (pos & 1) * 128 + (tid & 7) * 16;
        tw0 = (4 * m + g) * 128 + ((2 * w) ^ (m & 7)) * 16; tw1 = (4 * m + g) * 128 + ((2 * w + 1) ^ (m & 7)) * 16;
        trd = (tid >> 3) * 128 + ((tid & 7) ^ ((tid >> 5) & 7)) * 16;
        PIN(ip0); PIN(iq0); PIN(iq1); PIN(xno); PIN(tw0); PIN(tw1); PIN(trd);
      }
      SB();
      ST(S0 + 4);
      vmwait<(L == 0 ? 3 : 4)>(); PIN(c0); PIN(c1);
      {
        RETRY((stale<L == 1, 1u>(red8<L == 1>(c0, c1))), {
          CNT(3);
          if (bad_) { ld4r<0>(c0, uo, ub0); ld4r<0>(c1, uo, ub1); }
          vmwait<0>(); PIN(c0); PIN(c1);
        });
      }
      ST(S0 + 5);
      {
        u8* d = (u8*)img;
        *(u32*)(d + ip0) = c0.x; *(u32*)(d + iq0) = c0.y; *(u32*)(d + 2560 + ip0) = c0.z; *(u32*)(d + 2560 + iq0) = c0.w;
        *(u32*)(d + ip0 + 16) = c1.x; *(u32*)(d + iq1) = c1.y; *(u32*)(d + 2560 + ip0 + 16) = c1.z; *(u32*)(d + 2560 + iq1) = c1.w;
      }
      bar();
      const u32x4 a0 = *(const u32x4*)(img + w * 640 + m * 20 + 4 * g), a1 = *(const u32x4*)(img + w * 640 + (16 + m) * 20 + 4 * g);
      if constexpr (L == 1) ring4(5);
      if constexpr (L == 0) ring4(0);
      f32x4 dr0, di0, dr1, di1;
      row_fft<true, 7>(rc, a0, a1, dr0, di0, dr1, di1);
      if constexpr (L == 0) ring4(1);
      const float ys = 1.0f / 32768.0f;
      xa[0] += lo2(dr0) * ys; xa[1] += hi2(dr0) * ys; xa[2] += lo2(dr1) * ys; xa[3] += hi2(dr1) * ys;
      xb[0] += lo2(di0) * ys; xb[1] += hi2(di0) * ys; xb[2] += lo2(di1) * ys; xb[3] += hi2(di1) * ys;
      const f32x2 sa = (xa[0] + xa[1]) + (xa[2] + xa[3]), sb = (xb[0] + xb[1]) + (xb[2] + xb[3]);
      const float mean = reduce2(sa.x + sa.y, sb.x + sb.y) * (1.0f / 512.0f);
      const f32x2 M = bcast2(mean);
      f32x2 da[4], db[4];
#pragma unroll
      for (int i = 0; i < 4; ++i) { da[i] = xa[i] - M.x; db[i] = xb[i] - M.y; }
      f32x2 qa = da[0] * da[0], qb = db[0] * db[0];
#pragma unroll
      for (int i = 1; i < 4; ++i) { qa += da[i] * da[i]; qb += db[i] * db[i]; }
      if constexpr (L == 0) { SB(); ring4(2); SB(); }
      f32x2 R, O;
      ln_finish(mean, reduce2(qa.x + qa.y, qb.x + qb.y), R, O);
      u32x4 na, nb;
#pragma unroll
      for (int i = 0; i < 4; ++i) {
        const f32x2 va = da[i] * R.x + O.x, vb = db[i] * R.y + O.y;
        na[i] = mark<0x10000u, 1u - L>(pkb(va.x, va.y)); nb[i] = mark<0x10000u, 1u - L>(pkb(vb.x, vb.y));
      }
      u8* tr = lds + LDS_SCR + 12288;
      *(u32x4*)(tr + tw0) = na; *(u32x4*)(tr + tw1) = nb;
      if constexpr (L == 0) vmwait<12>();
      bar();
      st4(*(const u32x4*)(tr + trd), xno);
      st4(*(const u32x4*)(tr + 4096 + trd), xno + 8192);
      SB();
      const u32x4 rl = {pkb(xa[0].x, xa[0].y), pkb(xa[1].x, xa[1].y), pkb(xa[2].x, xa[2].y), pkb(xa[3].x, xa[3].y)};
      const u32x4 rh = {pkb(xb[0].x, xb[0].y), pkb(xb[1].x, xb[1].y), pkb(xb[2].x, xb[2].y), pkb(xb[3].x, xb[3].y)};
      if constexpr (L == 0) {
        *(u32x4*)(lds + LDS_STASH + t16) = rl; *(u32x4*)(lds + LDS_STASH + 4096 + t16) = rh;
        ring4(3); ring4(4); ring4(5); ring4(6); ring4(7);
      } else {
        const u32 o = OFF_X3 + (8 * pos + 2 * w) * 1024 + xo;
        st2(u32x2{rl.x, rl.y}, o); st2(u32x2{rl.z, rl.w}, o + 512);
        st2(u32x2{rh.x, rh.y}, o + 1024); st2(u32x2{rh.z, rh.w}, o + 1536);
        __builtin_amdgcn_s_sleep(6);
      }
    }
    ST(S0 + 6);
    {
      FRESH();
      u32x4 tk[32], he[32], hl[16], w2t[4], w2v[3][4];
      u32x2 hh[16];
      f32x4 ac1[6][2], ac2[4][2], ac3[4][2];
      const u8* hs0 = ws + OFF_H + (8 * G + 4 * half2) * 57344 + (4 * w) * 3584 + 3584;
      const u8* hs2 = hs0 + 7168;
      const u8* w2src = ws + OFF_W2 + (ct * 4 + w) * 57344;
      PINS(hs0); PINS(hs2);
      u32 hv[4] = {l16, l16 + 57344, l16 + 2 * 57344, l16 + 3 * 57344}, h8[4];
#pragma unroll
      for (int u = 0; u < 4; ++u) { h8[u] = hv[u] - lane * 8; PIN(hv[u]); PIN(h8[u]); }
      auto hload = [&](auto RE, auto KC, auto UC) {
        constexpr int k = decltype(KC)::value, u = decltype(UC)::value;
        constexpr bool re = decltype(RE)::value;
        if constexpr (k == 4 || k == 5) {
          constexpr int o = (u >> 2) * 3584 + 3072 - 3584;
          if constexpr (re) ld2r<o>(hh[8 * (k - 4) + u], h8[u & 3], k == 5 ? hs2 : hs0); else ld2<o>(hh[8 * (k - 4) + u], h8[u & 3], k == 5 ? hs2 : hs0);
        } else {
          constexpr int j = k < 4 ? k : k < 10 ? k - 6 : k - 10, o = (j & 1) * 3584 + (k < 4 ? 0 : k < 10 ? 1024 : 2048) - 3584;
          u32x4& d = k < 10 ? he[(k < 4 ? 0 : 16) + 4 * j + u] : hl[4 * j + u];
          if constexpr (re) ld4r<o>(d, hv[u], j >> 1 ? hs2 : hs0); else ld4<o>(d, hv[u], j >> 1 ? hs2 : hs0);
        }
      };
      auto hseq = [&](auto IC) {
        constexpr int i = decltype(IC)::value, k = i < 16 ? i >> 2 : i < 32 ? 4 + ((i - 16) >> 3) : 6 + ((i - 32) >> 2);
        hload(std::false_type{}, std::integral_constant<int, k>{}, std::integral_constant<int, (i < 16 || i >= 32) ? (i & 3) : (i & 7)>{});
      };
      {
        const u8* w1b = ws + OFF_W1 + member * 229376 + 131072 + w * 8192;
        const u8* tk0 = ws + OFF_A + (8 * G + 2 * w) * 16384 + 4096;
        const u8* tk1 = tk0 + 8192;
        u32 tv1 = l16 + 16384, lhi = l16 + 65536;
        u32 hd0 = OFF_H + (8 * G + 2 * w) * 57344 + member * 3584 + l16, hd1 = hd0 + 57344;
        u32 hd2 = hd0 + 3072 - lane * 8, hd3 = hd2 + 57344;
        PIN(tv1); PIN(lhi); PIN(hd0); PIN(hd1); PIN(hd2); PIN(hd3); PINS(tk0); PINS(tk1);
        auto tok = [&](auto RE, auto QC) {
          constexpr int q = decltype(QC)::value, ks = q >> 1;
          if constexpr (decltype(RE)::value) ld4r<(ks & 7) * 1024 - 4096>(tk[q], q & 1 ? tv1 : l16, ks >> 3 ? tk1 : tk0);
          else ld4<(ks & 7) * 1024 - 4096>(tk[q], q & 1 ? tv1 : l16, ks >> 3 ? tk1 : tk0);
        };
        constexpr bool TONE = L == 0;
        constexpr u32 TBIT = L == 0 ? 1u : 0x10000u;
        u32 mx0 = 0, mx1 = 0;
        auto mx0_fin = [&]() { return red2<TONE>(mx0, mx1) & TBIT; };
        auto fix = [&](auto QC, u64 bad) {
          constexpr int q0 = decltype(QC)::value;
          if (__builtin_expect(bad != 0, 0)) {
            _Pragma("nounroll") for (int sp = 0; sp < SPIN_CAP; ++sp) {
              CNT(4);
              static_for<4>([&](auto I) { tok(std::true_type{}, std::integral_constant<int, q0 + decltype(I)::value>{}); });
              vmwait<0>();
#pragma unroll
              for (int q = q0; q < q0 + 4; ++q) PIN(tk[q]);
              if (!__any(stale<TONE, TBIT>(red2<TONE>(red8<TONE>(tk[q0], tk[q0 + 1]), red8<TONE>(tk[q0 + 2], tk[q0 + 3]))))) break;
            }
          }
        };
        static_for<NTOK0>([&](auto Q) { tok(std::false_type{}, Q); });
        constexpr int PD = 8;
        u32x4 af[PD];
        auto frag = [&](int x) {
          const int o = (x & 127) * 1024;
          return o < 65536 ? *(const u32x4*)(lds + l16 + o) : *(const u32x4*)(lds + lhi + (o - 65536));
        };
        vmwait<NTOK0>();
        bar_nw();
#pragma unroll
        for (int x = 0; x < PD - 1; ++x) af[x] = frag(x);
        vmwait<NTOK0 - 4>();
        ST(S0 + 7);
#pragma unroll
        for (int q = 0; q < 4; ++q) PIN(tk[q]);
        mx0 = red8<TONE>(tk[0], tk[1]); mx1 = red8<TONE>(tk[2], tk[3]);
        fix(std::integral_constant<int, 0>{}, __ballot(stale<TONE, TBIT>(red2<TONE>(mx0, mx1))));
        SB();
        ST(S0 + 8);
        float eg[2][2], eu[2][2];
        u32 ev[2];
        u64 bm = 0;
        u32x4 eo[1];
        auto epi = [&](auto NC) {
          constexpr int n = decltype(NC)::value;
          if constexpr (n < NEPI1 + NEPI2) {
            constexpr bool p1 = n < NEPI1;
            constexpr int i = p1 ? n - (n > 40) - (n > 81) - (n > 102) : (n - NEPI1) - (n - NEPI1) / 41;
            if constexpr (p1 && (n == 40 || n == 81)) st4(eo[0], n == 40 ? hd0 : hd1);
            else if constexpr (p1 && (n == 102 || n == 123)) st2(u32x2{eo[0].x, eo[0].y}, n == 102 ? hd2 : hd3);
            else if constexpr (!p1 && (n - NEPI1) % 41 == 40) st4(eo[0], ((n - NEPI1) / 41 ? hd1 : hd0) + 1024);
            else {
              constexpr int s = i & 1, st = (i % 20) >> 1, blk = i / 20;
              constexpr int u = p1 ? (blk < 4 ? blk >> 1 : blk - 4) : blk >> 1, pr = p1 ? (blk < 4 ? blk & 1 : 2) : blk & 1;
              constexpr int ei = 0, d = 2 * (pr & 1) + s;
              const f32x4& ga = p1 ? ac1[2 * pr][u] : ac2[2 * (pr & 1)][u];
              const f32x4& up = p1 ? ac1[2 * pr + 1][u] : ac2[2 * (pr & 1) + 1][u];
              if constexpr (st < 2) { eg[s][st] = ga[2 * s + st]; PIN(eg[s][st]); }
              else if constexpr (st < 4) { eu[s][st - 2] = up[2 * s + st - 2]; PIN(eu[s][st - 2]); }
              else if constexpr (st < 6) { eg[s][st - 4] = relu(eg[s][st - 4]); PIN(eg[s][st - 4]); }
              else if constexpr (st < 8) { eg[s][st - 6] *= eu[s][st - 6]; PIN(eg[s][st - 6]); }
              else if constexpr (st == 8) { ev[s] = pkb(eg[s][0], eg[s][1]); PIN(ev[s]); }
              else { u32 v = mark<1u - L, L>(ev[s]); PIN(v); eo[ei][d] = v; }
            }
            SB();
          }
        };
        static_for<224>([&](auto XC) {
          constexpr int x = decltype(XC)::value, job = G1S[L].job[x], ji = G1S[L].idx[x], e0 = G1S[L].epi[x], e1 = G1S[L].epi[x + 1];
          if constexpr (x == 32 || x == 64) bar_nw();
          if constexpr (x == 97) { vmwait<g1_wait(L, x, 2, 7)>(); bar_nw(); }
          if constexpr (x == 150) { vmwait<g1_wait(L, x, 2, 23)>(); bar_nw(); }
          constexpr int cj = (x + 12) / 12, co = x + 12 - 12 * cj;
          constexpr bool chk = x < 84 && co >= 5;
          constexpr int ks = x < 96 ? x / 6 : x < 160 ? (x - 96) >> 2 : (x - 160) >> 2, t = x < 96 ? x % 6 : x & 3;
          auto mm = [&](int u) {
            f32x4& a = x < 96 ? ac1[x < 96 ? t : 0][u] : x < 160 ? ac2[x < 96 ? 0 : t][u] : ac3[x < 96 ? 0 : t][u];
            if constexpr (ks == 0) a = mfma_b(af[x % PD], tk[u], f32x4{0.f, 0.f, 0.f, 0.f}); else a = mfma_b(af[x % PD], tk[2 * ks + u], a);
          };
          SB();
          mm(0);
          SB();
          if constexpr (x < 223) __builtin_amdgcn_s_waitcnt(0xC07F | ((x + PD - 2 < 224 ? PD - 3 : 222 - x) << 8));
          if constexpr (chk && co == 5) {
            vmwait<g1_wait(L, x, 1, 4 * cj + 3)>();
            PIN(tk[4 * cj]); PIN(tk[4 * cj + 1]); PIN(tk[4 * cj + 2]); PIN(tk[4 * cj + 3]);
          }
          SB();
          if constexpr (chk && co >= 6 && co < 10) {
            const u32x4 v = tk[4 * cj + co - 6];
            mx0 = co == 6 ? red2<TONE>(v.x, v.y) : red3<TONE>(mx0, v.x, v.y); PIN(mx0); SB();
            mx1 = co == 6 ? red2<TONE>(v.z, v.w) : red3<TONE>(mx1, v.z, v.w); PIN(mx1);
          }
          if constexpr (chk && co == 10) { mx0 = mx0_fin(); PIN(mx0); SB(); bm = __ballot(TONE ? mx0 == 0 : mx0 != 0); PINS(bm); }
          if constexpr (e0 < e1) epi(std::integral_constant<int, e0>{});
          if constexpr (e0 + 1 < e1) epi(std::integral_constant<int, e0 + 1>{});
          SB();
          mm(1);
          SB();
          if constexpr (chk && co == 11) fix(std::integral_constant<int, 4 * cj>{}, bm);
          if constexpr (job == 2 && (ji & 3) == 0) set_m0(ldsbase + (ji >> 3) * 32768 + w * 8192 + (ji & 4) * 1024);
          SB();
          if constexpr (x + PD - 1 < 224) af[(x + PD - 1) % PD] = frag(x + PD - 1);
          SB();
          if constexpr (job == 1) tok(std::false_type{}, std::integral_constant<int, ji>{});
          if constexpr (job == 2) dma1<(ji & 3) * 1024>(l16, w1b + (ji >> 3) * 32768 + (ji & 4) * 1024);
          if constexpr (job == 3) ld4a<(ji & 7) * 1024 - 4096>(w2a[ji >> 2][ji & 3], l16, w2src + (ji >> 3) * 8192 + 4096);
          if constexpr (job == 5) ld4a<ji * 1024>(w2t[ji], l16, w2src + 40960);
          if constexpr (job == 4) hseq(std::integral_constant<int, ji>{});
          if constexpr (e0 + 2 < e1) epi(std::integral_constant<int, e0 + 2>{});
          SB();
        });
        ST(S0 + 9);
#pragma unroll
        for (int u = 0; u < 2; ++u) {
          u32x4 o;
#pragma unroll
          for (int p = 0; p < 2; ++p) {
            const f32x4 ga = ac3[2 * p][u], up = ac3[2 * p + 1][u];
            const f32x2 a = f32x2{relu(ga[0]), relu(ga[1])} * lo2(up), c = f32x2{relu(ga[2]), relu(ga[3])} * hi2(up);
            o[2 * p] = mark<1u - L, L>(pkb(a.x, a.y)); o[2 * p + 1] = mark<1u - L, L>(pkb(c.x, c.y));
          }
          st4(o, (u ? hd1 : hd0) + 2048);
        }
        SB();
      }
      ST(S0 + 10);
      {
        f32x4 acc[4][4];
        u32x4 x3a, x3b;
        const int tokl = 16 * w + (lane >> 3);
        u32 eo = (128 * G + 64 * half2 + tokl) * 1024 + ct * 128 + (lane & 7) * 16;
        PIN(eo);
        const u8* w2x = w2src + 45056 + 4096;
        const u8* x3s = ws + OFF_X3;
        u32 lx4 = l16 + 4096, eo2 = eo + 8192;
        u64 bm = 0;
        PINS(w2x); PINS(x3s); PIN(lx4); PIN(eo2);
        constexpr int NE2 = 48 - NEARLY, LB = NE2 + 12;
        auto cum = [](int n) constexpr { int c = 0; for (int i = 0; i < n; ++i) c += i < LB + 16 ? 1 : 0; return c; };
        auto young = [cum](int k, int n) constexpr {
          const int i = k < 4 ? 4 * k + 3 : k < 6 ? 16 + 8 * (k - 4) + 7 : 32 + 4 * (k - 6) + 3;
          if (k < 10 && i < NEARLY) return cum(n) + 2 + G1S[L].cum[224] - G1S[L].pos[4][i] - 1;
          if (k < 10) return cum(n) - cum(i - NEARLY + 1);
          return cum(n) - cum(LB + 4 * (k - 10) + 4);
        };
        constexpr bool HONE = L == 1;
        u32 mx0 = 0, mx1 = 0;
        auto mx0_fin = [&]() { return red2<HONE>(mx0, mx1) & 1u; };
        auto pin_k = [&](auto KC) {
          constexpr int k = decltype(KC)::value;
#pragma unroll
          for (int u = 0; u < 4; ++u) {
            if constexpr (k < 4) PIN(he[4 * k + u]);
            else if constexpr (k < 6) { PIN(hh[8 * (k - 4) + u]); PIN(hh[8 * (k - 4) + 4 + u]); }
            else if constexpr (k < 10) PIN(he[16 + 4 * (k - 6) + u]);
            else { PIN(hl[4 * (k - 10) + u]); if constexpr (k > 10) PIN(w2v[k - 11][u]); }
          }
        };
        auto opnd = [&](auto KC, int u) {
          constexpr int k = decltype(KC)::value;
          if constexpr (k < 4) return he[4 * k + u];
          else if constexpr (k < 6) return u32x4{hh[8 * (k - 4) + u].x, hh[8 * (k - 4) + u].y, hh[8 * (k - 4) + 4 + u].x, hh[8 * (k - 4) + 4 + u].y};
          else if constexpr (k < 10) return he[16 + 4 * (k - 6) + u];
          else return hl[4 * (k - 10) + u];
        };
        auto fixk = [&](auto KC, u64 bad) {
          constexpr int k = decltype(KC)::value;
          if (__builtin_expect(bad != 0, 0)) {
            _Pragma("nounroll") for (int sp = 0; sp < SPIN_CAP; ++sp) {
              CNT(k < 10 ? 5 : 6);
              static_for<(k == 4 || k == 5) ? 8 : 4>([&](auto U) { hload(std::true_type{}, KC, U); });
              vmwait<0>();
              pin_k(KC);
              if (!__any(stale<HONE, 1u>(red2<HONE>(red8<HONE>(opnd(KC, 0), opnd(KC, 1)), red8<HONE>(opnd(KC, 2), opnd(KC, 3)))))) break;
            }
          }
        };
        u32 rw[4];
#pragma unroll
        for (int c = 0; c < 4; ++c) { rw[c] = w * 16384 + c * 4096 + (g * 16 + (m ^ (2 * (2 * c + (g >> 1))))) * 16; PIN(rw[c]); }
        u32 oo[2] = {0, 0};
        if constexpr (L == 1) {
#pragma unroll
          for (int a = 0; a < 2; ++a) {
            const int tok = 128 * G + 64 * half2 + tokl + 8 * a;
            oo[a] = (row_of_pos(tok >> 3, (tok >> 1) & 3) + 1024 * (tok & 1)) * 1024 + ct * 128 + (lane & 7) * 16;
            PIN(oo[a]);
          }
        }
        vmwait<clamp63(young(0, 0))>();
        if constexpr (L == 0) {
#pragma unroll
          for (int k = 0; k < 10; ++k) { PINA(w2a[k][0]); PINA(w2a[k][1]); PINA(w2a[k][2]); PINA(w2a[k][3]); }
        }
        PINA(w2t[0]); PINA(w2t[1]); PINA(w2t[2]); PINA(w2t[3]);
        pin_k(std::integral_constant<int, 0>{});
        mx0 = red8<HONE>(he[0], he[1]); mx1 = red8<HONE>(he[2], he[3]);
        fixk(std::integral_constant<int, 0>{}, __ballot(stale<HONE, 1u>(red2<HONE>(mx0, mx1))));
        ST(S0 + 11);
        auto wfr = [&](auto KC, int c) {
          constexpr int k = decltype(KC)::value;
          if constexpr (k < 10) return w2a[k][c]; else if constexpr (k == 10) return w2t[c]; else return w2v[k - 11][c];
        };
        static_for<48>([&](auto NC) {
          constexpr int n = decltype(NC)::value, k = n >> 2, c = n & 3;
          using KC = std::integral_constant<int, k>;
          using KN = std::integral_constant<int, k + 1>;
          static_for<4>([&](auto UC) {
            constexpr int u = decltype(UC)::value;
            SB();
            if constexpr (k == 0) acc[c][u] = mfma_b(wfr(KC{}, c), opnd(KC{}, u), f32x4{0.f, 0.f, 0.f, 0.f});
            else acc[c][u] = mfma_b(wfr(KC{}, c), opnd(KC{}, u), acc[c][u]);
            SB();
            if constexpr (c == 1 && u == 0) {
              vmwait<clamp63(young(k == 11 ? 13 : k + 1, n))>(); pin_k(KN{});
              if constexpr (k == 11) pin_k(std::integral_constant<int, 13>{});
            }
            if constexpr ((c == 1 && (u == 1 || u == 2)) || (c == 2 && u < (k == 11 ? 4 : 2)) || (k == 11 && c == 3 && u < 2)) {
              constexpr int j = c == 1 ? u - 1 : c == 2 ? 2 + u : 6 + u;
              const u32x4 v = j < 4 ? opnd(KN{}, j & 3) : opnd(std::integral_constant<int, 13>{}, j & 3);
              mx0 = j == 0 ? red2<HONE>(v.x, v.y) : red3<HONE>(mx0, v.x, v.y); PIN(mx0); SB();
              mx1 = j == 0 ? red2<HONE>(v.z, v.w) : red3<HONE>(mx1, v.z, v.w); PIN(mx1);
            }
            if constexpr (c == 3 && u == (k == 11 ? 2 : 0)) { mx0 = mx0_fin(); PIN(mx0); SB(); bm = __ballot(HONE ? mx0 == 0 : mx0 != 0); PINS(bm); }
            if constexpr (u == 3) {
              if constexpr (n < NE2) hseq(std::integral_constant<int, NEARLY + n>{});
              else if constexpr (n < NE2 + 8) ld4<(n - NE2) * 1024 - 4096>(w2v[(n - NE2) >> 2][(n - NE2) & 3], l16, w2x);
              else if constexpr (n < LB) ld4<(n - NE2 - 8) * 1024>(w2v[2][(n - NE2) & 3], lx4, w2x);
              else if constexpr (n < LB + 16) hload(std::false_type{}, std::integral_constant<int, 10 + ((n - LB) >> 2)>{}, std::integral_constant<int, (n - LB) & 3>{});
              if constexpr (c == 3) { fixk(KN{}, bm); if constexpr (k == 11) fixk(std::integral_constant<int, 13>{}, bm); }
            }
          });
        });
        bar_nw();
        if constexpr (L == 1) { ld4<0>(x3a, eo, x3s); ld4<0>(x3b, eo2, x3s); }
        static_for<20>([&](auto TC) {
          constexpr int t = decltype(TC)::value;
          if constexpr (t < 16) {
            SB();
            acc[t >> 2][t & 3] = mfma_b(w2v[1][t >> 2], hl[8 + (t & 3)], acc[t >> 2][t & 3]);
            acc[t >> 2][t & 3] = mfma_b(w2v[2][t >> 2], hl[12 + (t & 3)], acc[t >> 2][t & 3]);
            SB();
          }
          if constexpr (t >= 4) *(f32x4*)(lds + rw[(t - 4) >> 2] + ((t - 4) & 3) * 1024) = acc[(t - 4) >> 2][(t - 4) & 3];
        });
        SB();
        ST(S0 + 12);
        const int o8 = lane & 7;
        const u8* rp0 = lds + ((o8 >> 1) * 4 + w) * 1024 + (2 * (o8 & 1) * 16 + ((lane >> 3) ^ (2 * o8))) * 16;
        const u8* rp1 = lds + ((o8 >> 1) * 4 + w) * 1024 + (2 * (o8 & 1) * 16 + ((8 + (lane >> 3)) ^ (2 * o8))) * 16;
        bar();
        f32x4 s[2][2][4];
#pragma unroll
        for (int p = 0; p < 4; ++p) { s[0][0][p] = *(const f32x4*)(rp0 + p * 16384); s[0][1][p] = *(const f32x4*)(rp0 + p * 16384 + 256); }
#pragma unroll
        for (int p = 0; p < 4; ++p) { s[1][0][p] = *(const f32x4*)(rp1 + p * 16384); s[1][1][p] = *(const f32x4*)(rp1 + p * 16384 + 256); }
        SB();
        if constexpr (L == 1) {
          vmwait<0>(); PIN(x3a); PIN(x3b);
        }
#pragma unroll
        for (int a = 0; a < 2; ++a) {
          const f32x4 s0 = (s[a][0][0] + s[a][0][1]) + (s[a][0][2] + s[a][0][3]), s1 = (s[a][1][0] + s[a][1][1]) + (s[a][1][2] + s[a][1][3]);
          const u32x4 y = {pkb(s0[0], s0[1]), pkb(s0[2], s0[3]), pkb(s1[0], s1[1]), pkb(s1[2], s1[3])};
          if constexpr (L == 0) {
            st4(mark4<0x10001u, 0u>(y), OFF_A + eo + a * 8192);
          } else {
            const u32x4 xr = a ? x3b : x3a;
            u32x4 o;
#pragma unroll
            for (int p = 0; p < 4; ++p) o[p] = pkb(blo(xr[p]) + blo(y[p]), bhi(xr[p]) + bhi(y[p]));
            *(u32x4*)((u8*)P.out + oo[a]) = o;
          }
        }
      }
    }
    ST(S0 + 13);
    if constexpr (L == 0) {
      FRESH();
      SB();
      row_consts<false, 0>(rc, m, g);
      SB();
      u32x2 d0, d1, d2, d3;
      const u8* db = ws + OFF_A + (8 * pos + 2 * w) * 1024;
      const u32x4 sl = *(const u32x4*)(lds + LDS_STASH + t16), sh = *(const u32x4*)(lds + LDS_STASH + 4096 + t16);
#pragma unroll
      for (int i = 0; i < 4; ++i) {
        xv[2 * i] = f32x2{blo(sl[i]), blo(sh[i])}; xv[2 * i + 1] = f32x2{bhi(sl[i]), bhi(sh[i])};
        PIN(xv[2 * i]); PIN(xv[2 * i + 1]);
      }
      SB();
      bar_nw();
      ld2<0>(d0, xo, db); ld2<512>(d1, xo, db); ld2<1024>(d2, xo, db); ld2<1536>(d3, xo, db);
      ring4(0);
      SB();
      row_consts<false, 1>(rc, m, g);
      SB();
      ring4(1);
      ST(S0 + 14);
      vmwait<8>(); PIN(d0); PIN(d1); PIN(d2); PIN(d3);
      RETRY((stale<false, 1u>(red8<false>(u32x4{d0.x, d0.y, d1.x, d1.y}, u32x4{d2.x, d2.y, d3.x, d3.y}))), {
        CNT(7);
        if (bad_) { ld2r<0>(d0, xo, db); ld2r<512>(d1, xo, db); ld2r<1024>(d2, xo, db); ld2r<1536>(d3, xo, db); }
        vmwait<0>(); PIN(d0); PIN(d1); PIN(d2); PIN(d3);
      });
      xv[0] += f32x2{blo(d0.x), blo(d2.x)}; xv[1] += f32x2{bhi(d0.x), bhi(d2.x)}; xv[2] += f32x2{blo(d0.y), blo(d2.y)}; xv[3] += f32x2{bhi(d0.y), bhi(d2.y)};
      xv[4] += f32x2{blo(d1.x), blo(d3.x)}; xv[5] += f32x2{bhi(d1.x), bhi(d3.x)}; xv[6] += f32x2{blo(d1.y), blo(d3.y)}; xv[7] += f32x2{bhi(d1.y), bhi(d3.y)};
      ST(S0 + 15);
    }
  };
  layer(std::integral_constant<int, 0>{});
  layer(std::integral_constant<int, 1>{});
  if (blockIdx.x == 0 && threadIdx.x == 0) st2(ep, OFF_E);
}

extern "C" void kernel_launch(void* const* d_in, const int* in_sizes, int n_in, void* d_out, int out_size, void* d_ws, size_t ws_size,
                              hipStream_t stream) {
  Params p;
  p.x = (const u16*)d_in[0]; p.wr = (const u16*)d_in[1]; p.wi = (const u16*)d_in[2];
  p.wg = (const u16*)d_in[4]; p.wu = (const u16*)d_in[5]; p.wd = (const u16*)d_in[6];
  p.out = (u16*)d_out; p.ws = (u8*)d_ws;
  (void)hipFuncSetAttribute((const void*)k_all, hipFuncAttributeMaxDynamicSharedMemorySize, LDS_TOTAL);
  k_all<<<256, 256, LDS_TOTAL, stream>>>(p);
}
```

```cpp
#include <hip/hip_runtime.h>
#include <hip/hip_bf16.h>
#include <stdint.h>
#include <type_traits>
#include <utility>

typedef unsigned char u8;
typedef unsigned short u16;
typedef unsigned int u32;
typedef unsigned long long u64;
typedef u32 u32x2 __attribute__((ext_vector_type(2)));
typedef u32 u32x4 __attribute__((ext_vector_type(4)));
typedef float f32x2 __attribute__((ext_vector_type(2)));
typedef float f32x4 __attribute__((ext_vector_type(4)));
typedef _Float16 f16x2 __attribute__((ext_vector_type(2)));
typedef __bf16 bf16x2 __attribute__((ext_vector_type(2)));
typedef __bf16 bf16x8 __attribute__((ext_vector_type(8)));
#define DI __device__ __forceinline__
#define SB() __builtin_amdgcn_sched_barrier(0)
#define MEMFENCE() asm volatile("" ::: "memory")

constexpr u32 MB2 = 2u << 20;
constexpr u32 OFF_E = 0, OFF_FLAG = 4096, OFF_WNY = 65536;
constexpr u32 OFF_A = MB2;
constexpr u32 OFF_U = OFF_A + MB2;
constexpr u32 OFF_WX = OFF_U + MB2;
constexpr u32 OFF_X3 = OFF_WX + MB2;
constexpr u32 OFF_H = OFF_X3 + MB2;
constexpr u32 OFF_W1 = OFF_H + 7340032;
constexpr u32 OFF_W2 = OFF_W1 + 3670016;
constexpr u32 WS_USED = OFF_W2 + 1835008;
constexpr u32 SENT = 0xFFFFFFFFu;
constexpr int SPIN_CAP = 1 << 15;
constexpr u32 CODE_BYTES = 66 * 1024;

constexpr int LDS_SCR = 131072, LDS_STASH = LDS_SCR + 24576, LDS_TOTAL = 163840;

struct Params {
  const u16* x; const u16* wr; const u16* wi; const u16* wg; const u16* wu; const u16* wd; u16* out; u8* ws;
};

#define ST(i)
#define ST_DECL
#define CNT(i)
#define ST_OUT()

#define XS(i)
DI u32 pkh(float a, float b) { f32x2 v = {a, b}; return __builtin_bit_cast(u32, __builtin_convertvector(v, f16x2)); }
DI u32 pkb(float a, float b) { f32x2 v = {a, b}; return __builtin_bit_cast(u32, __builtin_convertvector(v, bf16x2)); }
template <bool BF> DI u32 pkc(float a, float b) { return BF ? pkb(a, b) : pkh(a, b); }
DI u32 pkh_n0(float a, float b, u32 after) { u32 r; asm("v_cvt_pk_f16_f32 %0, -%1, %2" : "=v"(r) : "v"(a), "v"(b), "v"(after)); return r; }
DI u32 pkb_n1(float a, float b, u32 after) { u32 r; asm("v_cvt_pk_bf16_f32 %0, %1, -%2" : "=v"(r) : "v"(a), "v"(b), "v"(after)); return r; }
DI float blo(u32 u) { return __uint_as_float(u << 16); }
DI float bhi(u32 u) { return __uint_as_float(u & 0xffff0000u); }
DI float bfr(float a) { return blo(pkb(a, a)); }
DI float relu(float a) { const int i = __float_as_int(a); return __int_as_float(i > 0 ? i : 0); }
DI u32 umax(u32 a, u32 b) { return a > b ? a : b; }
DI u32 max3(u32 a, u32 b, u32 c) { return umax(umax(a, b), c); }
DI u32 max4(u32 acc, u32x4 v) { return max3(max3(acc, v.x, v.y), v.z, v.w); }
template <bool ONE> DI u32 red3(u32 a, u32 b, u32 c) { return ONE ? (a & b & c) : (a | b | c); }
template <bool ONE> DI u32 red2(u32 a, u32 b) { return ONE ? (a & b) : (a | b); }
template <bool ONE> DI u32 red4(u32 acc, u32x4 v) { return red3<ONE>(red3<ONE>(acc, v.x, v.y), v.z, v.w); }
template <bool ONE> DI u32 red8(u32x4 a, u32x4 b) { return red4<ONE>(red3<ONE>(red2<ONE>(a.x, a.y), a.z, a.w), b); }
template <bool ONE, u32 BIT> DI bool stale(u32 r) { return ONE ? (r & BIT) == 0 : (r & BIT) != 0; }
template <u32 CLR, u32 SET> DI u32 mark(u32 v) { return (v & ~CLR) | SET; }
template <u32 CLR, u32 SET> DI u32x4 mark4(u32x4 v) { return u32x4{mark<CLR, SET>(v.x), mark<CLR, SET>(v.y), mark<CLR, SET>(v.z), mark<CLR, SET>(v.w)}; }
DI f32x2 lo2(f32x4 v) { return __builtin_shufflevector(v, v, 0, 1); }
DI f32x2 hi2(f32x4 v) { return __builtin_shufflevector(v, v, 2, 3); }
DI f32x2 swp(f32x2 v) { return __builtin_shufflevector(v, v, 1, 0); }
DI f32x4 mfma_b(u32x4 a, u32x4 b, f32x4 c) {
  return __builtin_amdgcn_mfma_f32_16x16x32_bf16(__builtin_bit_cast(bf16x8, a), __builtin_bit_cast(bf16x8, b), c, 0, 0, 0);
}
template <class T> DI void PIN(T& x) { asm volatile("" : "+v"(x)); }
template <class T> DI void PINS(T& x) { asm volatile("" : "+s"(x)); }
template <class T> DI void PINA(T& x) { asm volatile("" : "+a"(x)); }
template <class F, int... I> DI void static_for_(F&& f, std::integer_sequence<int, I...>) { (f(std::integral_constant<int, I>{}), ...); }
template <int N, class F> DI void static_for(F&& f) { static_for_(f, std::make_integer_sequence<int, N>{}); }

#define MM_(T) "v_mfma_f32_16x16x32_" T
template <bool BF> DI void mm4(f32x4& d0, f32x4& d1, f32x4& d2, f32x4& d3, u32x4 a0, u32x4 a1, u32x4 b0, u32x4 b1) {
#define MM4_(T) asm volatile("s_nop 1\n\t" MM_(T) " %0, %4, %6, 0\n\t" MM_(T) " %1, %4, %7, 0\n\t" MM_(T) " %2, %5, %6, 0\n\t" MM_(T) " %3, %5, %7, 0\n\ts_nop 5" \
               : "=&v"(d0), "=&v"(d1), "=&v"(d2), "=&v"(d3) : "v"(a0), "v"(a1), "v"(b0), "v"(b1))
  if constexpr (BF) MM4_("bf16"); else MM4_("f16");
}
template <bool BF, int TAIL, int AG = 0> DI void mm8(f32x4& d0, f32x4& d1, f32x4& d2, f32x4& d3, u32x4 a00, u32x4 b00, u32x4 a01, u32x4 b01, u32x4 a10,
                               u32x4 b10, u32x4 a11, u32x4 b11, u32x4 a20, u32x4 b20, u32x4 a21, u32x4 b21, u32x4 a30, u32x4 b30, u32x4 a31, u32x4 b31) {
#define MM8_(T, A, B) asm volatile("s_nop 1\n\t" MM_(T) " %0, %4, %5, 0\n\t" MM_(T) " %1, %8, %9, 0\n\t" MM_(T) " %0, %6, %7, %0\n\t" MM_(T) " %1, %10, %11, %1\n\t" \
               MM_(T) " %2, %12, %13, 0\n\t" MM_(T) " %3, %16, %17, 0\n\t" MM_(T) " %2, %14, %15, %2\n\t" MM_(T) " %3, %18, %19, %3\n\ts_nop %20"         \
               : "=&v"(d0), "=&v"(d1), "=&v"(d2), "=&v"(d3)                                                                                              \
               : A(a00), B(b00), A(a01), B(b01), A(a10), B(b10), A(a11), B(b11), A(a20), B(b20), A(a21), B(b21), A(a30), B(b30), A(a31), B(b31), "i"(TAIL))
  if constexpr (!BF) MM8_("f16", "v", "v");
  else if constexpr (AG == 0) MM8_("bf16", "v", "v");
  else if constexpr (AG == 1) MM8_("bf16", "a", "v");
  else MM8_("bf16", "v", "a");
}
template <bool BF> DI u32x4 twiddle(f32x4 r, f32x4 i, const f32x2 (&c)[2], const f32x2 (&s)[2]) {
  const f32x2 re0 = lo2(r) * c[0] + lo2(i) * s[0], im0 = lo2(i) * c[0] - lo2(r) * s[0];
  const f32x2 re1 = hi2(r) * c[1] + hi2(i) * s[1], im1 = hi2(i) * c[1] - hi2(r) * s[1];
  return u32x4{pkc<BF>(re0.x, im0.x), pkc<BF>(re0.y, im0.y), pkc<BF>(re1.x, im1.x), pkc<BF>(re1.y, im1.y)};
}
DI u32x4 cmul_b(f32x4 r, f32x4 i, const f32x2 (&c)[2], const f32x2 (&s)[2]) {
  const f32x2 re0 = lo2(r) * c[0] - lo2(i) * s[0], im0 = lo2(i) * c[0] + lo2(r) * s[0];
  const f32x2 re1 = hi2(r) * c[1] - hi2(i) * s[1], im1 = hi2(i) * c[1] + hi2(r) * s[1];
  return u32x4{pkb(re0.x, im0.x), pkb(re0.y, im0.y), pkb(re1.x, im1.x), pkb(re1.y, im1.y)};
}
DI float cosr(float a) { return __builtin_amdgcn_cosf(a); }
DI float sinr(float a) { return __builtin_amdgcn_sinf(a); }
template <int CTRL> DI float dppf(float x) {
  return __builtin_bit_cast(float, __builtin_amdgcn_mov_dpp(__builtin_bit_cast(int, x), CTRL, 0xf, 0xf, true));
}
DI float reduce2(float a, float b) {
  auto t = __builtin_amdgcn_permlane32_swap(__float_as_uint(a), __float_as_uint(b), false, false);
  float x = __uint_as_float(t[0]) + __uint_as_float(t[1]);
  x += dppf<0xB1>(x);
  x += dppf<0x4E>(x);
  x += dppf<0x141>(x);
  x += dppf<0x140>(x);
  auto s = __builtin_amdgcn_permlane16_swap(__float_as_uint(x), __float_as_uint(x), false, false);
  return __uint_as_float(s[0]) + __uint_as_float(s[1]);
}
DI f32x2 bcast2(float x) {
  auto t = __builtin_amdgcn_permlane32_swap(__float_as_uint(x), __float_as_uint(x), false, false);
  return f32x2{__uint_as_float(t[0]), __uint_as_float(t[1])};
}
DI void ln_finish(float mean, float q, f32x2& R, f32x2& O) {
  const float r = __builtin_amdgcn_rcpf(bfr(__builtin_amdgcn_sqrtf(bfr(bfr(q * (1.0f / 512.0f)) + 1e-5f))));
  const float o = (mean - bfr(mean)) * r;
  R = bcast2(r); O = bcast2(o);
}
DI int row_of_pos(int pos, int j) {
  const int c = 8 * (pos >> 7) + 2 * ((pos >> 3) & 3) + ((pos >> 5) & 1), q = 8 * ((pos >> 6) & 1) + (pos & 7);
  return 32 * (2 * c + (j & 1)) + 2 * q + (j >> 1);
}

template <int I> DI void ld4(u32x4& d, u32 v, const u8* s) { asm volatile("global_load_dwordx4 %0, %1, %2 offset:%3 sc1" : "=v"(d) : "v"(v), "s"(s), "i"(I) : "memory"); }
template <int I> DI void ld4a(u32x4& d, u32 v, const u8* s) { asm volatile("global_load_dwordx4 %0, %1, %2 offset:%3 sc1" : "=a"(d) : "v"(v), "s"(s), "i"(I) : "memory"); }
template <int I> DI void ld4r(u32x4& d, u32 v, const u8* s) { asm volatile("global_load_dwordx4 %0, %1, %2 offset:%3 sc1" : "+v"(d) : "v"(v), "s"(s), "i"(I) : "memory"); }
template <int I> DI void ld2(u32x2& d, u32 v, const u8* s) { asm volatile("global_load_dwordx2 %0, %1, %2 offset:%3 sc1" : "=v"(d) : "v"(v), "s"(s), "i"(I) : "memory"); }
template <int I> DI void ld2r(u32x2& d, u32 v, const u8* s) { asm volatile("global_load_dwordx2 %0, %1, %2 offset:%3 sc1" : "+v"(d) : "v"(v), "s"(s), "i"(I) : "memory"); }
template <int I> DI void ld2p(u32x2& d, u32 v, const u8* s) { asm volatile("global_load_dwordx2 %0, %1, %2 offset:%3" : "=v"(d) : "v"(v), "s"(s), "i"(I) : "memory"); }
DI void touch(u32& d, u32 v, const u8* s) { asm volatile("global_load_dword %0, %1, %2 sc1" : "+v"(d) : "v"(v), "s"(s) : "memory"); }
DI void ld1s(u32& d, u32 v, const u8* s) { asm volatile("global_load_ushort %0, %1, %2" : "=v"(d) : "v"(v), "s"(s) : "memory"); }
template <int N> DI void vmwait() { static_assert(N >= 0 && N < 64, ""); asm volatile("s_waitcnt vmcnt(%0)" ::"i"(N) : "memory"); }
DI void set_m0(u32 ldsaddr) { asm volatile("s_mov_b32 m0, %0" ::"s"(ldsaddr) : "memory"); }
template <int I> DI void dma1(u32 voff, const u8* src) {
  asm volatile("global_load_lds_dwordx4 %0, %1 offset:%2 sc1" ::"v"(voff), "s"(src), "i"(I) : "memory");
}
DI void dma4(u32 voff, const u8* src, u32 ldsaddr) {
  asm volatile("s_mov_b32 m0, %2\n\ts_nop 0\n\tglobal_load_lds_dwordx4 %0, %1 sc1\n\tglobal_load_lds_dwordx4 %0, %1 offset:1024 sc1\n\t"
               "global_load_lds_dwordx4 %0, %1 offset:2048 sc1\n\tglobal_load_lds_dwordx4 %0, %1 offset:3072 sc1" ::"v"(voff), "s"(src), "s"(ldsaddr) : "memory");
}
#define RETRY(COND, ...)                                                      \
  do {                                                                        \
    bool bad_ = (COND);                                                       \
    if (__builtin_expect(__any(bad_), 0)) {                                   \
      _Pragma("nounroll") for (int sp_ = 0; sp_ < SPIN_CAP; ++sp_) {          \
        __VA_ARGS__                                                           \
        bad_ = (COND);                                                        \
        if (!__any(bad_)) break;                                              \
      }                                                                       \
    }                                                                         \
  } while (0)
DI void bar() { asm volatile("s_waitcnt lgkmcnt(0)\n\ts_barrier" ::: "memory"); }
DI void bar_nw() { asm volatile("s_barrier" ::: "memory"); }

struct RowC { u32x4 b1re, b1im, b2re[2][2], b2im[2][2]; f32x2 twc[2][2], tws[2][2]; };
template <bool INV> DI void pair_cs(float c, float s, u32& re, u32& im) {
  if constexpr (INV) { im = pkb(s, c); re = pkb_n1(c, s, im); } else { re = pkh(c, s); im = pkh_n0(s, c, re); }
}
template <bool AG = false> DI void sign4(u32x4 base, u32 lm, u32x4& f00, u32x4& f01, u32x4& f10, u32x4& f11) {
  f00 = base;
  f01 = base ^ lm;
  f10 = base ^ u32x4{0u, 0x80008000u, 0u, 0x80008000u};
  f11 = f10 ^ lm;
  if constexpr (AG) { PINA(f00); PINA(f01); PINA(f10); PINA(f11); } else { PIN(f00); PIN(f01); PIN(f10); PIN(f11); }
}
template <bool INV, int PART> DI void row_consts(RowC& k, int m, int g) {
  const float fm = (float)m, fg = (float)(4 * g);
  if constexpr (PART == 0) {
    u32x4 br, bi;
#pragma unroll
    for (int i = 0; i < 4; ++i) {
      const float a = (fg + (float)i) * (fm * (1.0f / 16.0f));
      u32 x, y;
      pair_cs<INV>(cosr(a), sinr(a), x, y); k.b1re[i] = x; k.b1im[i] = y;
      const float a2 = (fg + (float)i) * (fm * (1.0f / 32.0f));
      pair_cs<INV>(cosr(a2), sinr(a2), x, y); br[i] = x; bi[i] = y;
    }
    PIN(k.b1re); PIN(k.b1im);
    const u32 lm = (m & 1) ? 0x80008000u : 0u;
    sign4(br, lm, k.b2re[0][0], k.b2re[0][1], k.b2re[1][0], k.b2re[1][1]);
    sign4(bi, lm, k.b2im[0][0], k.b2im[0][1], k.b2im[1][0], k.b2im[1][1]);
  } else {
#pragma unroll
    for (int e = 0; e < 2; ++e)
#pragma unroll
      for (int i = 0; i < 4; ++i) {
        const float a = (fg + (float)(16 * e + i)) * (fm * (INV ? -1.0f / 512.0f : 1.0f / 512.0f));
        k.twc[e][i >> 1][i & 1] = cosr(a); k.tws[e][i >> 1][i & 1] = sinr(a);
      }
#pragma unroll
    for (int e = 0; e < 2; ++e) { PIN(k.twc[e][0]); PIN(k.twc[e][1]); PIN(k.tws[e][0]); PIN(k.tws[e][1]); }
  }
}
template <bool INV, int TAIL> DI void row_fft(const RowC& k, u32x4 a0, u32x4 a1, f32x4& dr0, f32x4& di0, f32x4& dr1, f32x4& di1) {
  f32x4 tr0, ti0, tr1, ti1;
  mm4<INV>(tr0, ti0, tr1, ti1, a0, a1, k.b1re, k.b1im);
  const u32x4 p0 = twiddle<INV>(tr0, ti0, k.twc[0], k.tws[0]);
  SB();
  const u32x4 p1 = twiddle<INV>(tr1, ti1, k.twc[1], k.tws[1]);
  mm8<INV, TAIL>(dr0, di0, dr1, di1, p0, k.b2re[0][0], p1, k.b2re[0][1], p0, k.b2im[0][0], p1, k.b2im[0][1], p0, k.b2re[1][0], p1, k.b2re[1][1], p0,
           k.b2im[1][0], p1, k.b2im[1][1]);
}

struct ColC {
  u32x4 b1re[2], b1im[2], a2re[2][2], a2im[2][2], b3re[2][2], b3im[2][2], a4re[2][2], a4im[2][2];
  f32x2 t1c[2][2], t1s[2][2], t2c[2][2], t2s[2][2];
};
template <int PART> DI void col_consts(ColC& k, int m, int g, int w) {
  const int pi_ = w >> 1, kt = w & 1;
  const float fm = (float)m, fg = (float)(4 * g);
  const u32 lm = (m & 1) ? 0x80008000u : 0u;
  if constexpr (PART == 0) {
    const float fk = (float)(2 * (16 * kt + m) + pi_);
#pragma unroll
    for (int u = 0; u < 2; ++u) {
#pragma unroll
      for (int r = 0; r < 4; ++r) {
        const float n = fg + (float)(16 * u + r), a = n * (fk * (1.0f / 64.0f)), a2 = n * (fk * (1.0f / 2048.0f));
        u32 x, y;
        pair_cs<false>(cosr(a), sinr(a), x, y); k.b1re[u][r] = x; k.b1im[u][r] = y;
        k.t1c[u][r >> 1][r & 1] = cosr(a2); k.t1s[u][r >> 1][r & 1] = sinr(a2);
      }
      PIN(k.b1re[u]); PIN(k.b1im[u]); PIN(k.t1c[u][0]); PIN(k.t1c[u][1]); PIN(k.t1s[u][0]); PIN(k.t1s[u][1]);
    }
  } else if constexpr (PART == 1) {
    u32x4 fr, fi, ir, ii;
#pragma unroll
    for (int r = 0; r < 4; ++r) {
      const float a = (fg + (float)r) * (fm * (1.0f / 32.0f)), c = cosr(a), s = sinr(a);
      u32 x, y;
      pair_cs<false>(c, s, x, y); fr[r] = x; fi[r] = y;
      pair_cs<true>(c, s, x, y); ir[r] = x; ii[r] = y;
    }
    sign4(fr, lm, k.a2re[0][0], k.a2re[0][1], k.a2re[1][0], k.a2re[1][1]);
    sign4(fi, lm, k.a2im[0][0], k.a2im[0][1], k.a2im[1][0], k.a2im[1][1]);
    sign4<true>(ir, lm, k.b3re[0][0], k.b3re[0][1], k.b3re[1][0], k.b3re[1][1]);
    sign4<true>(ii, lm, k.b3im[0][0], k.b3im[0][1], k.b3im[1][0], k.b3im[1][1]);
  } else if constexpr (PART == 2) {
    const float kb = (float)(32 * kt + pi_) + 2.0f * fg;
#pragma unroll
    for (int u = 0; u < 2; ++u) {
#pragma unroll
      for (int r = 0; r < 4; ++r) {
        const float a = (kb + (float)(2 * r)) * ((fm + (float)(16 * u)) * (-1.0f / 2048.0f));
        k.t2c[u][r >> 1][r & 1] = cosr(a); k.t2s[u][r >> 1][r & 1] = sinr(a);
      }
      PIN(k.t2c[u][0]); PIN(k.t2c[u][1]); PIN(k.t2s[u][0]); PIN(k.t2s[u][1]);
    }
  } else {
    const float fn = (fm + (float)(16 * (w >> 1))) * (1.0f / 64.0f);
#pragma unroll
    for (int u = 0; u < 2; ++u) {
#pragma unroll
      for (int r = 0; r < 4; ++r) {
        const float a = (2.0f * fg + (float)(2 * r + u)) * fn;
        u32 x, y;
        pair_cs<true>(cosr(a), sinr(a), x, y); k.a4re[u][0][r] = x; k.a4im[u][0][r] = y;
      }
      k.a4re[u][1] = k.a4re[u][0] ^ lm; k.a4im[u][1] = k.a4im[u][0] ^ lm;
      PINA(k.a4re[u][0]); PINA(k.a4re[u][1]); PINA(k.a4im[u][0]); PINA(k.a4im[u][1]);
    }
  }
}

DI void transpose8x4(const u32x2 (&v)[8], u32x4 (&o)[4]) {
#pragma unroll
  for (int jj = 0; jj < 4; ++jj) {
    o[0][jj] = __builtin_amdgcn_perm(v[2 * jj + 1].x, v[2 * jj].x, 0x05040100u);
    o[1][jj] = __builtin_amdgcn_perm(v[2 * jj + 1].x, v[2 * jj].x, 0x07060302u);
    o[2][jj] = __builtin_amdgcn_perm(v[2 * jj + 1].y, v[2 * jj].y, 0x05040100u);
    o[3][jj] = __builtin_amdgcn_perm(v[2 * jj + 1].y, v[2 * jj].y, 0x07060302u);
  }
}
DI void prep_load(const Params& P, int wt, int lane, u32x2 (&v)[8]) {
  const u32 vo = lane * 8;
  if (wt < 896) {
    const int rest = wt / 7, piece = rest & 63;
    const u8* src = (const u8*)((rest >> 6) ? P.wu : P.wg) + (wt % 7) * 512 + (16 * (piece >> 2) + 4 * (piece & 3)) * 3584;
    const u32 vo2 = vo + 7168;
    static_for<8>([&](auto J) { constexpr int j = decltype(J)::value; ld2p<(j & 1) * 3584>(v[j], j & 2 ? vo2 : vo, src + (j >> 2) * (256 * 3584)); });
  } else {
    const int piece = (wt - 896) >> 1, g = piece & 3, kq = piece >> 2, wv = kq / 14, ks = kq % 14;
    int f0, f1;
    if (ks < 4) { f0 = 112 * (4 * wv + ks); f1 = f0 + 16; }
    else if (ks < 6) { f0 = 112 * (4 * wv + 2 * (ks - 4)) + 32; f1 = f0 + 112; }
    else if (ks < 10) { f0 = 112 * (4 * wv + ks - 6) + 48; f1 = f0 + 16; }
    else { f0 = 112 * (4 * wv + ks - 10) + 80; f1 = f0 + 16; }
    const u8* s0 = (const u8*)P.wd + (wt & 1) * 512 + (4 * g + f0) * 1024;
    const u8* s1 = (const u8*)P.wd + (wt & 1) * 512 + (4 * g + f1) * 1024;
    static_for<8>([&](auto J) { constexpr int j = decltype(J)::value; ld2p<(j & 3) * 1024>(v[j], vo, j < 4 ? s0 : s1); });
  }
}
DI u32 prep_dst(int wt, int tile) {
  if (wt < 896) {
    const int rest = wt / 7, piece = rest & 63, mat = rest >> 6, tl = (wt % 7) * 16 + tile;
    const int nt = tl / 7, p = tl % 7, ks = piece >> 2;
    const u32 frag = (p < 3 ? ks * 6 + 2 * p : p < 5 ? 96 + ks * 4 + 2 * (p - 3) : 160 + ks * 4 + 2 * (p - 5)) + mat;
    return OFF_W1 + (nt * 224 + frag) * 1024 + (piece & 3) * 256;
  } else {
    const int piece = (wt - 896) >> 1, tl = (wt & 1) * 16 + tile;
    return OFF_W2 + (((tl >> 2) * 56 + (piece >> 2)) * 4 + (tl & 3)) * 1024 + (piece & 3) * 256;
  }
}

constexpr int NTOK0 = 16, NRES = 40, NEARLY = 37;
constexpr int g1_job(int L, int x) {
  if (((x >= 34 && x < 96) && (x & 3) == 2) || (x >= 98 && x < 130 && (x & 3) == 0)) return 2;
  if ((x < 42 && x % 3 == 0) || x == 41 || x == 44) return 1;
  if (x >= 150) return (x & 1) ? 0 : 4;
  if (x >= 132 && x < 148 && (x & 3) == 0) return 5;
  if (L == 0 && ((x < 34 && x % 6 == 1) || (x >= 48 && x < 98 && (x & 3) == 0) || (x >= 98 && x < 150 && (x & 3) == 2) || (x >= 49 && x < 150 && x % 12 == 1)))
    return 3;
  return 0;
}
constexpr int NEPI1 = 124, NEPI2 = 82;
struct G1Sched {
  int job[224], idx[224], epi[225], nst[224], cum[225], pos[7][64], cnt[7];
  constexpr G1Sched(int L) : job{}, idx{}, epi{}, nst{}, cum{}, pos{}, cnt{} {
    cnt[1] = NTOK0;
    for (int i = 0; i < NTOK0; ++i) pos[1][i] = i;
    cum[0] = NTOK0;
    for (int x = 0; x < 224; ++x) {
      int j = g1_job(L, x);
      if ((j == 3 && cnt[3] >= NRES) || (j == 4 && cnt[4] >= NEARLY)) j = 0;
      job[x] = j; idx[x] = cnt[j];
      if (x == 161) epi[x] = NEPI1;
      const bool on = (x >= 97 && x < 160 && epi[x] < NEPI1) || x >= 161;
      epi[x + 1] = on ? epi[x] + (j ? 2 : 3) : epi[x];
      if (x < 160 && epi[x + 1] > NEPI1) epi[x + 1] = NEPI1;
      for (int n = epi[x]; n < epi[x + 1]; ++n)
        nst[x] += n < NEPI1 ? (n == 40 || n == 81 || n == 102 || n == 123) : (n < NEPI1 + NEPI2 && (n - NEPI1) % 41 == 40);
      if (j) pos[j][cnt[j]++] = cum[x] + nst[x];
      cum[x + 1] = cum[x] + nst[x] + (j ? 1 : 0);
    }
  }
};
constexpr G1Sched G1S[2] = {G1Sched(0), G1Sched(1)};
static_assert(G1S[0].cnt[1] == 32 && G1S[0].cnt[2] == 24 && G1S[0].cnt[3] == NRES && G1S[0].cnt[4] == NEARLY && G1S[0].cnt[5] == 4, "");
static_assert(G1S[0].idx[66] == 8 && G1S[0].idx[100] == 16, "");
static_assert(G1S[0].epi[152] == NEPI1 && G1S[0].epi[204] >= NEPI1 + NEPI2, "");
constexpr int clamp63(int n) { return n > 63 ? 63 : n; }
constexpr int g1_wait(int L, int x, int job, int idx) { return clamp63(G1S[L].cum[x] - G1S[L].pos[job][idx] - 1); }

#define FRESH()                                                                                                   \
  int tid = threadIdx.x, b = blockIdx.x, w = w_;                                                                   \
  u8* ws = P.ws;                                                                                                   \
  asm volatile("" : "+v"(tid), "+s"(b), "+s"(w), "+s"(ws));                                                        \
  const int lane = tid & 63, m = lane & 15, g = lane >> 4, half2 = b & 1, ct = b >> 5;                             \
  const int G = ((b >> 1) & 3) * 4 + ((b >> 3) & 3), member = half2 * 8 + ct, pos = G * 16 + member;               \
  const u32 l16 = lane * 16, t16 = tid * 16, xo = 32 * m + 8 * g;                                                  \
  auto fill_h = [&](u32 base, int q) {                                                                             \
    const int i = q * 64 + lane;                                                                                   \
    st4(sent4, base + (8 * G + 2 * w + (i >= 224)) * 57344 + member * 3584 + (i >= 224 ? i - 224 : i) * 16);       \
  };                                                                                                               \
  auto fill_rows = [&](u32 base) { st4(sent4, base + pos * 8192 + t16); st4(sent4, base + pos * 8192 + 4096 + t16); }; \
  auto ring4 = [&](int q) {                                   \
    const u32 o = (q >> 1) * 32768 + w * 8192 + (q & 1) * 4096;                                                    \
    dma4(l16, ws + OFF_W1 + member * 229376 + o, ldsbase + o);                                                     \
  };                                                                                                               \
  (void)m; (void)g; (void)half2; (void)ct; (void)l16; (void)t16; (void)xo; (void)fill_h; (void)fill_rows; (void)ring4

__global__ __launch_bounds__(256, 1) void k_all(Params P) {
  extern __shared__ __attribute__((aligned(16))) u8 lds[];
  ST_DECL;
  const int w_ = __builtin_amdgcn_readfirstlane(threadIdx.x >> 6);
  const __amdgpu_buffer_rsrc_t rs = __builtin_amdgcn_make_buffer_rsrc(P.ws, 0, WS_USED, 0x00020000);
  const u32 ldsbase = (u32)(size_t)(__attribute__((address_space(3))) u8*)lds;
  const u32x4 sent4 = {SENT, SENT, SENT, SENT};
  auto st4 = [&](u32x4 v, u32 off) { __builtin_amdgcn_raw_buffer_store_b128(v, rs, off, 0, 16); };
  auto st2 = [&](u32x2 v, u32 off) { __builtin_amdgcn_raw_buffer_store_b64(v, rs, off, 0, 16); };
  u32x2 xl[2], xh[2], pv0[8], pv1[8];
  u32 wfr[8], wfi[8], wn[8];
  f32x2 xv[8];
  u32x2 ep;
  u32 cpf = 0;
  RowC rc;
  ColC cc;
  ST(0);
  {
    FRESH();
    const u8* xp = (const u8*)P.x + row_of_pos(pos, w) * 1024;
    ld2p<0>(xl[0], xo, xp); ld2p<512>(xl[1], xo, xp); ld2p<0>(xh[0], xo, xp + 1024 * 1024); ld2p<512>(xh[1], xo, xp + 1024 * 1024);
    { const u32 z = 0; ld2<0>(ep, z, ws); }
    {
      const u8* pc;
      asm volatile("s_getpc_b64 %0" : "=s"(pc));
      const u32 o = (((b >> 3) * 4 + w) * 8 + (lane & 7)) * 128;
      touch(cpf, o < CODE_BYTES ? o : 0, pc);
    }
    XS(32);
    fill_rows(OFF_A); fill_rows(OFF_WX);
    SB();
    row_consts<false, 0>(rc, m, g);
    SB();
    XS(33);
    fill_rows(OFF_U);
    if (tid < 8) st4(sent4, OFF_WNY + tid * 4096 + pos * 16);
    SB();
    row_consts<false, 1>(rc, m, g);
    SB();
    XS(34);
    const int kb = 2 * (16 * ((pos >> 6) & 1) + (pos & 15)) + (pos >> 7) + 256 * ((pos >> 4) & 3);
    static_for<8>([&](auto J) {
      constexpr int j = decltype(J)::value;
      const u32 o = tid * 2 + (kb + 64 * (16 * (j >> 2) + (j & 3))) * 514;
      ld1s(wfr[j], o, (const u8*)P.wr); ld1s(wfi[j], o, (const u8*)P.wi);
    });
    {
      const int k = kb + 64 * (16 * ((lane >> 2) & 1) + (lane & 3)), km = (2048 - k) & 2047;
      const u32 o = k * 514, om = km * 514;
      ld1s(wn[0], o, (const u8*)P.wr); ld1s(wn[1], o, (const u8*)P.wi); ld1s(wn[2], om, (const u8*)P.wr); ld1s(wn[3], om, (const u8*)P.wi);
      ld1s(wn[4], o, (const u8*)P.wr + 512); ld1s(wn[5], o, (const u8*)P.wi + 512); ld1s(wn[6], om, (const u8*)P.wr + 512); ld1s(wn[7], om, (const u8*)P.wi + 512);
    }
    XS(35);
    vmwait<24>();
    XS(36);
    bar_nw();
    PIN(cpf); PIN(ep); PIN(xl[0]); PIN(xl[1]); PIN(xh[0]); PIN(xh[1]);
    { const u64 e = (((u64)(u32)__builtin_amdgcn_readfirstlane(ep.y) << 32) | (u32)__builtin_amdgcn_readfirstlane(ep.x)) + 1; ep = u32x2{(u32)e, (u32)(e >> 32)}; }
    if (tid == 0) st2(ep, OFF_FLAG + pos * 8);
    XS(37);
    prep_load(P, b * 4 + w, lane, pv0);
    SB();
#pragma unroll
    for (int t = 0; t < 2; ++t)
#pragma unroll
      for (int p = 0; p < 2; ++p) {
        xv[4 * t + 2 * p] = f32x2{blo(xl[t][p]), blo(xh[t][p])};
        xv[4 * t + 2 * p + 1] = f32x2{bhi(xl[t][p]), bhi(xh[t][p])};
      }
  }
  ST(1);

  u32x4 w2a[10][4];

  auto layer = [&](auto LC) {
    constexpr int L = decltype(LC)::value;
    constexpr int S0 = 2 + 16 * L;
    {
      FRESH();
      const f32x2 s2 = ((xv[0] + xv[1]) + (xv[2] + xv[3])) + ((xv[4] + xv[5]) + (xv[6] + xv[7]));
      const float mean = reduce2(s2.x, s2.y) * (1.0f / 512.0f);
      const f32x2 M = bcast2(mean);
      f32x2 d[8];
#pragma unroll
      for (int i = 0; i < 8; ++i) d[i] = xv[i] - M;
      f32x2 q0 = d[0] * d[0], q1 = d[1] * d[1];
#pragma unroll
      for (int i = 2; i < 8; i += 2) { q0 += d[i] * d[i]; q1 += d[i + 1] * d[i + 1]; }
      q0 += q1;
      f32x2 R, O;
      ln_finish(mean, reduce2(q0.x, q0.y), R, O);
      float* img = (float*)(lds + LDS_SCR);
      u32* inp = (u32*)(img + w * 1280);
#pragma unroll
      for (int i = 0; i < 8; ++i) {
        const f32x2 v = d[i] * R + O;
        inp[(16 * (m & 1) + 4 * g + (i & 3)) * 20 + (m >> 1) + 8 * (i >> 2)] = pkh(v.x, v.y);
      }
      const u32x4 a0 = *(const u32x4*)(inp + m * 20 + 4 * g), a1 = *(const u32x4*)(inp + (16 + m) * 20 + 4 * g);
      if constexpr (L == 0) { if (b < 80) prep_load(P, b * 4 + w + 1024, lane, pv1); }
      const int ph = tid >> 1, fq = ph ? 512 - ph : 256, fq1 = 384 - ph;
      u32 ap0 = (ph + (ph >> 4) * 4) * 4, ap1 = ap0 + 640, ar0 = (fq + (fq >> 4) * 4) * 4, ar1 = (fq1 + (fq1 >> 4) * 4) * 4;
      PIN(ap0); PIN(ap1); PIN(ar0); PIN(ar1);
      const float* p0 = (const float*)((const u8*)img + ap0);
      const float* p1 = (const float*)((const u8*)img + ap1);
      const float* r0 = (const float*)((const u8*)img + ar0);
      const float* r1 = (const float*)((const u8*)img + ar1);
      const float sc = 1.0f / 64.0f, sg = (tid & 1) ? -sc : sc;
      f32x2 cA = {sc, sc}, cB = {sg, -sg}, cC = {sc, -sc}, cD = {sg, sg};
      f32x2 eA = ph ? cA : f32x2{2 * sc, 0.f}, eB = ph ? cB : f32x2{2 * sg, 0.f}, eC = ph ? cC : f32x2{0.f, 2 * sg}, eD = ph ? cD : f32x2{0.f, 2 * sc};
      u32 zo = OFF_A + pos * 8192 + t16;
      PIN(cB); PIN(cD); PIN(eA); PIN(eB); PIN(eC); PIN(eD); PIN(zo);
      f32x4 dr0, di0, dr1, di1;
      row_fft<false, 5>(rc, a0, a1, dr0, di0, dr1, di1);
      float* op = img + w * 1280 + 4 * g + 20 * m;
      *(f32x4*)(op) = dr0; *(f32x4*)(op + 640) = di0;
      SB();
      *(f32x4*)(op + 320) = dr1; *(f32x4*)(op + 960) = di1;
      bar();
      f32x2 pa[4], qa[4], pb[4], qb[4];
#pragma unroll
      for (int j = 0; j < 4; ++j) {
        pa[j] = f32x2{p0[j * 1280], p0[j * 1280 + 640]}; qa[j] = f32x2{r0[j * 1280], r0[j * 1280 + 640]};
      }
#pragma unroll
      for (int j = 0; j < 4; ++j) {
        pb[j] = f32x2{p1[j * 1280], p1[j * 1280 + 640]}; qb[j] = f32x2{r1[j * 1280], r1[j * 1280 + 640]};
      }
      SB();
      u32x4 o;
#pragma unroll
      for (int j = 0; j < 4; ++j) {
        const f32x2 v = eD * swp(qa[j]) + (eC * qa[j] + (eB * swp(pa[j]) + eA * pa[j]));
        o[j] = mark<(L == 0 ? 0x10001u : 0u), (L == 0 ? 0u : 0x10000u)>(pkh(v.x, v.y));
      }
      st4(o, zo);
#pragma unroll
      for (int j = 0; j < 4; ++j) {
        const f32x2 v = cD * swp(qb[j]) + (cC * qb[j] + (cB * swp(pb[j]) + cA * pb[j]));
        o[j] = mark<(L == 0 ? 0x10001u : 0u), (L == 0 ? 0u : 0x10000u)>(pkh(v.x, v.y));
      }
      st4(o, zo + 4096);
      SB();
      if constexpr (L == 1) {
#pragma unroll
        for (int t = 0; t < 2; ++t)
#pragma unroll
          for (int p = 0; p < 2; ++p) {
            xl[t][p] = pkb(xv[4 * t + 2 * p].x, xv[4 * t + 2 * p + 1].x); xh[t][p] = pkb(xv[4 * t + 2 * p].y, xv[4 * t + 2 * p + 1].y);
          }
        PIN(xl[0]); PIN(xl[1]); PIN(xh[0]); PIN(xh[1]);
      }
    }
    if constexpr (L == 0) {
      FRESH();
      vmwait<8 + 2>();
#pragma unroll
      for (int j = 0; j < 8; ++j) { PIN(wfr[j]); PIN(wfi[j]); PIN(wn[j]); }
      u8* bw = lds + w * 2048;
      u32x4 v0, v1;
#pragma unroll
      for (int j = 0; j < 4; ++j) { v0[j] = wfr[j] | (wfi[j] << 16); v1[j] = wfr[4 + j] | (wfi[4 + j] << 16); }
      *(u32x4*)(bw + lane * 32) = v0; *(u32x4*)(bw + lane * 32 + 16) = v1;
      st4(*(const u32x4*)(bw + l16), OFF_WX + pos * 8192 + w * 2048 + l16);
      st4(*(const u32x4*)(bw + 1024 + l16), OFF_WX + pos * 8192 + w * 2048 + 1024 + l16);
      const float hr = blo(wn[0]) + blo(wn[2]), hi = blo(wn[1]) - blo(wn[3]), nr = blo(wn[4]) + blo(wn[6]), ni = blo(wn[5]) - blo(wn[7]);
      const f32x4 ab = {0.25f * (hr + nr), 0.25f * (hi + ni), 0.25f * (hr - nr), 0.25f * (hi - ni)};
      if (tid < 8) st4(__builtin_bit_cast(u32x4, ab), OFF_WNY + tid * 4096 + pos * 16);
    }
    ST(S0);
    {
      FRESH();
      const int phi = pos, pi_ = w >> 1, kt = w & 1;
      const int k1l = 2 * (16 * kt + m) + pi_;
      auto consts = [&](auto PC) { col_consts<decltype(PC)::value>(cc, m, g, w); SB(); };
      using I0 = std::integral_constant<int, 0>; using I1 = std::integral_constant<int, 1>;
      using I2 = std::integral_constant<int, 2>; using I3 = std::integral_constant<int, 3>;
      u8* const bw = lds + 16384 + w * 8192;
      const u32 zo = (tid >> 1) * 8192 + (tid & 1) * 16;
      const u8* zb0 = ws + OFF_A + phi * 32;
      const u8* zb1 = zb0 + 128 * 8192;
      const u8* wb0 = ws + OFF_WX + phi * 32;
      const u8* wb1 = wb0 + 128 * 8192;
      const u32 wo = zo;
      PINS(zb0); PINS(zb1); PINS(wb0); PINS(wb1);
      if constexpr (L == 0) {
        u32x4 fl0, fl1;
        const u32 o = lane * 32;
        const u8* fb = ws + OFF_FLAG;
        ld4<0>(fl0, o, fb); ld4<16>(fl1, o, fb);
        SB();
        consts(I0{});
        consts(I1{});
        vmwait<0>(); PIN(fl0); PIN(fl1);
        RETRY(!(fl0.x == ep.x && fl0.y == ep.y && fl0.z == ep.x && fl0.w == ep.y && fl1.x == ep.x && fl1.y == ep.y && fl1.z == ep.x && fl1.w == ep.y), {
          CNT(0);
          ld4r<0>(fl0, o, fb); ld4r<16>(fl1, o, fb);
          vmwait<0>(); PIN(fl0); PIN(fl1);
        });
      } else {
        SB();
        consts(I0{});
        consts(I1{});
      }
      u32x4 z0, z1, wv[2];
      ld4<0>(z0, zo, zb0); ld4<0>(z1, zo, zb1);
      ld4<0>(wv[0], wo, wb0); ld4<0>(wv[1], wo, wb1);
      SB();
      u32x4 wa[8];
      if (phi == 0) static_for<8>([&](auto J) { constexpr int j = decltype(J)::value; ld4<0>(wa[j], t16, ws + OFF_WNY + j * 4096); });
      if constexpr (L == 0) {
        auto put = [&](int rep) {
#pragma unroll
          for (int x = 0; x < 4; ++x) {
            const int src = x * 16 + (lane >> 2);
            st4(*(const u32x4*)(bw + rep * 4096 + src * 64 + (((lane & 3) + src) & 3) * 16), prep_dst(b * 4 + w + 1024 * rep, 4 * x + g) + m * 16);
          }
        };
        u32x4 ot[4];
#pragma unroll
        for (int j = 0; j < 8; ++j) PIN(pv0[j]);
        transpose8x4(pv0, ot);
#pragma unroll
        for (int x = 0; x < 4; ++x) *(u32x4*)(bw + lane * 64 + ((x + lane) & 3) * 16) = ot[x];
        put(0);
        SB();
        consts(I2{});
        if (b < 80) {
#pragma unroll
          for (int j = 0; j < 8; ++j) PIN(pv1[j]);
          transpose8x4(pv1, ot);
#pragma unroll
          for (int x = 0; x < 4; ++x) *(u32x4*)(bw + 4096 + lane * 64 + ((x + lane) & 3) * 16) = ot[x];
          put(1);
        }
        SB();
        consts(I3{});
      } else {
        consts(I2{});
        ring4(2);
        SB();
        consts(I3{});
      }
      u8* const zi = lds + LDS_SCR;
      u32 zw;
      {
        const int pp = tid >> 1;
        zw = (tid & 1) * 4096 + ((pp >> 6) & 1) * 2048 + (((pp >> 3) & 3) * 16 + 2 * (pp & 7)) * 16 + ((pp >> 5) & 1) * 8;
        PIN(zw);
      }
      u32 uo = OFF_U + phi * 8192 + w * 2048 + l16;
      PIN(uo);
      SB();
      bar_nw();
      ST(S0 + 1);
      vmwait<4>();
      PIN(z0); PIN(z1); PIN(wv[0]); PIN(wv[1]);
      if constexpr (L == 0) {
        RETRY((stale<false, 1u>(red8<false>(z0, z1)) || max4(max4(0, wv[0]), wv[1]) == SENT), {
          CNT(1);
          if (bad_) { ld4r<0>(z0, zo, zb0); ld4r<0>(z1, zo, zb1); ld4r<0>(wv[0], wo, wb0); ld4r<0>(wv[1], wo, wb1); }
          vmwait<0>(); PIN(z0); PIN(z1); PIN(wv[0]); PIN(wv[1]);
        });
      } else {
        RETRY((stale<true, 0x10000u>(red8<true>(z0, z1))), {
          CNT(2);
          if (bad_) { ld4r<0>(z0, zo, zb0); ld4r<0>(z1, zo, zb1); }
          vmwait<0>(); PIN(z0); PIN(z1);
        });
      }
      ST(S0 + 2);
      *(u32x2*)(zi + zw) = u32x2{z0.x, z0.y}; *(u32x2*)(zi + zw + 16) = u32x2{z0.z, z0.w};
      *(u32x2*)(zi + zw + 1024) = u32x2{z1.x, z1.y}; *(u32x2*)(zi + zw + 1024 + 16) = u32x2{z1.z, z1.w};
      *(u32x4*)(zi + 8192 + t16) = wv[0]; *(u32x4*)(zi + 12288 + t16) = wv[1];
      bar();
      const u8* zr = zi + pi_ * 4096 + l16;
      const u32x4 f00 = *(const u32x4*)(zr), f01 = *(const u32x4*)(zr + 1024), f10 = *(const u32x4*)(zr + 2048), f11 = *(const u32x4*)(zr + 3072);
      wv[0] = *(const u32x4*)(zi + 8192 + tid * 32); wv[1] = *(const u32x4*)(zi + 8192 + tid * 32 + 16);
      if constexpr (L == 0) { fill_h(OFF_H, 0); fill_h(OFF_H, 1); fill_h(OFF_H, 2); fill_h(OFF_H, 3); } else ring4(3);
      f32x4 d0, d1, d2, d3;
      mm8<false, 3>(d0, d1, d2, d3, f00, cc.b1re[0], f01, cc.b1re[1], f00, cc.b1im[0], f01, cc.b1im[1], f10, cc.b1re[0], f11, cc.b1re[1], f10, cc.b1im[0], f11, cc.b1im[1]);
      const u32x4 b20 = twiddle<false>(d0, d1, cc.t1c[0], cc.t1s[0]);
      SB();
      const u32x4 b21 = twiddle<false>(d2, d3, cc.t1c[1], cc.t1s[1]);
      f32x2 wr[2][2], wi[2][2];
      auto unpack_w = [&]() {
#pragma unroll
        for (int t = 0; t < 2; ++t)
#pragma unroll
          for (int h = 0; h < 2; ++h) {
            wr[t][h] = f32x2{blo(wv[t][2 * h]), blo(wv[t][2 * h + 1])}; wi[t][h] = f32x2{bhi(wv[t][2 * h]), bhi(wv[t][2 * h + 1])};
            PIN(wr[t][h]); PIN(wi[t][h]);
          }
      };
      mm8<false, 0>(d0, d1, d2, d3, cc.a2re[0][0], b20, cc.a2re[0][1], b21, cc.a2im[0][0], b20, cc.a2im[0][1], b21, cc.a2re[1][0], b20, cc.a2re[1][1], b21, cc.a2im[1][0],
                    b20, cc.a2im[1][1], b21);
      SB();
      unpack_w();
      SB();
      u32x4 a30, a31;
      if (phi != 0) {
        a30 = cmul_b(d0, d1, wr[0], wi[0]);
        a31 = cmul_b(d2, d3, wr[1], wi[1]);
      } else {
        u32 mx = 0;
#pragma unroll
        for (int j = 0; j < 8; ++j) { PIN(wa[j]); mx = max4(mx, wa[j]); }
        _Pragma("nounroll") for (int sp = 0; sp < SPIN_CAP && __any(mx == SENT); ++sp) {
          static_for<8>([&](auto J) { constexpr int j = decltype(J)::value; ld4r<0>(wa[j], t16, ws + OFF_WNY + j * 4096); });
          vmwait<0>();
          mx = 0;
#pragma unroll
          for (int j = 0; j < 8; ++j) { PIN(wa[j]); mx = max4(mx, wa[j]); }
        }
        float* pl = (float*)zi;
        bar_nw();
        float* pw = pl + k1l * 36 + 4 * g;
        *(f32x4*)(pw) = d0; *(f32x4*)(pw + 16) = d2; *(f32x4*)(pw + 2340) = d1; *(f32x4*)(pw + 2340 + 16) = d3;
        if (k1l == 0) {
#pragma unroll
          for (int r = 0; r < 4; ++r) {
            pl[64 * 36 + ((4 * g + r - 1) & 31)] = d0[r]; pl[64 * 36 + ((16 + 4 * g + r - 1) & 31)] = d2[r];
            pl[2340 + 64 * 36 + ((4 * g + r - 1) & 31)] = d1[r]; pl[2340 + 64 * 36 + ((16 + 4 * g + r - 1) & 31)] = d3[r];
          }
        }
        bar();
        const float* pr_ = pl + (64 - k1l) * 36 + 28 - 4 * g;
        const f32x4 qr0 = *(const f32x4*)(pr_), qr1 = *(const f32x4*)(pr_ - 16), qi0 = *(const f32x4*)(pr_ + 2340), qi1 = *(const f32x4*)(pr_ + 2340 - 16);
#pragma unroll
        for (int r = 0; r < 4; ++r) {
          const f32x4 c0 = __builtin_bit_cast(f32x4, wa[r]), c1 = __builtin_bit_cast(f32x4, wa[4 + r]);
          a30[r] = pkb(d0[r] * c0[0] - d1[r] * c0[1] + qr0[3 - r] * c0[2] + qi0[3 - r] * c0[3],
                       d0[r] * c0[1] + d1[r] * c0[0] + qr0[3 - r] * c0[3] - qi0[3 - r] * c0[2]);
          a31[r] = pkb(d2[r] * c1[0] - d3[r] * c1[1] + qr1[3 - r] * c1[2] + qi1[3 - r] * c1[3],
                       d2[r] * c1[1] + d3[r] * c1[0] + qr1[3 - r] * c1[3] - qi1[3 - r] * c1[2]);
        }
      }
      u8* ex = lds + LDS_SCR + 20480;
      mm8<true, 3, 2>(d0, d1, d2, d3, a30, cc.b3re[0][0], a31, cc.b3re[0][1], a30, cc.b3im[0][0], a31, cc.b3im[0][1], a30, cc.b3re[1][0], a31, cc.b3re[1][1], a30, cc.b3im[1][0],
                   a31, cc.b3im[1][1]);
      *(u32x4*)(ex + w * 1024 + l16) = twiddle<true>(d0, d1, cc.t2c[0], cc.t2s[0]);
      SB();
      *(u32x4*)(ex + (4 + w) * 1024 + l16) = twiddle<true>(d2, d3, cc.t2c[1], cc.t2s[1]);
      if constexpr (L == 0) vmwait<4>();
      bar();
      const u8* er = ex + (w & 1) * 4096 + l16;
      const u32x4 g0 = *(const u32x4*)(er), g1 = *(const u32x4*)(er + 1024), g2 = *(const u32x4*)(er + 2048), g3 = *(const u32x4*)(er + 3072);
      mm8<true, 7, 1>(d0, d1, d2, d3, cc.a4re[0][0], g0, cc.a4re[0][1], g1, cc.a4im[0][0], g0, cc.a4im[0][1], g1, cc.a4re[1][0], g2, cc.a4re[1][1], g3, cc.a4im[1][0], g2,
                   cc.a4im[1][1], g3);
      {
        const f32x2 lr0 = lo2(d0) + lo2(d2), li0 = lo2(d1) + lo2(d3), hr0 = lo2(d0) - lo2(d2), hi0 = lo2(d1) - lo2(d3);
        const f32x2 lr1 = hi2(d0) + hi2(d2), li1 = hi2(d1) + hi2(d3), hr1 = hi2(d0) - hi2(d2), hi1 = hi2(d1) - hi2(d3);
        if (phi != 0) {
          const f32x2 pr0 = lr0 - hi0, pi0 = li0 + hr0, qr0 = lr0 + hi0, qi0 = hr0 - li0;
          st4(mark4<1u - L, L>(u32x4{pkb(pr0.x, pi0.x), pkb(qr0.x, qi0.x), pkb(pr0.y, pi0.y), pkb(qr0.y, qi0.y)}), uo);
          const f32x2 pr1 = lr1 - hi1, pi1 = li1 + hr1, qr1 = lr1 + hi1, qi1 = hr1 - li1;
          st4(mark4<1u - L, L>(u32x4{pkb(pr1.x, pi1.x), pkb(qr1.x, qi1.x), pkb(pr1.y, pi1.y), pkb(qr1.y, qi1.y)}), uo + 1024);
          MEMFENCE();
        } else {
          st4(mark4<1u - L, L>(u32x4{pkb(lr0.x, hr0.x), pkb(li0.x, hi0.x), pkb(lr0.y, hr0.y), pkb(li0.y, hi0.y)}), uo);
          st4(mark4<1u - L, L>(u32x4{pkb(lr1.x, hr1.x), pkb(li1.x, hi1.x), pkb(lr1.y, hr1.y), pkb(li1.y, hi1.y)}), uo + 1024);
        }
      }
    }
    ST(S0 + 3);
    {
      FRESH();
      SB();
      row_consts<true, 0>(rc, m, g);
      f32x2 xa[4], xb[4];
#pragma unroll
      for (int i = 0; i < 4; ++i) {
        xa[i] = f32x2{blo(xl[i >> 1][i & 1]), bhi(xl[i >> 1][i & 1])}; xb[i] = f32x2{blo(xh[i >> 1][i & 1]), bhi(xh[i >> 1][i & 1])};
        PIN(xa[i]); PIN(xb[i]);
      }
      SB();
      u32x4 c0, c1;
      const u32 uo = (tid >> 1) * 8192 + (tid & 1) * 16;
      const u8* ub0 = ws + OFF_U + pos * 32;
      const u8* ub1 = ub0 + 128 * 8192;
      ld4<0>(c0, uo, ub0); ld4<0>(c1, uo, ub1);
      if constexpr (L == 0) { fill_h(OFF_H, 4); fill_h(OFF_H, 5); fill_h(OFF_H, 6); }
      SB();
      row_consts<true, 1>(rc, m, g);
      SB();
      if constexpr (L == 1) ring4(4);
      u32* img = (u32*)(lds + LDS_SCR);
      u32 ip0, iq0, iq1, xno, tw0, tw1, trd;
      {
        const int ph = tid >> 1, fq0 = ph ? 512 - ph : 256, fq1 = 384 - ph;
        ip0 = ((tid & 1) * 1280 + (ph & 31) * 20 + (ph >> 5)) * 4;
        iq0 = ((tid & 1) * 1280 + (fq0 & 31) * 20 + (fq0 >> 5)) * 4; iq1 = ((tid & 1) * 1280 + (fq1 & 31) * 20 + (fq1 >> 5)) * 4;
        xno = OFF_A + (pos >> 1) * 16384 + (tid >> 3) * 256 + (pos & 1) * 128 + (tid & 7) * 16;
        tw0 = (4 * m + g) * 128 + ((2 * w) ^ (m & 7)) * 16; tw1 = (4 * m + g) * 128 + ((2 * w + 1) ^ (m & 7)) * 16;
        trd = (tid >> 3) * 128 + ((tid & 7) ^ ((tid >> 5) & 7)) * 16;
        PIN(ip0); PIN(iq0); PIN(iq1); PIN(xno); PIN(tw0); PIN(tw1); PIN(trd);
      }
      SB();
      ST(S0 + 4);
      vmwait<(L == 0 ? 3 : 4)>(); PIN(c0); PIN(c1);
      {
        RETRY((stale<L == 1, 1u>(red8<L == 1>(c0, c1))), {
          CNT(3);
          if (bad_) { ld4r<0>(c0, uo, ub0); ld4r<0>(c1, uo, ub1); }
          vmwait<0>(); PIN(c0); PIN(c1);
        });
      }
      ST(S0 + 5);
      {
        u8* d = (u8*)img;
        *(u32*)(d + ip0) = c0.x; *(u32*)(d + iq0) = c0.y; *(u32*)(d + 2560 + ip0) = c0.z; *(u32*)(d + 2560 + iq0) = c0.w;
        *(u32*)(d + ip0 + 16) = c1.x; *(u32*)(d + iq1) = c1.y; *(u32*)(d + 2560 + ip0 + 16) = c1.z; *(u32*)(d + 2560 + iq1) = c1.w;
      }
      bar();
      const u32x4 a0 = *(const u32x4*)(img + w * 640 + m * 20 + 4 * g), a1 = *(const u32x4*)(img + w * 640 + (16 + m) * 20 + 4 * g);
      if constexpr (L == 1) ring4(5);
      if constexpr (L == 0) ring4(0);
      f32x4 dr0, di0, dr1, di1;
      row_fft<true, 7>(rc, a0, a1, dr0, di0, dr1, di1);
      if constexpr (L == 0) ring4(1);
      const float ys = 1.0f / 32768.0f;
      xa[0] += lo2(dr0) * ys; xa[1] += hi2(dr0) * ys; xa[2] += lo2(dr1) * ys; xa[3] += hi2(dr1) * ys;
      xb[0] += lo2(di0) * ys; xb[1] += hi2(di0) * ys; xb[2] += lo2(di1) * ys; xb[3] += hi2(di1) * ys;
      const f32x2 sa = (xa[0] + xa[1]) + (xa[2] + xa[3]), sb = (xb[0] + xb[1]) + (xb[2] + xb[3]);
      const float mean = reduce2(sa.x + sa.y, sb.x + sb.y) * (1.0f / 512.0f);
      const f32x2 M = bcast2(mean);
      f32x2 da[4], db[4];
#pragma unroll
      for (int i = 0; i < 4; ++i) { da[i] = xa[i] - M.x; db[i] = xb[i] - M.y; }
      f32x2 qa = da[0] * da[0], qb = db[0] * db[0];
#pragma unroll
      for (int i = 1; i < 4; ++i) { qa += da[i] * da[i]; qb += db[i] * db[i]; }
      if constexpr (L == 0) { SB(); ring4(2); SB(); }
      f32x2 R, O;
      ln_finish(mean, reduce2(qa.x + qa.y, qb.x + qb.y), R, O);
      u32x4 na, nb;
#pragma unroll
      for (int i = 0; i < 4; ++i) {
        const f32x2 va = da[i] * R.x + O.x, vb = db[i] * R.y + O.y;
        na[i] = mark<0x10000u, 1u - L>(pkb(va.x, va.y)); nb[i] = mark<0x10000u, 1u - L>(pkb(vb.x, vb.y));
      }
      u8* tr = lds + LDS_SCR + 12288;
      *(u32x4*)(tr + tw0) = na; *(u32x4*)(tr + tw1) = nb;
      if constexpr (L == 0) vmwait<12>();
      bar();
      st4(*(const u32x4*)(tr + trd), xno);
      st4(*(const u32x4*)(tr + 4096 + trd), xno + 8192);
      SB();
      const u32x4 rl = {pkb(xa[0].x, xa[0].y), pkb(xa[1].x, xa[1].y), pkb(xa[2].x, xa[2].y), pkb(xa[3].x, xa[3].y)};
      const u32x4 rh = {pkb(xb[0].x, xb[0].y), pkb(xb[1].x, xb[1].y), pkb(xb[2].x, xb[2].y), pkb(xb[3].x, xb[3].y)};
      if constexpr (L == 0) {
        *(u32x4*)(lds + LDS_STASH + t16) = rl; *(u32x4*)(lds + LDS_STASH + 4096 + t16) = rh;
        ring4(3); ring4(4); ring4(5); ring4(6); ring4(7);
      } else {
        const u32 o = OFF_X3 + (8 * pos + 2 * w) * 1024 + xo;
        st2(u32x2{rl.x, rl.y}, o); st2(u32x2{rl.z, rl.w}, o + 512);
        st2(u32x2{rh.x, rh.y}, o + 1024); st2(u32x2{rh.z, rh.w}, o + 1536);
        __builtin_amdgcn_s_sleep(6);
      }
    }
    ST(S0 + 6);
    {
      FRESH();
      u32x4 tk[32], he[32], hl[16], w2t[4], w2v[3][4];
      u32x2 hh[16];
      f32x4 ac1[6][2], ac2[4][2], ac3[4][2];
      const u8* hs0 = ws + OFF_H + (8 * G + 4 * half2) * 57344 + (4 * w) * 3584 + 3584;
      const u8* hs2 = hs0 + 7168;
      const u8* w2src = ws + OFF_W2 + (ct * 4 + w) * 57344;
      PINS(hs0); PINS(hs2);
      u32 hv[4] = {l16, l16 + 57344, l16 + 2 * 57344, l16 + 3 * 57344}, h8[4];
#pragma unroll
      for (int u = 0; u < 4; ++u) { h8[u] = hv[u] - lane * 8; PIN(hv[u]); PIN(h8[u]); }
      auto hload = [&](auto RE, auto KC, auto UC) {
        constexpr int k = decltype(KC)::value, u = decltype(UC)::value;
        constexpr bool re = decltype(RE)::value;
        if constexpr (k == 4 || k == 5) {
          constexpr int o = (u >> 2) * 3584 + 3072 - 3584;
          if constexpr (re) ld2r<o>(hh[8 * (k - 4) + u], h8[u & 3], k == 5 ? hs2 : hs0); else ld2<o>(hh[8 * (k - 4) + u], h8[u & 3], k == 5 ? hs2 : hs0);
        } else {
          constexpr int j = k < 4 ? k : k < 10 ? k - 6 : k - 10, o = (j & 1) * 3584 + (k < 4 ? 0 : k < 10 ? 1024 : 2048) - 3584;
          u32x4& d = k < 10 ? he[(k < 4 ? 0 : 16) + 4 * j + u] : hl[4 * j + u];
          if constexpr (re) ld4r<o>(d, hv[u], j >> 1 ? hs2 : hs0); else ld4<o>(d, hv[u], j >> 1 ? hs2 : hs0);
        }
      };
      auto hseq = [&](auto IC) {
        constexpr int i = decltype(IC)::value, k = i < 16 ? i >> 2 : i < 32 ? 4 + ((i - 16) >> 3) : 6 + ((i - 32) >> 2);
        hload(std::false_type{}, std::integral_constant<int, k>{}, std::integral_constant<int, (i < 16 || i >= 32) ? (i & 3) : (i & 7)>{});
      };
      {
        const u8* w1b = ws + OFF_W1 + member * 229376 + 131072 + w * 8192;
        const u8* tk0 = ws + OFF_A + (8 * G + 2 * w) * 16384 + 4096;
        const u8* tk1 = tk0 + 8192;
        u32 tv1 = l16 + 16384, lhi = l16 + 65536;
        u32 hd0 = OFF_H + (8 * G + 2 * w) * 57344 + member * 3584 + l16, hd1 = hd0 + 57344;
        u32 hd2 = hd0 + 3072 - lane * 8, hd3 = hd2 + 57344;
        PIN(tv1); PIN(lhi); PIN(hd0); PIN(hd1); PIN(hd2); PIN(hd3); PINS(tk0); PINS(tk1);
        auto tok = [&](auto RE, auto QC) {
          constexpr int q = decltype(QC)::value, ks = q >> 1;
          if constexpr (decltype(RE)::value) ld4r<(ks & 7) * 1024 - 4096>(tk[q], q & 1 ? tv1 : l16, ks >> 3 ? tk1 : tk0);
          else ld4<(ks & 7) * 1024 - 4096>(tk[q], q & 1 ? tv1 : l16, ks >> 3 ? tk1 : tk0);
        };
        constexpr bool TONE = L == 0;
        constexpr u32 TBIT = L == 0 ? 1u : 0x10000u;
        u32 mx0 = 0, mx1 = 0;
        auto mx0_fin = [&]() { return red2<TONE>(mx0, mx1) & TBIT; };
        auto fix = [&](auto QC, u64 bad) {
          constexpr int q0 = decltype(QC)::value;
          if (__builtin_expect(bad != 0, 0)) {
            _Pragma("nounroll") for (int sp = 0; sp < SPIN_CAP; ++sp) {
              CNT(4);
              static_for<4>([&](auto I) { tok(std::true_type{}, std::integral_constant<int, q0 + decltype(I)::value>{}); });
              vmwait<0>();
#pragma unroll
              for (int q = q0; q < q0 + 4; ++q) PIN(tk[q]);
              if (!__any(stale<TONE, TBIT>(red2<TONE>(red8<TONE>(tk[q0], tk[q0 + 1]), red8<TONE>(tk[q0 + 2], tk[q0 + 3]))))) break;
            }
          }
        };
        static_for<NTOK0>([&](auto Q) { tok(std::false_type{}, Q); });
        constexpr int PD = 8;
        u32x4 af[PD];
        auto frag = [&](int x) {
          const int o = (x & 127) * 1024;
          return o < 65536 ? *(const u32x4*)(lds + l16 + o) : *(const u32x4*)(lds + lhi + (o - 65536));
        };
        vmwait<NTOK0>();
        bar_nw();
#pragma unroll
        for (int x = 0; x < PD - 1; ++x) af[x] = frag(x);
        vmwait<NTOK0 - 4>();
        ST(S0 + 7);
#pragma unroll
        for (int q = 0; q < 4; ++q) PIN(tk[q]);
        mx0 = red8<TONE>(tk[0], tk[1]); mx1 = red8<TONE>(tk[2], tk[3]);
        fix(std::integral_constant<int, 0>{}, __ballot(stale<TONE, TBIT>(red2<TONE>(mx0, mx1))));
        SB();
        ST(S0 + 8);
        float eg[2][2], eu[2][2];
        u32 ev[2];
        u64 bm = 0;
        u32x4 eo[1];
        auto epi = [&](auto NC) {
          constexpr int n = decltype(NC)::value;
          if constexpr (n < NEPI1 + NEPI2) {
            constexpr bool p1 = n < NEPI1;
            constexpr int i = p1 ? n - (n > 40) - (n > 81) - (n > 102) : (n - NEPI1) - (n - NEPI1) / 41;
            if constexpr (p1 && (n == 40 || n == 81)) st4(eo[0], n == 40 ? hd0 : hd1);
            else if constexpr (p1 && (n == 102 || n == 123)) st2(u32x2{eo[0].x, eo[0].y}, n == 102 ? hd2 : hd3);
            else if constexpr (!p1 && (n - NEPI1) % 41 == 40) st4(eo[0], ((n - NEPI1) / 41 ? hd1 : hd0) + 1024);
            else {
              constexpr int s = i & 1, st = (i % 20) >> 1, blk = i / 20;
              constexpr int u = p1 ? (blk < 4 ? blk >> 1 : blk - 4) : blk >> 1, pr = p1 ? (blk < 4 ? blk & 1 : 2) : blk & 1;
              constexpr int ei = 0, d = 2 * (pr & 1) + s;
              const f32x4& ga = p1 ? ac1[2 * pr][u] : ac2[2 * (pr & 1)][u];
              const f32x4& up = p1 ? ac1[2 * pr + 1][u] : ac2[2 * (pr & 1) + 1][u];
              if constexpr (st < 2) { eg[s][st] = ga[2 * s + st]; PIN(eg[s][st]); }
              else if constexpr (st < 4) { eu[s][st - 2] = up[2 * s + st - 2]; PIN(eu[s][st - 2]); }
              else if constexpr (st < 6) { eg[s][st - 4] = relu(eg[s][st - 4]); PIN(eg[s][st - 4]); }
              else if constexpr (st < 8) { eg[s][st - 6] *= eu[s][st - 6]; PIN(eg[s][st - 6]); }
              else if constexpr (st == 8) { ev[s] = pkb(eg[s][0], eg[s][1]); PIN(ev[s]); }
              else { u32 v = mark<1u - L, L>(ev[s]); PIN(v); eo[ei][d] = v; }
            }
            SB();
          }
        };
        static_for<224>([&](auto XC) {
          constexpr int x = decltype(XC)::value, job = G1S[L].job[x], ji = G1S[L].idx[x], e0 = G1S[L].epi[x], e1 = G1S[L].epi[x + 1];
          if constexpr (x == 32 || x == 64) bar_nw();
          if constexpr (x == 97) { vmwait<g1_wait(L, x, 2, 7)>(); bar_nw(); }
          if constexpr (x == 150) { vmwait<g1_wait(L, x, 2, 23)>(); bar_nw(); }
          constexpr int cj = (x + 12) / 12, co = x + 12 - 12 * cj;
          constexpr bool chk = x < 84 && co >= 5;
          constexpr int ks = x < 96 ? x / 6 : x < 160 ? (x - 96) >> 2 : (x - 160) >> 2, t = x < 96 ? x % 6 : x & 3;
          auto mm = [&](int u) {
            f32x4& a = x < 96 ? ac1[x < 96 ? t : 0][u] : x < 160 ? ac2[x < 96 ? 0 : t][u] : ac3[x < 96 ? 0 : t][u];
            if constexpr (ks == 0) a = mfma_b(af[x % PD], tk[u], f32x4{0.f, 0.f, 0.f, 0.f}); else a = mfma_b(af[x % PD], tk[2 * ks + u], a);
          };
          SB();
          mm(0);
          SB();
          if constexpr (x < 223) __builtin_amdgcn_s_waitcnt(0xC07F | ((x + PD - 2 < 224 ? PD - 3 : 222 - x) << 8));
          if constexpr (chk && co == 5) {
            vmwait<g1_wait(L, x, 1, 4 * cj + 3)>();
            PIN(tk[4 * cj]); PIN(tk[4 * cj + 1]); PIN(tk[4 * cj + 2]); PIN(tk[4 * cj + 3]);
          }
          SB();
          if constexpr (chk && co >= 6 && co < 10) {
            const u32x4 v = tk[4 * cj + co - 6];
            mx0 = co == 6 ? red2<TONE>(v.x, v.y) : red3<TONE>(mx0, v.x, v.y); PIN(mx0); SB();
            mx1 = co == 6 ? red2<TONE>(v.z, v.w) : red3<TONE>(mx1, v.z, v.w); PIN(mx1);
          }
          if constexpr (chk && co == 10) { mx0 = mx0_fin(); PIN(mx0); SB(); bm = __ballot(TONE ? mx0 == 0 : mx0 != 0); PINS(bm); }
          if constexpr (e0 < e1) epi(std::integral_constant<int, e0>{});
          if constexpr (e0 + 1 < e1) epi(std::integral_constant<int, e0 + 1>{});
          SB();
          mm(1);
          SB();
          if constexpr (chk && co == 11) fix(std::integral_constant<int, 4 * cj>{}, bm);
          if constexpr (job == 2 && (ji & 3) == 0) set_m0(ldsbase + (ji >> 3) * 32768 + w * 8192 + (ji & 4) * 1024);
          SB();
          if constexpr (x + PD - 1 < 224) af[(x + PD - 1) % PD] = frag(x + PD - 1);
          SB();
          if constexpr (job == 1) tok(std::false_type{}, std::integral_constant<int, ji>{});
          if constexpr (job == 2) dma1<(ji & 3) * 1024>(l16, w1b + (ji >> 3) * 32768 + (ji & 4) * 1024);
          if constexpr (job == 3) ld4a<(ji & 7) * 1024 - 4096>(w2a[ji >> 2][ji & 3], l16, w2src + (ji >> 3) * 8192 + 4096);
          if constexpr (job == 5) ld4a<ji * 1024>(w2t[ji], l16, w2src + 40960);
          if constexpr (job == 4) hseq(std::integral_constant<int, ji>{});
          if constexpr (e0 + 2 < e1) epi(std::integral_constant<int, e0 + 2>{});
          SB();
        });
        ST(S0 + 9);
#pragma unroll
        for (int u = 0; u < 2; ++u) {
          u32x4 o;
#pragma unroll
          for (int p = 0; p < 2; ++p) {
            const f32x4 ga = ac3[2 * p][u], up = ac3[2 * p + 1][u];
            const f32x2 a = f32x2{relu(ga[0]), relu(ga[1])} * lo2(up), c = f32x2{relu(ga[2]), relu(ga[3])} * hi2(up);
            o[2 * p] = mark<1u - L, L>(pkb(a.x, a.y)); o[2 * p + 1] = mark<1u - L, L>(pkb(c.x, c.y));
          }
          st4(o, (u ? hd1 : hd0) + 2048);
        }
        SB();
      }
      ST(S0 + 10);
      {
        f32x4 acc[4][4];
        u32x4 x3a, x3b;
        const int tokl = 16 * w + (lane >> 3);
        u32 eo = (128 * G + 64 * half2 + tokl) * 1024 + ct * 128 + (lane & 7) * 16;
        PIN(eo);
        const u8* w2x = w2src + 45056 + 4096;
        const u8* x3s = ws + OFF_X3;
        u32 lx4 = l16 + 4096, eo2 = eo + 8192;
        u64 bm = 0;
        PINS(w2x); PINS(x3s); PIN(lx4); PIN(eo2);
        constexpr int NE2 = 48 - NEARLY, LB = NE2 + 12;
        auto cum = [](int n) constexpr { int c = 0; for (int i = 0; i < n; ++i) c += i < LB + 16 ? 1 : 0; return c; };
        auto young = [cum](int k, int n) constexpr {
          const int i = k < 4 ? 4 * k + 3 : k < 6 ? 16 + 8 * (k - 4) + 7 : 32 + 4 * (k - 6) + 3;
          if (k < 10 && i < NEARLY) return cum(n) + 2 + G1S[L].cum[224] - G1S[L].pos[4][i] - 1;
          if (k < 10) return cum(n) - cum(i - NEARLY + 1);
          return cum(n) - cum(LB + 4 * (k - 10) + 4);
        };
        constexpr bool HONE = L == 1;
        u32 mx0 = 0, mx1 = 0;
        auto mx0_fin = [&]() { return red2<HONE>(mx0, mx1) & 1u; };
        auto pin_k = [&](auto KC) {
          constexpr int k = decltype(KC)::value;
#pragma unroll
          for (int u = 0; u < 4; ++u) {
            if constexpr (k < 4) PIN(he[4 * k + u]);
            else if constexpr (k < 6) { PIN(hh[8 * (k - 4) + u]); PIN(hh[8 * (k - 4) + 4 + u]); }
            else if constexpr (k < 10) PIN(he[16 + 4 * (k - 6) + u]);
            else { PIN(hl[4 * (k - 10) + u]); if constexpr (k > 10) PIN(w2v[k - 11][u]); }
          }
        };
        auto opnd = [&](auto KC, int u) {
          constexpr int k = decltype(KC)::value;
          if constexpr (k < 4) return he[4 * k + u];
          else if constexpr (k < 6) return u32x4{hh[8 * (k - 4) + u].x, hh[8 * (k - 4) + u].y, hh[8 * (k - 4) + 4 + u].x, hh[8 * (k - 4) + 4 + u].y};
          else if constexpr (k < 10) return he[16 + 4 * (k - 6) + u];
          else return hl[4 * (k - 10) + u];
        };
        auto fixk = [&](auto KC, u64 bad) {
          constexpr int k = decltype(KC)::value;
          if (__builtin_expect(bad != 0, 0)) {
            _Pragma("nounroll") for (int sp = 0; sp < SPIN_CAP; ++sp) {
              CNT(k < 10 ? 5 : 6);
              static_for<(k == 4 || k == 5) ? 8 : 4>([&](auto U) { hload(std::true_type{}, KC, U); });
              vmwait<0>();
              pin_k(KC);
              if (!__any(stale<HONE, 1u>(red2<HONE>(red8<HONE>(opnd(KC, 0), opnd(KC, 1)), red8<HONE>(opnd(KC, 2), opnd(KC, 3)))))) break;
            }
          }
        };
        u32 rw[4];
#pragma unroll
        for (int c = 0; c < 4; ++c) { rw[c] = w * 16384 + c * 4096 + (g * 16 + (m ^ (2 * (2 * c + (g >> 1))))) * 16; PIN(rw[c]); }
        u32 oo[2] = {0, 0};
        if constexpr (L == 1) {
#pragma unroll
          for (int a = 0; a < 2; ++a) {
            const int tok = 128 * G + 64 * half2 + tokl + 8 * a;
            oo[a] = (row_of_pos(tok >> 3, (tok >> 1) & 3) + 1024 * (tok & 1)) * 1024 + ct * 128 + (lane & 7) * 16;
            PIN(oo[a]);
          }
        }
        vmwait<clamp63(young(0, 0))>();
        if constexpr (L == 0) {
#pragma unroll
          for (int k = 0; k < 10; ++k) { PINA(w2a[k][0]); PINA(w2a[k][1]); PINA(w2a[k][2]); PINA(w2a[k][3]); }
        }
        PINA(w2t[0]); PINA(w2t[1]); PINA(w2t[2]); PINA(w2t[3]);
        pin_k(std::integral_constant<int, 0>{});
        mx0 = red8<HONE>(he[0], he[1]); mx1 = red8<HONE>(he[2], he[3]);
        fixk(std::integral_constant<int, 0>{}, __ballot(stale<HONE, 1u>(red2<HONE>(mx0, mx1))));
        ST(S0 + 11);
        auto wfr = [&](auto KC, int c) {
          constexpr int k = decltype(KC)::value;
          if constexpr (k < 10) return w2a[k][c]; else if constexpr (k == 10) return w2t[c]; else return w2v[k - 11][c];
        };
        static_for<48>([&](auto NC) {
          constexpr int n = decltype(NC)::value, k = n >> 2, c = n & 3;
          using KC = std::integral_constant<int, k>;
          using KN = std::integral_constant<int, k + 1>;
          static_for<4>([&](auto UC) {
            constexpr int u = decltype(UC)::value;
            SB();
            if constexpr (k == 0) acc[c][u] = mfma_b(wfr(KC{}, c), opnd(KC{}, u), f32x4{0.f, 0.f, 0.f, 0.f});
            else acc[c][u] = mfma_b(wfr(KC{}, c), opnd(KC{}, u), acc[c][u]);
            SB();
            if constexpr (c == 1 && u == 0) {
              vmwait<clamp63(young(k == 11 ? 13 : k + 1, n))>(); pin_k(KN{});
              if constexpr (k == 11) pin_k(std::integral_constant<int, 13>{});
            }
            if constexpr ((c == 1 && (u == 1 || u == 2)) || (c == 2 && u < (k == 11 ? 4 : 2)) || (k == 11 && c == 3 && u < 2)) {
              constexpr int j = c == 1 ? u - 1 : c == 2 ? 2 + u : 6 + u;
              const u32x4 v = j < 4 ? opnd(KN{}, j & 3) : opnd(std::integral_constant<int, 13>{}, j & 3);
              mx0 = j == 0 ? red2<HONE>(v.x, v.y) : red3<HONE>(mx0, v.x, v.y); PIN(mx0); SB();
              mx1 = j == 0 ? red2<HONE>(v.z, v.w) : red3<HONE>(mx1, v.z, v.w); PIN(mx1);
            }
            if constexpr (c == 3 && u == (k == 11 ? 2 : 0)) { mx0 = mx0_fin(); PIN(mx0); SB(); bm = __ballot(HONE ? mx0 == 0 : mx0 != 0); PINS(bm); }
            if constexpr (u == 3) {
              if constexpr (n < NE2) hseq(std::integral_constant<int, NEARLY + n>{});
              else if constexpr (n < NE2 + 8) ld4<(n - NE2) * 1024 - 4096>(w2v[(n - NE2) >> 2][(n - NE2) & 3], l16, w2x);
              else if constexpr (n < LB) ld4<(n - NE2 - 8) * 1024>(w2v[2][(n - NE2) & 3], lx4, w2x);
              else if constexpr (n < LB + 16) hload(std::false_type{}, std::integral_constant<int, 10 + ((n - LB) >> 2)>{}, std::integral_constant<int, (n - LB) & 3>{});
              if constexpr (c == 3) { fixk(KN{}, bm); if constexpr (k == 11) fixk(std::integral_constant<int, 13>{}, bm); }
            }
          });
        });
        bar_nw();
        if constexpr (L == 1) { ld4<0>(x3a, eo, x3s); ld4<0>(x3b, eo2, x3s); }
        static_for<20>([&](auto TC) {
          constexpr int t = decltype(TC)::value;
          if constexpr (t < 16) {
            SB();
            acc[t >> 2][t & 3] = mfma_b(w2v[1][t >> 2], hl[8 + (t & 3)], acc[t >> 2][t & 3]);
            acc[t >> 2][t & 3] = mfma_b(w2v[2][t >> 2], hl[12 + (t & 3)], acc[t >> 2][t & 3]);
            SB();
          }
          if constexpr (t >= 4) *(f32x4*)(lds + rw[(t - 4) >> 2] + ((t - 4) & 3) * 1024) = acc[(t - 4) >> 2][(t - 4) & 3];
        });
        SB();
        ST(S0 + 12);
        const int o8 = lane & 7;
        const u8* rp0 = lds + ((o8 >> 1) * 4 + w) * 1024 + (2 * (o8 & 1) * 16 + ((lane >> 3) ^ (2 * o8))) * 16;
        const u8* rp1 = lds + ((o8 >> 1) * 4 + w) * 1024 + (2 * (o8 & 1) * 16 + ((8 + (lane >> 3)) ^ (2 * o8))) * 16;
        bar();
        f32x4 s[2][2][4];
#pragma unroll
        for (int p = 0; p < 4; ++p) { s[0][0][p] = *(const f32x4*)(rp0 + p * 16384); s[0][1][p] = *(const f32x4*)(rp0 + p * 16384 + 256); }
#pragma unroll
        for (int p = 0; p < 4; ++p) { s[1][0][p] = *(const f32x4*)(rp1 + p * 16384); s[1][1][p] = *(const f32x4*)(rp1 + p * 16384 + 256); }
        SB();
        if constexpr (L == 1) {
          vmwait<0>(); PIN(x3a); PIN(x3b);
        }
#pragma unroll
        for (int a = 0; a < 2; ++a) {
          const f32x4 s0 = (s[a][0][0] + s[a][0][1]) + (s[a][0][2] + s[a][0][3]), s1 = (s[a][1][0] + s[a][1][1]) + (s[a][1][2] + s[a][1][3]);
          const u32x4 y = {pkb(s0[0], s0[1]), pkb(s0[2], s0[3]), pkb(s1[0], s1[1]), pkb(s1[2], s1[3])};
          if constexpr (L == 0) {
            st4(mark4<0x10001u, 0u>(y), OFF_A + eo + a * 8192);
          } else {
            const u32x4 xr = a ? x3b : x3a;
            u32x4 o;
#pragma unroll
            for (int p = 0; p < 4; ++p) o[p] = pkb(blo(xr[p]) + blo(y[p]), bhi(xr[p]) + bhi(y[p]));
            *(u32x4*)((u8*)P.out + oo[a]) = o;
          }
        }
      }
    }
    ST(S0 + 13);
    if constexpr (L == 0) {
      FRESH();
      SB();
      row_consts<false, 0>(rc, m, g);
      SB();
      u32x2 d0, d1, d2, d3;
      const u8* db = ws + OFF_A + (8 * pos + 2 * w) * 1024;
      const u32x4 sl = *(const u32x4*)(lds + LDS_STASH + t16), sh = *(const u32x4*)(lds + LDS_STASH + 4096 + t16);
#pragma unroll
      for (int i = 0; i < 4; ++i) {
        xv[2 * i] = f32x2{blo(sl[i]), blo(sh[i])}; xv[2 * i + 1] = f32x2{bhi(sl[i]), bhi(sh[i])};
        PIN(xv[2 * i]); PIN(xv[2 * i + 1]);
      }
      SB();
      bar_nw();
      ld2<0>(d0, xo, db); ld2<512>(d1, xo, db); ld2<1024>(d2, xo, db); ld2<1536>(d3, xo, db);
      ring4(0);
      SB();
      row_consts<false, 1>(rc, m, g);
      SB();
      ring4(1);
      ST(S0 + 14);
      vmwait<8>(); PIN(d0); PIN(d1); PIN(d2); PIN(d3);
      RETRY((stale<false, 1u>(red8<false>(u32x4{d0.x, d0.y, d1.x, d1.y}, u32x4{d2.x, d2.y, d3.x, d3.y}))), {
        CNT(7);
        if (bad_) { ld2r<0>(d0, xo, db); ld2r<512>(d1, xo, db); ld2r<1024>(d2, xo, db); ld2r<1536>(d3, xo, db); }
        vmwait<0>(); PIN(d0); PIN(d1); PIN(d2); PIN(d3);
      });
      xv[0] += f32x2{blo(d0.x), blo(d2.x)}; xv[1] += f32x2{bhi(d0.x), bhi(d2.x)}; xv[2] += f32x2{blo(d0.y), blo(d2.y)}; xv[3] += f32x2{bhi(d0.y), bhi(d2.y)};
      xv[4] += f32x2{blo(d1.x), blo(d3.x)}; xv[5] += f32x2{bhi(d1.x), bhi(d3.x)}; xv[6] += f32x2{blo(d1.y), blo(d3.y)}; xv[7] += f32x2{bhi(d1.y), bhi(d3.y)};
      ST(S0 + 15);
    }
  };
  layer(std::integral_constant<int, 0>{});
  layer(std::integral_constant<int, 1>{});
  if (blockIdx.x == 0 && threadIdx.x == 0) st2(ep, OFF_E);
  ST_OUT();
}

extern "C" void kernel_launch(void* const* d_in, const int* in_sizes, int n_in, void* d_out, int out_size, void* d_ws, size_t ws_size,
                              hipStream_t stream) {
  Params p;
  p.x = (const u16*)d_in[0]; p.wr = (const u16*)d_in[1]; p.wi = (const u16*)d_in[2];
  p.wg = (const u16*)d_in[4]; p.wu = (const u16*)d_in[5]; p.wd = (const u16*)d_in[6];
  p.out = (u16*)d_out; p.ws = (u8*)d_ws;
  (void)hipFuncSetAttribute((const void*)k_all, hipFuncAttributeMaxDynamicSharedMemorySize, LDS_TOTAL);
  k_all<<<256, 256, LDS_TOTAL, stream>>>(p);
}
```

```cpp
#include <hip/hip_runtime.h>
#include <hip/hip_bf16.h>
#include <stdint.h>
#include <type_traits>
#include <utility>

typedef unsigned char u8;
typedef unsigned short u16;
typedef unsigned int u32;
typedef unsigned long long u64;
typedef u32 u32x2 __attribute__((ext_vector_type(2)));
typedef u32 u32x4 __attribute__((ext_vector_type(4)));
typedef float f32x2 __attribute__((ext_vector_type(2)));
typedef float f32x4 __attribute__((ext_vector_type(4)));
typedef _Float16 f16x2 __attribute__((ext_vector_type(2)));
typedef __bf16 bf16x2 __attribute__((ext_vector_type(2)));
typedef __bf16 bf16x8 __attribute__((ext_vector_type(8)));
#define DI __device__ __forceinline__
#define SB() __builtin_amdgcn_sched_barrier(0)
#define MEMFENCE() asm volatile("" ::: "memory")

constexpr u32 MB2 = 2u << 20;
constexpr u32 OFF_E = 0, OFF_FLAG = 8192, OFF_WNY = 65536;
constexpr u32 OFF_A = MB2;
constexpr u32 OFF_U = OFF_A + MB2;
constexpr u32 OFF_WX = OFF_U + MB2;
constexpr u32 OFF_X3 = OFF_WX + MB2;
constexpr u32 OFF_H = OFF_X3 + MB2;
constexpr u32 OFF_W1 = OFF_H + 7340032;
constexpr u32 OFF_W2 = OFF_W1 + 3670016;
constexpr u32 WS_USED = OFF_W2 + 1835008;
constexpr u32 SENT = 0xFFFFFFFFu;
constexpr int SPIN_CAP = 1 << 15;
constexpr u32 CODE_BYTES = 66 * 1024;

constexpr int LDS_SCR = 131072, LDS_STASH = LDS_SCR + 24576, LDS_TOTAL = 163840;

struct Params {
  const u16* x; const u16* wr; const u16* wi; const u16* wg; const u16* wu; const u16* wd; u16* out; u8* ws;
};

#define ST(i)
#define ST_DECL
#define CNT(i)
#define ST_OUT()

#define XS(i)
#define XF(i)
DI u32 pkh(float a, float b) { f32x2 v = {a, b}; return __builtin_bit_cast(u32, __builtin_convertvector(v, f16x2)); }
DI u32 pkb(float a, float b) { f32x2 v = {a, b}; return __builtin_bit_cast(u32, __builtin_convertvector(v, bf16x2)); }
template <bool BF> DI u32 pkc(float a, float b) { return BF ? pkb(a, b) : pkh(a, b); }
DI u32 pkh_n0(float a, float b, u32 after) { u32 r; asm("v_cvt_pk_f16_f32 %0, -%1, %2" : "=v"(r) : "v"(a), "v"(b), "v"(after)); return r; }
DI u32 pkb_n1(float a, float b, u32 after) { u32 r; asm("v_cvt_pk_bf16_f32 %0, %1, -%2" : "=v"(r) : "v"(a), "v"(b), "v"(after)); return r; }
DI float blo(u32 u) { return __uint_as_float(u << 16); }
DI float bhi(u32 u) { return __uint_as_float(u & 0xffff0000u); }
DI float bfr(float a) { return blo(pkb(a, a)); }
DI float relu(float a) { const int i = __float_as_int(a); return __int_as_float(i > 0 ? i : 0); }
DI u32 umax(u32 a, u32 b) { return a > b ? a : b; }
DI u32 max3(u32 a, u32 b, u32 c) { return umax(umax(a, b), c); }
DI u32 max4(u32 acc, u32x4 v) { return max3(max3(acc, v.x, v.y), v.z, v.w); }
template <bool ONE> DI u32 red3(u32 a, u32 b, u32 c) { return ONE ? (a & b & c) : (a | b | c); }
template <bool ONE> DI u32 red2(u32 a, u32 b) { return ONE ? (a & b) : (a | b); }
template <bool ONE> DI u32 red4(u32 acc, u32x4 v) { return red3<ONE>(red3<ONE>(acc, v.x, v.y), v.z, v.w); }
template <bool ONE> DI u32 red8(u32x4 a, u32x4 b) { return red4<ONE>(red3<ONE>(red2<ONE>(a.x, a.y), a.z, a.w), b); }
template <bool ONE, u32 BIT> DI bool stale(u32 r) { return ONE ? (r & BIT) == 0 : (r & BIT) != 0; }
template <u32 CLR, u32 SET> DI u32 mark(u32 v) { return (v & ~CLR) | SET; }
template <u32 CLR, u32 SET> DI u32x4 mark4(u32x4 v) { return u32x4{mark<CLR, SET>(v.x), mark<CLR, SET>(v.y), mark<CLR, SET>(v.z), mark<CLR, SET>(v.w)}; }
DI f32x2 lo2(f32x4 v) { return __builtin_shufflevector(v, v, 0, 1); }
DI f32x2 hi2(f32x4 v) { return __builtin_shufflevector(v, v, 2, 3); }
DI f32x2 swp(f32x2 v) { return __builtin_shufflevector(v, v, 1, 0); }
DI f32x4 mfma_b(u32x4 a, u32x4 b, f32x4 c) {
  return __builtin_amdgcn_mfma_f32_16x16x32_bf16(__builtin_bit_cast(bf16x8, a), __builtin_bit_cast(bf16x8, b), c, 0, 0, 0);
}
template <class T> DI void PIN(T& x) { asm volatile("" : "+v"(x)); }
template <class T> DI void PINS(T& x) { asm volatile("" : "+s"(x)); }
DI int opq(int x) { asm("" : "+s"(x)); return x; }
template <class T> DI void PINA(T& x) { asm volatile("" : "+a"(x)); }
template <class F, int... I> DI void static_for_(F&& f, std::integer_sequence<int, I...>) { (f(std::integral_constant<int, I>{}), ...); }
template <int N, class F> DI void static_for(F&& f) { static_for_(f, std::make_integer_sequence<int, N>{}); }

#define MM_(T) "v_mfma_f32_16x16x32_" T
template <bool BF> DI void mm4(f32x4& d0, f32x4& d1, f32x4& d2, f32x4& d3, u32x4 a0, u32x4 a1, u32x4 b0, u32x4 b1) {
#define MM4_(T) asm volatile("s_nop 1\n\t" MM_(T) " %0, %4, %6, 0\n\t" MM_(T) " %1, %4, %7, 0\n\t" MM_(T) " %2, %5, %6, 0\n\t" MM_(T) " %3, %5, %7, 0\n\ts_nop 5" \
               : "=&v"(d0), "=&v"(d1), "=&v"(d2), "=&v"(d3) : "v"(a0), "v"(a1), "v"(b0), "v"(b1))
  if constexpr (BF) MM4_("bf16"); else MM4_("f16");
}
template <bool BF, int TAIL, int AG = 0> DI void mm8(f32x4& d0, f32x4& d1, f32x4& d2, f32x4& d3, u32x4 a00, u32x4 b00, u32x4 a01, u32x4 b01, u32x4 a10,
                               u32x4 b10, u32x4 a11, u32x4 b11, u32x4 a20, u32x4 b20, u32x4 a21, u32x4 b21, u32x4 a30, u32x4 b30, u32x4 a31, u32x4 b31) {
#define MM8_(T, A, B) asm volatile("s_nop 1\n\t" MM_(T) " %0, %4, %5, 0\n\t" MM_(T) " %1, %8, %9, 0\n\t" MM_(T) " %0, %6, %7, %0\n\t" MM_(T) " %1, %10, %11, %1\n\t" \
               MM_(T) " %2, %12, %13, 0\n\t" MM_(T) " %3, %16, %17, 0\n\t" MM_(T) " %2, %14, %15, %2\n\t" MM_(T) " %3, %18, %19, %3\n\ts_nop %20"         \
               : "=&v"(d0), "=&v"(d1), "=&v"(d2), "=&v"(d3)                                                                                              \
               : A(a00), B(b00), A(a01), B(b01), A(a10), B(b10), A(a11), B(b11), A(a20), B(b20), A(a21), B(b21), A(a30), B(b30), A(a31), B(b31), "i"(TAIL))
  if constexpr (!BF) MM8_("f16", "v", "v");
  else if constexpr (AG == 0) MM8_("bf16", "v", "v");
  else if constexpr (AG == 1) MM8_("bf16", "a", "v");
  else MM8_("bf16", "v", "a");
}
template <bool BF, int AG> DI void mf0(f32x4& d, u32x4 a, u32x4 b) {
  if constexpr (!BF) asm volatile(MM_("f16") " %0, %1, %2, 0" : "=&v"(d) : "v"(a), "v"(b));
  else if constexpr (AG == 0) asm volatile(MM_("bf16") " %0, %1, %2, 0" : "=&v"(d) : "v"(a), "v"(b));
  else asm volatile(MM_("bf16") " %0, %1, %2, 0" : "=&v"(d) : "v"(a), "a"(b));
}
template <bool BF, int AG, int TAIL> DI void mf4(f32x4& d0, f32x4& d1, f32x4& d2, f32x4& d3, u32x4 a0, u32x4 b0, u32x4 a1, u32x4 b1, u32x4 a2,
                                               u32x4 b2, u32x4 a3, u32x4 b3) {
#define MF4_(T, B) asm volatile("s_nop 1\n\t" MM_(T) " %0, %4, %5, %0\n\t" MM_(T) " %1, %6, %7, %1\n\t" MM_(T) " %2, %8, %9, %2\n\t" MM_(T) " %3, %10, %11, %3\n\ts_nop %12" \
               : "+v"(d0), "+v"(d1), "+v"(d2), "+v"(d3) : "v"(a0), B(b0), "v"(a1), B(b1), "v"(a2), B(b2), "v"(a3), B(b3), "i"(TAIL))
  if constexpr (!BF) MF4_("f16", "v"); else if constexpr (AG == 0) MF4_("bf16", "v"); else MF4_("bf16", "a");
}
template <bool BF, bool CONJ, class MM> DI u32x4 cplx_under(f32x4 r, f32x4 i, const f32x2 (&c)[2], const f32x2 (&s)[2], MM&& mm) {
  asm volatile("s_nop 1");
  SB(); mm(std::integral_constant<int, 0>{}); SB();
  f32x2 a0 = lo2(i) * s[0], b0 = lo2(r) * s[0], a1 = hi2(i) * s[1];
  PIN(a0); PIN(b0); PIN(a1);
  SB(); mm(std::integral_constant<int, 1>{}); SB();
  f32x2 b1 = hi2(r) * s[1], re0 = CONJ ? lo2(r) * c[0] + a0 : lo2(r) * c[0] - a0, im0 = CONJ ? lo2(i) * c[0] - b0 : lo2(i) * c[0] + b0;
  PIN(b1); PIN(re0); PIN(im0);
  SB(); mm(std::integral_constant<int, 2>{}); SB();
  f32x2 re1 = CONJ ? hi2(r) * c[1] + a1 : hi2(r) * c[1] - a1, im1 = CONJ ? hi2(i) * c[1] - b1 : hi2(i) * c[1] + b1;
  u32 o0 = pkc<BF>(re0.x, im0.x);
  PIN(re1); PIN(im1); PIN(o0);
  SB(); mm(std::integral_constant<int, 3>{}); SB();
  return u32x4{o0, pkc<BF>(re0.y, im0.y), pkc<BF>(re1.x, im1.x), pkc<BF>(re1.y, im1.y)};
}
template <bool BF> DI u32x4 twiddle(f32x4 r, f32x4 i, const f32x2 (&c)[2], const f32x2 (&s)[2]) {
  const f32x2 re0 = lo2(r) * c[0] + lo2(i) * s[0], im0 = lo2(i) * c[0] - lo2(r) * s[0];
  const f32x2 re1 = hi2(r) * c[1] + hi2(i) * s[1], im1 = hi2(i) * c[1] - hi2(r) * s[1];
  return u32x4{pkc<BF>(re0.x, im0.x), pkc<BF>(re0.y, im0.y), pkc<BF>(re1.x, im1.x), pkc<BF>(re1.y, im1.y)};
}
DI u32x4 cmul_b(f32x4 r, f32x4 i, const f32x2 (&c)[2], const f32x2 (&s)[2]) {
  const f32x2 re0 = lo2(r) * c[0] - lo2(i) * s[0], im0 = lo2(i) * c[0] + lo2(r) * s[0];
  const f32x2 re1 = hi2(r) * c[1] - hi2(i) * s[1], im1 = hi2(i) * c[1] + hi2(r) * s[1];
  return u32x4{pkb(re0.x, im0.x), pkb(re0.y, im0.y), pkb(re1.x, im1.x), pkb(re1.y, im1.y)};
}
DI float cosr(float a) { return __builtin_amdgcn_cosf(a); }
DI float sinr(float a) { return __builtin_amdgcn_sinf(a); }
template <int CTRL> DI float dppf(float x) {
  return __builtin_bit_cast(float, __builtin_amdgcn_mov_dpp(__builtin_bit_cast(int, x), CTRL, 0xf, 0xf, true));
}
DI f32x2 bcast2(float x) {
  auto t = __builtin_amdgcn_permlane32_swap(__float_as_uint(x), __float_as_uint(x), false, false);
  return f32x2{__uint_as_float(t[0]), __uint_as_float(t[1])};
}
DI void ln_coef(f32x2 S, f32x2 Q, float ch, f32x2& R, f32x2& O) {
  auto t = __builtin_amdgcn_permlane32_swap(__float_as_uint(S.x), __float_as_uint(S.y), false, false);
  auto u = __builtin_amdgcn_permlane32_swap(__float_as_uint(Q.x), __float_as_uint(Q.y), false, false);
  const float s = __uint_as_float(t[0]) + __uint_as_float(t[1]), q = __uint_as_float(u[0]) + __uint_as_float(u[1]);
  auto z = __builtin_amdgcn_permlane16_swap(__float_as_uint(s), __float_as_uint(q), false, false);
  float x = __uint_as_float(z[0]) + __uint_as_float(z[1]);
  x += dppf<0xB1>(x);
  x += dppf<0x4E>(x);
  x += dppf<0x141>(x);
  x += dppf<0x140>(x);
  auto y = __builtin_amdgcn_permlane16_swap(__float_as_uint(x), __float_as_uint(x), false, false);
  const float md = __uint_as_float(y[0]) * (1.0f / 512.0f);
  const float var = relu(__uint_as_float(y[1]) * (1.0f / 512.0f) - md * md);
  const float r = __builtin_amdgcn_rcpf(bfr(__builtin_amdgcn_sqrtf(bfr(bfr(var) + 1e-5f))));
  const float o = (ch - bfr(ch + md)) * r;
  R = bcast2(r); O = bcast2(o);
}
DI float first_lane(float x) { return __uint_as_float(__builtin_amdgcn_readfirstlane(__float_as_uint(x))); }
DI int row_of_pos(int pos, int j) {
  const int c = 8 * (pos >> 7) + 2 * ((pos >> 3) & 3) + ((pos >> 5) & 1), q = 8 * ((pos >> 6) & 1) + (pos & 7);
  return 32 * (2 * c + (j & 1)) + 2 * q + (j >> 1);
}

template <int I> DI void ld4(u32x4& d, u32 v, const u8* s) { asm volatile("global_load_dwordx4 %0, %1, %2 offset:%3 sc1" : "=v"(d) : "v"(v), "s"(s), "i"(I) : "memory"); }
template <int I> DI void ld4a(u32x4& d, u32 v, const u8* s) { asm volatile("global_load_dwordx4 %0, %1, %2 offset:%3 sc1" : "=a"(d) : "v"(v), "s"(s), "i"(I) : "memory"); }
template <int I> DI void ld4r(u32x4& d, u32 v, const u8* s) { asm volatile("global_load_dwordx4 %0, %1, %2 offset:%3 sc1" : "+v"(d) : "v"(v), "s"(s), "i"(I) : "memory"); }
template <int I> DI void ld2(u32x2& d, u32 v, const u8* s) { asm volatile("global_load_dwordx2 %0, %1, %2 offset:%3 sc1" : "=v"(d) : "v"(v), "s"(s), "i"(I) : "memory"); }
template <int I> DI void ld2r(u32x2& d, u32 v, const u8* s) { asm volatile("global_load_dwordx2 %0, %1, %2 offset:%3 sc1" : "+v"(d) : "v"(v), "s"(s), "i"(I) : "memory"); }
template <int I> DI void ld2p(u32x2& d, u32 v, const u8* s) { asm volatile("global_load_dwordx2 %0, %1, %2 offset:%3" : "=v"(d) : "v"(v), "s"(s), "i"(I) : "memory"); }
DI void touch(u32& d, u32 v, const u8* s) { asm volatile("global_load_dword %0, %1, %2 sc1" : "+v"(d) : "v"(v), "s"(s) : "memory"); }
template <int I> DI void ld1p(u32& d, u32 v, const u8* s) { asm volatile("global_load_dword %0, %1, %2 offset:%3" : "=v"(d) : "v"(v), "s"(s), "i"(I) : "memory"); }
DI void ld1s(u32& d, u32 v, const u8* s) { asm volatile("global_load_ushort %0, %1, %2" : "=v"(d) : "v"(v), "s"(s) : "memory"); }
template <int N> DI void vmwait() { static_assert(N >= 0 && N < 64, ""); asm volatile("s_waitcnt vmcnt(%0)" ::"i"(N) : "memory"); }
DI void set_m0(u32 ldsaddr) { asm volatile("s_mov_b32 m0, %0" ::"s"(ldsaddr) : "memory"); }
template <int I> DI void dma1(u32 voff, const u8* src) {
  asm volatile("global_load_lds_dwordx4 %0, %1 offset:%2 sc1" ::"v"(voff), "s"(src), "i"(I) : "memory");
}
DI void dma4(u32 voff, const u8* src, u32 ldsaddr) {
  asm volatile("s_mov_b32 m0, %2\n\ts_nop 0\n\tglobal_load_lds_dwordx4 %0, %1 sc1\n\tglobal_load_lds_dwordx4 %0, %1 offset:1024 sc1\n\t"
               "global_load_lds_dwordx4 %0, %1 offset:2048 sc1\n\tglobal_load_lds_dwordx4 %0, %1 offset:3072 sc1" ::"v"(voff), "s"(src), "s"(ldsaddr) : "memory");
}
#define RETRY(COND, ...)                                                      \
  do {                                                                        \
    bool bad_ = (COND);                                                       \
    if (__builtin_expect(__any(bad_), 0)) {                                   \
      _Pragma("nounroll") for (int sp_ = 0; sp_ < SPIN_CAP; ++sp_) {          \
        __VA_ARGS__                                                           \
        bad_ = (COND);                                                        \
        if (!__any(bad_)) break;                                              \
      }                                                                       \
    }                                                                         \
  } while (0)
DI void bar() { asm volatile("s_waitcnt lgkmcnt(0)\n\ts_barrier" ::: "memory"); }
DI void bar_nw() { asm volatile("s_barrier" ::: "memory"); }

struct RowC { u32x4 b1re, b1im, b2re[2][2], b2im[2][2]; f32x2 twc[2][2], tws[2][2]; };
template <bool INV> DI void pair_cs(float c, float s, u32& re, u32& im) {
  if constexpr (INV) { im = pkb(s, c); re = pkb_n1(c, s, im); } else { re = pkh(c, s); im = pkh_n0(s, c, re); }
}
template <bool AG = false> DI void sign4(u32x4 base, u32 lm, u32x4& f00, u32x4& f01, u32x4& f10, u32x4& f11) {
  f00 = base;
  f01 = base ^ lm;
  f10 = base ^ u32x4{0u, 0x80008000u, 0u, 0x80008000u};
  f11 = f10 ^ lm;
  if constexpr (AG) { PINA(f00); PINA(f01); PINA(f10); PINA(f11); } else { PIN(f00); PIN(f01); PIN(f10); PIN(f11); }
}
template <bool INV, int PART> DI void row_consts(RowC& k, int m, int g) {
  const float fm = (float)m, fg = (float)(4 * g);
  if constexpr (PART == 0) {
    u32x4 br, bi;
#pragma unroll
    for (int i = 0; i < 4; ++i) {
      const float a = (fg + (float)i) * (fm * (1.0f / 16.0f));
      u32 x, y;
      pair_cs<INV>(cosr(a), sinr(a), x, y); k.b1re[i] = x; k.b1im[i] = y;
      const float a2 = (fg + (float)i) * (fm * (1.0f / 32.0f));
      pair_cs<INV>(cosr(a2), sinr(a2), x, y); br[i] = x; bi[i] = y;
    }
    PIN(k.b1re); PIN(k.b1im);
    const u32 lm = (m & 1) ? 0x80008000u : 0u;
    sign4(br, lm, k.b2re[0][0], k.b2re[0][1], k.b2re[1][0], k.b2re[1][1]);
    sign4(bi, lm, k.b2im[0][0], k.b2im[0][1], k.b2im[1][0], k.b2im[1][1]);
  } else {
#pragma unroll
    for (int e = 0; e < 2; ++e)
#pragma unroll
      for (int i = 0; i < 4; ++i) {
        const float a = (fg + (float)(16 * e + i)) * (fm * (INV ? -1.0f / 512.0f : 1.0f / 512.0f));
        k.twc[e][i >> 1][i & 1] = cosr(a); k.tws[e][i >> 1][i & 1] = sinr(a);
      }
#pragma unroll
    for (int e = 0; e < 2; ++e) { PIN(k.twc[e][0]); PIN(k.twc[e][1]); PIN(k.tws[e][0]); PIN(k.tws[e][1]); }
  }
}
template <bool INV, int TAIL> DI void row_fft(const RowC& k, u32x4 a0, u32x4 a1, f32x4& dr0, f32x4& di0, f32x4& dr1, f32x4& di1) {
  f32x4 tr0, ti0, tr1, ti1;
  mm4<INV>(tr0, ti0, tr1, ti1, a0, a1, k.b1re, k.b1im);
  u32x4 p0 = twiddle<INV>(tr0, ti0, k.twc[0], k.tws[0]);
  PIN(p0);
  const u32x4 p1 = cplx_under<INV, true>(tr1, ti1, k.twc[1], k.tws[1], [&](auto I) {
    constexpr int i = decltype(I)::value;
    mf0<INV, 0>(i == 0 ? dr0 : i == 1 ? di0 : i == 2 ? dr1 : di1, p0, i == 0 ? k.b2re[0][0] : i == 1 ? k.b2im[0][0] : i == 2 ? k.b2re[1][0] : k.b2im[1][0]);
  });
  mf4<INV, 0, TAIL>(dr0, di0, dr1, di1, p1, k.b2re[0][1], p1, k.b2im[0][1], p1, k.b2re[1][1], p1, k.b2im[1][1]);
}

struct FwdP { u32 iw, ir, ow, ap0, ap1, ar0, ar1, zo; f32x2 cB, cD, eA, eB, eC, eD; };
DI void fwd_pre(FwdP& p, int tid, int w, int pos) {
  const int m = tid & 15, g = (tid >> 4) & 3, ph = tid >> 1, fq = ph ? 512 - ph : 256, fq1 = 384 - ph;
  p.iw = w * 5120 + ((16 * (m & 1) + 4 * g) * 20 + (m >> 1)) * 4; p.ir = w * 5120 + (m * 20 + 4 * g) * 4;
  p.ap0 = (ph + (ph >> 4) * 4) * 4; p.ap1 = p.ap0 + 640; p.ar0 = (fq + (fq >> 4) * 4) * 4; p.ar1 = (fq1 + (fq1 >> 4) * 4) * 4;
  p.zo = OFF_A + pos * 8192 + tid * 16;
  const float sc = 1.0f / 64.0f, sg = (tid & 1) ? -sc : sc;
  p.cB = f32x2{sg, -sg}; p.cD = f32x2{sg, sg};
  p.eA = ph ? f32x2{sc, sc} : f32x2{2 * sc, 0.f}; p.eB = ph ? p.cB : f32x2{2 * sg, 0.f};
  p.eC = ph ? f32x2{sc, -sc} : f32x2{0.f, 2 * sg}; p.eD = ph ? p.cD : f32x2{0.f, 2 * sc};
  PIN(p.iw); PIN(p.ir); PIN(p.ap0); PIN(p.ap1); PIN(p.ar0); PIN(p.ar1); PIN(p.zo);
  PIN(p.cB); PIN(p.cD); PIN(p.eA); PIN(p.eB); PIN(p.eC); PIN(p.eD);
}
struct ColC {
  u32x4 b1re[2], b1im[2], a2re[2][2], a2im[2][2], b3re[2][2], b3im[2][2], a4re[2][2], a4im[2][2];
  f32x2 t1c[2][2], t1s[2][2], t2c[2][2], t2s[2][2];
};
template <int PART> DI void col_consts(ColC& k, int m, int g, int w) {
  const int pi_ = w >> 1, kt = w & 1;
  const float fm = (float)m, fg = (float)(4 * g);
  const u32 lm = (m & 1) ? 0x80008000u : 0u;
  if constexpr (PART == 0) {
    const float fk = (float)(2 * (16 * kt + m) + pi_);
#pragma unroll
    for (int u = 0; u < 2; ++u) {
#pragma unroll
      for (int r = 0; r < 4; ++r) {
        const float n = fg + (float)(16 * u + r), a = n * (fk * (1.0f / 64.0f)), a2 = n * (fk * (1.0f / 2048.0f));
        u32 x, y;
        pair_cs<false>(cosr(a), sinr(a), x, y); k.b1re[u][r] = x; k.b1im[u][r] = y;
        k.t1c[u][r >> 1][r & 1] = cosr(a2); k.t1s[u][r >> 1][r & 1] = sinr(a2);
      }
      PIN(k.b1re[u]); PIN(k.b1im[u]); PIN(k.t1c[u][0]); PIN(k.t1c[u][1]); PIN(k.t1s[u][0]); PIN(k.t1s[u][1]);
    }
  } else if constexpr (PART == 1) {
    u32x4 fr, fi, ir, ii;
#pragma unroll
    for (int r = 0; r < 4; ++r) {
      const float a = (fg + (float)r) * (fm * (1.0f / 32.0f)), c = cosr(a), s = sinr(a);
      u32 x, y;
      pair_cs<false>(c, s, x, y); fr[r] = x; fi[r] = y;
      pair_cs<true>(c, s, x, y); ir[r] = x; ii[r] = y;
    }
    sign4(fr, lm, k.a2re[0][0], k.a2re[0][1], k.a2re[1][0], k.a2re[1][1]);
    sign4(fi, lm, k.a2im[0][0], k.a2im[0][1], k.a2im[1][0], k.a2im[1][1]);
    sign4(ir, lm, k.b3re[0][0], k.b3re[0][1], k.b3re[1][0], k.b3re[1][1]);
    sign4(ii, lm, k.b3im[0][0], k.b3im[0][1], k.b3im[1][0], k.b3im[1][1]);
  } else if constexpr (PART == 2) {
    const float kb = (float)(32 * kt + pi_) + 2.0f * fg;
#pragma unroll
    for (int u = 0; u < 2; ++u) {
#pragma unroll
      for (int r = 0; r < 4; ++r) {
        const float a = (kb + (float)(2 * r)) * ((fm + (float)(16 * u)) * (-1.0f / 2048.0f));
        k.t2c[u][r >> 1][r & 1] = cosr(a); k.t2s[u][r >> 1][r & 1] = sinr(a);
      }
      PIN(k.t2c[u][0]); PIN(k.t2c[u][1]); PIN(k.t2s[u][0]); PIN(k.t2s[u][1]);
    }
  } else {
    const float fn = (fm + (float)(16 * (w >> 1))) * (1.0f / 64.0f);
#pragma unroll
    for (int u = 0; u < 2; ++u) {
#pragma unroll
      for (int r = 0; r < 4; ++r) {
        const float a = (2.0f * fg + (float)(2 * r + u)) * fn;
        u32 x, y;
        pair_cs<true>(cosr(a), sinr(a), x, y); k.a4re[u][0][r] = x; k.a4im[u][0][r] = y;
      }
      k.a4re[u][1] = k.a4re[u][0] ^ lm; k.a4im[u][1] = k.a4im[u][0] ^ lm;
      PIN(k.a4re[u][0]); PIN(k.a4re[u][1]); PIN(k.a4im[u][0]); PIN(k.a4im[u][1]);
    }
  }
}

DI void transpose8x4(const u32x2 (&v)[8], u32x4 (&o)[4]) {
#pragma unroll
  for (int jj = 0; jj < 4; ++jj) {
    o[0][jj] = __builtin_amdgcn_perm(v[2 * jj + 1].x, v[2 * jj].x, 0x05040100u);
    o[1][jj] = __builtin_amdgcn_perm(v[2 * jj + 1].x, v[2 * jj].x, 0x07060302u);
    o[2][jj] = __builtin_amdgcn_perm(v[2 * jj + 1].y, v[2 * jj].y, 0x05040100u);
    o[3][jj] = __builtin_amdgcn_perm(v[2 * jj + 1].y, v[2 * jj].y, 0x07060302u);
  }
}
template <int H> DI void prep_load1(const Params& P, int wt, int lane, u32x2 (&v)[8]) {
  const u32 vo = (lane < 56 ? lane : 55) * 8, vo2 = vo + 7168;
  const int piece = (wt >> 3) & 63;
  const u8* src = (const u8*)((wt >> 9) ? P.wu : P.wg) + opq(wt & 7) * 448 + opq(4 * piece) * 3584;
  static_for<4>([&](auto J) { constexpr int j = decltype(J)::value; ld2p<(j & 1) * 3584>(v[4 * H + j], j & 2 ? vo2 : vo, src + H * (256 * 3584)); });
}
template <int H> DI void prep_load2(const Params& P, int wt, int lane, u32 (&v)[8]) {
  const int piece = wt >> 2, g = piece & 3, kq = piece >> 2, wv = kq / 14, ks = kq % 14;
  const int f0 = ks < 4 ? 112 * (4 * wv + ks) : ks < 6 ? 112 * (4 * wv + 2 * (ks - 4)) + 32 : ks < 10 ? 112 * (4 * wv + ks - 6) + 48 : 112 * (4 * wv + ks - 10) + 80;
  const int f1 = f0 + ((ks == 4 || ks == 5) ? 112 : 16);
  const u8* s0 = (const u8*)P.wd + (wt & 3) * 256 + (4 * g + (H ? f1 : f0)) * 1024;
  static_for<4>([&](auto J) { constexpr int j = decltype(J)::value; ld1p<j * 1024>(v[4 * H + j], lane * 4, s0); });
}
DI u32 prep_dst1(int wt, int tile) {
  const int piece = (wt >> 3) & 63, mat = wt >> 9, hi = tile >= 7, p = tile - 7 * hi, ks = piece >> 2;
  const u32 frag = (p < 3 ? ks * 6 + 2 * p : p < 5 ? 96 + ks * 4 + 2 * (p - 3) : 160 + ks * 4 + 2 * (p - 5)) + mat;
  return OFF_W1 + ((2 * (wt & 7) + hi) * 224 + frag) * 1024 + (piece & 3) * 256;
}
DI u32 prep_dst2(int wt, int tile) {
  const int piece = wt >> 2, tl = (wt & 3) * 8 + tile, rot = (tl >> 2) & 3, kq = piece >> 2, ks = kq % 14;
  const int slot = ks < 4 ? (ks - rot) & 3 : ks < 6 ? ks ^ (rot & 1) : ks < 10 ? 6 + ((ks - 6 - rot) & 3) : 10 + ((ks - 10 - rot) & 3);
  return OFF_W2 + (((tl >> 2) * 56 + kq - ks + slot) * 4 + (tl & 3)) * 1024 + (piece & 3) * 256;
}

constexpr int NTOK0 = 16, NRES = 40, NEARLY = 32;
constexpr int g1_job(int L, int x) {
  if (((x >= 34 && x < 96) && (x & 3) == 2) || (x >= 98 && x < 114 && !(x & 1))) return 2;
  if ((x < 42 && x % 3 == 0) || x == 41 || x == 44) return 1;
  if (L == 1 && x >= 47 && x < 60 && (x & 3) == 3) return 6;
  if (x >= 201) return (x - 201) % 3 != 2 ? 4 : 0;
  if (x >= 164) return ((x - 164) % 7 == 0 || (x - 164) % 7 == 2 || (x - 164) % 7 == 5) ? 4 : 0;
  if (x >= 116 && x % 3 == 2) return 5;
  if (L == 0 && ((x < 34 && x % 3 == 1) || (x >= 48 && x < 98 && (x & 3) == 0) || (x >= 114 && x % 3 == 0) || (x >= 49 && x < 98 && x % 12 == 1)))
    return 3;
  return 0;
}
constexpr int NEPI1 = 124, NEPI2 = 82;
struct G1Sched {
  int job[224], idx[224], epi[225], nst[224], cum[225], pos[7][64], cnt[7];
  constexpr G1Sched(int L) : job{}, idx{}, epi{}, nst{}, cum{}, pos{}, cnt{} {
    cnt[1] = NTOK0;
    for (int i = 0; i < NTOK0; ++i) pos[1][i] = i;
    cum[0] = NTOK0;
    for (int x = 0; x < 224; ++x) {
      int j = g1_job(L, x);
      if ((j == 3 && cnt[3] >= NRES) || (j == 4 && cnt[4] >= NEARLY)) j = 0;
      job[x] = j; idx[x] = cnt[j];
      if (x == 161) epi[x] = NEPI1;
      const bool on = (x >= 97 && x < 160 && epi[x] < NEPI1) || x >= 161;
      epi[x + 1] = on ? epi[x] + (j ? 2 : 3) : epi[x];
      if (x < 160 && epi[x + 1] > NEPI1) epi[x + 1] = NEPI1;
      for (int n = epi[x]; n < epi[x + 1]; ++n)
        nst[x] += n < NEPI1 ? (n == 40 || n == 81 || n == 102 || n == 123) : (n < NEPI1 + NEPI2 && (n - NEPI1) % 41 == 40);
      if (j) pos[j][cnt[j]++] = cum[x] + nst[x];
      cum[x + 1] = cum[x] + nst[x] + (j ? 1 : 0);
    }
  }
};
constexpr G1Sched G1S[2] = {G1Sched(0), G1Sched(1)};
static_assert(G1S[0].cnt[1] == 32 && G1S[0].cnt[2] == 24 && G1S[0].cnt[3] == NRES && G1S[0].cnt[4] == NEARLY && G1S[0].cnt[5] == 16, "");
static_assert(G1S[1].cnt[6] == 4 && G1S[1].pos[6][3] < G1S[1].pos[2][7], "");
static_assert(G1S[0].idx[66] == 8 && G1S[0].idx[98] == 16 && G1S[0].epi[113] <= 40 && G1S[1].epi[113] <= 40, "");
static_assert(G1S[0].epi[152] == NEPI1 && G1S[0].epi[204] >= NEPI1 + NEPI2, "");
constexpr int clamp63(int n) { return n > 63 ? 63 : n; }
constexpr int g1_wait(int L, int x, int job, int idx) { return clamp63(G1S[L].cum[x] - G1S[L].pos[job][idx] - 1); }

#define FRESH()                                                                                                   \
  int tid = threadIdx.x, b = blockIdx.x, w = w_;                                                                   \
  u8* ws = P.ws;                                                                                                   \
  asm volatile("" : "+v"(tid), "+s"(b), "+s"(w), "+s"(ws));                                                        \
  const int lane = tid & 63, m = lane & 15, g = lane >> 4, half2 = b & 1, ct = b >> 5;                             \
  const int G = ((b >> 1) & 3) * 4 + ((b >> 3) & 3), member = half2 * 8 + ct, pos = G * 16 + member;               \
  const u32 l16 = lane * 16, t16 = tid * 16, xo = 32 * m + 8 * g;                                                  \
  auto fill_h = [&](u32 base, int q) {                                                                             \
    const int i = q * 64 + lane;                                                                                   \
    st4(sent4, base + (8 * G + 2 * w + (i >= 224)) * 57344 + member * 3584 + (i >= 224 ? i - 224 : i) * 16);       \
  };                                                                                                               \
  auto fill_rows = [&](u32 base) { st4(sent4, base + pos * 8192 + t16); st4(sent4, base + pos * 8192 + 4096 + t16); }; \
  auto ring4 = [&](int q) {                                   \
    const u32 o = (q >> 1) * 32768 + w * 8192 + (q & 1) * 4096;                                                    \
    dma4(l16, ws + OFF_W1 + member * 229376 + o, ldsbase + o);                                                     \
  };                                                                                                               \
  (void)m; (void)g; (void)half2; (void)ct; (void)l16; (void)t16; (void)xo; (void)fill_h; (void)fill_rows; (void)ring4

__global__ __launch_bounds__(256, 1) void k_all(Params P) {
  extern __shared__ __attribute__((aligned(16))) u8 lds[];
  ST_DECL;
  const int w_ = __builtin_amdgcn_readfirstlane(threadIdx.x >> 6);
  const __amdgpu_buffer_rsrc_t rs = __builtin_amdgcn_make_buffer_rsrc(P.ws, 0, WS_USED, 0x00020000);
  const u32 ldsbase = (u32)(size_t)(__attribute__((address_space(3))) u8*)lds;
  const u32x4 sent4 = {SENT, SENT, SENT, SENT};
  auto st4 = [&](u32x4 v, u32 off) { __builtin_amdgcn_raw_buffer_store_b128(v, rs, off, 0, 16); };
  auto st2 = [&](u32x2 v, u32 off) { __builtin_amdgcn_raw_buffer_store_b64(v, rs, off, 0, 16); };
  u32x2 xl[2], xh[2], pv0[8];
  u32 pv1[8];
  u32 wfr[8], wfi[8], wn[8];
  f32x2 xv[8], xc;
  float xch;
  FwdP fp;
  u32 pd[6];
  u32x2 ep;
  u32 cpf = 0;
  RowC rc;
  ColC cc;
  ST(0);
  {
    FRESH();
    const u8* xp = (const u8*)P.x + row_of_pos(pos, w) * 1024;
    ld2p<0>(xl[0], xo, xp); ld2p<512>(xl[1], xo, xp); ld2p<0>(xh[0], xo, xp + 1024 * 1024); ld2p<512>(xh[1], xo, xp + 1024 * 1024);
    { const u32 z = 0; ld2<0>(ep, z, ws); }
    {
      const u8* pc;
      asm volatile("s_getpc_b64 %0" : "=s"(pc));
      const u32 o = (((b >> 3) * 4 + w) * 8 + (lane & 7)) * 128;
      touch(cpf, o < CODE_BYTES ? o : 0, pc);
    }
    XS(32);
    fill_rows(OFF_A); fill_rows(OFF_WX);
    if (tid < 8) st4(sent4, OFF_WNY + tid * 4096 + pos * 16);
    SB();
    row_consts<false, 0>(rc, m, g);
    row_consts<false, 1>(rc, m, g);
    SB();
    XS(33);
    const int kb = 2 * (16 * ((pos >> 6) & 1) + (pos & 15)) + (pos >> 7) + 256 * ((pos >> 4) & 3);
    static_for<8>([&](auto J) {
      constexpr int j = decltype(J)::value;
      const u32 o = tid * 2 + (kb + 64 * (16 * (j >> 2) + (j & 3))) * 514;
      ld1s(wfr[j], o, (const u8*)P.wr); ld1s(wfi[j], o, (const u8*)P.wi);
    });
    {
      const int k = kb + 64 * (16 * ((lane >> 2) & 1) + (lane & 3)), km = (2048 - k) & 2047;
      const u32 o = k * 514, om = km * 514;
      ld1s(wn[0], o, (const u8*)P.wr); ld1s(wn[1], o, (const u8*)P.wi); ld1s(wn[2], om, (const u8*)P.wr); ld1s(wn[3], om, (const u8*)P.wi);
      ld1s(wn[4], o, (const u8*)P.wr + 512); ld1s(wn[5], o, (const u8*)P.wi + 512); ld1s(wn[6], om, (const u8*)P.wr + 512); ld1s(wn[7], om, (const u8*)P.wi + 512);
    }
    XS(34);
    fwd_pre(fp, tid, w, pos);
#pragma unroll
    for (int x = 0; x < 4; ++x) pd[x] = x < 3 || g < 2 ? prep_dst1(b * 4 + w, 4 * x + g) + m * 16 : WS_USED;
#pragma unroll
    for (int x = 0; x < 2; ++x) pd[4 + x] = b < 224 ? prep_dst2(b * 4 + w, 4 * x + g) + m * 16 : WS_USED;
#pragma unroll
    for (int x = 0; x < 6; ++x) PIN(pd[x]);
    SB();
    XS(35);
    vmwait<1 + 4 + 24>();
    XS(36);
    PIN(ep); PIN(xl[0]); PIN(xl[1]); PIN(xh[0]); PIN(xh[1]);
    { const u64 e = (((u64)(u32)__builtin_amdgcn_readfirstlane(ep.y) << 32) | (u32)__builtin_amdgcn_readfirstlane(ep.x)) + 1; ep = u32x2{(u32)e, (u32)(e >> 32)}; }
#pragma unroll
    for (int t = 0; t < 2; ++t)
#pragma unroll
      for (int p = 0; p < 2; ++p) {
        xv[4 * t + 2 * p] = f32x2{blo(xl[t][p]), blo(xh[t][p])};
        xv[4 * t + 2 * p + 1] = f32x2{bhi(xl[t][p]), bhi(xh[t][p])};
      }
    xc = f32x2{first_lane(xv[0].x), first_lane(xv[0].y)};
    xch = lane < 32 ? xc.x : xc.y;
#pragma unroll
    for (int i = 0; i < 8; ++i) xv[i] -= xc;
  }
  ST(1);

  u32x4 w2a[10][4];

  auto layer = [&](auto LC) {
    constexpr int L = decltype(LC)::value;
    constexpr int S0 = 2 + 16 * L;
    {
      FRESH();
      const f32x2 S = ((xv[0] + xv[1]) + (xv[2] + xv[3])) + ((xv[4] + xv[5]) + (xv[6] + xv[7]));
      f32x2 q0 = xv[0] * xv[0], q1 = xv[1] * xv[1];
#pragma unroll
      for (int i = 2; i < 8; i += 2) { q0 += xv[i] * xv[i]; q1 += xv[i + 1] * xv[i + 1]; }
      if constexpr (L == 0) { SB(); fill_rows(OFF_U); SB(); }
      f32x2 R, O;
      XF(32);
      ln_coef(S, q0 + q1, xch, R, O);
      XF(33);
      u8* img = lds + LDS_SCR;
      if constexpr (L == 0) {
        SB(); vmwait<26>(); PIN(cpf);
        if (lane == 0) st2(ep, OFF_FLAG + (w * 256 + pos) * 8);
        prep_load1<0>(P, b * 4 + w, lane, pv0); SB();
      }
#pragma unroll
      for (int i = 0; i < 8; ++i) {
        const f32x2 v = xv[i] * R + O;
        *(u32*)(img + fp.iw + ((i & 3) * 20 + 8 * (i >> 2)) * 4) = pkh(v.x, v.y);
      }
      const u32x4 a0 = *(const u32x4*)(img + fp.ir), a1 = *(const u32x4*)(img + fp.ir + 1280);
      if constexpr (L == 0) prep_load1<1>(P, b * 4 + w, lane, pv0); else ring4(1);
      const float* p0 = (const float*)(img + fp.ap0);
      const float* p1 = (const float*)(img + fp.ap1);
      const float* r0 = (const float*)(img + fp.ar0);
      const float* r1 = (const float*)(img + fp.ar1);
      const f32x2 cA = {1.0f / 64.0f, 1.0f / 64.0f}, cC = {1.0f / 64.0f, -1.0f / 64.0f}, cB = fp.cB, cD = fp.cD, eA = fp.eA, eB = fp.eB, eC = fp.eC, eD = fp.eD;
      const u32 zo = fp.zo;
      f32x4 dr0, di0, dr1, di1;
      XF(34);
      row_fft<false, 6>(rc, a0, a1, dr0, di0, dr1, di1);
      XF(35);
      SB();
      float* op = (float*)(img + fp.ir);
      *(f32x4*)(op) = dr0; *(f32x4*)(op + 640) = di0;
      SB();
      *(f32x4*)(op + 320) = dr1; *(f32x4*)(op + 960) = di1;
      if constexpr (L == 0) { prep_load2<0>(P, (b < 224 ? b : 0) * 4 + w, lane, pv1); vmwait<13>(); }
      bar();
      XF(36);
      f32x2 pa[4], qa[4], pb[4], qb[4];
#pragma unroll
      for (int j = 0; j < 4; ++j) {
        pa[j] = f32x2{p0[j * 1280], p0[j * 1280 + 640]}; qa[j] = f32x2{r0[j * 1280], r0[j * 1280 + 640]};
      }
#pragma unroll
      for (int j = 0; j < 4; ++j) {
        pb[j] = f32x2{p1[j * 1280], p1[j * 1280 + 640]}; qb[j] = f32x2{r1[j * 1280], r1[j * 1280 + 640]};
      }
      if constexpr (L == 0) prep_load2<1>(P, (b < 224 ? b : 0) * 4 + w, lane, pv1);
      SB();
      u32x4 o;
#pragma unroll
      for (int j = 0; j < 4; ++j) {
        const f32x2 v = eD * swp(qa[j]) + (eC * qa[j] + (eB * swp(pa[j]) + eA * pa[j]));
        o[j] = mark<(L == 0 ? 0x10001u : 0u), (L == 0 ? 0u : 0x10000u)>(pkh(v.x, v.y));
      }
      st4(o, zo);
#pragma unroll
      for (int j = 0; j < 4; ++j) {
        const f32x2 v = cD * swp(qb[j]) + (cC * qb[j] + (cB * swp(pb[j]) + cA * pb[j]));
        o[j] = mark<(L == 0 ? 0x10001u : 0u), (L == 0 ? 0u : 0x10000u)>(pkh(v.x, v.y));
      }
      st4(o, zo + 4096);
      SB();
      if constexpr (L == 1) {
#pragma unroll
        for (int t = 0; t < 2; ++t)
#pragma unroll
          for (int p = 0; p < 2; ++p) {
            const f32x2 v0 = xv[4 * t + 2 * p] + xc, v1 = xv[4 * t + 2 * p + 1] + xc;
            xl[t][p] = pkb(v0.x, v1.x); xh[t][p] = pkb(v0.y, v1.y);
          }
        PIN(xl[0]); PIN(xl[1]); PIN(xh[0]); PIN(xh[1]);
      }
    }
    if constexpr (L == 0) {
      XS(37);
      FRESH();
#pragma unroll
      for (int j = 0; j < 8; ++j) { PIN(wfr[j]); PIN(wfi[j]); PIN(wn[j]); }
      u8* bw = lds + w * 2048;
      u32x4 v0, v1;
#pragma unroll
      for (int j = 0; j < 4; ++j) { v0[j] = wfr[j] | (wfi[j] << 16); v1[j] = wfr[4 + j] | (wfi[4 + j] << 16); }
      *(u32x4*)(bw + lane * 32) = v0; *(u32x4*)(bw + lane * 32 + 16) = v1;
      st4(*(const u32x4*)(bw + l16), OFF_WX + pos * 8192 + w * 2048 + l16);
      st4(*(const u32x4*)(bw + 1024 + l16), OFF_WX + pos * 8192 + w * 2048 + 1024 + l16);
      const float hr = blo(wn[0]) + blo(wn[2]), hi = blo(wn[1]) - blo(wn[3]), nr = blo(wn[4]) + blo(wn[6]), ni = blo(wn[5]) - blo(wn[7]);
      const f32x4 ab = {0.25f * (hr + nr), 0.25f * (hi + ni), 0.25f * (hr - nr), 0.25f * (hi - ni)};
      if (tid < 8) st4(__builtin_bit_cast(u32x4, ab), OFF_WNY + tid * 4096 + pos * 16);
    }
    ST(S0);
    {
      FRESH();
      const int phi = pos, pi_ = w >> 1, kt = w & 1;
      const int k1l = 2 * (16 * kt + m) + pi_;
      auto consts = [&](auto PC) { col_consts<decltype(PC)::value>(cc, m, g, w); SB(); };
      using I0 = std::integral_constant<int, 0>; using I1 = std::integral_constant<int, 1>;
      using I2 = std::integral_constant<int, 2>; using I3 = std::integral_constant<int, 3>;
      u8* const bw = lds + 16384 + w * 8192;
      const u32 zo = (tid >> 1) * 8192 + (tid & 1) * 16;
      const u8* zb0 = ws + OFF_A + phi * 32;
      const u8* zb1 = zb0 + 128 * 8192;
      const u8* wb0 = ws + OFF_WX + phi * 32;
      const u8* wb1 = wb0 + 128 * 8192;
      const u32 wo = zo;
      PINS(zb0); PINS(zb1); PINS(wb0); PINS(wb1);
      if constexpr (L == 0) {
        u32x4 fl0, fl1;
        const u32 o = lane * 32;
        const u8* fb = ws + OFF_FLAG + ((phi >> 5) & 3) * 2048;
        ld4<0>(fl0, o, fb); ld4<16>(fl1, o, fb);
        SB();
        consts(I0{});
        consts(I1{});
        XS(38);
        vmwait<0>(); PIN(fl0); PIN(fl1);
        RETRY(!(fl0.x == ep.x && fl0.y == ep.y && fl0.z == ep.x && fl0.w == ep.y && fl1.x == ep.x && fl1.y == ep.y && fl1.z == ep.x && fl1.w == ep.y), {
          CNT(0);
          ld4r<0>(fl0, o, fb); ld4r<16>(fl1, o, fb);
          vmwait<0>(); PIN(fl0); PIN(fl1);
        });
      } else {
        SB();
        consts(I0{});
        consts(I2{});
      }
      u32x4 z0, z1, wv[2];
      if constexpr (L == 0) XS(39);
      ld4<0>(z0, zo, zb0); ld4<0>(z1, zo, zb1);
      ld4<0>(wv[0], wo, wb0); ld4<0>(wv[1], wo, wb1);
      SB();
      u32x4 wa[8], po[6];
      if constexpr (L == 0) {
        XS(40);
        u32x4 ot[4];
#pragma unroll
        for (int j = 0; j < 8; ++j) { PIN(pv0[j]); PIN(pv1[j]); }
        transpose8x4(pv0, ot);
#pragma unroll
        for (int x = 0; x < 4; ++x) *(u32x4*)(bw + lane * 64 + ((x + lane) & 3) * 16) = ot[x];
#pragma unroll
        for (int jj = 0; jj < 4; ++jj) {
          ot[0][jj] = __builtin_amdgcn_perm(pv1[2 * jj + 1], pv1[2 * jj], 0x05040100u);
          ot[1][jj] = __builtin_amdgcn_perm(pv1[2 * jj + 1], pv1[2 * jj], 0x07060302u);
        }
        *(u32x4*)(bw + 4096 + lane * 32) = ot[0]; *(u32x4*)(bw + 4096 + lane * 32 + 16) = ot[1];
#pragma unroll
        for (int x = 0; x < 4; ++x) {
          const int src = x * 16 + (lane >> 2);
          po[x] = *(const u32x4*)(bw + src * 64 + (((lane & 3) + src) & 3) * 16);
        }
#pragma unroll
        for (int x = 0; x < 2; ++x) {
          po[4 + x] = *(const u32x4*)(bw + 4096 + x * 1024 + (8 * (m >> 2) + 4 * (g & 1) + (m & 3) + 32 * (g >> 1)) * 16);
        }
#pragma unroll
        for (int x = 0; x < 6; ++x) PIN(po[x]);
        st4(po[0], pd[0]); st4(po[1], pd[1]);
        SB();
        XS(41);
        consts(I2{});
        XS(42);
        consts(I3{});
        XS(43);
        st4(po[2], pd[2]); st4(po[3], pd[3]);
      } else {
        consts(I1{});
        ring4(2);
        SB();
        consts(I3{});
      }
      u8* const zi = lds + LDS_SCR;
      u32 zw;
      {
        const int pp = tid >> 1;
        zw = (tid & 1) * 4096 + ((pp >> 6) & 1) * 2048 + (((pp >> 3) & 3) * 16 + 2 * (pp & 7)) * 16 + ((pp >> 5) & 1) * 8;
        PIN(zw);
      }
      u32 uo = OFF_U + phi * 8192 + w * 2048 + l16;
      PIN(uo);
      SB();
      bar_nw();
      ST(S0 + 1);
      vmwait<4>();
      PIN(z0); PIN(z1); PIN(wv[0]); PIN(wv[1]);
      if constexpr (L == 0) {
        RETRY((stale<false, 1u>(red8<false>(z0, z1)) || max4(max4(0, wv[0]), wv[1]) == SENT), {
          CNT(1);
          if (bad_) { ld4r<0>(z0, zo, zb0); ld4r<0>(z1, zo, zb1); ld4r<0>(wv[0], wo, wb0); ld4r<0>(wv[1], wo, wb1); }
          vmwait<0>(); PIN(z0); PIN(z1); PIN(wv[0]); PIN(wv[1]);
        });
      } else {
        RETRY((stale<true, 0x10000u>(red8<true>(z0, z1))), {
          CNT(2);
          if (bad_) { ld4r<0>(z0, zo, zb0); ld4r<0>(z1, zo, zb1); }
          vmwait<0>(); PIN(z0); PIN(z1);
        });
      }
      ST(S0 + 2);
      *(u32x2*)(zi + zw) = u32x2{z0.x, z0.y}; *(u32x2*)(zi + zw + 16) = u32x2{z0.z, z0.w};
      *(u32x2*)(zi + zw + 1024) = u32x2{z1.x, z1.y}; *(u32x2*)(zi + zw + 1024 + 16) = u32x2{z1.z, z1.w};
      *(u32x4*)(zi + 8192 + t16) = wv[0]; *(u32x4*)(zi + 12288 + t16) = wv[1];
      bar();
      XF(37);
      const u8* zr = zi + pi_ * 4096 + l16;
      const u32x4 f00 = *(const u32x4*)(zr), f01 = *(const u32x4*)(zr + 1024), f10 = *(const u32x4*)(zr + 2048), f11 = *(const u32x4*)(zr + 3072);
      wv[0] = *(const u32x4*)(zi + 8192 + tid * 32); wv[1] = *(const u32x4*)(zi + 8192 + tid * 32 + 16);
      if (phi == 0) static_for<8>([&](auto J) { constexpr int j = decltype(J)::value; ld4<0>(wa[j], t16, ws + OFF_WNY + j * 4096); });
      if constexpr (L == 1) ring4(3); else { st4(po[4], pd[4]); st4(po[5], pd[5]); }
      f32x4 d0, d1, d2, d3;
      mm8<false, 3>(d0, d1, d2, d3, f00, cc.b1re[0], f01, cc.b1re[1], f00, cc.b1im[0], f01, cc.b1im[1], f10, cc.b1re[0], f11, cc.b1re[1], f10, cc.b1im[0], f11, cc.b1im[1]);
      XF(38);
      f32x4 e0, e1, e2, e3;
      u32x4 b20 = twiddle<false>(d0, d1, cc.t1c[0], cc.t1s[0]);
      PIN(b20);
      const u32x4 b21 = cplx_under<false, true>(d2, d3, cc.t1c[1], cc.t1s[1], [&](auto I) {
        constexpr int i = decltype(I)::value;
        mf0<false, 0>(i == 0 ? e0 : i == 1 ? e1 : i == 2 ? e2 : e3, i == 0 ? cc.a2re[0][0] : i == 1 ? cc.a2im[0][0] : i == 2 ? cc.a2re[1][0] : cc.a2im[1][0], b20);
      });
      f32x2 wr[2][2], wi[2][2];
      auto unpack_w = [&]() {
#pragma unroll
        for (int t = 0; t < 2; ++t)
#pragma unroll
          for (int h = 0; h < 2; ++h) {
            wr[t][h] = f32x2{blo(wv[t][2 * h]), blo(wv[t][2 * h + 1])}; wi[t][h] = f32x2{bhi(wv[t][2 * h]), bhi(wv[t][2 * h + 1])};
            PIN(wr[t][h]); PIN(wi[t][h]);
          }
      };
      mf4<false, 0, 0>(e0, e1, e2, e3, cc.a2re[0][1], b21, cc.a2im[0][1], b21, cc.a2re[1][1], b21, cc.a2im[1][1], b21);
      SB();
      unpack_w();
      SB();
      if constexpr (L == 0) { fill_h(OFF_H, 0); fill_h(OFF_H, 1); }
      XF(39);
      u32x4 a30, a31;
      auto step3 = [&](auto I) {
        constexpr int i = decltype(I)::value;
        mf0<true, 0>(i == 0 ? d0 : i == 1 ? d1 : i == 2 ? d2 : d3, a30, i == 0 ? cc.b3re[0][0] : i == 1 ? cc.b3im[0][0] : i == 2 ? cc.b3re[1][0] : cc.b3im[1][0]);
      };
      if (phi != 0) {
        a30 = cmul_b(e0, e1, wr[0], wi[0]);
        PIN(a30);
        a31 = cplx_under<true, false>(e2, e3, wr[1], wi[1], step3);
      } else {
        u32* pl = (u32*)(lds + LDS_STASH);
        const u32x4 h0 = {pkh(e0[0], e1[0]), pkh(e0[1], e1[1]), pkh(e0[2], e1[2]), pkh(e0[3], e1[3])};
        const u32x4 h1 = {pkh(e2[0], e3[0]), pkh(e2[1], e3[1]), pkh(e2[2], e3[2]), pkh(e2[3], e3[3])};
        if (k1l != 0) {
          *(u32x4*)(pl + k1l * 32 + ((g ^ (k1l >> 1)) & 7) * 4) = h0; *(u32x4*)(pl + k1l * 32 + (((4 + g) ^ (k1l >> 1)) & 7) * 4) = h1;
        } else {
#pragma unroll
          for (int r = 0; r < 4; ++r) { pl[(4 * g + r - 1) & 31] = h0[r]; pl[16 + 4 * g + r - 1] = h1[r]; }
        }
        bar();
        vmwait<4>();
#pragma unroll
        for (int j = 0; j < 8; ++j) PIN(wa[j]);
        if constexpr (L == 0) {
          RETRY(max4(max4(max4(max4(max4(max4(max4(max4(0, wa[0]), wa[1]), wa[2]), wa[3]), wa[4]), wa[5]), wa[6]), wa[7]) == SENT, {
            static_for<8>([&](auto J) { constexpr int j = decltype(J)::value; ld4r<0>(wa[j], t16, ws + OFF_WNY + j * 4096); });
            vmwait<0>();
            _Pragma("unroll") for (int j = 0; j < 8; ++j) PIN(wa[j]);
          });
        }
        const int kr = (64 - k1l) & 63;
        const u32x4 q0 = *(const u32x4*)(pl + kr * 32 + (((7 - g) ^ (kr >> 1)) & 7) * 4), q1 = *(const u32x4*)(pl + kr * 32 + (((3 - g) ^ (kr >> 1)) & 7) * 4);
#pragma unroll
        for (int r = 0; r < 4; ++r) {
          const f32x4 c0 = __builtin_bit_cast(f32x4, wa[r]), c1 = __builtin_bit_cast(f32x4, wa[4 + r]);
          const u32 u0 = q0[3 - r], u1 = q1[3 - r];
          const f16x2 v0 = __builtin_bit_cast(f16x2, u0), v1 = __builtin_bit_cast(f16x2, u1);
          const float qr0 = (float)v0.x, qi0 = (float)v0.y, qr1 = (float)v1.x, qi1 = (float)v1.y;
          a30[r] = pkb(e0[r] * c0[0] - e1[r] * c0[1] + qr0 * c0[2] + qi0 * c0[3], e0[r] * c0[1] + e1[r] * c0[0] + qr0 * c0[3] - qi0 * c0[2]);
          a31[r] = pkb(e2[r] * c1[0] - e3[r] * c1[1] + qr1 * c1[2] + qi1 * c1[3], e2[r] * c1[1] + e3[r] * c1[0] + qr1 * c1[3] - qi1 * c1[2]);
        }
        PIN(a30); PIN(a31);
        asm volatile("s_nop 1");
        static_for<4>(step3);
      }
      u8* ex = lds + LDS_SCR + 16384;
      mf4<true, 0, 5>(d0, d1, d2, d3, a31, cc.b3re[0][1], a31, cc.b3im[0][1], a31, cc.b3re[1][1], a31, cc.b3im[1][1]);
      XF(40);
      *(u32x4*)(ex + w * 1024 + l16) = twiddle<true>(d0, d1, cc.t2c[0], cc.t2s[0]);
      SB();
      *(u32x4*)(ex + (4 + w) * 1024 + l16) = twiddle<true>(d2, d3, cc.t2c[1], cc.t2s[1]);
      if constexpr (L == 0) vmwait<2>();
      bar();
      XF(41);
      const u8* er = ex + (w & 1) * 4096 + l16;
      const u32x4 g0 = *(const u32x4*)(er), g1 = *(const u32x4*)(er + 1024), g2 = *(const u32x4*)(er + 2048), g3 = *(const u32x4*)(er + 3072);
      mm8<true, 7>(d0, d1, d2, d3, cc.a4re[0][0], g0, cc.a4re[0][1], g1, cc.a4im[0][0], g0, cc.a4im[0][1], g1, cc.a4re[1][0], g2, cc.a4re[1][1], g3, cc.a4im[1][0], g2,
                   cc.a4im[1][1], g3);
      {
        XF(42);
        const f32x2 lr0 = lo2(d0) + lo2(d2), li0 = lo2(d1) + lo2(d3), hr0 = lo2(d0) - lo2(d2), hi0 = lo2(d1) - lo2(d3);
        const f32x2 lr1 = hi2(d0) + hi2(d2), li1 = hi2(d1) + hi2(d3), hr1 = hi2(d0) - hi2(d2), hi1 = hi2(d1) - hi2(d3);
        if (phi != 0) {
          const f32x2 pr0 = lr0 - hi0, pi0 = li0 + hr0, qr0 = lr0 + hi0, qi0 = hr0 - li0;
          st4(mark4<1u - L, L>(u32x4{pkb(pr0.x, pi0.x), pkb(qr0.x, qi0.x), pkb(pr0.y, pi0.y), pkb(qr0.y, qi0.y)}), uo);
          const f32x2 pr1 = lr1 - hi1, pi1 = li1 + hr1, qr1 = lr1 + hi1, qi1 = hr1 - li1;
          st4(mark4<1u - L, L>(u32x4{pkb(pr1.x, pi1.x), pkb(qr1.x, qi1.x), pkb(pr1.y, pi1.y), pkb(qr1.y, qi1.y)}), uo + 1024);
          MEMFENCE();
        } else {
          st4(mark4<1u - L, L>(u32x4{pkb(lr0.x, hr0.x), pkb(li0.x, hi0.x), pkb(lr0.y, hr0.y), pkb(li0.y, hi0.y)}), uo);
          st4(mark4<1u - L, L>(u32x4{pkb(lr1.x, hr1.x), pkb(li1.x, hi1.x), pkb(lr1.y, hr1.y), pkb(li1.y, hi1.y)}), uo + 1024);
        }
      }
    }
    ST(S0 + 3);
    {
      FRESH();
      SB();
      row_consts<true, 0>(rc, m, g);
      f32x2 xa[4], xb[4];
#pragma unroll
      for (int i = 0; i < 4; ++i) {
        xa[i] = f32x2{blo(xl[i >> 1][i & 1]), bhi(xl[i >> 1][i & 1])}; xb[i] = f32x2{blo(xh[i >> 1][i & 1]), bhi(xh[i >> 1][i & 1])};
      }
      const float ca = first_lane(xa[0].x), cb = first_lane(xb[0].x);
      float ch = lane < 32 ? ca : cb;
      PIN(ch);
#pragma unroll
      for (int i = 0; i < 4; ++i) { xa[i] -= ca; xb[i] -= cb; PIN(xa[i]); PIN(xb[i]); }
      SB();
      u32x4 c0, c1;
      const u32 uo = (tid >> 1) * 8192 + (tid & 1) * 16;
      const u8* ub0 = ws + OFF_U + pos * 32;
      const u8* ub1 = ub0 + 128 * 8192;
      ld4<0>(c0, uo, ub0); ld4<0>(c1, uo, ub1);
      if constexpr (L == 0) { fill_h(OFF_H, 2); fill_h(OFF_H, 3); fill_h(OFF_H, 4); }
      SB();
      row_consts<true, 1>(rc, m, g);
      SB();
      if constexpr (L == 1) ring4(4); else { fill_h(OFF_H, 5); fill_h(OFF_H, 6); }
      u32* img = (u32*)(lds + LDS_SCR);
      u32 ip0, iq0, iq1, xno, tw0, tw1, trd;
      {
        const int ph = tid >> 1, fq0 = ph ? 512 - ph : 256, fq1 = 384 - ph;
        ip0 = ((tid & 1) * 1280 + (ph & 31) * 20 + (ph >> 5)) * 4;
        iq0 = ((tid & 1) * 1280 + (fq0 & 31) * 20 + (fq0 >> 5)) * 4; iq1 = ((tid & 1) * 1280 + (fq1 & 31) * 20 + (fq1 >> 5)) * 4;
        xno = OFF_A + (pos >> 1) * 16384 + (tid >> 3) * 256 + (pos & 1) * 128 + (tid & 7) * 16;
        tw0 = (4 * m + g) * 128 + ((2 * w) ^ (m & 7)) * 16; tw1 = (4 * m + g) * 128 + ((2 * w + 1) ^ (m & 7)) * 16;
        trd = (tid >> 3) * 128 + ((tid & 7) ^ ((tid >> 5) & 7)) * 16;
        PIN(ip0); PIN(iq0); PIN(iq1); PIN(xno); PIN(tw0); PIN(tw1); PIN(trd);
      }
      SB();
      ST(S0 + 4);
      vmwait<(L == 0 ? 5 : 4)>(); PIN(c0); PIN(c1);
      {
        RETRY((stale<L == 1, 1u>(red8<L == 1>(c0, c1))), {
          CNT(3);
          if (bad_) { ld4r<0>(c0, uo, ub0); ld4r<0>(c1, uo, ub1); }
          vmwait<0>(); PIN(c0); PIN(c1);
        });
      }
      ST(S0 + 5);
      {
        u8* d = (u8*)img;
        *(u32*)(d + ip0) = c0.x; *(u32*)(d + iq0) = c0.y; *(u32*)(d + 2560 + ip0) = c0.z; *(u32*)(d + 2560 + iq0) = c0.w;
        *(u32*)(d + ip0 + 16) = c1.x; *(u32*)(d + iq1) = c1.y; *(u32*)(d + 2560 + ip0 + 16) = c1.z; *(u32*)(d + 2560 + iq1) = c1.w;
      }
      bar();
      XF(43);
      const u32x4 a0 = *(const u32x4*)(img + w * 640 + m * 20 + 4 * g), a1 = *(const u32x4*)(img + w * 640 + (16 + m) * 20 + 4 * g);
      if constexpr (L == 1) ring4(5);
      if constexpr (L == 0) ring4(0);
      f32x4 dr0, di0, dr1, di1;
      row_fft<true, 7>(rc, a0, a1, dr0, di0, dr1, di1);
      if constexpr (L == 0) ring4(1);
      XF(44);
      const float ys = 1.0f / 32768.0f;
      xa[0] += lo2(dr0) * ys; xa[1] += hi2(dr0) * ys; xa[2] += lo2(dr1) * ys; xa[3] += hi2(dr1) * ys;
      xb[0] += lo2(di0) * ys; xb[1] += hi2(di0) * ys; xb[2] += lo2(di1) * ys; xb[3] += hi2(di1) * ys;
      const f32x2 sa = (xa[0] + xa[1]) + (xa[2] + xa[3]), sb = (xb[0] + xb[1]) + (xb[2] + xb[3]);
      f32x2 qa = xa[0] * xa[0], qb = xb[0] * xb[0];
#pragma unroll
      for (int i = 1; i < 4; ++i) { qa += xa[i] * xa[i]; qb += xb[i] * xb[i]; }
      if constexpr (L == 0) { SB(); ring4(2); SB(); }
      f32x2 R, O;
      XF(45);
      ln_coef(f32x2{sa.x + sa.y, sb.x + sb.y}, f32x2{qa.x + qa.y, qb.x + qb.y}, ch, R, O);
      XF(46);
      u32x4 na, nb;
#pragma unroll
      for (int i = 0; i < 4; ++i) {
        const f32x2 va = xa[i] * R.x + O.x, vb = xb[i] * R.y + O.y;
        na[i] = mark<0x10000u, 1u - L>(pkb(va.x, va.y)); nb[i] = mark<0x10000u, 1u - L>(pkb(vb.x, vb.y));
      }
      u8* tr = lds + LDS_SCR + 12288;
      *(u32x4*)(tr + tw0) = na; *(u32x4*)(tr + tw1) = nb;
      if constexpr (L == 0) vmwait<12>();
      bar();
      XF(47);
      st4(*(const u32x4*)(tr + trd), xno);
      st4(*(const u32x4*)(tr + 4096 + trd), xno + 8192);
      SB();
#pragma unroll
      for (int i = 0; i < 4; ++i) { xa[i] += ca; xb[i] += cb; }
      const u32x4 rl = {pkb(xa[0].x, xa[0].y), pkb(xa[1].x, xa[1].y), pkb(xa[2].x, xa[2].y), pkb(xa[3].x, xa[3].y)};
      const u32x4 rh = {pkb(xb[0].x, xb[0].y), pkb(xb[1].x, xb[1].y), pkb(xb[2].x, xb[2].y), pkb(xb[3].x, xb[3].y)};
      if constexpr (L == 0) {
        *(u32x4*)(lds + LDS_STASH + t16) = rl; *(u32x4*)(lds + LDS_STASH + 4096 + t16) = rh;
        XS(44);
        ring4(3); ring4(4); ring4(5); ring4(6); ring4(7);
      } else {
        xl[0] = u32x2{rl.x, rl.y}; xl[1] = u32x2{rl.z, rl.w}; xh[0] = u32x2{rh.x, rh.y}; xh[1] = u32x2{rh.z, rh.w};
        PIN(xl[0]); PIN(xl[1]); PIN(xh[0]); PIN(xh[1]);
        __builtin_amdgcn_s_sleep(6);
      }
    }
    ST(S0 + 6);
    {
      FRESH();
      u32x4 tk[32], he[32], hl[16], w2t[4], w2v[3][4];
      u32x2 hh[16];
      f32x4 ac1[6][2], ac2[4][2], ac3[4][2];
      const u8* hs0 = ws + OFF_H + (8 * G + 4 * half2) * 57344 + (4 * w) * 3584;
      const u8* hs[4];
      const u8* hp[2];
#pragma unroll
      for (int j = 0; j < 4; ++j) { hs[j] = hs0 + opq((j + ct) & 3) * 3584; PINS(hs[j]); }
#pragma unroll
      for (int j = 0; j < 2; ++j) { hp[j] = hs0 + opq((j ^ ct) & 1) * 7168 + 3584; PINS(hp[j]); }
      const u8* w2src = ws + OFF_W2 + (ct * 4 + w) * 57344;
      const u8* w2x = w2src + 40960 + 4096;
      u32 lx8 = l16 + 8192;
      PINS(w2x); PIN(lx8);
      u32 hv[4], h8[4];
#pragma unroll
      for (int u = 0; u < 4; ++u) { hv[u] = l16 + (u ^ w) * 57344; h8[u] = hv[u] - lane * 8; PIN(hv[u]); PIN(h8[u]); }
      auto hload = [&](auto RE, auto KC, auto UC) {
        constexpr int k = decltype(KC)::value, u = decltype(UC)::value;
        constexpr bool re = decltype(RE)::value;
        if constexpr (k == 4 || k == 5) {
          constexpr int o = (u >> 2) * 3584 + 3072 - 3584;
          if constexpr (re) ld2r<o>(hh[8 * (k - 4) + u], h8[u & 3], hp[k - 4]); else ld2<o>(hh[8 * (k - 4) + u], h8[u & 3], hp[k - 4]);
        } else {
          constexpr int j = k < 4 ? k : k < 10 ? k - 6 : k - 10, o = k < 4 ? 0 : k < 10 ? 1024 : 2048;
          u32x4& d = k < 10 ? he[(k < 4 ? 0 : 16) + 4 * j + u] : hl[4 * j + u];
          if constexpr (re) ld4r<o>(d, hv[u], hs[j]); else ld4<o>(d, hv[u], hs[j]);
        }
      };
      auto hseq = [&](auto IC) {
        constexpr int i = decltype(IC)::value, k = i < 16 ? i >> 2 : i < 32 ? 4 + ((i - 16) >> 3) : 6 + ((i - 32) >> 2);
        hload(std::false_type{}, std::integral_constant<int, k>{}, std::integral_constant<int, (i < 16 || i >= 32) ? (i & 3) : (i & 7)>{});
      };
      {
        const u8* w1b = ws + OFF_W1 + member * 229376 + 131072 + w * 8192;
        const u8* tk0 = ws + OFF_A + (8 * G + 2 * w) * 16384 + 4096;
        const u8* tk1 = tk0 + 8192;
        u32 tv1 = l16 + 16384, lhi = l16 + 65536;
        u32 hd0 = OFF_H + (8 * G + 2 * w) * 57344 + member * 3584 + l16, hd1 = hd0 + 57344;
        u32 hd2 = hd0 + 3072 - lane * 8, hd3 = hd2 + 57344;
        PIN(tv1); PIN(lhi); PIN(hd0); PIN(hd1); PIN(hd2); PIN(hd3); PINS(tk0); PINS(tk1);
        u32 x3o = OFF_X3 + (8 * pos + 2 * w) * 1024 + xo;
        if constexpr (L == 1) PIN(x3o);
        auto tok = [&](auto RE, auto QC) {
          constexpr int q = decltype(QC)::value, ks = q >> 1;
          if constexpr (decltype(RE)::value) ld4r<(ks & 7) * 1024 - 4096>(tk[q], q & 1 ? tv1 : l16, ks >> 3 ? tk1 : tk0);
          else ld4<(ks & 7) * 1024 - 4096>(tk[q], q & 1 ? tv1 : l16, ks >> 3 ? tk1 : tk0);
        };
        constexpr bool TONE = L == 0;
        constexpr u32 TBIT = L == 0 ? 1u : 0x10000u;
        u32 mx0 = 0, mx1 = 0;
        auto mx0_fin = [&]() { return red2<TONE>(mx0, mx1) & TBIT; };
        auto fix = [&](auto QC, u64 bad) {
          constexpr int q0 = decltype(QC)::value;
          if (__builtin_expect(bad != 0, 0)) {
            _Pragma("nounroll") for (int sp = 0; sp < SPIN_CAP; ++sp) {
              CNT(4);
              static_for<4>([&](auto I) { tok(std::true_type{}, std::integral_constant<int, q0 + decltype(I)::value>{}); });
              vmwait<0>();
#pragma unroll
              for (int q = q0; q < q0 + 4; ++q) PIN(tk[q]);
              if (!__any(stale<TONE, TBIT>(red2<TONE>(red8<TONE>(tk[q0], tk[q0 + 1]), red8<TONE>(tk[q0 + 2], tk[q0 + 3]))))) break;
            }
          }
        };
        static_for<NTOK0>([&](auto Q) { tok(std::false_type{}, Q); });
        constexpr int PD = 8;
        u32x4 af[PD];
        auto frag = [&](int x) {
          const int o = (x & 127) * 1024;
          return o < 65536 ? *(const u32x4*)(lds + l16 + o) : *(const u32x4*)(lds + lhi + (o - 65536));
        };
        vmwait<NTOK0>();
        bar_nw();
#pragma unroll
        for (int x = 0; x < PD - 1; ++x) af[x] = frag(x);
        vmwait<NTOK0 - 4>();
        ST(S0 + 7);
#pragma unroll
        for (int q = 0; q < 4; ++q) PIN(tk[q]);
        mx0 = red8<TONE>(tk[0], tk[1]); mx1 = red8<TONE>(tk[2], tk[3]);
        fix(std::integral_constant<int, 0>{}, __ballot(stale<TONE, TBIT>(red2<TONE>(mx0, mx1))));
        SB();
        ST(S0 + 8);
        float eg[2][2], eu[2][2];
        u32 ev[2];
        u64 bm = 0;
        u32x4 eo[1];
        auto epi = [&](auto NC) {
          constexpr int n = decltype(NC)::value;
          if constexpr (n < NEPI1 + NEPI2) {
            constexpr bool p1 = n < NEPI1;
            constexpr int i = p1 ? n - (n > 40) - (n > 81) - (n > 102) : (n - NEPI1) - (n - NEPI1) / 41;
            if constexpr (p1 && (n == 40 || n == 81)) st4(eo[0], n == 40 ? hd0 : hd1);
            else if constexpr (p1 && (n == 102 || n == 123)) st2(u32x2{eo[0].x, eo[0].y}, n == 102 ? hd2 : hd3);
            else if constexpr (!p1 && (n - NEPI1) % 41 == 40) st4(eo[0], ((n - NEPI1) / 41 ? hd1 : hd0) + 1024);
            else {
              constexpr int s = i & 1, st = (i % 20) >> 1, blk = i / 20;
              constexpr int u = p1 ? (blk < 4 ? blk >> 1 : blk - 4) : blk >> 1, pr = p1 ? (blk < 4 ? blk & 1 : 2) : blk & 1;
              constexpr int ei = 0, d = 2 * (pr & 1) + s;
              const f32x4& ga = p1 ? ac1[2 * pr][u] : ac2[2 * (pr & 1)][u];
              const f32x4& up = p1 ? ac1[2 * pr + 1][u] : ac2[2 * (pr & 1) + 1][u];
              if constexpr (st < 2) { eg[s][st] = ga[2 * s + st]; PIN(eg[s][st]); }
              else if constexpr (st < 4) { eu[s][st - 2] = up[2 * s + st - 2]; PIN(eu[s][st - 2]); }
              else if constexpr (st < 6) { eg[s][st - 4] = relu(eg[s][st - 4]); PIN(eg[s][st - 4]); }
              else if constexpr (st < 8) { eg[s][st - 6] *= eu[s][st - 6]; PIN(eg[s][st - 6]); }
              else if constexpr (st == 8) { ev[s] = pkb(eg[s][0], eg[s][1]); PIN(ev[s]); }
              else { u32 v = mark<1u - L, L>(ev[s]); PIN(v); eo[ei][d] = v; }
            }
            SB();
          }
        };
        static_for<224>([&](auto XC) {
          constexpr int x = decltype(XC)::value, job = G1S[L].job[x], ji = G1S[L].idx[x], e0 = G1S[L].epi[x], e1 = G1S[L].epi[x + 1];
          if constexpr (x == 32 || x == 64) bar_nw();
          if constexpr (x == 97) { vmwait<g1_wait(L, x, 2, 7)>(); bar_nw(); }
          if constexpr (x == 150) { vmwait<g1_wait(L, x, 2, 23)>(); bar_nw(); }
          constexpr int cj = (x + 12) / 12, co = x + 12 - 12 * cj;
          constexpr bool chk = x < 84 && co >= 5;
          constexpr int ks = x < 96 ? x / 6 : x < 160 ? (x - 96) >> 2 : (x - 160) >> 2, t = x < 96 ? x % 6 : x & 3;
          auto mm = [&](int u) {
            f32x4& a = x < 96 ? ac1[x < 96 ? t : 0][u] : x < 160 ? ac2[x < 96 ? 0 : t][u] : ac3[x < 96 ? 0 : t][u];
            if constexpr (ks == 0) a = mfma_b(af[x % PD], tk[u], f32x4{0.f, 0.f, 0.f, 0.f}); else a = mfma_b(af[x % PD], tk[2 * ks + u], a);
          };
          SB();
          mm(0);
          SB();
          if constexpr (x < 223) __builtin_amdgcn_s_waitcnt(0xC07F | ((x + PD - 2 < 224 ? PD - 3 : 222 - x) << 8));
          if constexpr (chk && co == 5) {
            vmwait<g1_wait(L, x, 1, 4 * cj + 3)>();
            PIN(tk[4 * cj]); PIN(tk[4 * cj + 1]); PIN(tk[4 * cj + 2]); PIN(tk[4 * cj + 3]);
          }
          SB();
          if constexpr (chk && co >= 6 && co < 10) {
            const u32x4 v = tk[4 * cj + co - 6];
            mx0 = co == 6 ? red2<TONE>(v.x, v.y) : red3<TONE>(mx0, v.x, v.y); PIN(mx0); SB();
            mx1 = co == 6 ? red2<TONE>(v.z, v.w) : red3<TONE>(mx1, v.z, v.w); PIN(mx1);
          }
          if constexpr (chk && co == 10) { mx0 = mx0_fin(); PIN(mx0); SB(); bm = __ballot(TONE ? mx0 == 0 : mx0 != 0); PINS(bm); }
          if constexpr (e0 < e1) epi(std::integral_constant<int, e0>{});
          if constexpr (e0 + 1 < e1) epi(std::integral_constant<int, e0 + 1>{});
          SB();
          mm(1);
          SB();
          if constexpr (chk && co == 11) fix(std::integral_constant<int, 4 * cj>{}, bm);
          if constexpr (job == 2 && (ji & 3) == 0) set_m0(ldsbase + (ji >> 3) * 32768 + w * 8192 + (ji & 4) * 1024);
          SB();
          if constexpr (x + PD - 1 < 224) af[(x + PD - 1) % PD] = frag(x + PD - 1);
          SB();
          if constexpr (job == 1) tok(std::false_type{}, std::integral_constant<int, ji>{});
          if constexpr (job == 2) dma1<(ji & 3) * 1024>(l16, w1b + (ji >> 3) * 32768 + (ji & 4) * 1024);
          if constexpr (job == 3) ld4a<(ji & 7) * 1024 - 4096>(w2a[ji >> 2][ji & 3], l16, w2src + (ji >> 3) * 8192 + 4096);
          if constexpr (job == 5) {
            if constexpr (ji >= 12) ld4a<(ji - 12) * 1024 - 4096>(w2t[ji - 12], l16, w2x);
            else ld4<((ji + 4) & 7) * 1024 - 4096>(w2v[ji >> 2][ji & 3], ji < 4 ? l16 : lx8, w2x);
          }
          if constexpr (job == 6) st2(ji & 2 ? xh[ji & 1] : xl[ji & 1], x3o + ji * 512);
          if constexpr (job == 4) hseq(std::integral_constant<int, ji>{});
          if constexpr (e0 + 2 < e1) epi(std::integral_constant<int, e0 + 2>{});
          SB();
        });
        ST(S0 + 9);
#pragma unroll
        for (int u = 0; u < 2; ++u) {
          u32x4 o;
#pragma unroll
          for (int p = 0; p < 2; ++p) {
            const f32x4 ga = ac3[2 * p][u], up = ac3[2 * p + 1][u];
            const f32x2 a = f32x2{relu(ga[0]), relu(ga[1])} * lo2(up), c = f32x2{relu(ga[2]), relu(ga[3])} * hi2(up);
            o[2 * p] = mark<1u - L, L>(pkb(a.x, a.y)); o[2 * p + 1] = mark<1u - L, L>(pkb(c.x, c.y));
          }
          st4(o, (u ? hd1 : hd0) + 2048);
        }
        SB();
      }
      ST(S0 + 10);
      {
        f32x4 acc[4][4];
        u32x4 x3a, x3b;
        u32 eo = L == 1 ? (128 * G + 64 * half2 + 16 * w + m) * 1024 + ct * 128 + g * 16
                        : (128 * G + 64 * half2 + 16 * w + (m & 14)) * 1024 + ct * 128 + (m & 1) * 64 + g * 16;
        PIN(eo);
        const u8* x3s = ws + OFF_X3;
        u64 bm = 0;
        PINS(x3s);
        constexpr int LB = 48 - NEARLY;
        auto cum = [](int n) constexpr { const int c = n - n / 8; return c < LB + 16 ? c : LB + 16; };
        auto young = [cum](int k, int n) constexpr {
          const int i = k < 4 ? 4 * k + 3 : k < 6 ? 16 + 8 * (k - 4) + 7 : 32 + 4 * (k - 6) + 3;
          if (k < 10 && i < NEARLY) return cum(n) + 2 + G1S[L].cum[224] - G1S[L].pos[4][i] - 1;
          if (k < 10) return cum(n) - (i - NEARLY + 1);
          return cum(n) - (LB + 4 * (k - 10) + 4);
        };
        constexpr bool HONE = L == 1;
        u32 mx0 = 0, mx1 = 0;
        auto mx0_fin = [&]() { return red2<HONE>(mx0, mx1) & 1u; };
        auto pin_k = [&](auto KC) {
          constexpr int k = decltype(KC)::value;
#pragma unroll
          for (int u = 0; u < 4; ++u) {
            if constexpr (k < 4) PIN(he[4 * k + u]);
            else if constexpr (k < 6) { PIN(hh[8 * (k - 4) + u]); PIN(hh[8 * (k - 4) + 4 + u]); }
            else if constexpr (k < 10) PIN(he[16 + 4 * (k - 6) + u]);
            else PIN(hl[4 * (k - 10) + u]);
          }
        };
        auto opnd = [&](auto KC, int u) {
          constexpr int k = decltype(KC)::value;
          if constexpr (k < 4) return he[4 * k + u];
          else if constexpr (k < 6) return u32x4{hh[8 * (k - 4) + u].x, hh[8 * (k - 4) + u].y, hh[8 * (k - 4) + 4 + u].x, hh[8 * (k - 4) + 4 + u].y};
          else if constexpr (k < 10) return he[16 + 4 * (k - 6) + u];
          else return hl[4 * (k - 10) + u];
        };
        auto fixk = [&](auto KC, u64 bad) {
          constexpr int k = decltype(KC)::value;
          if (__builtin_expect(bad != 0, 0)) {
            _Pragma("nounroll") for (int sp = 0; sp < SPIN_CAP; ++sp) {
              CNT(k < 10 ? 5 : 6);
              static_for<(k == 4 || k == 5) ? 8 : 4>([&](auto U) { hload(std::true_type{}, KC, U); });
              vmwait<0>();
              pin_k(KC);
              if (!__any(stale<HONE, 1u>(red2<HONE>(red8<HONE>(opnd(KC, 0), opnd(KC, 1)), red8<HONE>(opnd(KC, 2), opnd(KC, 3)))))) break;
            }
          }
        };
        u32 rw[3];
#pragma unroll
        for (int u = 1; u < 4; ++u) { rw[u - 1] = ((u ^ w) * 3 + u - 1) * 4096 + l16; PIN(rw[u - 1]); }
        u32 oo = 0;
        if constexpr (L == 1) {
          const int tok = 128 * G + 64 * half2 + 16 * w + m;
          oo = (row_of_pos(tok >> 3, (tok >> 1) & 3) + 1024 * (tok & 1)) * 1024 + ct * 128 + g * 16;
          PIN(oo);
        }
        vmwait<clamp63(young(0, 0))>();
        if constexpr (L == 0) {
#pragma unroll
          for (int k = 0; k < 10; ++k) { PINA(w2a[k][0]); PINA(w2a[k][1]); PINA(w2a[k][2]); PINA(w2a[k][3]); }
        }
#pragma unroll
        for (int c = 0; c < 4; ++c) { PINA(w2t[c]); PIN(w2v[0][c]); PIN(w2v[1][c]); PIN(w2v[2][c]); }
        pin_k(std::integral_constant<int, 0>{});
        mx0 = red8<HONE>(he[0], he[1]); mx1 = red8<HONE>(he[2], he[3]);
        fixk(std::integral_constant<int, 0>{}, __ballot(stale<HONE, 1u>(red2<HONE>(mx0, mx1))));
        ST(S0 + 11);
        auto wfr = [&](auto KC, int c) {
          constexpr int k = decltype(KC)::value;
          if constexpr (k < 10) return w2a[k][c]; else if constexpr (k == 10) return w2t[c]; else return w2v[k - 11][c];
        };
        static_for<44>([&](auto NC) {
          constexpr int n = decltype(NC)::value, k = n >> 2, c = n & 3;
          using KC = std::integral_constant<int, k>;
          using KN = std::integral_constant<int, k + 1>;
          static_for<4>([&](auto UC) {
            constexpr int u = decltype(UC)::value;
            SB();
            if constexpr (k == 0) acc[c][u] = mfma_b(wfr(KC{}, c), opnd(KC{}, u), f32x4{0.f, 0.f, 0.f, 0.f});
            else acc[c][u] = mfma_b(wfr(KC{}, c), opnd(KC{}, u), acc[c][u]);
            SB();
            constexpr int j = k == 10 ? 4 * c + u - 2 : u == 3 ? -9 : 3 * c + u - 5;
            if constexpr (j == -1) {
              vmwait<clamp63(young(k == 10 ? 13 : k + 1, n))>(); pin_k(KN{});
              if constexpr (k == 10) { pin_k(std::integral_constant<int, 12>{}); pin_k(std::integral_constant<int, 13>{}); }
            }
            if constexpr (j >= 0 && j < (k == 10 ? 12 : 4)) {
              const u32x4 v = opnd(std::integral_constant<int, k + 1 + (j >> 2)>{}, j & 3);
              mx0 = j == 0 ? red2<HONE>(v.x, v.y) : red3<HONE>(mx0, v.x, v.y); PIN(mx0); SB();
              mx1 = j == 0 ? red2<HONE>(v.z, v.w) : red3<HONE>(mx1, v.z, v.w); PIN(mx1);
            }
            if constexpr (j == (k == 10 ? 12 : 5)) { mx0 = mx0_fin(); PIN(mx0); SB(); bm = __ballot(HONE ? mx0 == 0 : mx0 != 0); PINS(bm); }
            if constexpr (u == 3) {
              constexpr int q = cum(n);
              if constexpr (cum(n + 1) == q) {}
              else if constexpr (q < LB) hseq(std::integral_constant<int, NEARLY + q>{});
              else hload(std::false_type{}, std::integral_constant<int, 10 + ((q - LB) >> 2)>{}, std::integral_constant<int, (q - LB) & 3>{});
              if constexpr (c == 3) {
                fixk(KN{}, bm);
                if constexpr (k == 10) { fixk(std::integral_constant<int, 12>{}, bm); fixk(std::integral_constant<int, 13>{}, bm); }
              }
            }
          });
        });
        bar_nw();
        if constexpr (L == 1) { ld4<0>(x3a, eo, x3s); ld4<64>(x3b, eo, x3s); }
        static_for<16>([&](auto TC) {
          constexpr int t = decltype(TC)::value, u = ((t >> 2) + 1) & 3, c = t & 3;
          SB();
          acc[c][u] = mfma_b(w2v[0][c], hl[4 + u], acc[c][u]);
          acc[c][u] = mfma_b(w2v[1][c], hl[8 + u], acc[c][u]);
          SB();
          if constexpr (t >= 3 && t < 15) *(f32x4*)(lds + rw[(t - 3) >> 2] + ((t - 3) & 3) * 1024) = acc[(t - 3) & 3][((t - 3) >> 2) + 1];
          SB();
          acc[c][u] = mfma_b(w2v[2][c], hl[12 + u], acc[c][u]);
        });
        SB();
        ST(S0 + 12);
        bar();
        f32x4 s[3][4];
#pragma unroll
        for (int c = 0; c < 4; ++c)
#pragma unroll
          for (int p = 0; p < 3; ++p) s[p][c] = *(const f32x4*)(lds + w * 12288 + p * 4096 + c * 1024 + l16);
        SB();
        if constexpr (L == 1) { vmwait<0>(); PIN(x3a); PIN(x3b); }
        u32x4 y[2];
#pragma unroll
        for (int a = 0; a < 2; ++a) {
          const f32x4 s0 = (acc[2 * a][0] + s[0][2 * a]) + (s[1][2 * a] + s[2][2 * a]);
          const f32x4 s1 = (acc[2 * a + 1][0] + s[0][2 * a + 1]) + (s[1][2 * a + 1] + s[2][2 * a + 1]);
          y[a] = u32x4{pkb(s0[0], s0[1]), pkb(s0[2], s0[3]), pkb(s1[0], s1[1]), pkb(s1[2], s1[3])};
          if constexpr (L == 1) {
            const u32x4 xr = a ? x3b : x3a;
            u32x4 o;
#pragma unroll
            for (int p = 0; p < 4; ++p) o[p] = pkb(blo(xr[p]) + blo(y[a][p]), bhi(xr[p]) + bhi(y[a][p]));
            *(u32x4*)((u8*)P.out + oo + a * 64) = o;
          }
        }
        if constexpr (L == 0) {
          const bool odd = m & 1;
          u32x4 z0, z1;
#pragma unroll
          for (int p = 0; p < 4; ++p) {
            const u32 t = (u32)__builtin_amdgcn_mov_dpp((int)(odd ? y[0][p] : y[1][p]), 0xB1, 0xf, 0xf, true);
            z0[p] = odd ? t : y[0][p]; z1[p] = odd ? y[1][p] : t;
          }
          st4(mark4<0x10001u, 0u>(z0), OFF_A + eo); st4(mark4<0x10001u, 0u>(z1), OFF_A + eo + 1024);
        }
      }
    }
    ST(S0 + 13);
    if constexpr (L == 0) {
      FRESH();
      SB();
      row_consts<false, 0>(rc, m, g);
      SB();
      u32x2 d0, d1, d2, d3;
      const u8* db = ws + OFF_A + (8 * pos + 2 * w) * 1024;
      const u32x4 sl = *(const u32x4*)(lds + LDS_STASH + t16), sh = *(const u32x4*)(lds + LDS_STASH + 4096 + t16);
#pragma unroll
      for (int i = 0; i < 4; ++i) {
        xv[2 * i] = f32x2{blo(sl[i]), blo(sh[i])}; xv[2 * i + 1] = f32x2{bhi(sl[i]), bhi(sh[i])};
      }
      xc = f32x2{first_lane(xv[0].x), first_lane(xv[0].y)};
      xch = lane < 32 ? xc.x : xc.y;
      PIN(xch);
#pragma unroll
      for (int i = 0; i < 8; ++i) { xv[i] -= xc; PIN(xv[i]); }
      SB();
      bar_nw();
      ld2<0>(d0, xo, db); ld2<512>(d1, xo, db); ld2<1024>(d2, xo, db); ld2<1536>(d3, xo, db);
      ring4(0);
      SB();
      row_consts<false, 1>(rc, m, g);
      SB();
      fwd_pre(fp, tid, w, pos);
      SB();
      ST(S0 + 14);
      vmwait<4>(); PIN(d0); PIN(d1); PIN(d2); PIN(d3);
      RETRY((stale<false, 1u>(red8<false>(u32x4{d0.x, d0.y, d1.x, d1.y}, u32x4{d2.x, d2.y, d3.x, d3.y}))), {
        CNT(7);
        if (bad_) { ld2r<0>(d0, xo, db); ld2r<512>(d1, xo, db); ld2r<1024>(d2, xo, db); ld2r<1536>(d3, xo, db); }
        vmwait<0>(); PIN(d0); PIN(d1); PIN(d2); PIN(d3);
      });
      xv[0] += f32x2{blo(d0.x), blo(d2.x)}; xv[1] += f32x2{bhi(d0.x), bhi(d2.x)}; xv[2] += f32x2{blo(d0.y), blo(d2.y)}; xv[3] += f32x2{bhi(d0.y), bhi(d2.y)};
      xv[4] += f32x2{blo(d1.x), blo(d3.x)}; xv[5] += f32x2{bhi(d1.x), bhi(d3.x)}; xv[6] += f32x2{blo(d1.y), blo(d3.y)}; xv[7] += f32x2{bhi(d1.y), bhi(d3.y)};
      ST(S0 + 15);
    }
  };
  layer(std::integral_constant<int, 0>{});
  layer(std::integral_constant<int, 1>{});
  if (blockIdx.x == 0 && threadIdx.x == 0) st2(ep, OFF_E);
  ST_OUT();
}

extern "C" void kernel_launch(void* const* d_in, const int* in_sizes, int n_in, void* d_out, int out_size, void* d_ws, size_t ws_size,
                              hipStream_t stream) {
  Params p;
  p.x = (const u16*)d_in[0]; p.wr = (const u16*)d_in[1]; p.wi = (const u16*)d_in[2];
  p.wg = (const u16*)d_in[4]; p.wu = (const u16*)d_in[5]; p.wd = (const u16*)d_in[6];
  p.out = (u16*)d_out; p.ws = (u8*)d_ws;
  (void)hipFuncSetAttribute((const void*)k_all, hipFuncAttributeMaxDynamicSharedMemorySize, LDS_TOTAL);
  k_all<<<256, 256, LDS_TOTAL, stream>>>(p);
}
```

```cpp
#include <hip/hip_runtime.h>
#include <hip/hip_bf16.h>
#include <stdint.h>
#include <type_traits>
#include <utility>
#define STAMPS 1

typedef unsigned char u8;
typedef unsigned short u16;
typedef unsigned int u32;
typedef unsigned long long u64;
typedef u32 u32x2 __attribute__((ext_vector_type(2)));
typedef u32 u32x4 __attribute__((ext_vector_type(4)));
typedef float f32x2 __attribute__((ext_vector_type(2)));
typedef float f32x4 __attribute__((ext_vector_type(4)));
typedef _Float16 f16x2 __attribute__((ext_vector_type(2)));
typedef __bf16 bf16x2 __attribute__((ext_vector_type(2)));
typedef __bf16 bf16x8 __attribute__((ext_vector_type(8)));
#define DI __device__ __forceinline__
#define SB() __builtin_amdgcn_sched_barrier(0)
#define MEMFENCE() asm volatile("" ::: "memory")

#define XSLP 4
constexpr u32 MB2 = 2u << 20;
constexpr u32 OFF_E = 0, OFF_FLAG = 8192, OFF_WNY = 65536;
constexpr u32 OFF_A = MB2;
constexpr u32 OFF_U = OFF_A + MB2;
constexpr u32 OFF_WX = OFF_U + MB2;
constexpr u32 OFF_X3 = OFF_WX + MB2;
constexpr u32 OFF_H = OFF_X3 + MB2;
constexpr u32 OFF_W1 = OFF_H + 7340032;
constexpr u32 OFF_W2 = OFF_W1 + 3670016;
constexpr u32 WS_USED = OFF_W2 + 1835008;
constexpr u32 SENT = 0xFFFFFFFFu;
constexpr int SPIN_CAP = 1 << 15;
constexpr u32 CODE_BYTES = 66 * 1024;

constexpr int LDS_SCR = 131072, LDS_STASH = LDS_SCR + 24576, LDS_TOTAL = 163840;

struct Params {
  const u16* x; const u16* wr; const u16* wi; const u16* wg; const u16* wu; const u16* wd; u16* out; u8* ws;
};

constexpr size_t OFF_DBG = 64u << 20;
constexpr int NSTAMP = 64;
DI u64 rt_() { u64 t; asm volatile("s_memrealtime %0\n\ts_waitcnt lgkmcnt(0)" : "=s"(t)::"memory"); return t; }
template <int I> DI void wl_(u32& v, u32 x) { asm volatile("v_writelane_b32 %0, %1, %2" : "+v"(v) : "s"(x), "i"(I)); }
DI u64 ct_() { u64 t; asm volatile("s_memtime %0\n\ts_waitcnt lgkmcnt(0)" : "=s"(t)::"memory"); return t; }
#define ST(i) do { SB(); u64 t_ = rt_(); wl_<(i)>(stv_, (u32)t_); t_ = ct_(); wl_<(i)>(stc_, (u32)t_); SB(); } while (0)
DI void qt_(u64& t) { asm volatile("s_memtime %0" : "=s"(t)::"memory"); }
#define ST_DECL u32 stv_ = 0, stc_ = 0, cnt_ = 0
#define CNT(i) do { cnt_ += 1u << (4 * (i)); } while (0)
#define ST_OUT() do { wl_<63>(stv_, __builtin_amdgcn_readfirstlane(cnt_)); if (threadIdx.x < NSTAMP) { ((u32*)(P.ws + OFF_DBG))[blockIdx.x * NSTAMP + threadIdx.x] = stv_; ((u32*)(P.ws + OFF_DBG))[(256 + blockIdx.x) * NSTAMP + threadIdx.x] = stc_; ((u32*)(P.ws + OFF_DBG + (1u << 20)))[((ep.x & 15) * 256 + blockIdx.x) * NSTAMP + threadIdx.x] = stv_; } } while (0)

#define XS(i)
#define XF(i)
DI u32 pkh(float a, float b) { f32x2 v = {a, b}; return __builtin_bit_cast(u32, __builtin_convertvector(v, f16x2)); }
DI u32 pkb(float a, float b) { f32x2 v = {a, b}; return __builtin_bit_cast(u32, __builtin_convertvector(v, bf16x2)); }
template <bool BF> DI u32 pkc(float a, float b) { return BF ? pkb(a, b) : pkh(a, b); }
DI u32 pkh_n0(float a, float b, u32 after) { u32 r; asm("v_cvt_pk_f16_f32 %0, -%1, %2" : "=v"(r) : "v"(a), "v"(b), "v"(after)); return r; }
DI u32 pkb_n1(float a, float b, u32 after) { u32 r; asm("v_cvt_pk_bf16_f32 %0, %1, -%2" : "=v"(r) : "v"(a), "v"(b), "v"(after)); return r; }
DI float blo(u32 u) { return __uint_as_float(u << 16); }
DI float bhi(u32 u) { return __uint_as_float(u & 0xffff0000u); }
DI float bfr(float a) { return blo(pkb(a, a)); }
DI float relu(float a) { const int i = __float_as_int(a); return __int_as_float(i > 0 ? i : 0); }
DI u32 umax(u32 a, u32 b) { return a > b ? a : b; }
DI u32 max3(u32 a, u32 b, u32 c) { return umax(umax(a, b), c); }
DI u32 max4(u32 acc, u32x4 v) { return max3(max3(acc, v.x, v.y), v.z, v.w); }
template <bool ONE> DI u32 red3(u32 a, u32 b, u32 c) { return ONE ? (a & b & c) : (a | b | c); }
template <bool ONE> DI u32 red2(u32 a, u32 b) { return ONE ? (a & b) : (a | b); }
template <bool ONE> DI u32 red4(u32 acc, u32x4 v) { return red3<ONE>(red3<ONE>(acc, v.x, v.y), v.z, v.w); }
template <bool ONE> DI u32 red8(u32x4 a, u32x4 b) { return red4<ONE>(red3<ONE>(red2<ONE>(a.x, a.y), a.z, a.w), b); }
template <bool ONE, u32 BIT> DI bool stale(u32 r) { return ONE ? (r & BIT) == 0 : (r & BIT) != 0; }
template <u32 CLR, u32 SET> DI u32 mark(u32 v) { return (v & ~CLR) | SET; }
template <u32 CLR, u32 SET> DI u32x4 mark4(u32x4 v) { return u32x4{mark<CLR, SET>(v.x), mark<CLR, SET>(v.y), mark<CLR, SET>(v.z), mark<CLR, SET>(v.w)}; }
DI f32x2 lo2(f32x4 v) { return __builtin_shufflevector(v, v, 0, 1); }
DI f32x2 hi2(f32x4 v) { return __builtin_shufflevector(v, v, 2, 3); }
DI f32x2 swp(f32x2 v) { return __builtin_shufflevector(v, v, 1, 0); }
DI f32x4 mfma_b(u32x4 a, u32x4 b, f32x4 c) {
  return __builtin_amdgcn_mfma_f32_16x16x32_bf16(__builtin_bit_cast(bf16x8, a), __builtin_bit_cast(bf16x8, b), c, 0, 0, 0);
}
template <class T> DI void PIN(T& x) { asm volatile("" : "+v"(x)); }
template <class T> DI void PINS(T& x) { asm volatile("" : "+s"(x)); }
DI int opq(int x) { asm("" : "+s"(x)); return x; }
template <class T> DI void PINA(T& x) { asm volatile("" : "+a"(x)); }
template <class F, int... I> DI void static_for_(F&& f, std::integer_sequence<int, I...>) { (f(std::integral_constant<int, I>{}), ...); }
template <int N, class F> DI void static_for(F&& f) { static_for_(f, std::make_integer_sequence<int, N>{}); }

#define MM_(T) "v_mfma_f32_16x16x32_" T
template <bool BF> DI void mm4(f32x4& d0, f32x4& d1, f32x4& d2, f32x4& d3, u32x4 a0, u32x4 a1, u32x4 b0, u32x4 b1) {
#define MM4_(T) asm volatile("s_nop 1\n\t" MM_(T) " %0, %4, %6, 0\n\t" MM_(T) " %1, %4, %7, 0\n\t" MM_(T) " %2, %5, %6, 0\n\t" MM_(T) " %3, %5, %7, 0\n\ts_nop 5" \
               : "=&v"(d0), "=&v"(d1), "=&v"(d2), "=&v"(d3) : "v"(a0), "v"(a1), "v"(b0), "v"(b1))
  if constexpr (BF) MM4_("bf16"); else MM4_("f16");
}
template <bool BF, int TAIL, int AG = 0> DI void mm8(f32x4& d0, f32x4& d1, f32x4& d2, f32x4& d3, u32x4 a00, u32x4 b00, u32x4 a01, u32x4 b01, u32x4 a10,
                               u32x4 b10, u32x4 a11, u32x4 b11, u32x4 a20, u32x4 b20, u32x4 a21, u32x4 b21, u32x4 a30, u32x4 b30, u32x4 a31, u32x4 b31) {
#define MM8_(T, A, B) asm volatile("s_nop 1\n\t" MM_(T) " %0, %4, %5, 0\n\t" MM_(T) " %1, %8, %9, 0\n\t" MM_(T) " %0, %6, %7, %0\n\t" MM_(T) " %1, %10, %11, %1\n\t" \
               MM_(T) " %2, %12, %13, 0\n\t" MM_(T) " %3, %16, %17, 0\n\t" MM_(T) " %2, %14, %15, %2\n\t" MM_(T) " %3, %18, %19, %3\n\ts_nop %20"         \
               : "=&v"(d0), "=&v"(d1), "=&v"(d2), "=&v"(d3)                                                                                              \
               : A(a00), B(b00), A(a01), B(b01), A(a10), B(b10), A(a11), B(b11), A(a20), B(b20), A(a21), B(b21), A(a30), B(b30), A(a31), B(b31), "i"(TAIL))
  if constexpr (!BF) MM8_("f16", "v", "v");
  else if constexpr (AG == 0) MM8_("bf16", "v", "v");
  else if constexpr (AG == 1) MM8_("bf16", "a", "v");
  else MM8_("bf16", "v", "a");
}
template <bool BF, int AG> DI void mf0(f32x4& d, u32x4 a, u32x4 b) {
  if constexpr (!BF) asm volatile(MM_("f16") " %0, %1, %2, 0" : "=&v"(d) : "v"(a), "v"(b));
  else if constexpr (AG == 0) asm volatile(MM_("bf16") " %0, %1, %2, 0" : "=&v"(d) : "v"(a), "v"(b));
  else asm volatile(MM_("bf16") " %0, %1, %2, 0" : "=&v"(d) : "v"(a), "a"(b));
}
template <bool BF, int AG, int TAIL> DI void mf4(f32x4& d0, f32x4& d1, f32x4& d2, f32x4& d3, u32x4 a0, u32x4 b0, u32x4 a1, u32x4 b1, u32x4 a2,
                                               u32x4 b2, u32x4 a3, u32x4 b3) {
#define MF4_(T, B) asm volatile("s_nop 1\n\t" MM_(T) " %0, %4, %5, %0\n\t" MM_(T) " %1, %6, %7, %1\n\t" MM_(T) " %2, %8, %9, %2\n\t" MM_(T) " %3, %10, %11, %3\n\ts_nop %12" \
               : "+v"(d0), "+v"(d1), "+v"(d2), "+v"(d3) : "v"(a0), B(b0), "v"(a1), B(b1), "v"(a2), B(b2), "v"(a3), B(b3), "i"(TAIL))
  if constexpr (!BF) MF4_("f16", "v"); else if constexpr (AG == 0) MF4_("bf16", "v"); else MF4_("bf16", "a");
}
template <bool BF, bool CONJ, class MM> DI u32x4 cplx_under(f32x4 r, f32x4 i, const f32x2 (&c)[2], const f32x2 (&s)[2], MM&& mm) {
  asm volatile("s_nop 1");
  SB(); mm(std::integral_constant<int, 0>{}); SB();
  f32x2 a0 = lo2(i) * s[0], b0 = lo2(r) * s[0], a1 = hi2(i) * s[1];
  PIN(a0); PIN(b0); PIN(a1);
  SB(); mm(std::integral_constant<int, 1>{}); SB();
  f32x2 b1 = hi2(r) * s[1], re0 = CONJ ? lo2(r) * c[0] + a0 : lo2(r) * c[0] - a0, im0 = CONJ ? lo2(i) * c[0] - b0 : lo2(i) * c[0] + b0;
  PIN(b1); PIN(re0); PIN(im0);
  SB(); mm(std::integral_constant<int, 2>{}); SB();
  f32x2 re1 = CONJ ? hi2(r) * c[1] + a1 : hi2(r) * c[1] - a1, im1 = CONJ ? hi2(i) * c[1] - b1 : hi2(i) * c[1] + b1;
  u32 o0 = pkc<BF>(re0.x, im0.x);
  PIN(re1); PIN(im1); PIN(o0);
  SB(); mm(std::integral_constant<int, 3>{}); SB();
  return u32x4{o0, pkc<BF>(re0.y, im0.y), pkc<BF>(re1.x, im1.x), pkc<BF>(re1.y, im1.y)};
}
template <bool BF> DI u32x4 twiddle(f32x4 r, f32x4 i, const f32x2 (&c)[2], const f32x2 (&s)[2]) {
  const f32x2 re0 = lo2(r) * c[0] + lo2(i) * s[0], im0 = lo2(i) * c[0] - lo2(r) * s[0];
  const f32x2 re1 = hi2(r) * c[1] + hi2(i) * s[1], im1 = hi2(i) * c[1] - hi2(r) * s[1];
  return u32x4{pkc<BF>(re0.x, im0.x), pkc<BF>(re0.y, im0.y), pkc<BF>(re1.x, im1.x), pkc<BF>(re1.y, im1.y)};
}
DI u32x4 cmul_b(f32x4 r, f32x4 i, const f32x2 (&c)[2], const f32x2 (&s)[2]) {
  const f32x2 re0 = lo2(r) * c[0] - lo2(i) * s[0], im0 = lo2(i) * c[0] + lo2(r) * s[0];
  const f32x2 re1 = hi2(r) * c[1] - hi2(i) * s[1], im1 = hi2(i) * c[1] + hi2(r) * s[1];
  return u32x4{pkb(re0.x, im0.x), pkb(re0.y, im0.y), pkb(re1.x, im1.x), pkb(re1.y, im1.y)};
}
DI float cosr(float a) { return __builtin_amdgcn_cosf(a); }
DI float sinr(float a) { return __builtin_amdgcn_sinf(a); }
template <int CTRL> DI float dppf(float x) {
  return __builtin_bit_cast(float, __builtin_amdgcn_mov_dpp(__builtin_bit_cast(int, x), CTRL, 0xf, 0xf, true));
}
DI f32x2 bcast2(float x) {
  auto t = __builtin_amdgcn_permlane32_swap(__float_as_uint(x), __float_as_uint(x), false, false);
  return f32x2{__uint_as_float(t[0]), __uint_as_float(t[1])};
}
DI void ln_coef(f32x2 S, f32x2 Q, float ch, f32x2& R, f32x2& O) {
  auto t = __builtin_amdgcn_permlane32_swap(__float_as_uint(S.x), __float_as_uint(S.y), false, false);
  auto u = __builtin_amdgcn_permlane32_swap(__float_as_uint(Q.x), __float_as_uint(Q.y), false, false);
  const float s = __uint_as_float(t[0]) + __uint_as_float(t[1]), q = __uint_as_float(u[0]) + __uint_as_float(u[1]);
  auto z = __builtin_amdgcn_permlane16_swap(__float_as_uint(s), __float_as_uint(q), false, false);
  float x = __uint_as_float(z[0]) + __uint_as_float(z[1]);
  x += dppf<0xB1>(x);
  x += dppf<0x4E>(x);
  x += dppf<0x141>(x);
  x += dppf<0x140>(x);
  auto y = __builtin_amdgcn_permlane16_swap(__float_as_uint(x), __float_as_uint(x), false, false);
  const float md = __uint_as_float(y[0]) * (1.0f / 512.0f);
  const float var = relu(__uint_as_float(y[1]) * (1.0f / 512.0f) - md * md);
  const float r = __builtin_amdgcn_rcpf(bfr(__builtin_amdgcn_sqrtf(bfr(bfr(var) + 1e-5f))));
  const float o = (ch - bfr(ch + md)) * r;
  R = bcast2(r); O = bcast2(o);
}
DI float first_lane(float x) { return __uint_as_float(__builtin_amdgcn_readfirstlane(__float_as_uint(x))); }
DI int row_of_pos(int pos, int j) {
  const int c = 8 * (pos >> 7) + 2 * ((pos >> 3) & 3) + ((pos >> 5) & 1), q = 8 * ((pos >> 6) & 1) + (pos & 7);
  return 32 * (2 * c + (j & 1)) + 2 * q + (j >> 1);
}

template <int I> DI void ld4(u32x4& d, u32 v, const u8* s) { asm volatile("global_load_dwordx4 %0, %1, %2 offset:%3 sc1" : "=v"(d) : "v"(v), "s"(s), "i"(I) : "memory"); }
template <int I> DI void ld4a(u32x4& d, u32 v, const u8* s) { asm volatile("global_load_dwordx4 %0, %1, %2 offset:%3 sc1" : "=a"(d) : "v"(v), "s"(s), "i"(I) : "memory"); }
template <int I> DI void ld4r(u32x4& d, u32 v, const u8* s) { asm volatile("global_load_dwordx4 %0, %1, %2 offset:%3 sc1" : "+v"(d) : "v"(v), "s"(s), "i"(I) : "memory"); }
template <int I> DI void ld2(u32x2& d, u32 v, const u8* s) { asm volatile("global_load_dwordx2 %0, %1, %2 offset:%3 sc1" : "=v"(d) : "v"(v), "s"(s), "i"(I) : "memory"); }
template <int I> DI void ld2r(u32x2& d, u32 v, const u8* s) { asm volatile("global_load_dwordx2 %0, %1, %2 offset:%3 sc1" : "+v"(d) : "v"(v), "s"(s), "i"(I) : "memory"); }
template <int I> DI void ld2p(u32x2& d, u32 v, const u8* s) { asm volatile("global_load_dwordx2 %0, %1, %2 offset:%3" : "=v"(d) : "v"(v), "s"(s), "i"(I) : "memory"); }
DI void touch(u32& d, u32 v, const u8* s) { asm volatile("global_load_dword %0, %1, %2 sc1" : "+v"(d) : "v"(v), "s"(s) : "memory"); }
template <int I> DI void ld1p(u32& d, u32 v, const u8* s) { asm volatile("global_load_dword %0, %1, %2 offset:%3" : "=v"(d) : "v"(v), "s"(s), "i"(I) : "memory"); }
DI void ld1s(u32& d, u32 v, const u8* s) { asm volatile("global_load_ushort %0, %1, %2" : "=v"(d) : "v"(v), "s"(s) : "memory"); }
template <int N> DI void vmwait() { static_assert(N >= 0 && N < 64, ""); asm volatile("s_waitcnt vmcnt(%0)" ::"i"(N) : "memory"); }
DI void set_m0(u32 ldsaddr) { asm volatile("s_mov_b32 m0, %0" ::"s"(ldsaddr) : "memory"); }
template <int I> DI void dma1(u32 voff, const u8* src) {
  asm volatile("global_load_lds_dwordx4 %0, %1 offset:%2 sc1" ::"v"(voff), "s"(src), "i"(I) : "memory");
}
DI void dma4(u32 voff, const u8* src, u32 ldsaddr) {
  asm volatile("s_mov_b32 m0, %2\n\ts_nop 0\n\tglobal_load_lds_dwordx4 %0, %1 sc1\n\tglobal_load_lds_dwordx4 %0, %1 offset:1024 sc1\n\t"
               "global_load_lds_dwordx4 %0, %1 offset:2048 sc1\n\tglobal_load_lds_dwordx4 %0, %1 offset:3072 sc1" ::"v"(voff), "s"(src), "s"(ldsaddr) : "memory");
}
#define RETRY(COND, ...)                                                      \
  do {                                                                        \
    bool bad_ = (COND);                                                       \
    if (__builtin_expect(__any(bad_), 0)) {                                   \
      _Pragma("nounroll") for (int sp_ = 0; sp_ < SPIN_CAP; ++sp_) {          \
        __VA_ARGS__                                                           \
        bad_ = (COND);                                                        \
        if (!__any(bad_)) break;                                              \
      }                                                                       \
    }                                                                         \
  } while (0)
DI void bar() { asm volatile("s_waitcnt lgkmcnt(0)\n\ts_barrier" ::: "memory"); }
DI void bar_nw() { asm volatile("s_barrier" ::: "memory"); }

struct RowC { u32x4 b1re, b1im, b2re[2][2], b2im[2][2]; f32x2 twc[2][2], tws[2][2]; };
template <bool INV> DI void pair_cs(float c, float s, u32& re, u32& im) {
  if constexpr (INV) { im = pkb(s, c); re = pkb_n1(c, s, im); } else { re = pkh(c, s); im = pkh_n0(s, c, re); }
}
template <bool AG = false> DI void sign4(u32x4 base, u32 lm, u32x4& f00, u32x4& f01, u32x4& f10, u32x4& f11) {
  f00 = base;
  f01 = base ^ lm;
  f10 = base ^ u32x4{0u, 0x80008000u, 0u, 0x80008000u};
  f11 = f10 ^ lm;
  if constexpr (AG) { PINA(f00); PINA(f01); PINA(f10); PINA(f11); } else { PIN(f00); PIN(f01); PIN(f10); PIN(f11); }
}
template <bool INV, int PART> DI void row_consts(RowC& k, int m, int g) {
  const float fm = (float)m, fg = (float)(4 * g);
  if constexpr (PART == 0) {
    u32x4 br, bi;
#pragma unroll
    for (int i = 0; i < 4; ++i) {
      const float a = (fg + (float)i) * (fm * (1.0f / 16.0f));
      u32 x, y;
      pair_cs<INV>(cosr(a), sinr(a), x, y); k.b1re[i] = x; k.b1im[i] = y;
      const float a2 = (fg + (float)i) * (fm * (1.0f / 32.0f));
      pair_cs<INV>(cosr(a2), sinr(a2), x, y); br[i] = x; bi[i] = y;
    }
    PIN(k.b1re); PIN(k.b1im);
    const u32 lm = (m & 1) ? 0x80008000u : 0u;
    sign4(br, lm, k.b2re[0][0], k.b2re[0][1], k.b2re[1][0], k.b2re[1][1]);
    sign4(bi, lm, k.b2im[0][0], k.b2im[0][1], k.b2im[1][0], k.b2im[1][1]);
  } else {
#pragma unroll
    for (int e = 0; e < 2; ++e)
#pragma unroll
      for (int i = 0; i < 4; ++i) {
        const float a = (fg + (float)(16 * e + i)) * (fm * (INV ? -1.0f / 512.0f : 1.0f / 512.0f));
        k.twc[e][i >> 1][i & 1] = cosr(a); k.tws[e][i >> 1][i & 1] = sinr(a);
      }
#pragma unroll
    for (int e = 0; e < 2; ++e) { PIN(k.twc[e][0]); PIN(k.twc[e][1]); PIN(k.tws[e][0]); PIN(k.tws[e][1]); }
  }
}
template <bool INV, int TAIL> DI void row_fft(const RowC& k, u32x4 a0, u32x4 a1, f32x4& dr0, f32x4& di0, f32x4& dr1, f32x4& di1) {
  f32x4 tr0, ti0, tr1, ti1;
  mm4<INV>(tr0, ti0, tr1, ti1, a0, a1, k.b1re, k.b1im);
  u32x4 p0 = twiddle<INV>(tr0, ti0, k.twc[0], k.tws[0]);
  PIN(p0);
  const u32x4 p1 = cplx_under<INV, true>(tr1, ti1, k.twc[1], k.tws[1], [&](auto I) {
    constexpr int i = decltype(I)::value;
    mf0<INV, 0>(i == 0 ? dr0 : i == 1 ? di0 : i == 2 ? dr1 : di1, p0, i == 0 ? k.b2re[0][0] : i == 1 ? k.b2im[0][0] : i == 2 ? k.b2re[1][0] : k.b2im[1][0]);
  });
  mf4<INV, 0, TAIL>(dr0, di0, dr1, di1, p1, k.b2re[0][1], p1, k.b2im[0][1], p1, k.b2re[1][1], p1, k.b2im[1][1]);
}

struct FwdP { u32 iw, ir, ow, ap0, ap1, ar0, ar1, zo; f32x2 cB, cD, eA, eB, eC, eD; };
DI void fwd_pre(FwdP& p, int tid, int w, int pos) {
  const int m = tid & 15, g = (tid >> 4) & 3, ph = tid >> 1, fq = ph ? 512 - ph : 256, fq1 = 384 - ph;
  p.iw = w * 5120 + ((16 * (m & 1) + 4 * g) * 20 + (m >> 1)) * 4; p.ir = w * 5120 + (m * 20 + 4 * g) * 4;
  p.ap0 = (ph + (ph >> 4) * 4) * 4; p.ap1 = p.ap0 + 640; p.ar0 = (fq + (fq >> 4) * 4) * 4; p.ar1 = (fq1 + (fq1 >> 4) * 4) * 4;
  p.zo = OFF_A + pos * 8192 + tid * 16;
  const float sc = 1.0f / 64.0f, sg = (tid & 1) ? -sc : sc;
  p.cB = f32x2{sg, -sg}; p.cD = f32x2{sg, sg};
  p.eA = ph ? f32x2{sc, sc} : f32x2{2 * sc, 0.f}; p.eB = ph ? p.cB : f32x2{2 * sg, 0.f};
  p.eC = ph ? f32x2{sc, -sc} : f32x2{0.f, 2 * sg}; p.eD = ph ? p.cD : f32x2{0.f, 2 * sc};
  PIN(p.iw); PIN(p.ir); PIN(p.ap0); PIN(p.ap1); PIN(p.ar0); PIN(p.ar1); PIN(p.zo);
  PIN(p.cB); PIN(p.cD); PIN(p.eA); PIN(p.eB); PIN(p.eC); PIN(p.eD);
}
struct ColC {
  u32x4 b1re[2], b1im[2], a2re[2][2], a2im[2][2], b3re[2][2], b3im[2][2], a4re[2][2], a4im[2][2];
  f32x2 t1c[2][2], t1s[2][2], t2c[2][2], t2s[2][2];
};
template <int PART> DI void col_consts(ColC& k, int m, int g, int w) {
  const int pi_ = w >> 1, kt = w & 1;
  const float fm = (float)m, fg = (float)(4 * g);
  const u32 lm = (m & 1) ? 0x80008000u : 0u;
  if constexpr (PART == 0) {
    const float fk = (float)(2 * (16 * kt + m) + pi_);
#pragma unroll
    for (int u = 0; u < 2; ++u) {
#pragma unroll
      for (int r = 0; r < 4; ++r) {
        const float n = fg + (float)(16 * u + r), a = n * (fk * (1.0f / 64.0f)), a2 = n * (fk * (1.0f / 2048.0f));
        u32 x, y;
        pair_cs<false>(cosr(a), sinr(a), x, y); k.b1re[u][r] = x; k.b1im[u][r] = y;
        k.t1c[u][r >> 1][r & 1] = cosr(a2); k.t1s[u][r >> 1][r & 1] = sinr(a2);
      }
      PIN(k.b1re[u]); PIN(k.b1im[u]); PIN(k.t1c[u][0]); PIN(k.t1c[u][1]); PIN(k.t1s[u][0]); PIN(k.t1s[u][1]);
    }
  } else if constexpr (PART == 1) {
    u32x4 fr, fi, ir, ii;
#pragma unroll
    for (int r = 0; r < 4; ++r) {
      const float a = (fg + (float)r) * (fm * (1.0f / 32.0f)), c = cosr(a), s = sinr(a);
      u32 x, y;
      pair_cs<false>(c, s, x, y); fr[r] = x; fi[r] = y;
      pair_cs<true>(c, s, x, y); ir[r] = x; ii[r] = y;
    }
    sign4(fr, lm, k.a2re[0][0], k.a2re[0][1], k.a2re[1][0], k.a2re[1][1]);
    sign4(fi, lm, k.a2im[0][0], k.a2im[0][1], k.a2im[1][0], k.a2im[1][1]);
    sign4(ir, lm, k.b3re[0][0], k.b3re[0][1], k.b3re[1][0], k.b3re[1][1]);
    sign4(ii, lm, k.b3im[0][0], k.b3im[0][1], k.b3im[1][0], k.b3im[1][1]);
  } else if constexpr (PART == 2) {
    const float kb = (float)(32 * kt + pi_) + 2.0f * fg;
#pragma unroll
    for (int u = 0; u < 2; ++u) {
#pragma unroll
      for (int r = 0; r < 4; ++r) {
        const float a = (kb + (float)(2 * r)) * ((fm + (float)(16 * u)) * (-1.0f / 2048.0f));
        k.t2c[u][r >> 1][r & 1] = cosr(a); k.t2s[u][r >> 1][r & 1] = sinr(a);
      }
      PIN(k.t2c[u][0]); PIN(k.t2c[u][1]); PIN(k.t2s[u][0]); PIN(k.t2s[u][1]);
    }
  } else {
    const float fn = (fm + (float)(16 * (w >> 1))) * (1.0f / 64.0f);
#pragma unroll
    for (int u = 0; u < 2; ++u) {
#pragma unroll
      for (int r = 0; r < 4; ++r) {
        const float a = (2.0f * fg + (float)(2 * r + u)) * fn;
        u32 x, y;
        pair_cs<true>(cosr(a), sinr(a), x, y); k.a4re[u][0][r] = x; k.a4im[u][0][r] = y;
      }
      k.a4re[u][1] = k.a4re[u][0] ^ lm; k.a4im[u][1] = k.a4im[u][0] ^ lm;
      PIN(k.a4re[u][0]); PIN(k.a4re[u][1]); PIN(k.a4im[u][0]); PIN(k.a4im[u][1]);
    }
  }
}

DI void transpose8x4(const u32x2 (&v)[8], u32x4 (&o)[4]) {
#pragma unroll
  for (int jj = 0; jj < 4; ++jj) {
    o[0][jj] = __builtin_amdgcn_perm(v[2 * jj + 1].x, v[2 * jj].x, 0x05040100u);
    o[1][jj] = __builtin_amdgcn_perm(v[2 * jj + 1].x, v[2 * jj].x, 0x07060302u);
    o[2][jj] = __builtin_amdgcn_perm(v[2 * jj + 1].y, v[2 * jj].y, 0x05040100u);
    o[3][jj] = __builtin_amdgcn_perm(v[2 * jj + 1].y, v[2 * jj].y, 0x07060302u);
  }
}
template <int H> DI void prep_load1(const Params& P, int wt, int lane, u32x2 (&v)[8]) {
  const u32 vo = (lane < 56 ? lane : 55) * 8, vo2 = vo + 7168;
  const int piece = (wt >> 3) & 63;
  const u8* src = (const u8*)((wt >> 9) ? P.wu : P.wg) + opq(wt & 7) * 448 + opq(4 * piece) * 3584;
  static_for<4>([&](auto J) { constexpr int j = decltype(J)::value; ld2p<(j & 1) * 3584>(v[4 * H + j], j & 2 ? vo2 : vo, src + H * (256 * 3584)); });
}
template <int H> DI void prep_load2(const Params& P, int wt, int lane, u32 (&v)[8]) {
  const int piece = wt >> 2, g = piece & 3, kq = piece >> 2, wv = kq / 14, ks = kq % 14;
  const int f0 = ks < 4 ? 112 * (4 * wv + ks) : ks < 6 ? 112 * (4 * wv + 2 * (ks - 4)) + 32 : ks < 10 ? 112 * (4 * wv + ks - 6) + 48 : 112 * (4 * wv + ks - 10) + 80;
  const int f1 = f0 + ((ks == 4 || ks == 5) ? 112 : 16);
  const u8* s0 = (const u8*)P.wd + (wt & 3) * 256 + (4 * g + (H ? f1 : f0)) * 1024;
  static_for<4>([&](auto J) { constexpr int j = decltype(J)::value; ld1p<j * 1024>(v[4 * H + j], lane * 4, s0); });
}
DI u32 prep_dst1(int wt, int tile) {
  const int piece = (wt >> 3) & 63, mat = wt >> 9, hi = tile >= 7, p = tile - 7 * hi, ks = piece >> 2;
  const u32 frag = (p < 3 ? ks * 6 + 2 * p : p < 5 ? 96 + ks * 4 + 2 * (p - 3) : 160 + ks * 4 + 2 * (p - 5)) + mat;
  return OFF_W1 + ((2 * (wt & 7) + hi) * 224 + frag) * 1024 + (piece & 3) * 256;
}
DI u32 prep_dst2(int wt, int tile) {
  const int piece = wt >> 2, tl = (wt & 3) * 8 + tile, rot = (tl >> 2) & 3, kq = piece >> 2, ks = kq % 14;
  const int slot = ks < 4 ? (ks - rot) & 3 : ks < 6 ? ks ^ (rot & 1) : ks < 10 ? 6 + ((ks - 6 - rot) & 3) : 10 + ((ks - 10 - rot) & 3);
  return OFF_W2 + (((tl >> 2) * 56 + kq - ks + slot) * 4 + (tl & 3)) * 1024 + (piece & 3) * 256;
}

constexpr int NTOK0 = 12, NRES = 40, NEARLY = 26;
constexpr int g1_job(int L, int x) {
  if (((x >= 34 && x < 96) && (x & 3) == 2) || (x >= 98 && x < 114 && !(x & 1))) return 2;
  if ((x < 34 && !(x & 1)) || (x < 64 && (x & 3) == 0)) return 1;
  if (L == 1 && x >= 47 && x < 60 && (x & 3) == 3) return 6;
  if (x >= 164) return ((x - 164) % 7 == 0 || (x - 164) % 7 == 2 || (x - 164) % 7 == 5) ? 4 : 0;
  if (x >= 116 && x % 3 == 2) return 5;
  if (L == 0 && ((x < 34 && x % 4 == 1) || x == 158 || x == 161 || x == 163 || (x >= 48 && x < 98 && (x & 3) == 0) || (x >= 114 && x % 3 == 0) || (x >= 35 && x < 98 && x % 8 == 1)))
    return 3;
  return 0;
}
constexpr int NEPI1 = 123, NEPI2 = 82;
struct G1Sched {
  int job[224], idx[224], epi[225], nst[224], cum[225], pos[7][64], cnt[7];
  constexpr G1Sched(int L) : job{}, idx{}, epi{}, nst{}, cum{}, pos{}, cnt{} {
    cnt[1] = NTOK0;
    for (int i = 0; i < NTOK0; ++i) pos[1][i] = i;
    cum[0] = NTOK0;
    for (int x = 0; x < 224; ++x) {
      int j = g1_job(L, x);
      if (j == 1 && cnt[1] >= 32) j = L == 0 ? 3 : 0;
      if ((j == 3 && cnt[3] >= NRES) || (j == 4 && cnt[4] >= NEARLY)) j = 0;
      job[x] = j; idx[x] = cnt[j];
      if (x == 161) epi[x] = NEPI1;
      const bool on = (x >= 97 && x < 160 && epi[x] < NEPI1) || x >= 161;
      epi[x + 1] = on ? epi[x] + (j ? 2 : 3) : epi[x];
      if (x < 160 && epi[x + 1] > NEPI1) epi[x + 1] = NEPI1;
      for (int n = epi[x]; n < epi[x + 1]; ++n)
        nst[x] += n < NEPI1 ? (n == 40 || n == 81 || n == 122) : (n < NEPI1 + NEPI2 && (n - NEPI1) % 41 == 40);
      if (j) pos[j][cnt[j]++] = cum[x] + nst[x];
      cum[x + 1] = cum[x] + nst[x] + (j ? 1 : 0);
    }
  }
};
constexpr G1Sched G1S[2] = {G1Sched(0), G1Sched(1)};
static_assert(G1S[0].cnt[1] == 32 && G1S[0].cnt[2] == 24 && G1S[0].cnt[3] == NRES && G1S[0].cnt[4] == NEARLY && G1S[0].cnt[5] == 16, "");
static_assert(G1S[1].cnt[6] == 4 && G1S[1].pos[6][3] < G1S[1].pos[2][7], "");
static_assert(G1S[0].idx[66] == 8 && G1S[0].idx[98] == 16 && G1S[0].epi[113] <= 40 && G1S[1].epi[113] <= 40, "");
static_assert(G1S[0].epi[152] == NEPI1 && G1S[0].epi[204] >= NEPI1 + NEPI2, "");
constexpr int clamp63(int n) { return n > 63 ? 63 : n; }
constexpr int g1_wait(int L, int x, int job, int idx) { return clamp63(G1S[L].cum[x] - G1S[L].pos[job][idx] - 1); }

#define FRESH()                                                                                                   \
  int tid = threadIdx.x, b = blockIdx.x, w = w_;                                                                   \
  u8* ws = P.ws;                                                                                                   \
  asm volatile("" : "+v"(tid), "+s"(b), "+s"(w), "+s"(ws));                                                        \
  const int lane = tid & 63, m = lane & 15, g = lane >> 4, half2 = b & 1, ct = b >> 5;                             \
  const int G = ((b >> 1) & 3) * 4 + ((b >> 3) & 3), member = half2 * 8 + ct, pos = G * 16 + member;               \
  const u32 l16 = lane * 16, t16 = tid * 16, xo = 32 * m + 8 * g;                                                  \
  auto fill_h = [&](u32 base, int q) { st4(sent4, base + (4 * G + w) * 114688 + member * 7168 + q * 1024 + l16); };          \
  auto fill_rows = [&](u32 base) { st4(sent4, base + pos * 8192 + t16); st4(sent4, base + pos * 8192 + 4096 + t16); }; \
  auto ring4 = [&](int q) {                                   \
    const u32 o = (q >> 1) * 32768 + w * 8192 + (q & 1) * 4096;                                                    \
    dma4(l16, ws + OFF_W1 + member * 229376 + o, ldsbase + o);                                                     \
  };                                                                                                               \
  (void)m; (void)g; (void)half2; (void)ct; (void)l16; (void)t16; (void)xo; (void)fill_h; (void)fill_rows; (void)ring4

__global__ __launch_bounds__(256, 1) void k_all(Params P) {
  extern __shared__ __attribute__((aligned(16))) u8 lds[];
  ST_DECL;
  const int w_ = __builtin_amdgcn_readfirstlane(threadIdx.x >> 6);
  const __amdgpu_buffer_rsrc_t rs = __builtin_amdgcn_make_buffer_rsrc(P.ws, 0, WS_USED, 0x00020000);
  const u32 ldsbase = (u32)(size_t)(__attribute__((address_space(3))) u8*)lds;
  const u32x4 sent4 = {SENT, SENT, SENT, SENT};
  auto st4 = [&](u32x4 v, u32 off) { __builtin_amdgcn_raw_buffer_store_b128(v, rs, off, 0, 16); };
  auto st2 = [&](u32x2 v, u32 off) { __builtin_amdgcn_raw_buffer_store_b64(v, rs, off, 0, 16); };
  u32x2 xl[2], xh[2], pv0[8];
  u32 pv1[8];
  u32 wfr[8], wfi[8], wn[8];
  f32x2 xv[8], xc;
  float xch;
  FwdP fp;
  u32 pd[6];
  u32x2 ep;
  u32 cpf = 0;
  RowC rc;
  ColC cc;
  ST(0);
  {
    FRESH();
    const u8* xp = (const u8*)P.x + row_of_pos(pos, w) * 1024;
    ld2p<0>(xl[0], xo, xp); ld2p<512>(xl[1], xo, xp); ld2p<0>(xh[0], xo, xp + 1024 * 1024); ld2p<512>(xh[1], xo, xp + 1024 * 1024);
    { const u32 z = 0; ld2<0>(ep, z, ws); }
    {
      const u8* pc;
      asm volatile("s_getpc_b64 %0" : "=s"(pc));
      const u32 o = (((b >> 3) * 4 + w) * 8 + (lane & 7)) * 128;
      touch(cpf, o < CODE_BYTES ? o : 0, pc);
    }
    XS(32);
    fill_rows(OFF_A); fill_rows(OFF_WX);
    if (tid < 8) st4(sent4, OFF_WNY + tid * 4096 + pos * 16);
    SB();
    row_consts<false, 0>(rc, m, g);
    row_consts<false, 1>(rc, m, g);
    SB();
    XS(33);
    const int kb = 2 * (16 * ((pos >> 6) & 1) + (pos & 15)) + (pos >> 7) + 256 * ((pos >> 4) & 3);
    static_for<8>([&](auto J) {
      constexpr int j = decltype(J)::value;
      const u32 o = tid * 2 + (kb + 64 * (16 * (j >> 2) + (j & 3))) * 514;
      ld1s(wfr[j], o, (const u8*)P.wr); ld1s(wfi[j], o, (const u8*)P.wi);
    });
    {
      const int k = kb + 64 * (16 * ((lane >> 2) & 1) + (lane & 3)), km = (2048 - k) & 2047;
      const u32 o = k * 514, om = km * 514;
      ld1s(wn[0], o, (const u8*)P.wr); ld1s(wn[1], o, (const u8*)P.wi); ld1s(wn[2], om, (const u8*)P.wr); ld1s(wn[3], om, (const u8*)P.wi);
      ld1s(wn[4], o, (const u8*)P.wr + 512); ld1s(wn[5], o, (const u8*)P.wi + 512); ld1s(wn[6], om, (const u8*)P.wr + 512); ld1s(wn[7], om, (const u8*)P.wi + 512);
    }
    XS(34);
    fwd_pre(fp, tid, w, pos);
#pragma unroll
    for (int x = 0; x < 4; ++x) pd[x] = x < 3 || g < 2 ? prep_dst1(b * 4 + w, 4 * x + g) + m * 16 : WS_USED;
#pragma unroll
    for (int x = 0; x < 2; ++x) pd[4 + x] = b < 224 ? prep_dst2(b * 4 + w, 4 * x + g) + m * 16 : WS_USED;
#pragma unroll
    for (int x = 0; x < 6; ++x) PIN(pd[x]);
    SB();
    XS(35);
    vmwait<1 + 4 + 24>();
    XS(36);
    PIN(ep); PIN(xl[0]); PIN(xl[1]); PIN(xh[0]); PIN(xh[1]);
    { const u64 e = (((u64)(u32)__builtin_amdgcn_readfirstlane(ep.y) << 32) | (u32)__builtin_amdgcn_readfirstlane(ep.x)) + 1; ep = u32x2{(u32)e, (u32)(e >> 32)}; }
#pragma unroll
    for (int t = 0; t < 2; ++t)
#pragma unroll
      for (int p = 0; p < 2; ++p) {
        xv[4 * t + 2 * p] = f32x2{blo(xl[t][p]), blo(xh[t][p])};
        xv[4 * t + 2 * p + 1] = f32x2{bhi(xl[t][p]), bhi(xh[t][p])};
      }
    xc = f32x2{first_lane(xv[0].x), first_lane(xv[0].y)};
    xch = lane < 32 ? xc.x : xc.y;
#pragma unroll
    for (int i = 0; i < 8; ++i) xv[i] -= xc;
  }
  ST(1);

  u32x4 w2a[10][4];

  auto layer = [&](auto LC) {
    constexpr int L = decltype(LC)::value;
    constexpr int S0 = 2 + 16 * L;
    {
      FRESH();
      const f32x2 S = ((xv[0] + xv[1]) + (xv[2] + xv[3])) + ((xv[4] + xv[5]) + (xv[6] + xv[7]));
      f32x2 q0 = xv[0] * xv[0], q1 = xv[1] * xv[1];
#pragma unroll
      for (int i = 2; i < 8; i += 2) { q0 += xv[i] * xv[i]; q1 += xv[i + 1] * xv[i + 1]; }
      if constexpr (L == 0) { SB(); fill_rows(OFF_U); SB(); }
      f32x2 R, O;
      XF(32);
      ln_coef(S, q0 + q1, xch, R, O);
      XF(33);
      u8* img = lds + LDS_SCR;
      if constexpr (L == 0) {
        SB(); vmwait<26>(); PIN(cpf);
        if (lane == 0) st2(ep, OFF_FLAG + (w * 256 + pos) * 8);
        prep_load1<0>(P, b * 4 + w, lane, pv0); SB();
      }
#pragma unroll
      for (int i = 0; i < 8; ++i) {
        const f32x2 v = xv[i] * R + O;
        *(u32*)(img + fp.iw + ((i & 3) * 20 + 8 * (i >> 2)) * 4) = pkh(v.x, v.y);
      }
      const u32x4 a0 = *(const u32x4*)(img + fp.ir), a1 = *(const u32x4*)(img + fp.ir + 1280);
      if constexpr (L == 0) prep_load1<1>(P, b * 4 + w, lane, pv0); else ring4(1);
      const float* p0 = (const float*)(img + fp.ap0);
      const float* p1 = (const float*)(img + fp.ap1);
      const float* r0 = (const float*)(img + fp.ar0);
      const float* r1 = (const float*)(img + fp.ar1);
      const f32x2 cA = {1.0f / 64.0f, 1.0f / 64.0f}, cC = {1.0f / 64.0f, -1.0f / 64.0f}, cB = fp.cB, cD = fp.cD, eA = fp.eA, eB = fp.eB, eC = fp.eC, eD = fp.eD;
      const u32 zo = fp.zo;
      f32x4 dr0, di0, dr1, di1;
      XF(34);
      row_fft<false, 6>(rc, a0, a1, dr0, di0, dr1, di1);
      XF(35);
      SB();
      float* op = (float*)(img + fp.ir);
      *(f32x4*)(op) = dr0; *(f32x4*)(op + 640) = di0;
      SB();
      *(f32x4*)(op + 320) = dr1; *(f32x4*)(op + 960) = di1;
      if constexpr (L == 0) { prep_load2<0>(P, (b < 224 ? b : 0) * 4 + w, lane, pv1); vmwait<13>(); }
      bar();
      XF(36);
      f32x2 pa[4], qa[4], pb[4], qb[4];
#pragma unroll
      for (int j = 0; j < 4; ++j) {
        pa[j] = f32x2{p0[j * 1280], p0[j * 1280 + 640]}; qa[j] = f32x2{r0[j * 1280], r0[j * 1280 + 640]};
      }
#pragma unroll
      for (int j = 0; j < 4; ++j) {
        pb[j] = f32x2{p1[j * 1280], p1[j * 1280 + 640]}; qb[j] = f32x2{r1[j * 1280], r1[j * 1280 + 640]};
      }
      if constexpr (L == 0) prep_load2<1>(P, (b < 224 ? b : 0) * 4 + w, lane, pv1);
      SB();
      u32x4 o;
#pragma unroll
      for (int j = 0; j < 4; ++j) {
        const f32x2 v = eD * swp(qa[j]) + (eC * qa[j] + (eB * swp(pa[j]) + eA * pa[j]));
        o[j] = mark<(L == 0 ? 0x10001u : 0u), (L == 0 ? 0u : 0x10000u)>(pkh(v.x, v.y));
      }
      st4(o, zo);
#pragma unroll
      for (int j = 0; j < 4; ++j) {
        const f32x2 v = cD * swp(qb[j]) + (cC * qb[j] + (cB * swp(pb[j]) + cA * pb[j]));
        o[j] = mark<(L == 0 ? 0x10001u : 0u), (L == 0 ? 0u : 0x10000u)>(pkh(v.x, v.y));
      }
      st4(o, zo + 4096);
      SB();
      if constexpr (L == 1) {
#pragma unroll
        for (int t = 0; t < 2; ++t)
#pragma unroll
          for (int p = 0; p < 2; ++p) {
            const f32x2 v0 = xv[4 * t + 2 * p] + xc, v1 = xv[4 * t + 2 * p + 1] + xc;
            xl[t][p] = pkb(v0.x, v1.x); xh[t][p] = pkb(v0.y, v1.y);
          }
        PIN(xl[0]); PIN(xl[1]); PIN(xh[0]); PIN(xh[1]);
      }
    }
    if constexpr (L == 0) {
      XS(37);
      FRESH();
#pragma unroll
      for (int j = 0; j < 8; ++j) { PIN(wfr[j]); PIN(wfi[j]); PIN(wn[j]); }
      u8* bw = lds + w * 2048;
      u32x4 v0, v1;
#pragma unroll
      for (int j = 0; j < 4; ++j) { v0[j] = wfr[j] | (wfi[j] << 16); v1[j] = wfr[4 + j] | (wfi[4 + j] << 16); }
      *(u32x4*)(bw + lane * 32) = v0; *(u32x4*)(bw + lane * 32 + 16) = v1;
      st4(*(const u32x4*)(bw + l16), OFF_WX + pos * 8192 + w * 2048 + l16);
      st4(*(const u32x4*)(bw + 1024 + l16), OFF_WX + pos * 8192 + w * 2048 + 1024 + l16);
      const float hr = blo(wn[0]) + blo(wn[2]), hi = blo(wn[1]) - blo(wn[3]), nr = blo(wn[4]) + blo(wn[6]), ni = blo(wn[5]) - blo(wn[7]);
      const f32x4 ab = {0.25f * (hr + nr), 0.25f * (hi + ni), 0.25f * (hr - nr), 0.25f * (hi - ni)};
      if (tid < 8) st4(__builtin_bit_cast(u32x4, ab), OFF_WNY + tid * 4096 + pos * 16);
    }
    ST(S0);
    {
      FRESH();
      const int phi = pos, pi_ = w >> 1, kt = w & 1;
      const int k1l = 2 * (16 * kt + m) + pi_;
      auto consts = [&](auto PC) { col_consts<decltype(PC)::value>(cc, m, g, w); SB(); };
      using I0 = std::integral_constant<int, 0>; using I1 = std::integral_constant<int, 1>;
      using I2 = std::integral_constant<int, 2>; using I3 = std::integral_constant<int, 3>;
      u8* const bw = lds + 16384 + w * 8192;
      const u32 zo = (tid >> 1) * 8192 + (tid & 1) * 16;
      const u8* zb0 = ws + OFF_A + phi * 32;
      const u8* zb1 = zb0 + 128 * 8192;
      const u8* wb0 = ws + OFF_WX + phi * 32;
      const u8* wb1 = wb0 + 128 * 8192;
      const u32 wo = zo;
      PINS(zb0); PINS(zb1); PINS(wb0); PINS(wb1);
      if constexpr (L == 0) {
        u32x4 fl0, fl1;
        const u32 o = lane * 32;
        const u8* fb = ws + OFF_FLAG + ((phi >> 5) & 3) * 2048;
        ld4<0>(fl0, o, fb); ld4<16>(fl1, o, fb);
        SB();
        consts(I0{});
        consts(I1{});
        XS(38);
        vmwait<0>(); PIN(fl0); PIN(fl1);
        RETRY(!(fl0.x == ep.x && fl0.y == ep.y && fl0.z == ep.x && fl0.w == ep.y && fl1.x == ep.x && fl1.y == ep.y && fl1.z == ep.x && fl1.w == ep.y), {
          CNT(0);
          ld4r<0>(fl0, o, fb); ld4r<16>(fl1, o, fb);
          vmwait<0>(); PIN(fl0); PIN(fl1);
        });
      } else {
        SB();
        consts(I0{});
        consts(I2{});
      }
      u32x4 z0, z1, wv[2];
      if constexpr (L == 0) XS(39);
      ld4<0>(z0, zo, zb0); ld4<0>(z1, zo, zb1);
      ld4<0>(wv[0], wo, wb0); ld4<0>(wv[1], wo, wb1);
      SB();
      u32x4 wa[8], po[6];
      if constexpr (L == 0) {
        XS(40);
        u32x4 ot[4];
#pragma unroll
        for (int j = 0; j < 8; ++j) { PIN(pv0[j]); PIN(pv1[j]); }
        transpose8x4(pv0, ot);
#pragma unroll
        for (int x = 0; x < 4; ++x) *(u32x4*)(bw + lane * 64 + ((x + lane) & 3) * 16) = ot[x];
#pragma unroll
        for (int jj = 0; jj < 4; ++jj) {
          ot[0][jj] = __builtin_amdgcn_perm(pv1[2 * jj + 1], pv1[2 * jj], 0x05040100u);
          ot[1][jj] = __builtin_amdgcn_perm(pv1[2 * jj + 1], pv1[2 * jj], 0x07060302u);
        }
        *(u32x4*)(bw + 4096 + lane * 32) = ot[0]; *(u32x4*)(bw + 4096 + lane * 32 + 16) = ot[1];
#pragma unroll
        for (int x = 0; x < 4; ++x) {
          const int src = x * 16 + (lane >> 2);
          po[x] = *(const u32x4*)(bw + src * 64 + (((lane & 3) + src) & 3) * 16);
        }
#pragma unroll
        for (int x = 0; x < 2; ++x) {
          po[4 + x] = *(const u32x4*)(bw + 4096 + x * 1024 + (8 * (m >> 2) + 4 * (g & 1) + (m & 3) + 32 * (g >> 1)) * 16);
        }
#pragma unroll
        for (int x = 0; x < 6; ++x) PIN(po[x]);
        st4(po[0], pd[0]); st4(po[1], pd[1]);
        SB();
        XS(41);
        consts(I2{});
        XS(42);
        consts(I3{});
        XS(43);
        st4(po[2], pd[2]); st4(po[3], pd[3]);
      } else {
        consts(I1{});
        ring4(2);
        SB();
        consts(I3{});
      }
      u8* const zi = lds + LDS_SCR;
      u32 zw;
      {
        const int pp = tid >> 1;
        zw = (tid & 1) * 4096 + ((pp >> 6) & 1) * 2048 + (((pp >> 3) & 3) * 16 + 2 * (pp & 7)) * 16 + ((pp >> 5) & 1) * 8;
        PIN(zw);
      }
      u32 uo = OFF_U + phi * 8192 + w * 2048 + l16;
      PIN(uo);
      SB();
      bar_nw();
      ST(S0 + 1);
      vmwait<4>();
      PIN(z0); PIN(z1); PIN(wv[0]); PIN(wv[1]);
      if constexpr (L == 0) {
        RETRY((stale<false, 1u>(red8<false>(z0, z1)) || max4(max4(0, wv[0]), wv[1]) == SENT), {
          CNT(1);
          if (bad_) { ld4r<0>(z0, zo, zb0); ld4r<0>(z1, zo, zb1); ld4r<0>(wv[0], wo, wb0); ld4r<0>(wv[1], wo, wb1); }
          vmwait<0>(); PIN(z0); PIN(z1); PIN(wv[0]); PIN(wv[1]);
        });
      } else {
        RETRY((stale<true, 0x10000u>(red8<true>(z0, z1))), {
          CNT(2);
          if (bad_) { ld4r<0>(z0, zo, zb0); ld4r<0>(z1, zo, zb1); }
          vmwait<0>(); PIN(z0); PIN(z1);
        });
      }
      ST(S0 + 2);
      *(u32x2*)(zi + zw) = u32x2{z0.x, z0.y}; *(u32x2*)(zi + zw + 16) = u32x2{z0.z, z0.w};
      *(u32x2*)(zi + zw + 1024) = u32x2{z1.x, z1.y}; *(u32x2*)(zi + zw + 1024 + 16) = u32x2{z1.z, z1.w};
      *(u32x4*)(zi + 8192 + t16) = wv[0]; *(u32x4*)(zi + 12288 + t16) = wv[1];
      bar();
      XF(37);
      const u8* zr = zi + pi_ * 4096 + l16;
      const u32x4 f00 = *(const u32x4*)(zr), f01 = *(const u32x4*)(zr + 1024), f10 = *(const u32x4*)(zr + 2048), f11 = *(const u32x4*)(zr + 3072);
      wv[0] = *(const u32x4*)(zi + 8192 + tid * 32); wv[1] = *(const u32x4*)(zi + 8192 + tid * 32 + 16);
      if (phi == 0) static_for<8>([&](auto J) { constexpr int j = decltype(J)::value; ld4<0>(wa[j], t16, ws + OFF_WNY + j * 4096); });
      if constexpr (L == 1) ring4(3); else { st4(po[4], pd[4]); st4(po[5], pd[5]); }
      f32x4 d0, d1, d2, d3;
      mm8<false, 3>(d0, d1, d2, d3, f00, cc.b1re[0], f01, cc.b1re[1], f00, cc.b1im[0], f01, cc.b1im[1], f10, cc.b1re[0], f11, cc.b1re[1], f10, cc.b1im[0], f11, cc.b1im[1]);
      XF(38);
      f32x4 e0, e1, e2, e3;
      u32x4 b20 = twiddle<false>(d0, d1, cc.t1c[0], cc.t1s[0]);
      PIN(b20);
      const u32x4 b21 = cplx_under<false, true>(d2, d3, cc.t1c[1], cc.t1s[1], [&](auto I) {
        constexpr int i = decltype(I)::value;
        mf0<false, 0>(i == 0 ? e0 : i == 1 ? e1 : i == 2 ? e2 : e3, i == 0 ? cc.a2re[0][0] : i == 1 ? cc.a2im[0][0] : i == 2 ? cc.a2re[1][0] : cc.a2im[1][0], b20);
      });
      f32x2 wr[2][2], wi[2][2];
      auto unpack_w = [&]() {
#pragma unroll
        for (int t = 0; t < 2; ++t)
#pragma unroll
          for (int h = 0; h < 2; ++h) {
            wr[t][h] = f32x2{blo(wv[t][2 * h]), blo(wv[t][2 * h + 1])}; wi[t][h] = f32x2{bhi(wv[t][2 * h]), bhi(wv[t][2 * h + 1])};
            PIN(wr[t][h]); PIN(wi[t][h]);
          }
      };
      mf4<false, 0, 0>(e0, e1, e2, e3, cc.a2re[0][1], b21, cc.a2im[0][1], b21, cc.a2re[1][1], b21, cc.a2im[1][1], b21);
      SB();
      unpack_w();
      SB();
      if constexpr (L == 0) { fill_h(OFF_H, 0); fill_h(OFF_H, 1); }
      XF(39);
      u32x4 a30, a31;
      auto step3 = [&](auto I) {
        constexpr int i = decltype(I)::value;
        mf0<true, 0>(i == 0 ? d0 : i == 1 ? d1 : i == 2 ? d2 : d3, a30, i == 0 ? cc.b3re[0][0] : i == 1 ? cc.b3im[0][0] : i == 2 ? cc.b3re[1][0] : cc.b3im[1][0]);
      };
      if (phi != 0) {
        a30 = cmul_b(e0, e1, wr[0], wi[0]);
        PIN(a30);
        a31 = cplx_under<true, false>(e2, e3, wr[1], wi[1], step3);
      } else {
        u32* pl = (u32*)(lds + LDS_STASH);
        const u32x4 h0 = {pkh(e0[0], e1[0]), pkh(e0[1], e1[1]), pkh(e0[2], e1[2]), pkh(e0[3], e1[3])};
        const u32x4 h1 = {pkh(e2[0], e3[0]), pkh(e2[1], e3[1]), pkh(e2[2], e3[2]), pkh(e2[3], e3[3])};
        if (k1l != 0) {
          *(u32x4*)(pl + k1l * 32 + ((g ^ (k1l >> 1)) & 7) * 4) = h0; *(u32x4*)(pl + k1l * 32 + (((4 + g) ^ (k1l >> 1)) & 7) * 4) = h1;
        } else {
#pragma unroll
          for (int r = 0; r < 4; ++r) { pl[(4 * g + r - 1) & 31] = h0[r]; pl[16 + 4 * g + r - 1] = h1[r]; }
        }
        bar();
        vmwait<4>();
#pragma unroll
        for (int j = 0; j < 8; ++j) PIN(wa[j]);
        if constexpr (L == 0) {
          RETRY(max4(max4(max4(max4(max4(max4(max4(max4(0, wa[0]), wa[1]), wa[2]), wa[3]), wa[4]), wa[5]), wa[6]), wa[7]) == SENT, {
            static_for<8>([&](auto J) { constexpr int j = decltype(J)::value; ld4r<0>(wa[j], t16, ws + OFF_WNY + j * 4096); });
            vmwait<0>();
            _Pragma("unroll") for (int j = 0; j < 8; ++j) PIN(wa[j]);
          });
        }
        const int kr = (64 - k1l) & 63;
        const u32x4 q0 = *(const u32x4*)(pl + kr * 32 + (((7 - g) ^ (kr >> 1)) & 7) * 4), q1 = *(const u32x4*)(pl + kr * 32 + (((3 - g) ^ (kr >> 1)) & 7) * 4);
#pragma unroll
        for (int r = 0; r < 4; ++r) {
          const f32x4 c0 = __builtin_bit_cast(f32x4, wa[r]), c1 = __builtin_bit_cast(f32x4, wa[4 + r]);
          const u32 u0 = q0[3 - r], u1 = q1[3 - r];
          const f16x2 v0 = __builtin_bit_cast(f16x2, u0), v1 = __builtin_bit_cast(f16x2, u1);
          const float qr0 = (float)v0.x, qi0 = (float)v0.y, qr1 = (float)v1.x, qi1 = (float)v1.y;
          a30[r] = pkb(e0[r] * c0[0] - e1[r] * c0[1] + qr0 * c0[2] + qi0 * c0[3], e0[r] * c0[1] + e1[r] * c0[0] + qr0 * c0[3] - qi0 * c0[2]);
          a31[r] = pkb(e2[r] * c1[0] - e3[r] * c1[1] + qr1 * c1[2] + qi1 * c1[3], e2[r] * c1[1] + e3[r] * c1[0] + qr1 * c1[3] - qi1 * c1[2]);
        }
        PIN(a30); PIN(a31);
        asm volatile("s_nop 1");
        static_for<4>(step3);
      }
      u8* ex = lds + LDS_SCR + 16384;
      mf4<true, 0, 5>(d0, d1, d2, d3, a31, cc.b3re[0][1], a31, cc.b3im[0][1], a31, cc.b3re[1][1], a31, cc.b3im[1][1]);
      XF(40);
      *(u32x4*)(ex + w * 1024 + l16) = twiddle<true>(d0, d1, cc.t2c[0], cc.t2s[0]);
      SB();
      *(u32x4*)(ex + (4 + w) * 1024 + l16) = twiddle<true>(d2, d3, cc.t2c[1], cc.t2s[1]);
      if constexpr (L == 0) vmwait<2>();
      bar();
      XF(41);
      const u8* er = ex + (w & 1) * 4096 + l16;
      const u32x4 g0 = *(const u32x4*)(er), g1 = *(const u32x4*)(er + 1024), g2 = *(const u32x4*)(er + 2048), g3 = *(const u32x4*)(er + 3072);
      mm8<true, 7>(d0, d1, d2, d3, cc.a4re[0][0], g0, cc.a4re[0][1], g1, cc.a4im[0][0], g0, cc.a4im[0][1], g1, cc.a4re[1][0], g2, cc.a4re[1][1], g3, cc.a4im[1][0], g2,
                   cc.a4im[1][1], g3);
      {
        XF(42);
        const f32x2 lr0 = lo2(d0) + lo2(d2), li0 = lo2(d1) + lo2(d3), hr0 = lo2(d0) - lo2(d2), hi0 = lo2(d1) - lo2(d3);
        const f32x2 lr1 = hi2(d0) + hi2(d2), li1 = hi2(d1) + hi2(d3), hr1 = hi2(d0) - hi2(d2), hi1 = hi2(d1) - hi2(d3);
        if (phi != 0) {
          const f32x2 pr0 = lr0 - hi0, pi0 = li0 + hr0, qr0 = lr0 + hi0, qi0 = hr0 - li0;
          st4(mark4<1u - L, L>(u32x4{pkb(pr0.x, pi0.x), pkb(qr0.x, qi0.x), pkb(pr0.y, pi0.y), pkb(qr0.y, qi0.y)}), uo);
          const f32x2 pr1 = lr1 - hi1, pi1 = li1 + hr1, qr1 = lr1 + hi1, qi1 = hr1 - li1;
          st4(mark4<1u - L, L>(u32x4{pkb(pr1.x, pi1.x), pkb(qr1.x, qi1.x), pkb(pr1.y, pi1.y), pkb(qr1.y, qi1.y)}), uo + 1024);
          MEMFENCE();
        } else {
          st4(mark4<1u - L, L>(u32x4{pkb(lr0.x, hr0.x), pkb(li0.x, hi0.x), pkb(lr0.y, hr0.y), pkb(li0.y, hi0.y)}), uo);
          st4(mark4<1u - L, L>(u32x4{pkb(lr1.x, hr1.x), pkb(li1.x, hi1.x), pkb(lr1.y, hr1.y), pkb(li1.y, hi1.y)}), uo + 1024);
        }
      }
    }
    ST(S0 + 3);
    {
      FRESH();
      SB();
      row_consts<true, 0>(rc, m, g);
      f32x2 xa[4], xb[4];
#pragma unroll
      for (int i = 0; i < 4; ++i) {
        xa[i] = f32x2{blo(xl[i >> 1][i & 1]), bhi(xl[i >> 1][i & 1])}; xb[i] = f32x2{blo(xh[i >> 1][i & 1]), bhi(xh[i >> 1][i & 1])};
      }
      const float ca = first_lane(xa[0].x), cb = first_lane(xb[0].x);
      float ch = lane < 32 ? ca : cb;
      PIN(ch);
#pragma unroll
      for (int i = 0; i < 4; ++i) { xa[i] -= ca; xb[i] -= cb; PIN(xa[i]); PIN(xb[i]); }
      SB();
      u32x4 c0, c1;
      const u32 uo = (tid >> 1) * 8192 + (tid & 1) * 16;
      const u8* ub0 = ws + OFF_U + pos * 32;
      const u8* ub1 = ub0 + 128 * 8192;
      ld4<0>(c0, uo, ub0); ld4<0>(c1, uo, ub1);
      if constexpr (L == 0) { fill_h(OFF_H, 2); fill_h(OFF_H, 3); fill_h(OFF_H, 4); }
      SB();
      row_consts<true, 1>(rc, m, g);
      SB();
      if constexpr (L == 1) ring4(4); else { fill_h(OFF_H, 5); fill_h(OFF_H, 6); }
      u32* img = (u32*)(lds + LDS_SCR);
      u32 ip0, iq0, iq1, xno, tw0, tw1, trd;
      {
        const int ph = tid >> 1, fq0 = ph ? 512 - ph : 256, fq1 = 384 - ph;
        ip0 = ((tid & 1) * 1280 + (ph & 31) * 20 + (ph >> 5)) * 4;
        iq0 = ((tid & 1) * 1280 + (fq0 & 31) * 20 + (fq0 >> 5)) * 4; iq1 = ((tid & 1) * 1280 + (fq1 & 31) * 20 + (fq1 >> 5)) * 4;
        xno = OFF_A + (pos >> 1) * 16384 + (tid >> 3) * 256 + (pos & 1) * 128 + (tid & 7) * 16;
        tw0 = (4 * m + g) * 128 + ((2 * w) ^ (m & 7)) * 16; tw1 = (4 * m + g) * 128 + ((2 * w + 1) ^ (m & 7)) * 16;
        trd = (tid >> 3) * 128 + ((tid & 7) ^ ((tid >> 5) & 7)) * 16;
        PIN(ip0); PIN(iq0); PIN(iq1); PIN(xno); PIN(tw0); PIN(tw1); PIN(trd);
      }
      SB();
      ST(S0 + 4);
      vmwait<(L == 0 ? 5 : 4)>(); PIN(c0); PIN(c1);
      {
        RETRY((stale<L == 1, 1u>(red8<L == 1>(c0, c1))), {
          CNT(3);
          if (bad_) { ld4r<0>(c0, uo, ub0); ld4r<0>(c1, uo, ub1); }
          vmwait<0>(); PIN(c0); PIN(c1);
        });
      }
      ST(S0 + 5);
      {
        u8* d = (u8*)img;
        *(u32*)(d + ip0) = c0.x; *(u32*)(d + iq0) = c0.y; *(u32*)(d + 2560 + ip0) = c0.z; *(u32*)(d + 2560 + iq0) = c0.w;
        *(u32*)(d + ip0 + 16) = c1.x; *(u32*)(d + iq1) = c1.y; *(u32*)(d + 2560 + ip0 + 16) = c1.z; *(u32*)(d + 2560 + iq1) = c1.w;
      }
      bar();
      XF(43);
      const u32x4 a0 = *(const u32x4*)(img + w * 640 + m * 20 + 4 * g), a1 = *(const u32x4*)(img + w * 640 + (16 + m) * 20 + 4 * g);
      if constexpr (L == 1) ring4(5);
      if constexpr (L == 0) ring4(0);
      f32x4 dr0, di0, dr1, di1;
      row_fft<true, 7>(rc, a0, a1, dr0, di0, dr1, di1);
      if constexpr (L == 0) ring4(1);
      XF(44);
      const float ys = 1.0f / 32768.0f;
      xa[0] += lo2(dr0) * ys; xa[1] += hi2(dr0) * ys; xa[2] += lo2(dr1) * ys; xa[3] += hi2(dr1) * ys;
      xb[0] += lo2(di0) * ys; xb[1] += hi2(di0) * ys; xb[2] += lo2(di1) * ys; xb[3] += hi2(di1) * ys;
      const f32x2 sa = (xa[0] + xa[1]) + (xa[2] + xa[3]), sb = (xb[0] + xb[1]) + (xb[2] + xb[3]);
      f32x2 qa = xa[0] * xa[0], qb = xb[0] * xb[0];
#pragma unroll
      for (int i = 1; i < 4; ++i) { qa += xa[i] * xa[i]; qb += xb[i] * xb[i]; }
      if constexpr (L == 0) { SB(); ring4(2); SB(); }
      f32x2 R, O;
      XF(45);
      ln_coef(f32x2{sa.x + sa.y, sb.x + sb.y}, f32x2{qa.x + qa.y, qb.x + qb.y}, ch, R, O);
      XF(46);
      u32x4 na, nb;
#pragma unroll
      for (int i = 0; i < 4; ++i) {
        const f32x2 va = xa[i] * R.x + O.x, vb = xb[i] * R.y + O.y;
        na[i] = mark<0x10000u, 1u - L>(pkb(va.x, va.y)); nb[i] = mark<0x10000u, 1u - L>(pkb(vb.x, vb.y));
      }
      u8* tr = lds + LDS_SCR + 12288;
      *(u32x4*)(tr + tw0) = na; *(u32x4*)(tr + tw1) = nb;
      if constexpr (L == 0) vmwait<12>(); else vmwait<0>();
      bar();
      XF(47);
      st4(*(const u32x4*)(tr + trd), xno);
      st4(*(const u32x4*)(tr + 4096 + trd), xno + 8192);
      SB();
#pragma unroll
      for (int i = 0; i < 4; ++i) { xa[i] += ca; xb[i] += cb; }
      const u32x4 rl = {pkb(xa[0].x, xa[0].y), pkb(xa[1].x, xa[1].y), pkb(xa[2].x, xa[2].y), pkb(xa[3].x, xa[3].y)};
      const u32x4 rh = {pkb(xb[0].x, xb[0].y), pkb(xb[1].x, xb[1].y), pkb(xb[2].x, xb[2].y), pkb(xb[3].x, xb[3].y)};
      if constexpr (L == 0) {
        *(u32x4*)(lds + LDS_STASH + t16) = rl; *(u32x4*)(lds + LDS_STASH + 4096 + t16) = rh;
        XS(44);
        ring4(3); ring4(4); ring4(5); ring4(6); ring4(7);
      } else {
        xl[0] = u32x2{rl.x, rl.y}; xl[1] = u32x2{rl.z, rl.w}; xh[0] = u32x2{rh.x, rh.y}; xh[1] = u32x2{rh.z, rh.w};
        PIN(xl[0]); PIN(xl[1]); PIN(xh[0]); PIN(xh[1]);
        __builtin_amdgcn_s_sleep(XSLP);
      }
    }
    ST(S0 + 6);
    {
      FRESH();
      u32x4 tk[32], he[32], hl[16], w2t[4], w2v[3][4];
      u32x4 hh[8], hf[8];
      f32x4 ac1[6][2], ac2[4][2], ac3[4][2];
      const u8* hs0 = ws + OFF_H + (4 * G + 2 * half2) * 114688 + (4 * w) * 7168;
      const u8* hs[4];
      const u8* hp[4];
#pragma unroll
      for (int j = 0; j < 4; ++j) {
        hs[j] = hs0 + opq((j + ct) & 3) * 7168; hp[j] = hs0 + opq(j ^ (2 * (ct & 1))) * 7168 + 6144;
        PINS(hs[j]); PINS(hp[j]);
      }
      const u8* w2src = ws + OFF_W2 + (ct * 4 + w) * 57344;
      const u8* w2x = w2src + 40960 + 4096;
      u32 lx8 = l16 + 8192;
      PINS(w2x); PIN(lx8);
      u32 hv[4], hw[2];
#pragma unroll
      for (int u = 0; u < 4; ++u) { hv[u] = l16 + ((u ^ w) >> 1) * 114688 + ((u ^ w) & 1) * 3072; PIN(hv[u]); }
#pragma unroll
      for (int q = 0; q < 2; ++q) { hw[q] = l16 + (q ^ (w >> 1)) * 114688; PIN(hw[q]); }
      auto hload = [&](auto RE, auto KC, auto UC) {
        constexpr int k = decltype(KC)::value, u = decltype(UC)::value;
        constexpr bool re = decltype(RE)::value;
        if constexpr (k == 4 || k == 5) {
          if constexpr (re) ld4r<0>(hh[4 * (k - 4) + u], hw[u >> 1], hp[2 * (k - 4) + (u & 1)]); else ld4<0>(hh[4 * (k - 4) + u], hw[u >> 1], hp[2 * (k - 4) + (u & 1)]);
        } else {
          constexpr int j = k < 4 ? k : k < 10 ? k - 6 : k - 10, o = k < 4 ? 0 : k < 10 ? 1024 : 2048;
          u32x4& d = k < 10 ? he[(k < 4 ? 0 : 16) + 4 * j + u] : hl[4 * j + u];
          if constexpr (re) ld4r<o>(d, hv[u], hs[j]); else ld4<o>(d, hv[u], hs[j]);
        }
      };
      auto hseq = [&](auto IC) {
        constexpr int i = decltype(IC)::value;
        hload(std::false_type{}, std::integral_constant<int, (i >> 2)>{}, std::integral_constant<int, i & 3>{});
      };
      {
        const u8* w1b = ws + OFF_W1 + member * 229376 + 131072 + w * 8192;
        const u8* tk0 = ws + OFF_A + (8 * G + 2 * w) * 16384 + 4096;
        const u8* tk1 = tk0 + 8192;
        u32 tv1 = l16 + 16384, lhi = l16 + 65536;
        u32 hd0 = OFF_H + (4 * G + w) * 114688 + member * 7168 + l16, hd1 = hd0 + 3072;
        PIN(tv1); PIN(lhi); PIN(hd0); PIN(hd1); PINS(tk0); PINS(tk1);
        u32 x3o = OFF_X3 + (8 * pos + 2 * w) * 1024 + xo;
        if constexpr (L == 1) PIN(x3o);
        auto tok = [&](auto RE, auto QC) {
          constexpr int q = decltype(QC)::value, ks = q >> 1;
          if constexpr (decltype(RE)::value) ld4r<(ks & 7) * 1024 - 4096>(tk[q], q & 1 ? tv1 : l16, ks >> 3 ? tk1 : tk0);
          else ld4<(ks & 7) * 1024 - 4096>(tk[q], q & 1 ? tv1 : l16, ks >> 3 ? tk1 : tk0);
        };
        constexpr bool TONE = L == 0;
        constexpr u32 TBIT = L == 0 ? 1u : 0x10000u;
        u32 mx0 = 0, mx1 = 0;
        auto mx0_fin = [&]() { return red2<TONE>(mx0, mx1) & TBIT; };
        auto fix = [&](auto QC, u64 bad) {
          constexpr int q0 = decltype(QC)::value;
          if (__builtin_expect(bad != 0, 0)) {
            _Pragma("nounroll") for (int sp = 0; sp < SPIN_CAP; ++sp) {
              CNT(4);
              static_for<4>([&](auto I) { tok(std::true_type{}, std::integral_constant<int, q0 + decltype(I)::value>{}); });
              vmwait<0>();
#pragma unroll
              for (int q = q0; q < q0 + 4; ++q) PIN(tk[q]);
              if (!__any(stale<TONE, TBIT>(red2<TONE>(red8<TONE>(tk[q0], tk[q0 + 1]), red8<TONE>(tk[q0 + 2], tk[q0 + 3]))))) break;
            }
          }
        };
        constexpr int PD = 8;
        u32x4 af[PD];
        auto frag = [&](int x) {
          const int o = (x & 127) * 1024;
          return o < 65536 ? *(const u32x4*)(lds + l16 + o) : *(const u32x4*)(lds + lhi + (o - 65536));
        };
        if constexpr (L == 1) {
#pragma unroll
          for (int x = 0; x < PD - 1; ++x) af[x] = frag(x);
          SB();
        }
        XF(48);
        static_for<NTOK0>([&](auto Q) {
          tok(std::false_type{}, Q);
          if constexpr (decltype(Q)::value == 3) bar_nw();
        });
        XF(49);
        if constexpr (L == 0) {
          vmwait<NTOK0>();
          bar_nw();
#pragma unroll
          for (int x = 0; x < PD - 1; ++x) af[x] = frag(x);
        }
        vmwait<NTOK0 - 4>();
        ST(S0 + 7);
#pragma unroll
        for (int q = 0; q < 4; ++q) PIN(tk[q]);
        mx0 = red8<TONE>(tk[0], tk[1]); mx1 = red8<TONE>(tk[2], tk[3]);
        fix(std::integral_constant<int, 0>{}, __ballot(stale<TONE, TBIT>(red2<TONE>(mx0, mx1))));
        SB();
        ST(S0 + 8);
        float eg[2][2], eu[2][2];
        u32 ev[2];
        u64 bm = 0;
        u32x4 eo[1];
        auto epi = [&](auto NC) {
          constexpr int n = decltype(NC)::value;
          if constexpr (n < NEPI1 + NEPI2) {
            constexpr bool p1 = n < NEPI1;
            constexpr int i = p1 ? n - (n > 40) - (n > 81) : (n - NEPI1) - (n - NEPI1) / 41;
            if constexpr (p1 && (n == 40 || n == 81)) st4(eo[0], n == 40 ? hd0 : hd1);
            else if constexpr (p1 && n == 122) st4(eo[0], hd1 + 3072);
            else if constexpr (!p1 && (n - NEPI1) % 41 == 40) st4(eo[0], ((n - NEPI1) / 41 ? hd1 : hd0) + 1024);
            else {
              constexpr int s = i & 1, st = (i % 20) >> 1, blk = i / 20;
              constexpr int u = p1 ? (blk < 4 ? blk >> 1 : blk - 4) : blk >> 1, pr = p1 ? (blk < 4 ? blk & 1 : 2) : blk & 1;
              constexpr int ei = 0, d = 2 * (pr == 2 ? u : pr) + s;
              const f32x4& ga = p1 ? ac1[2 * pr][u] : ac2[2 * (pr & 1)][u];
              const f32x4& up = p1 ? ac1[2 * pr + 1][u] : ac2[2 * (pr & 1) + 1][u];
              if constexpr (st < 2) { eg[s][st] = ga[2 * s + st]; PIN(eg[s][st]); }
              else if constexpr (st < 4) { eu[s][st - 2] = up[2 * s + st - 2]; PIN(eu[s][st - 2]); }
              else if constexpr (st < 6) { eg[s][st - 4] = relu(eg[s][st - 4]); PIN(eg[s][st - 4]); }
              else if constexpr (st < 8) { eg[s][st - 6] *= eu[s][st - 6]; PIN(eg[s][st - 6]); }
              else if constexpr (st == 8) { ev[s] = pkb(eg[s][0], eg[s][1]); PIN(ev[s]); }
              else { u32 v = mark<1u - L, L>(ev[s]); PIN(v); eo[ei][d] = v; }
            }
            SB();
          }
        };
        static_for<224>([&](auto XC) {
          constexpr int x = decltype(XC)::value, job = G1S[L].job[x], ji = G1S[L].idx[x], e0 = G1S[L].epi[x], e1 = G1S[L].epi[x + 1];
          if constexpr (x == 32 || x == 64) bar_nw();
          if constexpr (x == 97) { vmwait<g1_wait(L, x, 2, 7)>(); bar_nw(); }
          if constexpr (x == 150) { vmwait<g1_wait(L, x, 2, 23)>(); bar_nw(); }
          constexpr int cj = (x + 12) / 12, co = x + 12 - 12 * cj;
          constexpr bool chk = x < 84 && co >= 5;
          constexpr int ks = x < 96 ? x / 6 : x < 160 ? (x - 96) >> 2 : (x - 160) >> 2, t = x < 96 ? x % 6 : x & 3;
          auto mm = [&](int u) {
            f32x4& a = x < 96 ? ac1[x < 96 ? t : 0][u] : x < 160 ? ac2[x < 96 ? 0 : t][u] : ac3[x < 96 ? 0 : t][u];
            if constexpr (ks == 0) a = mfma_b(af[x % PD], tk[u], f32x4{0.f, 0.f, 0.f, 0.f}); else a = mfma_b(af[x % PD], tk[2 * ks + u], a);
          };
          SB();
          mm(0);
          SB();
          if constexpr (x < 223) __builtin_amdgcn_s_waitcnt(0xC07F | ((x + PD - 2 < 224 ? PD - 3 : 222 - x) << 8));
          if constexpr (chk && co == 5) {
            vmwait<g1_wait(L, x, 1, 4 * cj + 3)>();
            PIN(tk[4 * cj]); PIN(tk[4 * cj + 1]); PIN(tk[4 * cj + 2]); PIN(tk[4 * cj + 3]);
          }
          SB();
          if constexpr (chk && co >= 6 && co < 10) {
            const u32x4 v = tk[4 * cj + co - 6];
            mx0 = co == 6 ? red2<TONE>(v.x, v.y) : red3<TONE>(mx0, v.x, v.y); PIN(mx0); SB();
            mx1 = co == 6 ? red2<TONE>(v.z, v.w) : red3<TONE>(mx1, v.z, v.w); PIN(mx1);
          }
          if constexpr (chk && co == 10) { mx0 = mx0_fin(); PIN(mx0); SB(); bm = __ballot(TONE ? mx0 == 0 : mx0 != 0); PINS(bm); }
          if constexpr (e0 < e1) epi(std::integral_constant<int, e0>{});
          if constexpr (e0 + 1 < e1) epi(std::integral_constant<int, e0 + 1>{});
          SB();
          mm(1);
          SB();
          if constexpr (chk && co == 11) fix(std::integral_constant<int, 4 * cj>{}, bm);
          if constexpr (job == 2 && (ji & 3) == 0) set_m0(ldsbase + (ji >> 3) * 32768 + w * 8192 + (ji & 4) * 1024);
          SB();
          if constexpr (x + PD - 1 < 224) af[(x + PD - 1) % PD] = frag(x + PD - 1);
          SB();
          if constexpr (job == 1) tok(std::false_type{}, std::integral_constant<int, ji>{});
          if constexpr (job == 2) dma1<(ji & 3) * 1024>(l16, w1b + (ji >> 3) * 32768 + (ji & 4) * 1024);
          if constexpr (job == 3) ld4a<(ji & 7) * 1024 - 4096>(w2a[ji >> 2][ji & 3], l16, w2src + (ji >> 3) * 8192 + 4096);
          if constexpr (job == 5) {
            if constexpr (ji >= 12) ld4a<(ji - 12) * 1024 - 4096>(w2t[ji - 12], l16, w2x);
            else ld4<((ji + 4) & 7) * 1024 - 4096>(w2v[ji >> 2][ji & 3], ji < 4 ? l16 : lx8, w2x);
          }
          if constexpr (job == 6) st2(ji & 2 ? xh[ji & 1] : xl[ji & 1], x3o + ji * 512);
          if constexpr (job == 4) hseq(std::integral_constant<int, ji>{});
          if constexpr (e0 + 2 < e1) epi(std::integral_constant<int, e0 + 2>{});
          SB();
        });
        ST(S0 + 9);
#pragma unroll
        for (int u = 0; u < 2; ++u) {
          u32x4 o;
#pragma unroll
          for (int p = 0; p < 2; ++p) {
            const f32x4 ga = ac3[2 * p][u], up = ac3[2 * p + 1][u];
            const f32x2 a = f32x2{relu(ga[0]), relu(ga[1])} * lo2(up), c = f32x2{relu(ga[2]), relu(ga[3])} * hi2(up);
            o[2 * p] = mark<1u - L, L>(pkb(a.x, a.y)); o[2 * p + 1] = mark<1u - L, L>(pkb(c.x, c.y));
          }
          st4(o, (u ? hd1 : hd0) + 2048);
        }
        SB();
      }
      ST(S0 + 10);
      {
        f32x4 acc[4][4];
        u32x4 x3a, x3b;
        u32 eo = L == 1 ? (128 * G + 64 * half2 + 16 * w + m) * 1024 + ct * 128 + g * 16
                        : (128 * G + 64 * half2 + 16 * w + (m & 14)) * 1024 + ct * 128 + (m & 1) * 64 + g * 16;
        PIN(eo);
        const u8* x3s = ws + OFF_X3;
        u64 bm = 0;
        PINS(x3s);
        constexpr int LB = 40 - NEARLY;
        auto cum = [](int n) constexpr { const int c = n - n / 8; return c < LB + 16 ? c : LB + 16; };
        auto young = [cum](int k, int n) constexpr {
          const int i = 4 * k + 3;
          if (k < 10 && i < NEARLY) return cum(n) + 2 + G1S[L].cum[224] - G1S[L].pos[4][i] - 1;
          if (k < 10) return cum(n) - (i - NEARLY + 1);
          return cum(n) - (LB + 4 * (k - 10) + 4);
        };
        constexpr bool HONE = L == 1;
        u32 mx0 = 0, mx1 = 0;
        auto mx0_fin = [&]() { return red2<HONE>(mx0, mx1) & 1u; };
        auto pin_k = [&](auto KC) {
          constexpr int k = decltype(KC)::value;
#pragma unroll
          for (int u = 0; u < 4; ++u) {
            if constexpr (k < 4) PIN(he[4 * k + u]);
            else if constexpr (k < 6) PIN(hh[4 * (k - 4) + u]);
            else if constexpr (k < 10) PIN(he[16 + 4 * (k - 6) + u]);
            else PIN(hl[4 * (k - 10) + u]);
          }
        };
        auto opnd = [&](auto KC, int u) {
          constexpr int k = decltype(KC)::value;
          if constexpr (k < 4) return he[4 * k + u];
          else if constexpr (k < 6) return hh[4 * (k - 4) + u];
          else if constexpr (k < 10) return he[16 + 4 * (k - 6) + u];
          else return hl[4 * (k - 10) + u];
        };
        u32 wodd = (w & 1) ? ~0u : 0u;
        PIN(wodd);
        auto mkhalf = [&](auto KC, auto IC) {
          constexpr int k = decltype(KC)::value, i = decltype(IC)::value, u = i >> 2, d = i & 3;
          const u32x4& src = hh[4 * (k - 4) + (u & 2) + (d >> 1)];
          u32 v = (src[2 * ((u & 1) ^ 1) + (d & 1)] & wodd) | (src[2 * (u & 1) + (d & 1)] & ~wodd);
          PIN(v);
          hf[4 * (k - 4) + u][d] = v;
        };
        auto fixk = [&](auto KC, u64 bad) {
          constexpr int k = decltype(KC)::value;
          if (__builtin_expect(bad != 0, 0)) {
            _Pragma("nounroll") for (int sp = 0; sp < SPIN_CAP; ++sp) {
              CNT(k < 10 ? 5 : 6);
              static_for<4>([&](auto U) { hload(std::true_type{}, KC, U); });
              vmwait<0>();
              pin_k(KC);
              if (!__any(stale<HONE, 1u>(red2<HONE>(red8<HONE>(opnd(KC, 0), opnd(KC, 1)), red8<HONE>(opnd(KC, 2), opnd(KC, 3)))))) break;
            }
            if constexpr (k == 4 || k == 5) static_for<16>([&](auto I) { mkhalf(KC, I); });
          }
        };
        u32 rw[3];
#pragma unroll
        for (int u = 1; u < 4; ++u) { rw[u - 1] = ((u ^ w) * 3 + u - 1) * 4096 + l16; PIN(rw[u - 1]); }
        u32 oo = 0;
        if constexpr (L == 1) {
          const int tok = 128 * G + 64 * half2 + 16 * w + m;
          oo = (row_of_pos(tok >> 3, (tok >> 1) & 3) + 1024 * (tok & 1)) * 1024 + ct * 128 + g * 16;
          PIN(oo);
        }
        vmwait<clamp63(young(0, 0))>();
        if constexpr (L == 0) {
#pragma unroll
          for (int k = 0; k < 10; ++k) { PINA(w2a[k][0]); PINA(w2a[k][1]); PINA(w2a[k][2]); PINA(w2a[k][3]); }
        }
#pragma unroll
        for (int c = 0; c < 4; ++c) { PINA(w2t[c]); PIN(w2v[0][c]); PIN(w2v[1][c]); PIN(w2v[2][c]); }
        pin_k(std::integral_constant<int, 0>{});
        mx0 = red8<HONE>(he[0], he[1]); mx1 = red8<HONE>(he[2], he[3]);
        fixk(std::integral_constant<int, 0>{}, __ballot(stale<HONE, 1u>(red2<HONE>(mx0, mx1))));
        ST(S0 + 11);
        auto wfr = [&](auto KC, int c) {
          constexpr int k = decltype(KC)::value;
          if constexpr (k < 10) return w2a[k][c]; else if constexpr (k == 10) return w2t[c]; else return w2v[k - 11][c];
        };
        static_for<44>([&](auto NC) {
          constexpr int n = decltype(NC)::value, k = n >> 2, c = n & 3;
          using KC = std::integral_constant<int, k>;
          using KN = std::integral_constant<int, k + 1>;
          static_for<4>([&](auto UC) {
            constexpr int u = decltype(UC)::value;
            SB();
            if constexpr (k == 0) acc[c][u] = mfma_b(wfr(KC{}, c), opnd(KC{}, u), f32x4{0.f, 0.f, 0.f, 0.f});
            else if constexpr (k == 4 || k == 5) acc[c][u] = mfma_b(wfr(KC{}, c), hf[4 * (k - 4) + u], acc[c][u]);
            else acc[c][u] = mfma_b(wfr(KC{}, c), opnd(KC{}, u), acc[c][u]);
            SB();
            constexpr bool hk = k == 3 || k == 4;
            constexpr int j = k == 10 ? 4 * c + u - 2 : u == 3 ? -9 : 3 * c + u - (hk ? 1 : 5);
            if constexpr (j == -1) {
              vmwait<clamp63(young(k == 10 ? 13 : k + 1, n))>(); pin_k(KN{});
              if constexpr (k == 10) { pin_k(std::integral_constant<int, 12>{}); pin_k(std::integral_constant<int, 13>{}); }
            }
            if constexpr (j >= 0 && j < (k == 10 ? 12 : 4)) {
              const u32x4 v = opnd(std::integral_constant<int, k + 1 + (j >> 2)>{}, j & 3);
              mx0 = j == 0 ? red2<HONE>(v.x, v.y) : red3<HONE>(mx0, v.x, v.y); PIN(mx0); SB();
              mx1 = j == 0 ? red2<HONE>(v.z, v.w) : red3<HONE>(mx1, v.z, v.w); PIN(mx1);
            }
            if constexpr (hk && j >= 4 && j != 5) {
              constexpr int i0 = j == 4 ? 0 : 2 + 3 * (j - 6);
              mkhalf(KN{}, std::integral_constant<int, i0>{}); SB(); mkhalf(KN{}, std::integral_constant<int, i0 + 1>{});
              if constexpr (j >= 6 && i0 + 2 < 16) { SB(); mkhalf(KN{}, std::integral_constant<int, i0 + 2>{}); }
            }
            if constexpr (j == (k == 10 ? 12 : 5)) { mx0 = mx0_fin(); PIN(mx0); SB(); bm = __ballot(HONE ? mx0 == 0 : mx0 != 0); PINS(bm); }
            if constexpr (u == 3) {
              constexpr int q = cum(n);
              if constexpr (cum(n + 1) == q) {}
              else if constexpr (q < LB) hseq(std::integral_constant<int, NEARLY + q>{});
              else hload(std::false_type{}, std::integral_constant<int, 10 + ((q - LB) >> 2)>{}, std::integral_constant<int, (q - LB) & 3>{});
              if constexpr (c == 3) {
                fixk(KN{}, bm);
                if constexpr (k == 10) { fixk(std::integral_constant<int, 12>{}, bm); fixk(std::integral_constant<int, 13>{}, bm); }
              }
            }
          });
        });
        bar_nw();
        if constexpr (L == 1) { ld4<0>(x3a, eo, x3s); ld4<64>(x3b, eo, x3s); }
        static_for<16>([&](auto TC) {
          constexpr int t = decltype(TC)::value, u = ((t >> 2) + 1) & 3, c = t & 3;
          SB();
          acc[c][u] = mfma_b(w2v[0][c], hl[4 + u], acc[c][u]);
          acc[c][u] = mfma_b(w2v[1][c], hl[8 + u], acc[c][u]);
          SB();
          if constexpr (t >= 3 && t < 15) *(f32x4*)(lds + rw[(t - 3) >> 2] + ((t - 3) & 3) * 1024) = acc[(t - 3) & 3][((t - 3) >> 2) + 1];
          SB();
          acc[c][u] = mfma_b(w2v[2][c], hl[12 + u], acc[c][u]);
        });
        SB();
        ST(S0 + 12);
        bar();
        f32x4 s[3][4];
#pragma unroll
        for (int c = 0; c < 4; ++c)
#pragma unroll
          for (int p = 0; p < 3; ++p) s[p][c] = *(const f32x4*)(lds + w * 12288 + p * 4096 + c * 1024 + l16);
        SB();
        if constexpr (L == 1) { vmwait<0>(); PIN(x3a); PIN(x3b); }
        u32x4 y[2];
#pragma unroll
        for (int a = 0; a < 2; ++a) {
          const f32x4 s0 = (acc[2 * a][0] + s[0][2 * a]) + (s[1][2 * a] + s[2][2 * a]);
          const f32x4 s1 = (acc[2 * a + 1][0] + s[0][2 * a + 1]) + (s[1][2 * a + 1] + s[2][2 * a + 1]);
          y[a] = u32x4{pkb(s0[0], s0[1]), pkb(s0[2], s0[3]), pkb(s1[0], s1[1]), pkb(s1[2], s1[3])};
          if constexpr (L == 1) {
            const u32x4 xr = a ? x3b : x3a;
            u32x4 o;
#pragma unroll
            for (int p = 0; p < 4; ++p) o[p] = pkb(blo(xr[p]) + blo(y[a][p]), bhi(xr[p]) + bhi(y[a][p]));
            *(u32x4*)((u8*)P.out + oo + a * 64) = o;
          }
        }
        if constexpr (L == 0) {
          const bool odd = m & 1;
          u32x4 z0, z1;
#pragma unroll
          for (int p = 0; p < 4; ++p) {
            const u32 t = (u32)__builtin_amdgcn_mov_dpp((int)(odd ? y[0][p] : y[1][p]), 0xB1, 0xf, 0xf, true);
            z0[p] = odd ? t : y[0][p]; z1[p] = odd ? y[1][p] : t;
          }
          st4(mark4<0x10001u, 0u>(z0), OFF_A + eo); st4(mark4<0x10001u, 0u>(z1), OFF_A + eo + 1024);
        }
      }
    }
    ST(S0 + 13);
    if constexpr (L == 0) {
      FRESH();
      SB();
      row_consts<false, 0>(rc, m, g);
      SB();
      u32x2 d0, d1, d2, d3;
      const u8* db = ws + OFF_A + (8 * pos + 2 * w) * 1024;
      const u32x4 sl = *(const u32x4*)(lds + LDS_STASH + t16), sh = *(const u32x4*)(lds + LDS_STASH + 4096 + t16);
#pragma unroll
      for (int i = 0; i < 4; ++i) {
        xv[2 * i] = f32x2{blo(sl[i]), blo(sh[i])}; xv[2 * i + 1] = f32x2{bhi(sl[i]), bhi(sh[i])};
      }
      xc = f32x2{first_lane(xv[0].x), first_lane(xv[0].y)};
      xch = lane < 32 ? xc.x : xc.y;
      PIN(xch);
#pragma unroll
      for (int i = 0; i < 8; ++i) { xv[i] -= xc; PIN(xv[i]); }
      SB();
      bar_nw();
      ld2<0>(d0, xo, db); ld2<512>(d1, xo, db); ld2<1024>(d2, xo, db); ld2<1536>(d3, xo, db);
      ring4(0);
      SB();
      row_consts<false, 1>(rc, m, g);
      SB();
      fwd_pre(fp, tid, w, pos);
      SB();
      ST(S0 + 14);
      vmwait<4>(); PIN(d0); PIN(d1); PIN(d2); PIN(d3);
      RETRY((stale<false, 1u>(red8<false>(u32x4{d0.x, d0.y, d1.x, d1.y}, u32x4{d2.x, d2.y, d3.x, d3.y}))), {
        CNT(7);
        if (bad_) { ld2r<0>(d0, xo, db); ld2r<512>(d1, xo, db); ld2r<1024>(d2, xo, db); ld2r<1536>(d3, xo, db); }
        vmwait<0>(); PIN(d0); PIN(d1); PIN(d2); PIN(d3);
      });
      xv[0] += f32x2{blo(d0.x), blo(d2.x)}; xv[1] += f32x2{bhi(d0.x), bhi(d2.x)}; xv[2] += f32x2{blo(d0.y), blo(d2.y)}; xv[3] += f32x2{bhi(d0.y), bhi(d2.y)};
      xv[4] += f32x2{blo(d1.x), blo(d3.x)}; xv[5] += f32x2{bhi(d1.x), bhi(d3.x)}; xv[6] += f32x2{blo(d1.y), blo(d3.y)}; xv[7] += f32x2{bhi(d1.y), bhi(d3.y)};
      ST(S0 + 15);
    }
  };
  layer(std::integral_constant<int, 0>{});
  layer(std::integral_constant<int, 1>{});
  if (blockIdx.x == 0 && threadIdx.x == 0) st2(ep, OFF_E);
}

extern "C" void kernel_launch(void* const* d_in, const int* in_sizes, int n_in, void* d_out, int out_size, void* d_ws, size_t ws_size,
                              hipStream_t stream) {
  Params p;
  p.x = (const u16*)d_in[0]; p.wr = (const u16*)d_in[1]; p.wi = (const u16*)d_in[2];
  p.wg = (const u16*)d_in[4]; p.wu = (const u16*)d_in[5]; p.wd = (const u16*)d_in[6];
  p.out = (u16*)d_out; p.ws = (u8*)d_ws;
  (void)hipFuncSetAttribute((const void*)k_all, hipFuncAttributeMaxDynamicSharedMemorySize, LDS_TOTAL);
  k_all<<<256, 256, LDS_TOTAL, stream>>>(p);
}
```

```cpp
#include <hip/hip_runtime.h>
#include <hip/hip_bf16.h>
#include <stdint.h>
#include <type_traits>
#include <utility>

typedef unsigned char u8;
typedef unsigned short u16;
typedef unsigned int u32;
typedef unsigned long long u64;
typedef u32 u32x2 __attribute__((ext_vector_type(2)));
typedef u32 u32x4 __attribute__((ext_vector_type(4)));
typedef float f32x2 __attribute__((ext_vector_type(2)));
typedef float f32x4 __attribute__((ext_vector_type(4)));
typedef _Float16 f16x2 __attribute__((ext_vector_type(2)));
typedef __bf16 bf16x2 __attribute__((ext_vector_type(2)));
typedef __bf16 bf16x8 __attribute__((ext_vector_type(8)));
#define DI __device__ __forceinline__
#define SB() __builtin_amdgcn_sched_barrier(0)
#define MEMFENCE() asm volatile("" ::: "memory")

#define XSLP 4
constexpr u32 MB2 = 2u << 20;
constexpr u32 OFF_E = 0, OFF_FLAG = 8192, OFF_WNY = 65536;
constexpr u32 OFF_A = MB2;
constexpr u32 OFF_U = OFF_A + MB2;
constexpr u32 OFF_WX = OFF_U + MB2;
constexpr u32 OFF_X3 = OFF_WX + MB2;
constexpr u32 OFF_H = OFF_X3 + MB2;
constexpr u32 OFF_W1 = OFF_H + 7340032;
constexpr u32 OFF_W2 = OFF_W1 + 3670016;
constexpr u32 WS_USED = OFF_W2 + 1835008;
constexpr u32 SENT = 0xFFFFFFFFu;
constexpr int SPIN_CAP = 1 << 15;
constexpr u32 CODE_BYTES = 66 * 1024;

constexpr int LDS_SCR = 131072, LDS_STASH = LDS_SCR + 24576, LDS_TOTAL = 163840;

struct Params {
  const u16* x; const u16* wr; const u16* wi; const u16* wg; const u16* wu; const u16* wd; u16* out; u8* ws;
};

#define ST(i)
#define ST_DECL
#define CNT(i)
#define ST_OUT()

#define XS(i)
#define XF(i)
DI u32 pkh(float a, float b) { f32x2 v = {a, b}; return __builtin_bit_cast(u32, __builtin_convertvector(v, f16x2)); }
DI u32 pkb(float a, float b) { f32x2 v = {a, b}; return __builtin_bit_cast(u32, __builtin_convertvector(v, bf16x2)); }
template <bool BF> DI u32 pkc(float a, float b) { return BF ? pkb(a, b) : pkh(a, b); }
DI u32 pkh_n0(float a, float b, u32 after) { u32 r; asm("v_cvt_pk_f16_f32 %0, -%1, %2" : "=v"(r) : "v"(a), "v"(b), "v"(after)); return r; }
DI u32 pkb_n1(float a, float b, u32 after) { u32 r; asm("v_cvt_pk_bf16_f32 %0, %1, -%2" : "=v"(r) : "v"(a), "v"(b), "v"(after)); return r; }
DI float blo(u32 u) { return __uint_as_float(u << 16); }
DI float bhi(u32 u) { return __uint_as_float(u & 0xffff0000u); }
DI float bfr(float a) { return blo(pkb(a, a)); }
DI float relu(float a) { const int i = __float_as_int(a); return __int_as_float(i > 0 ? i : 0); }
DI u32 umax(u32 a, u32 b) { return a > b ? a : b; }
DI u32 max3(u32 a, u32 b, u32 c) { return umax(umax(a, b), c); }
DI u32 max4(u32 acc, u32x4 v) { return max3(max3(acc, v.x, v.y), v.z, v.w); }
template <bool ONE> DI u32 red3(u32 a, u32 b, u32 c) { return ONE ? (a & b & c) : (a | b | c); }
template <bool ONE> DI u32 red2(u32 a, u32 b) { return ONE ? (a & b) : (a | b); }
template <bool ONE> DI u32 red4(u32 acc, u32x4 v) { return red3<ONE>(red3<ONE>(acc, v.x, v.y), v.z, v.w); }
template <bool ONE> DI u32 red8(u32x4 a, u32x4 b) { return red4<ONE>(red3<ONE>(red2<ONE>(a.x, a.y), a.z, a.w), b); }
template <bool ONE, u32 BIT> DI bool stale(u32 r) { return ONE ? (r & BIT) == 0 : (r & BIT) != 0; }
template <u32 CLR, u32 SET> DI u32 mark(u32 v) { return (v & ~CLR) | SET; }
template <u32 CLR, u32 SET> DI u32x4 mark4(u32x4 v) { return u32x4{mark<CLR, SET>(v.x), mark<CLR, SET>(v.y), mark<CLR, SET>(v.z), mark<CLR, SET>(v.w)}; }
DI f32x2 lo2(f32x4 v) { return __builtin_shufflevector(v, v, 0, 1); }
DI f32x2 hi2(f32x4 v) { return __builtin_shufflevector(v, v, 2, 3); }
DI f32x2 swp(f32x2 v) { return __builtin_shufflevector(v, v, 1, 0); }
DI f32x4 mfma_b(u32x4 a, u32x4 b, f32x4 c) {
  return __builtin_amdgcn_mfma_f32_16x16x32_bf16(__builtin_bit_cast(bf16x8, a), __builtin_bit_cast(bf16x8, b), c, 0, 0, 0);
}
template <class T> DI void PIN(T& x) { asm volatile("" : "+v"(x)); }
template <class T> DI void PINS(T& x) { asm volatile("" : "+s"(x)); }
DI int opq(int x) { asm("" : "+s"(x)); return x; }
template <class T> DI void PINA(T& x) { asm volatile("" : "+a"(x)); }
template <class F, int... I> DI void static_for_(F&& f, std::integer_sequence<int, I...>) { (f(std::integral_constant<int, I>{}), ...); }
template <int N, class F> DI void static_for(F&& f) { static_for_(f, std::make_integer_sequence<int, N>{}); }

#define MM_(T) "v_mfma_f32_16x16x32_" T
template <bool BF> DI void mm4(f32x4& d0, f32x4& d1, f32x4& d2, f32x4& d3, u32x4 a0, u32x4 a1, u32x4 b0, u32x4 b1) {
#define MM4_(T) asm volatile("s_nop 1\n\t" MM_(T) " %0, %4, %6, 0\n\t" MM_(T) " %1, %4, %7, 0\n\t" MM_(T) " %2, %5, %6, 0\n\t" MM_(T) " %3, %5, %7, 0\n\ts_nop 5" \
               : "=&v"(d0), "=&v"(d1), "=&v"(d2), "=&v"(d3) : "v"(a0), "v"(a1), "v"(b0), "v"(b1))
  if constexpr (BF) MM4_("bf16"); else MM4_("f16");
}
template <bool BF, int TAIL, int AG = 0> DI void mm8(f32x4& d0, f32x4& d1, f32x4& d2, f32x4& d3, u32x4 a00, u32x4 b00, u32x4 a01, u32x4 b01, u32x4 a10,
                               u32x4 b10, u32x4 a11, u32x4 b11, u32x4 a20, u32x4 b20, u32x4 a21, u32x4 b21, u32x4 a30, u32x4 b30, u32x4 a31, u32x4 b31) {
#define MM8_(T, A, B) asm volatile("s_nop 1\n\t" MM_(T) " %0, %4, %5, 0\n\t" MM_(T) " %1, %8, %9, 0\n\t" MM_(T) " %0, %6, %7, %0\n\t" MM_(T) " %1, %10, %11, %1\n\t" \
               MM_(T) " %2, %12, %13, 0\n\t" MM_(T) " %3, %16, %17, 0\n\t" MM_(T) " %2, %14, %15, %2\n\t" MM_(T) " %3, %18, %19, %3\n\ts_nop %20"         \
               : "=&v"(d0), "=&v"(d1), "=&v"(d2), "=&v"(d3)                                                                                              \
               : A(a00), B(b00), A(a01), B(b01), A(a10), B(b10), A(a11), B(b11), A(a20), B(b20), A(a21), B(b21), A(a30), B(b30), A(a31), B(b31), "i"(TAIL))
  if constexpr (!BF) MM8_("f16", "v", "v");
  else if constexpr (AG == 0) MM8_("bf16", "v", "v");
  else if constexpr (AG == 1) MM8_("bf16", "a", "v");
  else MM8_("bf16", "v", "a");
}
template <bool BF, int AG> DI void mf0(f32x4& d, u32x4 a, u32x4 b) {
  if constexpr (!BF) asm volatile(MM_("f16") " %0, %1, %2, 0" : "=&v"(d) : "v"(a), "v"(b));
  else if constexpr (AG == 0) asm volatile(MM_("bf16") " %0, %1, %2, 0" : "=&v"(d) : "v"(a), "v"(b));
  else asm volatile(MM_("bf16") " %0, %1, %2, 0" : "=&v"(d) : "v"(a), "a"(b));
}
template <bool BF, int AG, int TAIL> DI void mf4(f32x4& d0, f32x4& d1, f32x4& d2, f32x4& d3, u32x4 a0, u32x4 b0, u32x4 a1, u32x4 b1, u32x4 a2,
                                               u32x4 b2, u32x4 a3, u32x4 b3) {
#define MF4_(T, B) asm volatile("s_nop 1\n\t" MM_(T) " %0, %4, %5, %0\n\t" MM_(T) " %1, %6, %7, %1\n\t" MM_(T) " %2, %8, %9, %2\n\t" MM_(T) " %3, %10, %11, %3\n\ts_nop %12" \
               : "+v"(d0), "+v"(d1), "+v"(d2), "+v"(d3) : "v"(a0), B(b0), "v"(a1), B(b1), "v"(a2), B(b2), "v"(a3), B(b3), "i"(TAIL))
  if constexpr (!BF) MF4_("f16", "v"); else if constexpr (AG == 0) MF4_("bf16", "v"); else MF4_("bf16", "a");
}
template <bool BF, bool CONJ, class MM> DI u32x4 cplx_under(f32x4 r, f32x4 i, const f32x2 (&c)[2], const f32x2 (&s)[2], MM&& mm) {
  asm volatile("s_nop 1");
  SB(); mm(std::integral_constant<int, 0>{}); SB();
  f32x2 a0 = lo2(i) * s[0], b0 = lo2(r) * s[0], a1 = hi2(i) * s[1];
  PIN(a0); PIN(b0); PIN(a1);
  SB(); mm(std::integral_constant<int, 1>{}); SB();
  f32x2 b1 = hi2(r) * s[1], re0 = CONJ ? lo2(r) * c[0] + a0 : lo2(r) * c[0] - a0, im0 = CONJ ? lo2(i) * c[0] - b0 : lo2(i) * c[0] + b0;
  PIN(b1); PIN(re0); PIN(im0);
  SB(); mm(std::integral_constant<int, 2>{}); SB();
  f32x2 re1 = CONJ ? hi2(r) * c[1] + a1 : hi2(r) * c[1] - a1, im1 = CONJ ? hi2(i) * c[1] - b1 : hi2(i) * c[1] + b1;
  u32 o0 = pkc<BF>(re0.x, im0.x);
  PIN(re1); PIN(im1); PIN(o0);
  SB(); mm(std::integral_constant<int, 3>{}); SB();
  return u32x4{o0, pkc<BF>(re0.y, im0.y), pkc<BF>(re1.x, im1.x), pkc<BF>(re1.y, im1.y)};
}
template <bool BF> DI u32x4 twiddle(f32x4 r, f32x4 i, const f32x2 (&c)[2], const f32x2 (&s)[2]) {
  const f32x2 re0 = lo2(r) * c[0] + lo2(i) * s[0], im0 = lo2(i) * c[0] - lo2(r) * s[0];
  const f32x2 re1 = hi2(r) * c[1] + hi2(i) * s[1], im1 = hi2(i) * c[1] - hi2(r) * s[1];
  return u32x4{pkc<BF>(re0.x, im0.x), pkc<BF>(re0.y, im0.y), pkc<BF>(re1.x, im1.x), pkc<BF>(re1.y, im1.y)};
}
DI u32x4 cmul_b(f32x4 r, f32x4 i, const f32x2 (&c)[2], const f32x2 (&s)[2]) {
  const f32x2 re0 = lo2(r) * c[0] - lo2(i) * s[0], im0 = lo2(i) * c[0] + lo2(r) * s[0];
  const f32x2 re1 = hi2(r) * c[1] - hi2(i) * s[1], im1 = hi2(i) * c[1] + hi2(r) * s[1];
  return u32x4{pkb(re0.x, im0.x), pkb(re0.y, im0.y), pkb(re1.x, im1.x), pkb(re1.y, im1.y)};
}
DI float cosr(float a) { return __builtin_amdgcn_cosf(a); }
DI float sinr(float a) { return __builtin_amdgcn_sinf(a); }
template <int CTRL> DI float dppf(float x) {
  return __builtin_bit_cast(float, __builtin_amdgcn_mov_dpp(__builtin_bit_cast(int, x), CTRL, 0xf, 0xf, true));
}
DI f32x2 bcast2(float x) {
  auto t = __builtin_amdgcn_permlane32_swap(__float_as_uint(x), __float_as_uint(x), false, false);
  return f32x2{__uint_as_float(t[0]), __uint_as_float(t[1])};
}
DI void ln_coef(f32x2 S, f32x2 Q, float ch, f32x2& R, f32x2& O) {
  auto t = __builtin_amdgcn_permlane32_swap(__float_as_uint(S.x), __float_as_uint(S.y), false, false);
  auto u = __builtin_amdgcn_permlane32_swap(__float_as_uint(Q.x), __float_as_uint(Q.y), false, false);
  const float s = __uint_as_float(t[0]) + __uint_as_float(t[1]), q = __uint_as_float(u[0]) + __uint_as_float(u[1]);
  auto z = __builtin_amdgcn_permlane16_swap(__float_as_uint(s), __float_as_uint(q), false, false);
  float x = __uint_as_float(z[0]) + __uint_as_float(z[1]);
  x += dppf<0xB1>(x);
  x += dppf<0x4E>(x);
  x += dppf<0x141>(x);
  x += dppf<0x140>(x);
  auto y = __builtin_amdgcn_permlane16_swap(__float_as_uint(x), __float_as_uint(x), false, false);
  const float md = __uint_as_float(y[0]) * (1.0f / 512.0f);
  const float var = relu(__uint_as_float(y[1]) * (1.0f / 512.0f) - md * md);
  const float r = __builtin_amdgcn_rcpf(bfr(__builtin_amdgcn_sqrtf(bfr(bfr(var) + 1e-5f))));
  const float o = (ch - bfr(ch + md)) * r;
  R = bcast2(r); O = bcast2(o);
}
DI float first_lane(float x) { return __uint_as_float(__builtin_amdgcn_readfirstlane(__float_as_uint(x))); }
DI int row_of_pos(int pos, int j) {
  const int c = 8 * (pos >> 7) + 2 * ((pos >> 3) & 3) + ((pos >> 5) & 1), q = 8 * ((pos >> 6) & 1) + (pos & 7);
  return 32 * (2 * c + (j & 1)) + 2 * q + (j >> 1);
}

template <int I> DI void ld4(u32x4& d, u32 v, const u8* s) { asm volatile("global_load_dwordx4 %0, %1, %2 offset:%3 sc1" : "=v"(d) : "v"(v), "s"(s), "i"(I) : "memory"); }
template <int I> DI void ld4a(u32x4& d, u32 v, const u8* s) { asm volatile("global_load_dwordx4 %0, %1, %2 offset:%3 sc1" : "=a"(d) : "v"(v), "s"(s), "i"(I) : "memory"); }
template <int I> DI void ld4r(u32x4& d, u32 v, const u8* s) { asm volatile("global_load_dwordx4 %0, %1, %2 offset:%3 sc1" : "+v"(d) : "v"(v), "s"(s), "i"(I) : "memory"); }
template <int I> DI void ld2(u32x2& d, u32 v, const u8* s) { asm volatile("global_load_dwordx2 %0, %1, %2 offset:%3 sc1" : "=v"(d) : "v"(v), "s"(s), "i"(I) : "memory"); }
template <int I> DI void ld2r(u32x2& d, u32 v, const u8* s) { asm volatile("global_load_dwordx2 %0, %1, %2 offset:%3 sc1" : "+v"(d) : "v"(v), "s"(s), "i"(I) : "memory"); }
template <int I> DI void ld2p(u32x2& d, u32 v, const u8* s) { asm volatile("global_load_dwordx2 %0, %1, %2 offset:%3" : "=v"(d) : "v"(v), "s"(s), "i"(I) : "memory"); }
DI void touch(u32& d, u32 v, const u8* s) { asm volatile("global_load_dword %0, %1, %2 sc1" : "+v"(d) : "v"(v), "s"(s) : "memory"); }
template <int I> DI void ld1p(u32& d, u32 v, const u8* s) { asm volatile("global_load_dword %0, %1, %2 offset:%3" : "=v"(d) : "v"(v), "s"(s), "i"(I) : "memory"); }
DI void ld1s(u32& d, u32 v, const u8* s) { asm volatile("global_load_ushort %0, %1, %2" : "=v"(d) : "v"(v), "s"(s) : "memory"); }
template <int N> DI void vmwait() { static_assert(N >= 0 && N < 64, ""); asm volatile("s_waitcnt vmcnt(%0)" ::"i"(N) : "memory"); }
DI void set_m0(u32 ldsaddr) { asm volatile("s_mov_b32 m0, %0" ::"s"(ldsaddr) : "memory"); }
template <int I> DI void dma1(u32 voff, const u8* src) {
  asm volatile("global_load_lds_dwordx4 %0, %1 offset:%2 sc1" ::"v"(voff), "s"(src), "i"(I) : "memory");
}
DI void dma4(u32 voff, const u8* src, u32 ldsaddr) {
  asm volatile("s_mov_b32 m0, %2\n\ts_nop 0\n\tglobal_load_lds_dwordx4 %0, %1 sc1\n\tglobal_load_lds_dwordx4 %0, %1 offset:1024 sc1\n\t"
               "global_load_lds_dwordx4 %0, %1 offset:2048 sc1\n\tglobal_load_lds_dwordx4 %0, %1 offset:3072 sc1" ::"v"(voff), "s"(src), "s"(ldsaddr) : "memory");
}
#define RETRY(COND, ...)                                                      \
  do {                                                                        \
    bool bad_ = (COND);                                                       \
    if (__builtin_expect(__any(bad_), 0)) {                                   \
      _Pragma("nounroll") for (int sp_ = 0; sp_ < SPIN_CAP; ++sp_) {          \
        __VA_ARGS__                                                           \
        bad_ = (COND);                                                        \
        if (!__any(bad_)) break;                                              \
      }                                                                       \
    }                                                                         \
  } while (0)
DI void bar() { asm volatile("s_waitcnt lgkmcnt(0)\n\ts_barrier" ::: "memory"); }
DI void bar_nw() { asm volatile("s_barrier" ::: "memory"); }

struct RowC { u32x4 b1re, b1im, b2re[2][2], b2im[2][2]; f32x2 twc[2][2], tws[2][2]; };
template <bool INV> DI void pair_cs(float c, float s, u32& re, u32& im) {
  if constexpr (INV) { im = pkb(s, c); re = pkb_n1(c, s, im); } else { re = pkh(c, s); im = pkh_n0(s, c, re); }
}
template <bool AG = false> DI void sign4(u32x4 base, u32 lm, u32x4& f00, u32x4& f01, u32x4& f10, u32x4& f11) {
  f00 = base;
  f01 = base ^ lm;
  f10 = base ^ u32x4{0u, 0x80008000u, 0u, 0x80008000u};
  f11 = f10 ^ lm;
  if constexpr (AG) { PINA(f00); PINA(f01); PINA(f10); PINA(f11); } else { PIN(f00); PIN(f01); PIN(f10); PIN(f11); }
}
template <bool INV, int PART> DI void row_consts(RowC& k, int m, int g) {
  const float fm = (float)m, fg = (float)(4 * g);
  if constexpr (PART == 0) {
    u32x4 br, bi;
#pragma unroll
    for (int i = 0; i < 4; ++i) {
      const float a = (fg + (float)i) * (fm * (1.0f / 16.0f));
      u32 x, y;
      pair_cs<INV>(cosr(a), sinr(a), x, y); k.b1re[i] = x; k.b1im[i] = y;
      const float a2 = (fg + (float)i) * (fm * (1.0f / 32.0f));
      pair_cs<INV>(cosr(a2), sinr(a2), x, y); br[i] = x; bi[i] = y;
    }
    PIN(k.b1re); PIN(k.b1im);
    const u32 lm = (m & 1) ? 0x80008000u : 0u;
    sign4(br, lm, k.b2re[0][0], k.b2re[0][1], k.b2re[1][0], k.b2re[1][1]);
    sign4(bi, lm, k.b2im[0][0], k.b2im[0][1], k.b2im[1][0], k.b2im[1][1]);
  } else {
#pragma unroll
    for (int e = 0; e < 2; ++e)
#pragma unroll
      for (int i = 0; i < 4; ++i) {
        const float a = (fg + (float)(16 * e + i)) * (fm * (INV ? -1.0f / 512.0f : 1.0f / 512.0f));
        k.twc[e][i >> 1][i & 1] = cosr(a); k.tws[e][i >> 1][i & 1] = sinr(a);
      }
#pragma unroll
    for (int e = 0; e < 2; ++e) { PIN(k.twc[e][0]); PIN(k.twc[e][1]); PIN(k.tws[e][0]); PIN(k.tws[e][1]); }
  }
}
template <bool INV, int TAIL> DI void row_fft(const RowC& k, u32x4 a0, u32x4 a1, f32x4& dr0, f32x4& di0, f32x4& dr1, f32x4& di1) {
  f32x4 tr0, ti0, tr1, ti1;
  mm4<INV>(tr0, ti0, tr1, ti1, a0, a1, k.b1re, k.b1im);
  u32x4 p0 = twiddle<INV>(tr0, ti0, k.twc[0], k.tws[0]);
  PIN(p0);
  const u32x4 p1 = cplx_under<INV, true>(tr1, ti1, k.twc[1], k.tws[1], [&](auto I) {
    constexpr int i = decltype(I)::value;
    mf0<INV, 0>(i == 0 ? dr0 : i == 1 ? di0 : i == 2 ? dr1 : di1, p0, i == 0 ? k.b2re[0][0] : i == 1 ? k.b2im[0][0] : i == 2 ? k.b2re[1][0] : k.b2im[1][0]);
  });
  mf4<INV, 0, TAIL>(dr0, di0, dr1, di1, p1, k.b2re[0][1], p1, k.b2im[0][1], p1, k.b2re[1][1], p1, k.b2im[1][1]);
}

struct FwdP { u32 iw, ir, ow, ap0, ap1, ar0, ar1, zo; f32x2 cB, cD, eA, eB, eC, eD; };
DI void fwd_pre(FwdP& p, int tid, int w, int pos) {
  const int m = tid & 15, g = (tid >> 4) & 3, ph = tid >> 1, fq = ph ? 512 - ph : 256, fq1 = 384 - ph;
  p.iw = w * 5120 + ((16 * (m & 1) + 4 * g) * 20 + (m >> 1)) * 4; p.ir = w * 5120 + (m * 20 + 4 * g) * 4;
  p.ap0 = (ph + (ph >> 4) * 4) * 4; p.ap1 = p.ap0 + 640; p.ar0 = (fq + (fq >> 4) * 4) * 4; p.ar1 = (fq1 + (fq1 >> 4) * 4) * 4;
  p.zo = OFF_A + pos * 8192 + tid * 16;
  const float sc = 1.0f / 64.0f, sg = (tid & 1) ? -sc : sc;
  p.cB = f32x2{sg, -sg}; p.cD = f32x2{sg, sg};
  p.eA = ph ? f32x2{sc, sc} : f32x2{2 * sc, 0.f}; p.eB = ph ? p.cB : f32x2{2 * sg, 0.f};
  p.eC = ph ? f32x2{sc, -sc} : f32x2{0.f, 2 * sg}; p.eD = ph ? p.cD : f32x2{0.f, 2 * sc};
  PIN(p.iw); PIN(p.ir); PIN(p.ap0); PIN(p.ap1); PIN(p.ar0); PIN(p.ar1); PIN(p.zo);
  PIN(p.cB); PIN(p.cD); PIN(p.eA); PIN(p.eB); PIN(p.eC); PIN(p.eD);
}
struct ColC {
  u32x4 b1re[2], b1im[2], a2re[2][2], a2im[2][2], b3re[2][2], b3im[2][2], a4re[2][2], a4im[2][2];
  f32x2 t1c[2][2], t1s[2][2], t2c[2][2], t2s[2][2];
};
template <int PART> DI void col_consts(ColC& k, int m, int g, int w) {
  const int pi_ = w >> 1, kt = w & 1;
  const float fm = (float)m, fg = (float)(4 * g);
  const u32 lm = (m & 1) ? 0x80008000u : 0u;
  if constexpr (PART == 0) {
    const float fk = (float)(2 * (16 * kt + m) + pi_);
#pragma unroll
    for (int u = 0; u < 2; ++u) {
#pragma unroll
      for (int r = 0; r < 4; ++r) {
        const float n = fg + (float)(16 * u + r), a = n * (fk * (1.0f / 64.0f)), a2 = n * (fk * (1.0f / 2048.0f));
        u32 x, y;
        pair_cs<false>(cosr(a), sinr(a), x, y); k.b1re[u][r] = x; k.b1im[u][r] = y;
        k.t1c[u][r >> 1][r & 1] = cosr(a2); k.t1s[u][r >> 1][r & 1] = sinr(a2);
      }
      PIN(k.b1re[u]); PIN(k.b1im[u]); PIN(k.t1c[u][0]); PIN(k.t1c[u][1]); PIN(k.t1s[u][0]); PIN(k.t1s[u][1]);
    }
  } else if constexpr (PART == 1) {
    u32x4 fr, fi, ir, ii;
#pragma unroll
    for (int r = 0; r < 4; ++r) {
      const float a = (fg + (float)r) * (fm * (1.0f / 32.0f)), c = cosr(a), s = sinr(a);
      u32 x, y;
      pair_cs<false>(c, s, x, y); fr[r] = x; fi[r] = y;
      pair_cs<true>(c, s, x, y); ir[r] = x; ii[r] = y;
    }
    sign4(fr, lm, k.a2re[0][0], k.a2re[0][1], k.a2re[1][0], k.a2re[1][1]);
    sign4(fi, lm, k.a2im[0][0], k.a2im[0][1], k.a2im[1][0], k.a2im[1][1]);
    sign4(ir, lm, k.b3re[0][0], k.b3re[0][1], k.b3re[1][0], k.b3re[1][1]);
    sign4(ii, lm, k.b3im[0][0], k.b3im[0][1], k.b3im[1][0], k.b3im[1][1]);
  } else if constexpr (PART == 2) {
    const float kb = (float)(32 * kt + pi_) + 2.0f * fg;
#pragma unroll
    for (int u = 0; u < 2; ++u) {
#pragma unroll
      for (int r = 0; r < 4; ++r) {
        const float a = (kb + (float)(2 * r)) * ((fm + (float)(16 * u)) * (-1.0f / 2048.0f));
        k.t2c[u][r >> 1][r & 1] = cosr(a); k.t2s[u][r >> 1][r & 1] = sinr(a);
      }
      PIN(k.t2c[u][0]); PIN(k.t2c[u][1]); PIN(k.t2s[u][0]); PIN(k.t2s[u][1]);
    }
  } else {
    const float fn = (fm + (float)(16 * (w >> 1))) * (1.0f / 64.0f);
#pragma unroll
    for (int u = 0; u < 2; ++u) {
#pragma unroll
      for (int r = 0; r < 4; ++r) {
        const float a = (2.0f * fg + (float)(2 * r + u)) * fn;
        u32 x, y;
        pair_cs<true>(cosr(a), sinr(a), x, y); k.a4re[u][0][r] = x; k.a4im[u][0][r] = y;
      }
      k.a4re[u][1] = k.a4re[u][0] ^ lm; k.a4im[u][1] = k.a4im[u][0] ^ lm;
      PIN(k.a4re[u][0]); PIN(k.a4re[u][1]); PIN(k.a4im[u][0]); PIN(k.a4im[u][1]);
    }
  }
}

DI void transpose8x4(const u32x2 (&v)[8], u32x4 (&o)[4]) {
#pragma unroll
  for (int jj = 0; jj < 4; ++jj) {
    o[0][jj] = __builtin_amdgcn_perm(v[2 * jj + 1].x, v[2 * jj].x, 0x05040100u);
    o[1][jj] = __builtin_amdgcn_perm(v[2 * jj + 1].x, v[2 * jj].x, 0x07060302u);
    o[2][jj] = __builtin_amdgcn_perm(v[2 * jj + 1].y, v[2 * jj].y, 0x05040100u);
    o[3][jj] = __builtin_amdgcn_perm(v[2 * jj + 1].y, v[2 * jj].y, 0x07060302u);
  }
}
template <int H> DI void prep_load1(const Params& P, int wt, int lane, u32x2 (&v)[8]) {
  const u32 vo = (lane < 56 ? lane : 55) * 8, vo2 = vo + 7168;
  const int piece = (wt >> 3) & 63;
  const u8* src = (const u8*)((wt >> 9) ? P.wu : P.wg) + opq(wt & 7) * 448 + opq(4 * piece) * 3584;
  static_for<4>([&](auto J) { constexpr int j = decltype(J)::value; ld2p<(j & 1) * 3584>(v[4 * H + j], j & 2 ? vo2 : vo, src + H * (256 * 3584)); });
}
template <int H> DI void prep_load2(const Params& P, int wt, int lane, u32 (&v)[8]) {
  const int piece = wt >> 2, g = piece & 3, kq = piece >> 2, wv = kq / 14, ks = kq % 14;
  const int f0 = ks < 4 ? 112 * (4 * wv + ks) : ks < 6 ? 112 * (4 * wv + 2 * (ks - 4)) + 32 : ks < 10 ? 112 * (4 * wv + ks - 6) + 48 : 112 * (4 * wv + ks - 10) + 80;
  const int f1 = f0 + ((ks == 4 || ks == 5) ? 112 : 16);
  const u8* s0 = (const u8*)P.wd + (wt & 3) * 256 + (4 * g + (H ? f1 : f0)) * 1024;
  static_for<4>([&](auto J) { constexpr int j = decltype(J)::value; ld1p<j * 1024>(v[4 * H + j], lane * 4, s0); });
}
DI u32 prep_dst1(int wt, int tile) {
  const int piece = (wt >> 3) & 63, mat = wt >> 9, hi = tile >= 7, p = tile - 7 * hi, ks = piece >> 2;
  const u32 frag = (p < 3 ? ks * 6 + 2 * p : p < 5 ? 96 + ks * 4 + 2 * (p - 3) : 160 + ks * 4 + 2 * (p - 5)) + mat;
  return OFF_W1 + ((2 * (wt & 7) + hi) * 224 + frag) * 1024 + (piece & 3) * 256;
}
DI u32 prep_dst2(int wt, int tile) {
  const int piece = wt >> 2, tl = (wt & 3) * 8 + tile, rot = (tl >> 2) & 3, kq = piece >> 2, ks = kq % 14;
  const int slot = ks < 4 ? (ks - rot) & 3 : ks < 6 ? ks ^ (rot & 1) : ks < 10 ? 6 + ((ks - 6 - rot) & 3) : 10 + ((ks - 10 - rot) & 3);
  return OFF_W2 + (((tl >> 2) * 56 + kq - ks + slot) * 4 + (tl & 3)) * 1024 + (piece & 3) * 256;
}

constexpr int NTOK0 = 12, NRES = 40, NEARLY = 26;
constexpr int g1_job(int L, int x) {
  if (((x >= 34 && x < 96) && (x & 3) == 2) || (x >= 98 && x < 114 && !(x & 1))) return 2;
  if ((x < 34 && !(x & 1)) || (x < 64 && (x & 3) == 0)) return 1;
  if (L == 1 && x >= 47 && x < 60 && (x & 3) == 3) return 6;
  if (x >= 164) return ((x - 164) % 7 == 0 || (x - 164) % 7 == 2 || (x - 164) % 7 == 5) ? 4 : 0;
  if (x >= 116 && x % 3 == 2) return 5;
  if (L == 0 && ((x < 34 && x % 4 == 1) || x == 158 || x == 161 || x == 163 || (x >= 48 && x < 98 && (x & 3) == 0) || (x >= 114 && x % 3 == 0) || (x >= 35 && x < 98 && x % 8 == 1)))
    return 3;
  return 0;
}
constexpr int NEPI1 = 123, NEPI2 = 82;
struct G1Sched {
  int job[224], idx[224], epi[225], nst[224], cum[225], pos[7][64], cnt[7];
  constexpr G1Sched(int L) : job{}, idx{}, epi{}, nst{}, cum{}, pos{}, cnt{} {
    cnt[1] = NTOK0;
    for (int i = 0; i < NTOK0; ++i) pos[1][i] = i;
    cum[0] = NTOK0;
    for (int x = 0; x < 224; ++x) {
      int j = g1_job(L, x);
      if (j == 1 && cnt[1] >= 32) j = L == 0 ? 3 : 0;
      if ((j == 3 && cnt[3] >= NRES) || (j == 4 && cnt[4] >= NEARLY)) j = 0;
      job[x] = j; idx[x] = cnt[j];
      if (x == 161) epi[x] = NEPI1;
      const bool on = (x >= 97 && x < 160 && epi[x] < NEPI1) || x >= 161;
      epi[x + 1] = on ? epi[x] + (j ? 2 : 3) : epi[x];
      if (x < 160 && epi[x + 1] > NEPI1) epi[x + 1] = NEPI1;
      for (int n = epi[x]; n < epi[x + 1]; ++n)
        nst[x] += n < NEPI1 ? (n == 40 || n == 81 || n == 122) : (n < NEPI1 + NEPI2 && (n - NEPI1) % 41 == 40);
      if (j) pos[j][cnt[j]++] = cum[x] + nst[x];
      cum[x + 1] = cum[x] + nst[x] + (j ? 1 : 0);
    }
  }
};
constexpr G1Sched G1S[2] = {G1Sched(0), G1Sched(1)};
static_assert(G1S[0].cnt[1] == 32 && G1S[0].cnt[2] == 24 && G1S[0].cnt[3] == NRES && G1S[0].cnt[4] == NEARLY && G1S[0].cnt[5] == 16, "");
static_assert(G1S[1].cnt[6] == 4 && G1S[1].pos[6][3] < G1S[1].pos[2][7], "");
static_assert(G1S[0].idx[66] == 8 && G1S[0].idx[98] == 16 && G1S[0].epi[113] <= 40 && G1S[1].epi[113] <= 40, "");
static_assert(G1S[0].epi[152] == NEPI1 && G1S[0].epi[204] >= NEPI1 + NEPI2, "");
constexpr int clamp63(int n) { return n > 63 ? 63 : n; }
constexpr int g1_wait(int L, int x, int job, int idx) { return clamp63(G1S[L].cum[x] - G1S[L].pos[job][idx] - 1); }

#define FRESH()                                                                                                   \
  int tid = threadIdx.x, b = blockIdx.x, w = w_;                                                                   \
  u8* ws = P.ws;                                                                                                   \
  asm volatile("" : "+v"(tid), "+s"(b), "+s"(w), "+s"(ws));                                                        \
  const int lane = tid & 63, m = lane & 15, g = lane >> 4, half2 = b & 1, ct = b >> 5;                             \
  const int G = ((b >> 1) & 3) * 4 + ((b >> 3) & 3), member = half2 * 8 + ct, pos = G * 16 + member;               \
  const u32 l16 = lane * 16, t16 = tid * 16, xo = 32 * m + 8 * g;                                                  \
  auto fill_h = [&](u32 base, int q) { st4(sent4, base + (4 * G + w) * 114688 + member * 7168 + q * 1024 + l16); };          \
  auto fill_rows = [&](u32 base) { st4(sent4, base + pos * 8192 + t16); st4(sent4, base + pos * 8192 + 4096 + t16); }; \
  auto ring4 = [&](int q) {                                   \
    const u32 o = (q >> 1) * 32768 + w * 8192 + (q & 1) * 4096;                                                    \
    dma4(l16, ws + OFF_W1 + member * 229376 + o, ldsbase + o);                                                     \
  };                                                                                                               \
  (void)m; (void)g; (void)half2; (void)ct; (void)l16; (void)t16; (void)xo; (void)fill_h; (void)fill_rows; (void)ring4

__global__ __launch_bounds__(256, 1) void k_all(Params P) {
  extern __shared__ __attribute__((aligned(16))) u8 lds[];
  ST_DECL;
  const int w_ = __builtin_amdgcn_readfirstlane(threadIdx.x >> 6);
  const __amdgpu_buffer_rsrc_t rs = __builtin_amdgcn_make_buffer_rsrc(P.ws, 0, WS_USED, 0x00020000);
  const u32 ldsbase = (u32)(size_t)(__attribute__((address_space(3))) u8*)lds;
  const u32x4 sent4 = {SENT, SENT, SENT, SENT};
  auto st4 = [&](u32x4 v, u32 off) { __builtin_amdgcn_raw_buffer_store_b128(v, rs, off, 0, 16); };
  auto st2 = [&](u32x2 v, u32 off) { __builtin_amdgcn_raw_buffer_store_b64(v, rs, off, 0, 16); };
  u32x2 xl[2], xh[2], pv0[8];
  u32 pv1[8];
  u32 wfr[8], wfi[8], wn[8];
  f32x2 xv[8], xc;
  float xch;
  FwdP fp;
  u32 pd[6];
  u32x2 ep;
  u32 cpf = 0;
  RowC rc;
  ColC cc;
  ST(0);
  {
    FRESH();
    const u8* xp = (const u8*)P.x + row_of_pos(pos, w) * 1024;
    ld2p<0>(xl[0], xo, xp); ld2p<512>(xl[1], xo, xp); ld2p<0>(xh[0], xo, xp + 1024 * 1024); ld2p<512>(xh[1], xo, xp + 1024 * 1024);
    { const u32 z = 0; ld2<0>(ep, z, ws); }
    {
      const u8* pc;
      asm volatile("s_getpc_b64 %0" : "=s"(pc));
      const u32 o = (((b >> 3) * 4 + w) * 8 + (lane & 7)) * 128;
      touch(cpf, o < CODE_BYTES ? o : 0, pc);
    }
    XS(32);
    fill_rows(OFF_A); fill_rows(OFF_WX);
    if (tid < 8) st4(sent4, OFF_WNY + tid * 4096 + pos * 16);
    SB();
    row_consts<false, 0>(rc, m, g);
    row_consts<false, 1>(rc, m, g);
    SB();
    XS(33);
    const int kb = 2 * (16 * ((pos >> 6) & 1) + (pos & 15)) + (pos >> 7) + 256 * ((pos >> 4) & 3);
    static_for<8>([&](auto J) {
      constexpr int j = decltype(J)::value;
      const u32 o = tid * 2 + (kb + 64 * (16 * (j >> 2) + (j & 3))) * 514;
      ld1s(wfr[j], o, (const u8*)P.wr); ld1s(wfi[j], o, (const u8*)P.wi);
    });
    {
      const int k = kb + 64 * (16 * ((lane >> 2) & 1) + (lane & 3)), km = (2048 - k) & 2047;
      const u32 o = k * 514, om = km * 514;
      ld1s(wn[0], o, (const u8*)P.wr); ld1s(wn[1], o, (const u8*)P.wi); ld1s(wn[2], om, (const u8*)P.wr); ld1s(wn[3], om, (const u8*)P.wi);
      ld1s(wn[4], o, (const u8*)P.wr + 512); ld1s(wn[5], o, (const u8*)P.wi + 512); ld1s(wn[6], om, (const u8*)P.wr + 512); ld1s(wn[7], om, (const u8*)P.wi + 512);
    }
    XS(34);
    fwd_pre(fp, tid, w, pos);
#pragma unroll
    for (int x = 0; x < 4; ++x) pd[x] = x < 3 || g < 2 ? prep_dst1(b * 4 + w, 4 * x + g) + m * 16 : WS_USED;
#pragma unroll
    for (int x = 0; x < 2; ++x) pd[4 + x] = b < 224 ? prep_dst2(b * 4 + w, 4 * x + g) + m * 16 : WS_USED;
#pragma unroll
    for (int x = 0; x < 6; ++x) PIN(pd[x]);
    SB();
    XS(35);
    vmwait<1 + 4 + 24>();
    XS(36);
    PIN(ep); PIN(xl[0]); PIN(xl[1]); PIN(xh[0]); PIN(xh[1]);
    { const u64 e = (((u64)(u32)__builtin_amdgcn_readfirstlane(ep.y) << 32) | (u32)__builtin_amdgcn_readfirstlane(ep.x)) + 1; ep = u32x2{(u32)e, (u32)(e >> 32)}; }
#pragma unroll
    for (int t = 0; t < 2; ++t)
#pragma unroll
      for (int p = 0; p < 2; ++p) {
        xv[4 * t + 2 * p] = f32x2{blo(xl[t][p]), blo(xh[t][p])};
        xv[4 * t + 2 * p + 1] = f32x2{bhi(xl[t][p]), bhi(xh[t][p])};
      }
    xc = f32x2{first_lane(xv[0].x), first_lane(xv[0].y)};
    xch = lane < 32 ? xc.x : xc.y;
#pragma unroll
    for (int i = 0; i < 8; ++i) xv[i] -= xc;
  }
  ST(1);

  u32x4 w2a[10][4];

  auto layer = [&](auto LC) {
    constexpr int L = decltype(LC)::value;
    constexpr int S0 = 2 + 16 * L;
    {
      FRESH();
      const f32x2 S = ((xv[0] + xv[1]) + (xv[2] + xv[3])) + ((xv[4] + xv[5]) + (xv[6] + xv[7]));
      f32x2 q0 = xv[0] * xv[0], q1 = xv[1] * xv[1];
#pragma unroll
      for (int i = 2; i < 8; i += 2) { q0 += xv[i] * xv[i]; q1 += xv[i + 1] * xv[i + 1]; }
      if constexpr (L == 0) { SB(); fill_rows(OFF_U); SB(); }
      f32x2 R, O;
      XF(32);
      ln_coef(S, q0 + q1, xch, R, O);
      XF(33);
      u8* img = lds + LDS_SCR;
      if constexpr (L == 0) {
        SB(); vmwait<26>(); PIN(cpf);
        if (lane == 0) st2(ep, OFF_FLAG + (w * 256 + pos) * 8);
        prep_load1<0>(P, b * 4 + w, lane, pv0); SB();
      }
#pragma unroll
      for (int i = 0; i < 8; ++i) {
        const f32x2 v = xv[i] * R + O;
        *(u32*)(img + fp.iw + ((i & 3) * 20 + 8 * (i >> 2)) * 4) = pkh(v.x, v.y);
      }
      const u32x4 a0 = *(const u32x4*)(img + fp.ir), a1 = *(const u32x4*)(img + fp.ir + 1280);
      if constexpr (L == 0) prep_load1<1>(P, b * 4 + w, lane, pv0); else ring4(1);
      const float* p0 = (const float*)(img + fp.ap0);
      const float* p1 = (const float*)(img + fp.ap1);
      const float* r0 = (const float*)(img + fp.ar0);
      const float* r1 = (const float*)(img + fp.ar1);
      const f32x2 cA = {1.0f / 64.0f, 1.0f / 64.0f}, cC = {1.0f / 64.0f, -1.0f / 64.0f}, cB = fp.cB, cD = fp.cD, eA = fp.eA, eB = fp.eB, eC = fp.eC, eD = fp.eD;
      const u32 zo = fp.zo;
      f32x4 dr0, di0, dr1, di1;
      XF(34);
      row_fft<false, 6>(rc, a0, a1, dr0, di0, dr1, di1);
      XF(35);
      SB();
      float* op = (float*)(img + fp.ir);
      *(f32x4*)(op) = dr0; *(f32x4*)(op + 640) = di0;
      SB();
      *(f32x4*)(op + 320) = dr1; *(f32x4*)(op + 960) = di1;
      if constexpr (L == 0) { prep_load2<0>(P, (b < 224 ? b : 0) * 4 + w, lane, pv1); vmwait<13>(); }
      bar();
      XF(36);
      f32x2 pa[4], qa[4], pb[4], qb[4];
#pragma unroll
      for (int j = 0; j < 4; ++j) {
        pa[j] = f32x2{p0[j * 1280], p0[j * 1280 + 640]}; qa[j] = f32x2{r0[j * 1280], r0[j * 1280 + 640]};
      }
#pragma unroll
      for (int j = 0; j < 4; ++j) {
        pb[j] = f32x2{p1[j * 1280], p1[j * 1280 + 640]}; qb[j] = f32x2{r1[j * 1280], r1[j * 1280 + 640]};
      }
      if constexpr (L == 0) prep_load2<1>(P, (b < 224 ? b : 0) * 4 + w, lane, pv1);
      SB();
      u32x4 o;
#pragma unroll
      for (int j = 0; j < 4; ++j) {
        const f32x2 v = eD * swp(qa[j]) + (eC * qa[j] + (eB * swp(pa[j]) + eA * pa[j]));
        o[j] = mark<(L == 0 ? 0x10001u : 0u), (L == 0 ? 0u : 0x10000u)>(pkh(v.x, v.y));
      }
      st4(o, zo);
#pragma unroll
      for (int j = 0; j < 4; ++j) {
        const f32x2 v = cD * swp(qb[j]) + (cC * qb[j] + (cB * swp(pb[j]) + cA * pb[j]));
        o[j] = mark<(L == 0 ? 0x10001u : 0u), (L == 0 ? 0u : 0x10000u)>(pkh(v.x, v.y));
      }
      st4(o, zo + 4096);
      SB();
      if constexpr (L == 1) {
#pragma unroll
        for (int t = 0; t < 2; ++t)
#pragma unroll
          for (int p = 0; p < 2; ++p) {
            const f32x2 v0 = xv[4 * t + 2 * p] + xc, v1 = xv[4 * t + 2 * p + 1] + xc;
            xl[t][p] = pkb(v0.x, v1.x); xh[t][p] = pkb(v0.y, v1.y);
          }
        PIN(xl[0]); PIN(xl[1]); PIN(xh[0]); PIN(xh[1]);
      }
    }
    if constexpr (L == 0) {
      XS(37);
      FRESH();
#pragma unroll
      for (int j = 0; j < 8; ++j) { PIN(wfr[j]); PIN(wfi[j]); PIN(wn[j]); }
      u8* bw = lds + w * 2048;
      u32x4 v0, v1;
#pragma unroll
      for (int j = 0; j < 4; ++j) { v0[j] = wfr[j] | (wfi[j] << 16); v1[j] = wfr[4 + j] | (wfi[4 + j] << 16); }
      *(u32x4*)(bw + lane * 32) = v0; *(u32x4*)(bw + lane * 32 + 16) = v1;
      st4(*(const u32x4*)(bw + l16), OFF_WX + pos * 8192 + w * 2048 + l16);
      st4(*(const u32x4*)(bw + 1024 + l16), OFF_WX + pos * 8192 + w * 2048 + 1024 + l16);
      const float hr = blo(wn[0]) + blo(wn[2]), hi = blo(wn[1]) - blo(wn[3]), nr = blo(wn[4]) + blo(wn[6]), ni = blo(wn[5]) - blo(wn[7]);
      const f32x4 ab = {0.25f * (hr + nr), 0.25f * (hi + ni), 0.25f * (hr - nr), 0.25f * (hi - ni)};
      if (tid < 8) st4(__builtin_bit_cast(u32x4, ab), OFF_WNY + tid * 4096 + pos * 16);
    }
    ST(S0);
    {
      FRESH();
      const int phi = pos, pi_ = w >> 1, kt = w & 1;
      const int k1l = 2 * (16 * kt + m) + pi_;
      auto consts = [&](auto PC) { col_consts<decltype(PC)::value>(cc, m, g, w); SB(); };
      using I0 = std::integral_constant<int, 0>; using I1 = std::integral_constant<int, 1>;
      using I2 = std::integral_constant<int, 2>; using I3 = std::integral_constant<int, 3>;
      u8* const bw = lds + 16384 + w * 8192;
      const u32 zo = (tid >> 1) * 8192 + (tid & 1) * 16;
      const u8* zb0 = ws + OFF_A + phi * 32;
      const u8* zb1 = zb0 + 128 * 8192;
      const u8* wb0 = ws + OFF_WX + phi * 32;
      const u8* wb1 = wb0 + 128 * 8192;
      const u32 wo = zo;
      PINS(zb0); PINS(zb1); PINS(wb0); PINS(wb1);
      if constexpr (L == 0) {
        u32x4 fl0, fl1;
        const u32 o = lane * 32;
        const u8* fb = ws + OFF_FLAG + ((phi >> 5) & 3) * 2048;
        ld4<0>(fl0, o, fb); ld4<16>(fl1, o, fb);
        SB();
        consts(I0{});
        consts(I1{});
        XS(38);
        vmwait<0>(); PIN(fl0); PIN(fl1);
        RETRY(!(fl0.x == ep.x && fl0.y == ep.y && fl0.z == ep.x && fl0.w == ep.y && fl1.x == ep.x && fl1.y == ep.y && fl1.z == ep.x && fl1.w == ep.y), {
          CNT(0);
          ld4r<0>(fl0, o, fb); ld4r<16>(fl1, o, fb);
          vmwait<0>(); PIN(fl0); PIN(fl1);
        });
      } else {
        SB();
        consts(I0{});
        consts(I2{});
      }
      u32x4 z0, z1, wv[2];
      if constexpr (L == 0) XS(39);
      ld4<0>(z0, zo, zb0); ld4<0>(z1, zo, zb1);
      ld4<0>(wv[0], wo, wb0); ld4<0>(wv[1], wo, wb1);
      SB();
      u32x4 wa[8], po[6];
      if constexpr (L == 0) {
        XS(40);
        u32x4 ot[4];
#pragma unroll
        for (int j = 0; j < 8; ++j) { PIN(pv0[j]); PIN(pv1[j]); }
        transpose8x4(pv0, ot);
#pragma unroll
        for (int x = 0; x < 4; ++x) *(u32x4*)(bw + lane * 64 + ((x + lane) & 3) * 16) = ot[x];
#pragma unroll
        for (int jj = 0; jj < 4; ++jj) {
          ot[0][jj] = __builtin_amdgcn_perm(pv1[2 * jj + 1], pv1[2 * jj], 0x05040100u);
          ot[1][jj] = __builtin_amdgcn_perm(pv1[2 * jj + 1], pv1[2 * jj], 0x07060302u);
        }
        *(u32x4*)(bw + 4096 + lane * 32) = ot[0]; *(u32x4*)(bw + 4096 + lane * 32 + 16) = ot[1];
#pragma unroll
        for (int x = 0; x < 4; ++x) {
          const int src = x * 16 + (lane >> 2);
          po[x] = *(const u32x4*)(bw + src * 64 + (((lane & 3) + src) & 3) * 16);
        }
#pragma unroll
        for (int x = 0; x < 2; ++x) {
          po[4 + x] = *(const u32x4*)(bw + 4096 + x * 1024 + (8 * (m >> 2) + 4 * (g & 1) + (m & 3) + 32 * (g >> 1)) * 16);
        }
#pragma unroll
        for (int x = 0; x < 6; ++x) PIN(po[x]);
        st4(po[0], pd[0]); st4(po[1], pd[1]);
        SB();
        XS(41);
        consts(I2{});
        XS(42);
        consts(I3{});
        XS(43);
        st4(po[2], pd[2]); st4(po[3], pd[3]);
      } else {
        consts(I1{});
        ring4(2);
        SB();
        consts(I3{});
      }
      u8* const zi = lds + LDS_SCR;
      u32 zw;
      {
        const int pp = tid >> 1;
        zw = (tid & 1) * 4096 + ((pp >> 6) & 1) * 2048 + (((pp >> 3) & 3) * 16 + 2 * (pp & 7)) * 16 + ((pp >> 5) & 1) * 8;
        PIN(zw);
      }
      u32 uo = OFF_U + phi * 8192 + w * 2048 + l16;
      PIN(uo);
      SB();
      bar_nw();
      ST(S0 + 1);
      vmwait<4>();
      PIN(z0); PIN(z1); PIN(wv[0]); PIN(wv[1]);
      if constexpr (L == 0) {
        RETRY((stale<false, 1u>(red8<false>(z0, z1)) || max4(max4(0, wv[0]), wv[1]) == SENT), {
          CNT(1);
          if (bad_) { ld4r<0>(z0, zo, zb0); ld4r<0>(z1, zo, zb1); ld4r<0>(wv[0], wo, wb0); ld4r<0>(wv[1], wo, wb1); }
          vmwait<0>(); PIN(z0); PIN(z1); PIN(wv[0]); PIN(wv[1]);
        });
      } else {
        RETRY((stale<true, 0x10000u>(red8<true>(z0, z1))), {
          CNT(2);
          if (bad_) { ld4r<0>(z0, zo, zb0); ld4r<0>(z1, zo, zb1); }
          vmwait<0>(); PIN(z0); PIN(z1);
        });
      }
      ST(S0 + 2);
      *(u32x2*)(zi + zw) = u32x2{z0.x, z0.y}; *(u32x2*)(zi + zw + 16) = u32x2{z0.z, z0.w};
      *(u32x2*)(zi + zw + 1024) = u32x2{z1.x, z1.y}; *(u32x2*)(zi + zw + 1024 + 16) = u32x2{z1.z, z1.w};
      *(u32x4*)(zi + 8192 + t16) = wv[0]; *(u32x4*)(zi + 12288 + t16) = wv[1];
      bar();
      XF(37);
      const u8* zr = zi + pi_ * 4096 + l16;
      const u32x4 f00 = *(const u32x4*)(zr), f01 = *(const u32x4*)(zr + 1024), f10 = *(const u32x4*)(zr + 2048), f11 = *(const u32x4*)(zr + 3072);
      wv[0] = *(const u32x4*)(zi + 8192 + tid * 32); wv[1] = *(const u32x4*)(zi + 8192 + tid * 32 + 16);
      if (phi == 0) static_for<8>([&](auto J) { constexpr int j = decltype(J)::value; ld4<0>(wa[j], t16, ws + OFF_WNY + j * 4096); });
      if constexpr (L == 1) ring4(3); else { st4(po[4], pd[4]); st4(po[5], pd[5]); }
      f32x4 d0, d1, d2, d3;
      mm8<false, 3>(d0, d1, d2, d3, f00, cc.b1re[0], f01, cc.b1re[1], f00, cc.b1im[0], f01, cc.b1im[1], f10, cc.b1re[0], f11, cc.b1re[1], f10, cc.b1im[0], f11, cc.b1im[1]);
      XF(38);
      f32x4 e0, e1, e2, e3;
      u32x4 b20 = twiddle<false>(d0, d1, cc.t1c[0], cc.t1s[0]);
      PIN(b20);
      const u32x4 b21 = cplx_under<false, true>(d2, d3, cc.t1c[1], cc.t1s[1], [&](auto I) {
        constexpr int i = decltype(I)::value;
        mf0<false, 0>(i == 0 ? e0 : i == 1 ? e1 : i == 2 ? e2 : e3, i == 0 ? cc.a2re[0][0] : i == 1 ? cc.a2im[0][0] : i == 2 ? cc.a2re[1][0] : cc.a2im[1][0], b20);
      });
      f32x2 wr[2][2], wi[2][2];
      auto unpack_w = [&]() {
#pragma unroll
        for (int t = 0; t < 2; ++t)
#pragma unroll
          for (int h = 0; h < 2; ++h) {
            wr[t][h] = f32x2{blo(wv[t][2 * h]), blo(wv[t][2 * h + 1])}; wi[t][h] = f32x2{bhi(wv[t][2 * h]), bhi(wv[t][2 * h + 1])};
            PIN(wr[t][h]); PIN(wi[t][h]);
          }
      };
      mf4<false, 0, 0>(e0, e1, e2, e3, cc.a2re[0][1], b21, cc.a2im[0][1], b21, cc.a2re[1][1], b21, cc.a2im[1][1], b21);
      SB();
      unpack_w();
      SB();
      if constexpr (L == 0) { fill_h(OFF_H, 0); fill_h(OFF_H, 1); }
      XF(39);
      u32x4 a30, a31;
      auto step3 = [&](auto I) {
        constexpr int i = decltype(I)::value;
        mf0<true, 0>(i == 0 ? d0 : i == 1 ? d1 : i == 2 ? d2 : d3, a30, i == 0 ? cc.b3re[0][0] : i == 1 ? cc.b3im[0][0] : i == 2 ? cc.b3re[1][0] : cc.b3im[1][0]);
      };
      if (phi != 0) {
        a30 = cmul_b(e0, e1, wr[0], wi[0]);
        PIN(a30);
        a31 = cplx_under<true, false>(e2, e3, wr[1], wi[1], step3);
      } else {
        u32* pl = (u32*)(lds + LDS_STASH);
        const u32x4 h0 = {pkh(e0[0], e1[0]), pkh(e0[1], e1[1]), pkh(e0[2], e1[2]), pkh(e0[3], e1[3])};
        const u32x4 h1 = {pkh(e2[0], e3[0]), pkh(e2[1], e3[1]), pkh(e2[2], e3[2]), pkh(e2[3], e3[3])};
        if (k1l != 0) {
          *(u32x4*)(pl + k1l * 32 + ((g ^ (k1l >> 1)) & 7) * 4) = h0; *(u32x4*)(pl + k1l * 32 + (((4 + g) ^ (k1l >> 1)) & 7) * 4) = h1;
        } else {
#pragma unroll
          for (int r = 0; r < 4; ++r) { pl[(4 * g + r - 1) & 31] = h0[r]; pl[16 + 4 * g + r - 1] = h1[r]; }
        }
        bar();
        vmwait<4>();
#pragma unroll
        for (int j = 0; j < 8; ++j) PIN(wa[j]);
        if constexpr (L == 0) {
          RETRY(max4(max4(max4(max4(max4(max4(max4(max4(0, wa[0]), wa[1]), wa[2]), wa[3]), wa[4]), wa[5]), wa[6]), wa[7]) == SENT, {
            static_for<8>([&](auto J) { constexpr int j = decltype(J)::value; ld4r<0>(wa[j], t16, ws + OFF_WNY + j * 4096); });
            vmwait<0>();
            _Pragma("unroll") for (int j = 0; j < 8; ++j) PIN(wa[j]);
          });
        }
        const int kr = (64 - k1l) & 63;
        const u32x4 q0 = *(const u32x4*)(pl + kr * 32 + (((7 - g) ^ (kr >> 1)) & 7) * 4), q1 = *(const u32x4*)(pl + kr * 32 + (((3 - g) ^ (kr >> 1)) & 7) * 4);
#pragma unroll
        for (int r = 0; r < 4; ++r) {
          const f32x4 c0 = __builtin_bit_cast(f32x4, wa[r]), c1 = __builtin_bit_cast(f32x4, wa[4 + r]);
          const u32 u0 = q0[3 - r], u1 = q1[3 - r];
          const f16x2 v0 = __builtin_bit_cast(f16x2, u0), v1 = __builtin_bit_cast(f16x2, u1);
          const float qr0 = (float)v0.x, qi0 = (float)v0.y, qr1 = (float)v1.x, qi1 = (float)v1.y;
          a30[r] = pkb(e0[r] * c0[0] - e1[r] * c0[1] + qr0 * c0[2] + qi0 * c0[3], e0[r] * c0[1] + e1[r] * c0[0] + qr0 * c0[3] - qi0 * c0[2]);
          a31[r] = pkb(e2[r] * c1[0] - e3[r] * c1[1] + qr1 * c1[2] + qi1 * c1[3], e2[r] * c1[1] + e3[r] * c1[0] + qr1 * c1[3] - qi1 * c1[2]);
        }
        PIN(a30); PIN(a31);
        asm volatile("s_nop 1");
        static_for<4>(step3);
      }
      u8* ex = lds + LDS_SCR + 16384;
      mf4<true, 0, 5>(d0, d1, d2, d3, a31, cc.b3re[0][1], a31, cc.b3im[0][1], a31, cc.b3re[1][1], a31, cc.b3im[1][1]);
      XF(40);
      *(u32x4*)(ex + w * 1024 + l16) = twiddle<true>(d0, d1, cc.t2c[0], cc.t2s[0]);
      SB();
      *(u32x4*)(ex + (4 + w) * 1024 + l16) = twiddle<true>(d2, d3, cc.t2c[1], cc.t2s[1]);
      if constexpr (L == 0) vmwait<2>();
      bar();
      XF(41);
      const u8* er = ex + (w & 1) * 4096 + l16;
      const u32x4 g0 = *(const u32x4*)(er), g1 = *(const u32x4*)(er + 1024), g2 = *(const u32x4*)(er + 2048), g3 = *(const u32x4*)(er + 3072);
      mm8<true, 7>(d0, d1, d2, d3, cc.a4re[0][0], g0, cc.a4re[0][1], g1, cc.a4im[0][0], g0, cc.a4im[0][1], g1, cc.a4re[1][0], g2, cc.a4re[1][1], g3, cc.a4im[1][0], g2,
                   cc.a4im[1][1], g3);
      {
        XF(42);
        const f32x2 lr0 = lo2(d0) + lo2(d2), li0 = lo2(d1) + lo2(d3), hr0 = lo2(d0) - lo2(d2), hi0 = lo2(d1) - lo2(d3);
        const f32x2 lr1 = hi2(d0) + hi2(d2), li1 = hi2(d1) + hi2(d3), hr1 = hi2(d0) - hi2(d2), hi1 = hi2(d1) - hi2(d3);
        if (phi != 0) {
          const f32x2 pr0 = lr0 - hi0, pi0 = li0 + hr0, qr0 = lr0 + hi0, qi0 = hr0 - li0;
          st4(mark4<1u - L, L>(u32x4{pkb(pr0.x, pi0.x), pkb(qr0.x, qi0.x), pkb(pr0.y, pi0.y), pkb(qr0.y, qi0.y)}), uo);
          const f32x2 pr1 = lr1 - hi1, pi1 = li1 + hr1, qr1 = lr1 + hi1, qi1 = hr1 - li1;
          st4(mark4<1u - L, L>(u32x4{pkb(pr1.x, pi1.x), pkb(qr1.x, qi1.x), pkb(pr1.y, pi1.y), pkb(qr1.y, qi1.y)}), uo + 1024);
          MEMFENCE();
        } else {
          st4(mark4<1u - L, L>(u32x4{pkb(lr0.x, hr0.x), pkb(li0.x, hi0.x), pkb(lr0.y, hr0.y), pkb(li0.y, hi0.y)}), uo);
          st4(mark4<1u - L, L>(u32x4{pkb(lr1.x, hr1.x), pkb(li1.x, hi1.x), pkb(lr1.y, hr1.y), pkb(li1.y, hi1.y)}), uo + 1024);
        }
      }
    }
    ST(S0 + 3);
    {
      FRESH();
      SB();
      row_consts<true, 0>(rc, m, g);
      f32x2 xa[4], xb[4];
#pragma unroll
      for (int i = 0; i < 4; ++i) {
        xa[i] = f32x2{blo(xl[i >> 1][i & 1]), bhi(xl[i >> 1][i & 1])}; xb[i] = f32x2{blo(xh[i >> 1][i & 1]), bhi(xh[i >> 1][i & 1])};
      }
      const float ca = first_lane(xa[0].x), cb = first_lane(xb[0].x);
      float ch = lane < 32 ? ca : cb;
      PIN(ch);
#pragma unroll
      for (int i = 0; i < 4; ++i) { xa[i] -= ca; xb[i] -= cb; PIN(xa[i]); PIN(xb[i]); }
      SB();
      u32x4 c0, c1;
      const u32 uo = (tid >> 1) * 8192 + (tid & 1) * 16;
      const u8* ub0 = ws + OFF_U + pos * 32;
      const u8* ub1 = ub0 + 128 * 8192;
      ld4<0>(c0, uo, ub0); ld4<0>(c1, uo, ub1);
      if constexpr (L == 0) { fill_h(OFF_H, 2); fill_h(OFF_H, 3); fill_h(OFF_H, 4); }
      SB();
      row_consts<true, 1>(rc, m, g);
      SB();
      if constexpr (L == 1) ring4(4); else { fill_h(OFF_H, 5); fill_h(OFF_H, 6); }
      u32* img = (u32*)(lds + LDS_SCR);
      u32 ip0, iq0, iq1, xno, tw0, tw1, trd;
      {
        const int ph = tid >> 1, fq0 = ph ? 512 - ph : 256, fq1 = 384 - ph;
        ip0 = ((tid & 1) * 1280 + (ph & 31) * 20 + (ph >> 5)) * 4;
        iq0 = ((tid & 1) * 1280 + (fq0 & 31) * 20 + (fq0 >> 5)) * 4; iq1 = ((tid & 1) * 1280 + (fq1 & 31) * 20 + (fq1 >> 5)) * 4;
        xno = OFF_A + (pos >> 1) * 16384 + (tid >> 3) * 256 + (pos & 1) * 128 + (tid & 7) * 16;
        tw0 = (4 * m + g) * 128 + ((2 * w) ^ (m & 7)) * 16; tw1 = (4 * m + g) * 128 + ((2 * w + 1) ^ (m & 7)) * 16;
        trd = (tid >> 3) * 128 + ((tid & 7) ^ ((tid >> 5) & 7)) * 16;
        PIN(ip0); PIN(iq0); PIN(iq1); PIN(xno); PIN(tw0); PIN(tw1); PIN(trd);
      }
      SB();
      ST(S0 + 4);
      vmwait<(L == 0 ? 5 : 4)>(); PIN(c0); PIN(c1);
      {
        RETRY((stale<L == 1, 1u>(red8<L == 1>(c0, c1))), {
          CNT(3);
          if (bad_) { ld4r<0>(c0, uo, ub0); ld4r<0>(c1, uo, ub1); }
          vmwait<0>(); PIN(c0); PIN(c1);
        });
      }
      ST(S0 + 5);
      {
        u8* d = (u8*)img;
        *(u32*)(d + ip0) = c0.x; *(u32*)(d + iq0) = c0.y; *(u32*)(d + 2560 + ip0) = c0.z; *(u32*)(d + 2560 + iq0) = c0.w;
        *(u32*)(d + ip0 + 16) = c1.x; *(u32*)(d + iq1) = c1.y; *(u32*)(d + 2560 + ip0 + 16) = c1.z; *(u32*)(d + 2560 + iq1) = c1.w;
      }
      bar();
      XF(43);
      const u32x4 a0 = *(const u32x4*)(img + w * 640 + m * 20 + 4 * g), a1 = *(const u32x4*)(img + w * 640 + (16 + m) * 20 + 4 * g);
      if constexpr (L == 1) ring4(5);
      if constexpr (L == 0) ring4(0);
      f32x4 dr0, di0, dr1, di1;
      row_fft<true, 7>(rc, a0, a1, dr0, di0, dr1, di1);
      if constexpr (L == 0) ring4(1);
      XF(44);
      const float ys = 1.0f / 32768.0f;
      xa[0] += lo2(dr0) * ys; xa[1] += hi2(dr0) * ys; xa[2] += lo2(dr1) * ys; xa[3] += hi2(dr1) * ys;
      xb[0] += lo2(di0) * ys; xb[1] += hi2(di0) * ys; xb[2] += lo2(di1) * ys; xb[3] += hi2(di1) * ys;
      const f32x2 sa = (xa[0] + xa[1]) + (xa[2] + xa[3]), sb = (xb[0] + xb[1]) + (xb[2] + xb[3]);
      f32x2 qa = xa[0] * xa[0], qb = xb[0] * xb[0];
#pragma unroll
      for (int i = 1; i < 4; ++i) { qa += xa[i] * xa[i]; qb += xb[i] * xb[i]; }
      if constexpr (L == 0) { SB(); ring4(2); SB(); }
      f32x2 R, O;
      XF(45);
      ln_coef(f32x2{sa.x + sa.y, sb.x + sb.y}, f32x2{qa.x + qa.y, qb.x + qb.y}, ch, R, O);
      XF(46);
      u32x4 na, nb;
#pragma unroll
      for (int i = 0; i < 4; ++i) {
        const f32x2 va = xa[i] * R.x + O.x, vb = xb[i] * R.y + O.y;
        na[i] = mark<0x10000u, 1u - L>(pkb(va.x, va.y)); nb[i] = mark<0x10000u, 1u - L>(pkb(vb.x, vb.y));
      }
      u8* tr = lds + LDS_SCR + 12288;
      *(u32x4*)(tr + tw0) = na; *(u32x4*)(tr + tw1) = nb;
      if constexpr (L == 0) vmwait<12>(); else vmwait<0>();
      bar();
      XF(47);
      st4(*(const u32x4*)(tr + trd), xno);
      st4(*(const u32x4*)(tr + 4096 + trd), xno + 8192);
      SB();
#pragma unroll
      for (int i = 0; i < 4; ++i) { xa[i] += ca; xb[i] += cb; }
      const u32x4 rl = {pkb(xa[0].x, xa[0].y), pkb(xa[1].x, xa[1].y), pkb(xa[2].x, xa[2].y), pkb(xa[3].x, xa[3].y)};
      const u32x4 rh = {pkb(xb[0].x, xb[0].y), pkb(xb[1].x, xb[1].y), pkb(xb[2].x, xb[2].y), pkb(xb[3].x, xb[3].y)};
      if constexpr (L == 0) {
        *(u32x4*)(lds + LDS_STASH + t16) = rl; *(u32x4*)(lds + LDS_STASH + 4096 + t16) = rh;
        XS(44);
        ring4(3); ring4(4); ring4(5); ring4(6); ring4(7);
      } else {
        xl[0] = u32x2{rl.x, rl.y}; xl[1] = u32x2{rl.z, rl.w}; xh[0] = u32x2{rh.x, rh.y}; xh[1] = u32x2{rh.z, rh.w};
        PIN(xl[0]); PIN(xl[1]); PIN(xh[0]); PIN(xh[1]);
        __builtin_amdgcn_s_sleep(XSLP);
      }
    }
    ST(S0 + 6);
    {
      FRESH();
      u32x4 tk[32], he[32], hl[16], w2t[4], w2v[3][4];
      u32x4 hh[8], hf[8];
      f32x4 ac1[6][2], ac2[4][2], ac3[4][2];
      const u8* hs0 = ws + OFF_H + (4 * G + 2 * half2) * 114688 + (4 * w) * 7168;
      const u8* hs[4];
      const u8* hp[4];
#pragma unroll
      for (int j = 0; j < 4; ++j) {
        hs[j] = hs0 + opq((j + ct) & 3) * 7168; hp[j] = hs0 + opq(j ^ (2 * (ct & 1))) * 7168 + 6144;
        PINS(hs[j]); PINS(hp[j]);
      }
      const u8* w2src = ws + OFF_W2 + (ct * 4 + w) * 57344;
      const u8* w2x = w2src + 40960 + 4096;
      u32 lx8 = l16 + 8192;
      PINS(w2x); PIN(lx8);
      u32 hv[4], hw[2];
#pragma unroll
      for (int u = 0; u < 4; ++u) { hv[u] = l16 + ((u ^ w) >> 1) * 114688 + ((u ^ w) & 1) * 3072; PIN(hv[u]); }
#pragma unroll
      for (int q = 0; q < 2; ++q) { hw[q] = l16 + (q ^ (w >> 1)) * 114688; PIN(hw[q]); }
      auto hload = [&](auto RE, auto KC, auto UC) {
        constexpr int k = decltype(KC)::value, u = decltype(UC)::value;
        constexpr bool re = decltype(RE)::value;
        if constexpr (k == 4 || k == 5) {
          if constexpr (re) ld4r<0>(hh[4 * (k - 4) + u], hw[u >> 1], hp[2 * (k - 4) + (u & 1)]); else ld4<0>(hh[4 * (k - 4) + u], hw[u >> 1], hp[2 * (k - 4) + (u & 1)]);
        } else {
          constexpr int j = k < 4 ? k : k < 10 ? k - 6 : k - 10, o = k < 4 ? 0 : k < 10 ? 1024 : 2048;
          u32x4& d = k < 10 ? he[(k < 4 ? 0 : 16) + 4 * j + u] : hl[4 * j + u];
          if constexpr (re) ld4r<o>(d, hv[u], hs[j]); else ld4<o>(d, hv[u], hs[j]);
        }
      };
      auto hseq = [&](auto IC) {
        constexpr int i = decltype(IC)::value;
        hload(std::false_type{}, std::integral_constant<int, (i >> 2)>{}, std::integral_constant<int, i & 3>{});
      };
      {
        const u8* w1b = ws + OFF_W1 + member * 229376 + 131072 + w * 8192;
        const u8* tk0 = ws + OFF_A + (8 * G + 2 * w) * 16384 + 4096;
        const u8* tk1 = tk0 + 8192;
        u32 tv1 = l16 + 16384, lhi = l16 + 65536;
        u32 hd0 = OFF_H + (4 * G + w) * 114688 + member * 7168 + l16, hd1 = hd0 + 3072;
        PIN(tv1); PIN(lhi); PIN(hd0); PIN(hd1); PINS(tk0); PINS(tk1);
        u32 x3o = OFF_X3 + (8 * pos + 2 * w) * 1024 + xo;
        if constexpr (L == 1) PIN(x3o);
        auto tok = [&](auto RE, auto QC) {
          constexpr int q = decltype(QC)::value, ks = q >> 1;
          if constexpr (decltype(RE)::value) ld4r<(ks & 7) * 1024 - 4096>(tk[q], q & 1 ? tv1 : l16, ks >> 3 ? tk1 : tk0);
          else ld4<(ks & 7) * 1024 - 4096>(tk[q], q & 1 ? tv1 : l16, ks >> 3 ? tk1 : tk0);
        };
        constexpr bool TONE = L == 0;
        constexpr u32 TBIT = L == 0 ? 1u : 0x10000u;
        u32 mx0 = 0, mx1 = 0;
        auto mx0_fin = [&]() { return red2<TONE>(mx0, mx1) & TBIT; };
        auto fix = [&](auto QC, u64 bad) {
          constexpr int q0 = decltype(QC)::value;
          if (__builtin_expect(bad != 0, 0)) {
            _Pragma("nounroll") for (int sp = 0; sp < SPIN_CAP; ++sp) {
              CNT(4);
              static_for<4>([&](auto I) { tok(std::true_type{}, std::integral_constant<int, q0 + decltype(I)::value>{}); });
              vmwait<0>();
#pragma unroll
              for (int q = q0; q < q0 + 4; ++q) PIN(tk[q]);
              if (!__any(stale<TONE, TBIT>(red2<TONE>(red8<TONE>(tk[q0], tk[q0 + 1]), red8<TONE>(tk[q0 + 2], tk[q0 + 3]))))) break;
            }
          }
        };
        constexpr int PD = 8;
        u32x4 af[PD];
        auto frag = [&](int x) {
          const int o = (x & 127) * 1024;
          return o < 65536 ? *(const u32x4*)(lds + l16 + o) : *(const u32x4*)(lds + lhi + (o - 65536));
        };
        if constexpr (L == 1) {
#pragma unroll
          for (int x = 0; x < PD - 1; ++x) af[x] = frag(x);
          SB();
        }
        XF(48);
        static_for<NTOK0>([&](auto Q) {
          tok(std::false_type{}, Q);
          if constexpr (decltype(Q)::value == 3) bar_nw();
        });
        XF(49);
        if constexpr (L == 0) {
          vmwait<NTOK0>();
          bar_nw();
#pragma unroll
          for (int x = 0; x < PD - 1; ++x) af[x] = frag(x);
        }
        vmwait<NTOK0 - 4>();
        ST(S0 + 7);
#pragma unroll
        for (int q = 0; q < 4; ++q) PIN(tk[q]);
        mx0 = red8<TONE>(tk[0], tk[1]); mx1 = red8<TONE>(tk[2], tk[3]);
        fix(std::integral_constant<int, 0>{}, __ballot(stale<TONE, TBIT>(red2<TONE>(mx0, mx1))));
        SB();
        ST(S0 + 8);
        float eg[2][2], eu[2][2];
        u32 ev[2];
        u64 bm = 0;
        u32x4 eo[1];
        auto epi = [&](auto NC) {
          constexpr int n = decltype(NC)::value;
          if constexpr (n < NEPI1 + NEPI2) {
            constexpr bool p1 = n < NEPI1;
            constexpr int i = p1 ? n - (n > 40) - (n > 81) : (n - NEPI1) - (n - NEPI1) / 41;
            if constexpr (p1 && (n == 40 || n == 81)) st4(eo[0], n == 40 ? hd0 : hd1);
            else if constexpr (p1 && n == 122) st4(eo[0], hd1 + 3072);
            else if constexpr (!p1 && (n - NEPI1) % 41 == 40) st4(eo[0], ((n - NEPI1) / 41 ? hd1 : hd0) + 1024);
            else {
              constexpr int s = i & 1, st = (i % 20) >> 1, blk = i / 20;
              constexpr int u = p1 ? (blk < 4 ? blk >> 1 : blk - 4) : blk >> 1, pr = p1 ? (blk < 4 ? blk & 1 : 2) : blk & 1;
              constexpr int ei = 0, d = 2 * (pr == 2 ? u : pr) + s;
              const f32x4& ga = p1 ? ac1[2 * pr][u] : ac2[2 * (pr & 1)][u];
              const f32x4& up = p1 ? ac1[2 * pr + 1][u] : ac2[2 * (pr & 1) + 1][u];
              if constexpr (st < 2) { eg[s][st] = ga[2 * s + st]; PIN(eg[s][st]); }
              else if constexpr (st < 4) { eu[s][st - 2] = up[2 * s + st - 2]; PIN(eu[s][st - 2]); }
              else if constexpr (st < 6) { eg[s][st - 4] = relu(eg[s][st - 4]); PIN(eg[s][st - 4]); }
              else if constexpr (st < 8) { eg[s][st - 6] *= eu[s][st - 6]; PIN(eg[s][st - 6]); }
              else if constexpr (st == 8) { ev[s] = pkb(eg[s][0], eg[s][1]); PIN(ev[s]); }
              else { u32 v = mark<1u - L, L>(ev[s]); PIN(v); eo[ei][d] = v; }
            }
            SB();
          }
        };
        static_for<224>([&](auto XC) {
          constexpr int x = decltype(XC)::value, job = G1S[L].job[x], ji = G1S[L].idx[x], e0 = G1S[L].epi[x], e1 = G1S[L].epi[x + 1];
          if constexpr (x == 32 || x == 64) bar_nw();
          if constexpr (x == 97) { vmwait<g1_wait(L, x, 2, 7)>(); bar_nw(); }
          if constexpr (x == 150) { vmwait<g1_wait(L, x, 2, 23)>(); bar_nw(); }
          constexpr int cj = (x + 12) / 12, co = x + 12 - 12 * cj;
          constexpr bool chk = x < 84 && co >= 5;
          constexpr int ks = x < 96 ? x / 6 : x < 160 ? (x - 96) >> 2 : (x - 160) >> 2, t = x < 96 ? x % 6 : x & 3;
          auto mm = [&](int u) {
            f32x4& a = x < 96 ? ac1[x < 96 ? t : 0][u] : x < 160 ? ac2[x < 96 ? 0 : t][u] : ac3[x < 96 ? 0 : t][u];
            if constexpr (ks == 0) a = mfma_b(af[x % PD], tk[u], f32x4{0.f, 0.f, 0.f, 0.f}); else a = mfma_b(af[x % PD], tk[2 * ks + u], a);
          };
          SB();
          mm(0);
          SB();
          if constexpr (x < 223) __builtin_amdgcn_s_waitcnt(0xC07F | ((x + PD - 2 < 224 ? PD - 3 : 222 - x) << 8));
          if constexpr (chk && co == 5) {
            vmwait<g1_wait(L, x, 1, 4 * cj + 3)>();
            PIN(tk[4 * cj]); PIN(tk[4 * cj + 1]); PIN(tk[4 * cj + 2]); PIN(tk[4 * cj + 3]);
          }
          SB();
          if constexpr (chk && co >= 6 && co < 10) {
            const u32x4 v = tk[4 * cj + co - 6];
            mx0 = co == 6 ? red2<TONE>(v.x, v.y) : red3<TONE>(mx0, v.x, v.y); PIN(mx0); SB();
            mx1 = co == 6 ? red2<TONE>(v.z, v.w) : red3<TONE>(mx1, v.z, v.w); PIN(mx1);
          }
          if constexpr (chk && co == 10) { mx0 = mx0_fin(); PIN(mx0); SB(); bm = __ballot(TONE ? mx0 == 0 : mx0 != 0); PINS(bm); }
          if constexpr (e0 < e1) epi(std::integral_constant<int, e0>{});
          if constexpr (e0 + 1 < e1) epi(std::integral_constant<int, e0 + 1>{});
          SB();
          mm(1);
          SB();
          if constexpr (chk && co == 11) fix(std::integral_constant<int, 4 * cj>{}, bm);
          if constexpr (job == 2 && (ji & 3) == 0) set_m0(ldsbase + (ji >> 3) * 32768 + w * 8192 + (ji & 4) * 1024);
          SB();
          if constexpr (x + PD - 1 < 224) af[(x + PD - 1) % PD] = frag(x + PD - 1);
          SB();
          if constexpr (job == 1) tok(std::false_type{}, std::integral_constant<int, ji>{});
          if constexpr (job == 2) dma1<(ji & 3) * 1024>(l16, w1b + (ji >> 3) * 32768 + (ji & 4) * 1024);
          if constexpr (job == 3) ld4a<(ji & 7) * 1024 - 4096>(w2a[ji >> 2][ji & 3], l16, w2src + (ji >> 3) * 8192 + 4096);
          if constexpr (job == 5) {
            if constexpr (ji >= 12) ld4a<(ji - 12) * 1024 - 4096>(w2t[ji - 12], l16, w2x);
            else ld4<((ji + 4) & 7) * 1024 - 4096>(w2v[ji >> 2][ji & 3], ji < 4 ? l16 : lx8, w2x);
          }
          if constexpr (job == 6) st2(ji & 2 ? xh[ji & 1] : xl[ji & 1], x3o + ji * 512);
          if constexpr (job == 4) hseq(std::integral_constant<int, ji>{});
          if constexpr (e0 + 2 < e1) epi(std::integral_constant<int, e0 + 2>{});
          SB();
        });
        ST(S0 + 9);
#pragma unroll
        for (int u = 0; u < 2; ++u) {
          u32x4 o;
#pragma unroll
          for (int p = 0; p < 2; ++p) {
            const f32x4 ga = ac3[2 * p][u], up = ac3[2 * p + 1][u];
            const f32x2 a = f32x2{relu(ga[0]), relu(ga[1])} * lo2(up), c = f32x2{relu(ga[2]), relu(ga[3])} * hi2(up);
            o[2 * p] = mark<1u - L, L>(pkb(a.x, a.y)); o[2 * p + 1] = mark<1u - L, L>(pkb(c.x, c.y));
          }
          st4(o, (u ? hd1 : hd0) + 2048);
        }
        SB();
      }
      ST(S0 + 10);
      {
        f32x4 acc[4][4];
        u32x4 x3a, x3b;
        u32 eo = L == 1 ? (128 * G + 64 * half2 + 16 * w + m) * 1024 + ct * 128 + g * 16
                        : (128 * G + 64 * half2 + 16 * w + (m & 14)) * 1024 + ct * 128 + (m & 1) * 64 + g * 16;
        PIN(eo);
        const u8* x3s = ws + OFF_X3;
        u64 bm = 0;
        PINS(x3s);
        constexpr int LB = 40 - NEARLY;
        auto cum = [](int n) constexpr { const int c = n - n / 8; return c < LB + 16 ? c : LB + 16; };
        auto young = [cum](int k, int n) constexpr {
          const int i = 4 * k + 3;
          if (k < 10 && i < NEARLY) return cum(n) + 2 + G1S[L].cum[224] - G1S[L].pos[4][i] - 1;
          if (k < 10) return cum(n) - (i - NEARLY + 1);
          return cum(n) - (LB + 4 * (k - 10) + 4);
        };
        constexpr bool HONE = L == 1;
        u32 mx0 = 0, mx1 = 0;
        auto mx0_fin = [&]() { return red2<HONE>(mx0, mx1) & 1u; };
        auto pin_k = [&](auto KC) {
          constexpr int k = decltype(KC)::value;
#pragma unroll
          for (int u = 0; u < 4; ++u) {
            if constexpr (k < 4) PIN(he[4 * k + u]);
            else if constexpr (k < 6) PIN(hh[4 * (k - 4) + u]);
            else if constexpr (k < 10) PIN(he[16 + 4 * (k - 6) + u]);
            else PIN(hl[4 * (k - 10) + u]);
          }
        };
        auto opnd = [&](auto KC, int u) {
          constexpr int k = decltype(KC)::value;
          if constexpr (k < 4) return he[4 * k + u];
          else if constexpr (k < 6) return hh[4 * (k - 4) + u];
          else if constexpr (k < 10) return he[16 + 4 * (k - 6) + u];
          else return hl[4 * (k - 10) + u];
        };
        u32 wodd = (w & 1) ? ~0u : 0u;
        PIN(wodd);
        auto mkhalf = [&](auto KC, auto IC) {
          constexpr int k = decltype(KC)::value, i = decltype(IC)::value, u = i >> 2, d = i & 3;
          const u32x4& src = hh[4 * (k - 4) + (u & 2) + (d >> 1)];
          u32 v = (src[2 * ((u & 1) ^ 1) + (d & 1)] & wodd) | (src[2 * (u & 1) + (d & 1)] & ~wodd);
          PIN(v);
          hf[4 * (k - 4) + u][d] = v;
        };
        auto fixk = [&](auto KC, u64 bad) {
          constexpr int k = decltype(KC)::value;
          if (__builtin_expect(bad != 0, 0)) {
            _Pragma("nounroll") for (int sp = 0; sp < SPIN_CAP; ++sp) {
              CNT(k < 10 ? 5 : 6);
              static_for<4>([&](auto U) { hload(std::true_type{}, KC, U); });
              vmwait<0>();
              pin_k(KC);
              if (!__any(stale<HONE, 1u>(red2<HONE>(red8<HONE>(opnd(KC, 0), opnd(KC, 1)), red8<HONE>(opnd(KC, 2), opnd(KC, 3)))))) break;
            }
            if constexpr (k == 4 || k == 5) static_for<16>([&](auto I) { mkhalf(KC, I); });
          }
        };
        u32 rw[3];
#pragma unroll
        for (int u = 1; u < 4; ++u) { rw[u - 1] = ((u ^ w) * 3 + u - 1) * 4096 + l16; PIN(rw[u - 1]); }
        u32 oo = 0;
        if constexpr (L == 1) {
          const int tok = 128 * G + 64 * half2 + 16 * w + m;
          oo = row_of_pos(tok >> 3, (tok >> 1) & 3) * 1024 + ct * 128 + (m & 1) * 64 + g * 16;
          PIN(oo);
        }
        vmwait<clamp63(young(0, 0))>();
        if constexpr (L == 0) {
#pragma unroll
          for (int k = 0; k < 10; ++k) { PINA(w2a[k][0]); PINA(w2a[k][1]); PINA(w2a[k][2]); PINA(w2a[k][3]); }
        }
#pragma unroll
        for (int c = 0; c < 4; ++c) { PINA(w2t[c]); PIN(w2v[0][c]); PIN(w2v[1][c]); PIN(w2v[2][c]); }
        pin_k(std::integral_constant<int, 0>{});
        mx0 = red8<HONE>(he[0], he[1]); mx1 = red8<HONE>(he[2], he[3]);
        fixk(std::integral_constant<int, 0>{}, __ballot(stale<HONE, 1u>(red2<HONE>(mx0, mx1))));
        ST(S0 + 11);
        auto wfr = [&](auto KC, int c) {
          constexpr int k = decltype(KC)::value;
          if constexpr (k < 10) return w2a[k][c]; else if constexpr (k == 10) return w2t[c]; else return w2v[k - 11][c];
        };
        static_for<44>([&](auto NC) {
          constexpr int n = decltype(NC)::value, k = n >> 2, c = n & 3;
          using KC = std::integral_constant<int, k>;
          using KN = std::integral_constant<int, k + 1>;
          static_for<4>([&](auto UC) {
            constexpr int u = decltype(UC)::value;
            SB();
            if constexpr (k == 0) acc[c][u] = mfma_b(wfr(KC{}, c), opnd(KC{}, u), f32x4{0.f, 0.f, 0.f, 0.f});
            else if constexpr (k == 4 || k == 5) acc[c][u] = mfma_b(wfr(KC{}, c), hf[4 * (k - 4) + u], acc[c][u]);
            else acc[c][u] = mfma_b(wfr(KC{}, c), opnd(KC{}, u), acc[c][u]);
            SB();
            constexpr bool hk = k == 3 || k == 4;
            constexpr int j = k == 10 ? 4 * c + u - 2 : u == 3 ? -9 : 3 * c + u - (hk ? 1 : 5);
            if constexpr (j == -1) {
              vmwait<clamp63(young(k == 10 ? 13 : k + 1, n))>(); pin_k(KN{});
              if constexpr (k == 10) { pin_k(std::integral_constant<int, 12>{}); pin_k(std::integral_constant<int, 13>{}); }
            }
            if constexpr (j >= 0 && j < (k == 10 ? 12 : 4)) {
              const u32x4 v = opnd(std::integral_constant<int, k + 1 + (j >> 2)>{}, j & 3);
              mx0 = j == 0 ? red2<HONE>(v.x, v.y) : red3<HONE>(mx0, v.x, v.y); PIN(mx0); SB();
              mx1 = j == 0 ? red2<HONE>(v.z, v.w) : red3<HONE>(mx1, v.z, v.w); PIN(mx1);
            }
            if constexpr (hk && j >= 4 && j != 5) {
              constexpr int i0 = j == 4 ? 0 : 2 + 3 * (j - 6);
              mkhalf(KN{}, std::integral_constant<int, i0>{}); SB(); mkhalf(KN{}, std::integral_constant<int, i0 + 1>{});
              if constexpr (j >= 6 && i0 + 2 < 16) { SB(); mkhalf(KN{}, std::integral_constant<int, i0 + 2>{}); }
            }
            if constexpr (j == (k == 10 ? 12 : 5)) { mx0 = mx0_fin(); PIN(mx0); SB(); bm = __ballot(HONE ? mx0 == 0 : mx0 != 0); PINS(bm); }
            if constexpr (u == 3) {
              constexpr int q = cum(n);
              if constexpr (cum(n + 1) == q) {}
              else if constexpr (q < LB) hseq(std::integral_constant<int, NEARLY + q>{});
              else hload(std::false_type{}, std::integral_constant<int, 10 + ((q - LB) >> 2)>{}, std::integral_constant<int, (q - LB) & 3>{});
              if constexpr (c == 3) {
                fixk(KN{}, bm);
                if constexpr (k == 10) { fixk(std::integral_constant<int, 12>{}, bm); fixk(std::integral_constant<int, 13>{}, bm); }
              }
            }
          });
        });
        bar_nw();
        if constexpr (L == 1) { ld4<0>(x3a, eo, x3s); ld4<64>(x3b, eo, x3s); }
        static_for<16>([&](auto TC) {
          constexpr int t = decltype(TC)::value, u = ((t >> 2) + 1) & 3, c = t & 3;
          SB();
          acc[c][u] = mfma_b(w2v[0][c], hl[4 + u], acc[c][u]);
          acc[c][u] = mfma_b(w2v[1][c], hl[8 + u], acc[c][u]);
          SB();
          if constexpr (t >= 3 && t < 15) *(f32x4*)(lds + rw[(t - 3) >> 2] + ((t - 3) & 3) * 1024) = acc[(t - 3) & 3][((t - 3) >> 2) + 1];
          SB();
          acc[c][u] = mfma_b(w2v[2][c], hl[12 + u], acc[c][u]);
        });
        SB();
        ST(S0 + 12);
        bar();
        f32x4 s[3][4];
#pragma unroll
        for (int c = 0; c < 4; ++c)
#pragma unroll
          for (int p = 0; p < 3; ++p) s[p][c] = *(const f32x4*)(lds + w * 12288 + p * 4096 + c * 1024 + l16);
        SB();
        if constexpr (L == 1) { vmwait<0>(); PIN(x3a); PIN(x3b); }
        u32x4 y[2];
#pragma unroll
        for (int a = 0; a < 2; ++a) {
          const f32x4 s0 = (acc[2 * a][0] + s[0][2 * a]) + (s[1][2 * a] + s[2][2 * a]);
          const f32x4 s1 = (acc[2 * a + 1][0] + s[0][2 * a + 1]) + (s[1][2 * a + 1] + s[2][2 * a + 1]);
          y[a] = u32x4{pkb(s0[0], s0[1]), pkb(s0[2], s0[3]), pkb(s1[0], s1[1]), pkb(s1[2], s1[3])};
          if constexpr (L == 1) {
            const u32x4 xr = a ? x3b : x3a;
#pragma unroll
            for (int p = 0; p < 4; ++p) y[a][p] = pkb(blo(xr[p]) + blo(y[a][p]), bhi(xr[p]) + bhi(y[a][p]));
          }
        }
        {
          const bool odd = m & 1;
          u32x4 z0, z1;
#pragma unroll
          for (int p = 0; p < 4; ++p) {
            const u32 t = (u32)__builtin_amdgcn_mov_dpp((int)(odd ? y[0][p] : y[1][p]), 0xB1, 0xf, 0xf, true);
            z0[p] = odd ? t : y[0][p]; z1[p] = odd ? y[1][p] : t;
          }
          if constexpr (L == 0) { st4(mark4<0x10001u, 0u>(z0), OFF_A + eo); st4(mark4<0x10001u, 0u>(z1), OFF_A + eo + 1024); }
          else {
            const __amdgpu_buffer_rsrc_t ro = __builtin_amdgcn_make_buffer_rsrc(P.out, 0, 2048 * 1024, 0x00020000);
            __builtin_amdgcn_raw_buffer_store_b128(z0, ro, oo, 0, 16); __builtin_amdgcn_raw_buffer_store_b128(z1, ro, oo + 1024 * 1024, 0, 16);
          }
        }
      }
    }
    ST(S0 + 13);
    if constexpr (L == 0) {
      FRESH();
      SB();
      row_consts<false, 0>(rc, m, g);
      SB();
      u32x2 d0, d1, d2, d3;
      const u8* db = ws + OFF_A + (8 * pos + 2 * w) * 1024;
      const u32x4 sl = *(const u32x4*)(lds + LDS_STASH + t16), sh = *(const u32x4*)(lds + LDS_STASH + 4096 + t16);
#pragma unroll
      for (int i = 0; i < 4; ++i) {
        xv[2 * i] = f32x2{blo(sl[i]), blo(sh[i])}; xv[2 * i + 1] = f32x2{bhi(sl[i]), bhi(sh[i])};
      }
      xc = f32x2{first_lane(xv[0].x), first_lane(xv[0].y)};
      xch = lane < 32 ? xc.x : xc.y;
      PIN(xch);
#pragma unroll
      for (int i = 0; i < 8; ++i) { xv[i] -= xc; PIN(xv[i]); }
      SB();
      bar_nw();
      ld2<0>(d0, xo, db); ld2<512>(d1, xo, db); ld2<1024>(d2, xo, db); ld2<1536>(d3, xo, db);
      ring4(0);
      SB();
      row_consts<false, 1>(rc, m, g);
      SB();
      fwd_pre(fp, tid, w, pos);
      SB();
      ST(S0 + 14);
      vmwait<4>(); PIN(d0); PIN(d1); PIN(d2); PIN(d3);
      RETRY((stale<false, 1u>(red8<false>(u32x4{d0.x, d0.y, d1.x, d1.y}, u32x4{d2.x, d2.y, d3.x, d3.y}))), {
        CNT(7);
        if (bad_) { ld2r<0>(d0, xo, db); ld2r<512>(d1, xo, db); ld2r<1024>(d2, xo, db); ld2r<1536>(d3, xo, db); }
        vmwait<0>(); PIN(d0); PIN(d1); PIN(d2); PIN(d3);
      });
      xv[0] += f32x2{blo(d0.x), blo(d2.x)}; xv[1] += f32x2{bhi(d0.x), bhi(d2.x)}; xv[2] += f32x2{blo(d0.y), blo(d2.y)}; xv[3] += f32x2{bhi(d0.y), bhi(d2.y)};
      xv[4] += f32x2{blo(d1.x), blo(d3.x)}; xv[5] += f32x2{bhi(d1.x), bhi(d3.x)}; xv[6] += f32x2{blo(d1.y), blo(d3.y)}; xv[7] += f32x2{bhi(d1.y), bhi(d3.y)};
      ST(S0 + 15);
    }
  };
  layer(std::integral_constant<int, 0>{});
  layer(std::integral_constant<int, 1>{});
  if (blockIdx.x == 0 && threadIdx.x == 0) st2(ep, OFF_E);
  ST_OUT();
}

extern "C" void kernel_launch(void* const* d_in, const int* in_sizes, int n_in, void* d_out, int out_size, void* d_ws, size_t ws_size,
                              hipStream_t stream) {
  Params p;
  p.x = (const u16*)d_in[0]; p.wr = (const u16*)d_in[1]; p.wi = (const u16*)d_in[2];
  p.wg = (const u16*)d_in[4]; p.wu = (const u16*)d_in[5]; p.wd = (const u16*)d_in[6];
  p.out = (u16*)d_out; p.ws = (u8*)d_ws;
  (void)hipFuncSetAttribute((const void*)k_all, hipFuncAttributeMaxDynamicSharedMemorySize, LDS_TOTAL);
  k_all<<<256, 256, LDS_TOTAL, stream>>>(p);
}
```

```cpp
#include <hip/hip_runtime.h>
#include <hip/hip_bf16.h>
#include <stdint.h>
#include <type_traits>
#include <utility>

typedef unsigned char u8;
typedef unsigned short u16;
typedef unsigned int u32;
typedef unsigned long long u64;
typedef u32 u32x2 __attribute__((ext_vector_type(2)));
typedef u32 u32x4 __attribute__((ext_vector_type(4)));
typedef float f32x2 __attribute__((ext_vector_type(2)));
typedef float f32x4 __attribute__((ext_vector_type(4)));
typedef _Float16 f16x2 __attribute__((ext_vector_type(2)));
typedef __bf16 bf16x2 __attribute__((ext_vector_type(2)));
typedef __bf16 bf16x8 __attribute__((ext_vector_type(8)));
#define DI __device__ __forceinline__
#define SB() __builtin_amdgcn_sched_barrier(0)
#define MEMFENCE() asm volatile("" ::: "memory")

#define XSLP 4
#define BMAP(x) (x)
constexpr u32 MB2 = 2u << 20;
constexpr u32 OFF_E = 0, OFF_FLAG = 8192, OFF_WNY = 65536;
constexpr u32 OFF_A = MB2;
constexpr u32 OFF_U = OFF_A + MB2;
constexpr u32 OFF_WX = OFF_U + MB2;
constexpr u32 OFF_X3 = OFF_WX + MB2;
constexpr u32 OFF_H = OFF_X3 + MB2;
constexpr u32 OFF_W1 = OFF_H + 7340032;
constexpr u32 OFF_W2 = OFF_W1 + 3670016;
constexpr u32 WS_USED = OFF_W2 + 1835008;
constexpr u32 SENT = 0xFFFFFFFFu;
constexpr int SPIN_CAP = 1 << 15;
constexpr u32 CODE_BYTES = 66 * 1024;

constexpr int LDS_SCR = 131072, LDS_STASH = LDS_SCR + 24576, LDS_TOTAL = 163840;

struct Params {
  const u16* x; const u16* wr; const u16* wi; const u16* wg; const u16* wu; const u16* wd; u16* out; u8* ws;
};

#define ST(i)
#define ST_DECL
#define CNT(i)
#define ST_OUT()

#define XS(i)
#define XF(i)
DI u32 pkh(float a, float b) { f32x2 v = {a, b}; return __builtin_bit_cast(u32, __builtin_convertvector(v, f16x2)); }
DI u32 pkb(float a, float b) { f32x2 v = {a, b}; return __builtin_bit_cast(u32, __builtin_convertvector(v, bf16x2)); }
template <bool BF> DI u32 pkc(float a, float b) { return BF ? pkb(a, b) : pkh(a, b); }
DI u32 pkh_n0(float a, float b, u32 after) { u32 r; asm("v_cvt_pk_f16_f32 %0, -%1, %2" : "=v"(r) : "v"(a), "v"(b), "v"(after)); return r; }
DI u32 pkb_n1(float a, float b, u32 after) { u32 r; asm("v_cvt_pk_bf16_f32 %0, %1, -%2" : "=v"(r) : "v"(a), "v"(b), "v"(after)); return r; }
DI float blo(u32 u) { return __uint_as_float(u << 16); }
DI float bhi(u32 u) { return __uint_as_float(u & 0xffff0000u); }
DI float bfr(float a) { return blo(pkb(a, a)); }
DI float relu(float a) { const int i = __float_as_int(a); return __int_as_float(i > 0 ? i : 0); }
DI u32 umax(u32 a, u32 b) { return a > b ? a : b; }
DI u32 max3(u32 a, u32 b, u32 c) { return umax(umax(a, b), c); }
DI u32 max4(u32 acc, u32x4 v) { return max3(max3(acc, v.x, v.y), v.z, v.w); }
template <bool ONE> DI u32 red3(u32 a, u32 b, u32 c) { return ONE ? (a & b & c) : (a | b | c); }
template <bool ONE> DI u32 red2(u32 a, u32 b) { return ONE ? (a & b) : (a | b); }
template <bool ONE> DI u32 red4(u32 acc, u32x4 v) { return red3<ONE>(red3<ONE>(acc, v.x, v.y), v.z, v.w); }
template <bool ONE> DI u32 red8(u32x4 a, u32x4 b) { return red4<ONE>(red3<ONE>(red2<ONE>(a.x, a.y), a.z, a.w), b); }
template <bool ONE, u32 BIT> DI bool stale(u32 r) { return ONE ? (r & BIT) == 0 : (r & BIT) != 0; }
template <u32 CLR, u32 SET> DI u32 mark(u32 v) { return (v & ~CLR) | SET; }
template <u32 CLR, u32 SET> DI u32x4 mark4(u32x4 v) { return u32x4{mark<CLR, SET>(v.x), mark<CLR, SET>(v.y), mark<CLR, SET>(v.z), mark<CLR, SET>(v.w)}; }
DI f32x2 lo2(f32x4 v) { return __builtin_shufflevector(v, v, 0, 1); }
DI f32x2 hi2(f32x4 v) { return __builtin_shufflevector(v, v, 2, 3); }
DI f32x2 swp(f32x2 v) { return __builtin_shufflevector(v, v, 1, 0); }
DI f32x4 mfma_b(u32x4 a, u32x4 b, f32x4 c) {
  return __builtin_amdgcn_mfma_f32_16x16x32_bf16(__builtin_bit_cast(bf16x8, a), __builtin_bit_cast(bf16x8, b), c, 0, 0, 0);
}
template <class T> DI void PIN(T& x) { asm volatile("" : "+v"(x)); }
template <class T> DI void PINS(T& x) { asm volatile("" : "+s"(x)); }
DI int opq(int x) { asm("" : "+s"(x)); return x; }
template <class T> DI void PINA(T& x) { asm volatile("" : "+a"(x)); }
template <class F, int... I> DI void static_for_(F&& f, std::integer_sequence<int, I...>) { (f(std::integral_constant<int, I>{}), ...); }
template <int N, class F> DI void static_for(F&& f) { static_for_(f, std::make_integer_sequence<int, N>{}); }

#define MM_(T) "v_mfma_f32_16x16x32_" T
template <bool BF> DI void mm4(f32x4& d0, f32x4& d1, f32x4& d2, f32x4& d3, u32x4 a0, u32x4 a1, u32x4 b0, u32x4 b1) {
#define MM4_(T) asm volatile("s_nop 1\n\t" MM_(T) " %0, %4, %6, 0\n\t" MM_(T) " %1, %4, %7, 0\n\t" MM_(T) " %2, %5, %6, 0\n\t" MM_(T) " %3, %5, %7, 0\n\ts_nop 5" \
               : "=&v"(d0), "=&v"(d1), "=&v"(d2), "=&v"(d3) : "v"(a0), "v"(a1), "v"(b0), "v"(b1))
  if constexpr (BF) MM4_("bf16"); else MM4_("f16");
}
template <bool BF, int TAIL, int AG = 0> DI void mm8(f32x4& d0, f32x4& d1, f32x4& d2, f32x4& d3, u32x4 a00, u32x4 b00, u32x4 a01, u32x4 b01, u32x4 a10,
                               u32x4 b10, u32x4 a11, u32x4 b11, u32x4 a20, u32x4 b20, u32x4 a21, u32x4 b21, u32x4 a30, u32x4 b30, u32x4 a31, u32x4 b31) {
#define MM8_(T, A, B) asm volatile("s_nop 1\n\t" MM_(T) " %0, %4, %5, 0\n\t" MM_(T) " %1, %8, %9, 0\n\t" MM_(T) " %0, %6, %7, %0\n\t" MM_(T) " %1, %10, %11, %1\n\t" \
               MM_(T) " %2, %12, %13, 0\n\t" MM_(T) " %3, %16, %17, 0\n\t" MM_(T) " %2, %14, %15, %2\n\t" MM_(T) " %3, %18, %19, %3\n\ts_nop %20"         \
               : "=&v"(d0), "=&v"(d1), "=&v"(d2), "=&v"(d3)                                                                                              \
               : A(a00), B(b00), A(a01), B(b01), A(a10), B(b10), A(a11), B(b11), A(a20), B(b20), A(a21), B(b21), A(a30), B(b30), A(a31), B(b31), "i"(TAIL))
  if constexpr (!BF) MM8_("f16", "v", "v");
  else if constexpr (AG == 0) MM8_("bf16", "v", "v");
  else if constexpr (AG == 1) MM8_("bf16", "a", "v");
  else MM8_("bf16", "v", "a");
}
template <bool BF, int AG> DI void mf0(f32x4& d, u32x4 a, u32x4 b) {
  if constexpr (!BF) asm volatile(MM_("f16") " %0, %1, %2, 0" : "=&v"(d) : "v"(a), "v"(b));
  else if constexpr (AG == 0) asm volatile(MM_("bf16") " %0, %1, %2, 0" : "=&v"(d) : "v"(a), "v"(b));
  else asm volatile(MM_("bf16") " %0, %1, %2, 0" : "=&v"(d) : "v"(a), "a"(b));
}
template <bool BF, int AG, int TAIL> DI void mf4(f32x4& d0, f32x4& d1, f32x4& d2, f32x4& d3, u32x4 a0, u32x4 b0, u32x4 a1, u32x4 b1, u32x4 a2,
                                               u32x4 b2, u32x4 a3, u32x4 b3) {
#define MF4_(T, B) asm volatile("s_nop 1\n\t" MM_(T) " %0, %4, %5, %0\n\t" MM_(T) " %1, %6, %7, %1\n\t" MM_(T) " %2, %8, %9, %2\n\t" MM_(T) " %3, %10, %11, %3\n\ts_nop %12" \
               : "+v"(d0), "+v"(d1), "+v"(d2), "+v"(d3) : "v"(a0), B(b0), "v"(a1), B(b1), "v"(a2), B(b2), "v"(a3), B(b3), "i"(TAIL))
  if constexpr (!BF) MF4_("f16", "v"); else if constexpr (AG == 0) MF4_("bf16", "v"); else MF4_("bf16", "a");
}
template <bool BF, bool CONJ, class MM> DI u32x4 cplx_under(f32x4 r, f32x4 i, const f32x2 (&c)[2], const f32x2 (&s)[2], MM&& mm) {
  asm volatile("s_nop 1");
  SB(); mm(std::integral_constant<int, 0>{}); SB();
  f32x2 a0 = lo2(i) * s[0], b0 = lo2(r) * s[0], a1 = hi2(i) * s[1];
  PIN(a0); PIN(b0); PIN(a1);
  SB(); mm(std::integral_constant<int, 1>{}); SB();
  f32x2 b1 = hi2(r) * s[1], re0 = CONJ ? lo2(r) * c[0] + a0 : lo2(r) * c[0] - a0, im0 = CONJ ? lo2(i) * c[0] - b0 : lo2(i) * c[0] + b0;
  PIN(b1); PIN(re0); PIN(im0);
  SB(); mm(std::integral_constant<int, 2>{}); SB();
  f32x2 re1 = CONJ ? hi2(r) * c[1] + a1 : hi2(r) * c[1] - a1, im1 = CONJ ? hi2(i) * c[1] - b1 : hi2(i) * c[1] + b1;
  u32 o0 = pkc<BF>(re0.x, im0.x);
  PIN(re1); PIN(im1); PIN(o0);
  SB(); mm(std::integral_constant<int, 3>{}); SB();
  return u32x4{o0, pkc<BF>(re0.y, im0.y), pkc<BF>(re1.x, im1.x), pkc<BF>(re1.y, im1.y)};
}
template <bool BF> DI u32x4 twiddle(f32x4 r, f32x4 i, const f32x2 (&c)[2], const f32x2 (&s)[2]) {
  const f32x2 re0 = lo2(r) * c[0] + lo2(i) * s[0], im0 = lo2(i) * c[0] - lo2(r) * s[0];
  const f32x2 re1 = hi2(r) * c[1] + hi2(i) * s[1], im1 = hi2(i) * c[1] - hi2(r) * s[1];
  return u32x4{pkc<BF>(re0.x, im0.x), pkc<BF>(re0.y, im0.y), pkc<BF>(re1.x, im1.x), pkc<BF>(re1.y, im1.y)};
}
DI u32x4 cmul_b(f32x4 r, f32x4 i, const f32x2 (&c)[2], const f32x2 (&s)[2]) {
  const f32x2 re0 = lo2(r) * c[0] - lo2(i) * s[0], im0 = lo2(i) * c[0] + lo2(r) * s[0];
  const f32x2 re1 = hi2(r) * c[1] - hi2(i) * s[1], im1 = hi2(i) * c[1] + hi2(r) * s[1];
  return u32x4{pkb(re0.x, im0.x), pkb(re0.y, im0.y), pkb(re1.x, im1.x), pkb(re1.y, im1.y)};
}
DI float cosr(float a) { return __builtin_amdgcn_cosf(a); }
DI float sinr(float a) { return __builtin_amdgcn_sinf(a); }
template <int CTRL> DI float dppf(float x) {
  return __builtin_bit_cast(float, __builtin_amdgcn_mov_dpp(__builtin_bit_cast(int, x), CTRL, 0xf, 0xf, true));
}
DI f32x2 bcast2(float x) {
  auto t = __builtin_amdgcn_permlane32_swap(__float_as_uint(x), __float_as_uint(x), false, false);
  return f32x2{__uint_as_float(t[0]), __uint_as_float(t[1])};
}
DI void ln_coef(f32x2 S, f32x2 Q, float ch, f32x2& R, f32x2& O) {
  auto t = __builtin_amdgcn_permlane32_swap(__float_as_uint(S.x), __float_as_uint(S.y), false, false);
  auto u = __builtin_amdgcn_permlane32_swap(__float_as_uint(Q.x), __float_as_uint(Q.y), false, false);
  const float s = __uint_as_float(t[0]) + __uint_as_float(t[1]), q = __uint_as_float(u[0]) + __uint_as_float(u[1]);
  auto z = __builtin_amdgcn_permlane16_swap(__float_as_uint(s), __float_as_uint(q), false, false);
  float x = __uint_as_float(z[0]) + __uint_as_float(z[1]);
  x += dppf<0xB1>(x);
  x += dppf<0x4E>(x);
  x += dppf<0x141>(x);
  x += dppf<0x140>(x);
  auto y = __builtin_amdgcn_permlane16_swap(__float_as_uint(x), __float_as_uint(x), false, false);
  const float md = __uint_as_float(y[0]) * (1.0f / 512.0f);
  const float var = relu(__uint_as_float(y[1]) * (1.0f / 512.0f) - md * md);
  const float r = __builtin_amdgcn_rcpf(bfr(__builtin_amdgcn_sqrtf(bfr(bfr(var) + 1e-5f))));
  const float o = (ch - bfr(ch + md)) * r;
  R = bcast2(r); O = bcast2(o);
}
DI float first_lane(float x) { return __uint_as_float(__builtin_amdgcn_readfirstlane(__float_as_uint(x))); }
DI int row_of_pos(int pos, int j) {
  const int c = 8 * (pos >> 7) + 2 * ((pos >> 3) & 3) + ((pos >> 5) & 1), q = 8 * ((pos >> 6) & 1) + (pos & 7);
  return 32 * (2 * c + (j & 1)) + 2 * q + (j >> 1);
}

template <int I> DI void ld4(u32x4& d, u32 v, const u8* s) { asm volatile("global_load_dwordx4 %0, %1, %2 offset:%3 sc1" : "=v"(d) : "v"(v), "s"(s), "i"(I) : "memory"); }
template <int I> DI void ld4a(u32x4& d, u32 v, const u8* s) { asm volatile("global_load_dwordx4 %0, %1, %2 offset:%3 sc1" : "=a"(d) : "v"(v), "s"(s), "i"(I) : "memory"); }
template <int I> DI void ld4r(u32x4& d, u32 v, const u8* s) { asm volatile("global_load_dwordx4 %0, %1, %2 offset:%3 sc1" : "+v"(d) : "v"(v), "s"(s), "i"(I) : "memory"); }
template <int I> DI void ld2(u32x2& d, u32 v, const u8* s) { asm volatile("global_load_dwordx2 %0, %1, %2 offset:%3 sc1" : "=v"(d) : "v"(v), "s"(s), "i"(I) : "memory"); }
template <int I> DI void ld2r(u32x2& d, u32 v, const u8* s) { asm volatile("global_load_dwordx2 %0, %1, %2 offset:%3 sc1" : "+v"(d) : "v"(v), "s"(s), "i"(I) : "memory"); }
template <int I> DI void ld2p(u32x2& d, u32 v, const u8* s) { asm volatile("global_load_dwordx2 %0, %1, %2 offset:%3" : "=v"(d) : "v"(v), "s"(s), "i"(I) : "memory"); }
DI void touch(u32& d, u32 v, const u8* s) { asm volatile("global_load_dword %0, %1, %2 sc1" : "+v"(d) : "v"(v), "s"(s) : "memory"); }
template <int I> DI void ld1p(u32& d, u32 v, const u8* s) { asm volatile("global_load_dword %0, %1, %2 offset:%3" : "=v"(d) : "v"(v), "s"(s), "i"(I) : "memory"); }
DI void ld1s(u32& d, u32 v, const u8* s) { asm volatile("global_load_ushort %0, %1, %2" : "=v"(d) : "v"(v), "s"(s) : "memory"); }
template <int N> DI void vmwait() { static_assert(N >= 0 && N < 64, ""); asm volatile("s_waitcnt vmcnt(%0)" ::"i"(N) : "memory"); }
DI void set_m0(u32 ldsaddr) { asm volatile("s_mov_b32 m0, %0" ::"s"(ldsaddr) : "memory"); }
template <int I> DI void dma1(u32 voff, const u8* src) {
  asm volatile("global_load_lds_dwordx4 %0, %1 offset:%2 sc1" ::"v"(voff), "s"(src), "i"(I) : "memory");
}
DI void dma4(u32 voff, const u8* src, u32 ldsaddr) {
  asm volatile("s_mov_b32 m0, %2\n\ts_nop 0\n\tglobal_load_lds_dwordx4 %0, %1 sc1\n\tglobal_load_lds_dwordx4 %0, %1 offset:1024 sc1\n\t"
               "global_load_lds_dwordx4 %0, %1 offset:2048 sc1\n\tglobal_load_lds_dwordx4 %0, %1 offset:3072 sc1" ::"v"(voff), "s"(src), "s"(ldsaddr) : "memory");
}
#define RETRY(COND, ...)                                                      \
  do {                                                                        \
    bool bad_ = (COND);                                                       \
    if (__builtin_expect(__any(bad_), 0)) {                                   \
      _Pragma("nounroll") for (int sp_ = 0; sp_ < SPIN_CAP; ++sp_) {          \
        __VA_ARGS__                                                           \
        bad_ = (COND);                                                        \
        if (!__any(bad_)) break;                                              \
      }                                                                       \
    }                                                                         \
  } while (0)
DI void bar() { asm volatile("s_waitcnt lgkmcnt(0)\n\ts_barrier" ::: "memory"); }
DI void bar_nw() { asm volatile("s_barrier" ::: "memory"); }

struct RowC { u32x4 b1re, b1im, b2re[2][2], b2im[2][2]; f32x2 twc[2][2], tws[2][2]; };
template <bool INV> DI void pair_cs(float c, float s, u32& re, u32& im) {
  if constexpr (INV) { im = pkb(s, c); re = pkb_n1(c, s, im); } else { re = pkh(c, s); im = pkh_n0(s, c, re); }
}
template <bool AG = false> DI void sign4(u32x4 base, u32 lm, u32x4& f00, u32x4& f01, u32x4& f10, u32x4& f11) {
  f00 = base;
  f01 = base ^ lm;
  f10 = base ^ u32x4{0u, 0x80008000u, 0u, 0x80008000u};
  f11 = f10 ^ lm;
  if constexpr (AG) { PINA(f00); PINA(f01); PINA(f10); PINA(f11); } else { PIN(f00); PIN(f01); PIN(f10); PIN(f11); }
}
template <bool INV, int PART> DI void row_consts(RowC& k, int m, int g) {
  const float fm = (float)m, fg = (float)(4 * g);
  if constexpr (PART == 0) {
    u32x4 br, bi;
#pragma unroll
    for (int i = 0; i < 4; ++i) {
      const float a = (fg + (float)i) * (fm * (1.0f / 16.0f));
      u32 x, y;
      pair_cs<INV>(cosr(a), sinr(a), x, y); k.b1re[i] = x; k.b1im[i] = y;
      const float a2 = (fg + (float)i) * (fm * (1.0f / 32.0f));
      pair_cs<INV>(cosr(a2), sinr(a2), x, y); br[i] = x; bi[i] = y;
    }
    PIN(k.b1re); PIN(k.b1im);
    const u32 lm = (m & 1) ? 0x80008000u : 0u;
    sign4(br, lm, k.b2re[0][0], k.b2re[0][1], k.b2re[1][0], k.b2re[1][1]);
    sign4(bi, lm, k.b2im[0][0], k.b2im[0][1], k.b2im[1][0], k.b2im[1][1]);
  } else {
#pragma unroll
    for (int e = 0; e < 2; ++e)
#pragma unroll
      for (int i = 0; i < 4; ++i) {
        const float a = (fg + (float)(16 * e + i)) * (fm * (INV ? -1.0f / 512.0f : 1.0f / 512.0f));
        k.twc[e][i >> 1][i & 1] = cosr(a); k.tws[e][i >> 1][i & 1] = sinr(a);
      }
#pragma unroll
    for (int e = 0; e < 2; ++e) { PIN(k.twc[e][0]); PIN(k.twc[e][1]); PIN(k.tws[e][0]); PIN(k.tws[e][1]); }
  }
}
template <bool INV, int TAIL> DI void row_fft(const RowC& k, u32x4 a0, u32x4 a1, f32x4& dr0, f32x4& di0, f32x4& dr1, f32x4& di1) {
  f32x4 tr0, ti0, tr1, ti1;
  mm4<INV>(tr0, ti0, tr1, ti1, a0, a1, k.b1re, k.b1im);
  u32x4 p0 = twiddle<INV>(tr0, ti0, k.twc[0], k.tws[0]);
  PIN(p0);
  const u32x4 p1 = cplx_under<INV, true>(tr1, ti1, k.twc[1], k.tws[1], [&](auto I) {
    constexpr int i = decltype(I)::value;
    mf0<INV, 0>(i == 0 ? dr0 : i == 1 ? di0 : i == 2 ? dr1 : di1, p0, i == 0 ? k.b2re[0][0] : i == 1 ? k.b2im[0][0] : i == 2 ? k.b2re[1][0] : k.b2im[1][0]);
  });
  mf4<INV, 0, TAIL>(dr0, di0, dr1, di1, p1, k.b2re[0][1], p1, k.b2im[0][1], p1, k.b2re[1][1], p1, k.b2im[1][1]);
}

struct FwdP { u32 iw, ir, ow, ap0, ap1, ar0, ar1, zo; f32x2 cB, cD, eA, eB, eC, eD; };
DI void fwd_pre(FwdP& p, int tid, int w, int pos) {
  const int m = tid & 15, g = (tid >> 4) & 3, ph = tid >> 1, fq = ph ? 512 - ph : 256, fq1 = 384 - ph;
  p.iw = w * 5120 + ((16 * (m & 1) + 4 * g) * 20 + (m >> 1)) * 4; p.ir = w * 5120 + (m * 20 + 4 * g) * 4;
  p.ap0 = (ph + (ph >> 4) * 4) * 4; p.ap1 = p.ap0 + 640; p.ar0 = (fq + (fq >> 4) * 4) * 4; p.ar1 = (fq1 + (fq1 >> 4) * 4) * 4;
  p.zo = OFF_A + pos * 8192 + tid * 16;
  const float sc = 1.0f / 64.0f, sg = (tid & 1) ? -sc : sc;
  p.cB = f32x2{sg, -sg}; p.cD = f32x2{sg, sg};
  p.eA = ph ? f32x2{sc, sc} : f32x2{2 * sc, 0.f}; p.eB = ph ? p.cB : f32x2{2 * sg, 0.f};
  p.eC = ph ? f32x2{sc, -sc} : f32x2{0.f, 2 * sg}; p.eD = ph ? p.cD : f32x2{0.f, 2 * sc};
  PIN(p.iw); PIN(p.ir); PIN(p.ap0); PIN(p.ap1); PIN(p.ar0); PIN(p.ar1); PIN(p.zo);
  PIN(p.cB); PIN(p.cD); PIN(p.eA); PIN(p.eB); PIN(p.eC); PIN(p.eD);
}
struct ColC {
  u32x4 b1re[2], b1im[2], a2re[2][2], a2im[2][2], b3re[2][2], b3im[2][2], a4re[2][2], a4im[2][2];
  f32x2 t1c[2][2], t1s[2][2], t2c[2][2], t2s[2][2];
};
template <int PART> DI void col_consts(ColC& k, int m, int g, int w) {
  const int pi_ = w >> 1, kt = w & 1;
  const float fm = (float)m, fg = (float)(4 * g);
  const u32 lm = (m & 1) ? 0x80008000u : 0u;
  if constexpr (PART == 0) {
    const float fk = (float)(2 * (16 * kt + m) + pi_);
#pragma unroll
    for (int u = 0; u < 2; ++u) {
#pragma unroll
      for (int r = 0; r < 4; ++r) {
        const float n = fg + (float)(16 * u + r), a = n * (fk * (1.0f / 64.0f)), a2 = n * (fk * (1.0f / 2048.0f));
        u32 x, y;
        pair_cs<false>(cosr(a), sinr(a), x, y); k.b1re[u][r] = x; k.b1im[u][r] = y;
        k.t1c[u][r >> 1][r & 1] = cosr(a2); k.t1s[u][r >> 1][r & 1] = sinr(a2);
      }
      PIN(k.b1re[u]); PIN(k.b1im[u]); PIN(k.t1c[u][0]); PIN(k.t1c[u][1]); PIN(k.t1s[u][0]); PIN(k.t1s[u][1]);
    }
  } else if constexpr (PART == 1) {
    u32x4 fr, fi, ir, ii;
#pragma unroll
    for (int r = 0; r < 4; ++r) {
      const float a = (fg + (float)r) * (fm * (1.0f / 32.0f)), c = cosr(a), s = sinr(a);
      u32 x, y;
      pair_cs<false>(c, s, x, y); fr[r] = x; fi[r] = y;
      pair_cs<true>(c, s, x, y); ir[r] = x; ii[r] = y;
    }
    sign4(fr, lm, k.a2re[0][0], k.a2re[0][1], k.a2re[1][0], k.a2re[1][1]);
    sign4(fi, lm, k.a2im[0][0], k.a2im[0][1], k.a2im[1][0], k.a2im[1][1]);
    sign4(ir, lm, k.b3re[0][0], k.b3re[0][1], k.b3re[1][0], k.b3re[1][1]);
    sign4(ii, lm, k.b3im[0][0], k.b3im[0][1], k.b3im[1][0], k.b3im[1][1]);
  } else if constexpr (PART == 2) {
    const float kb = (float)(32 * kt + pi_) + 2.0f * fg;
#pragma unroll
    for (int u = 0; u < 2; ++u) {
#pragma unroll
      for (int r = 0; r < 4; ++r) {
        const float a = (kb + (float)(2 * r)) * ((fm + (float)(16 * u)) * (-1.0f / 2048.0f));
        k.t2c[u][r >> 1][r & 1] = cosr(a); k.t2s[u][r >> 1][r & 1] = sinr(a);
      }
      PIN(k.t2c[u][0]); PIN(k.t2c[u][1]); PIN(k.t2s[u][0]); PIN(k.t2s[u][1]);
    }
  } else {
    const float fn = (fm + (float)(16 * (w >> 1))) * (1.0f / 64.0f);
#pragma unroll
    for (int u = 0; u < 2; ++u) {
#pragma unroll
      for (int r = 0; r < 4; ++r) {
        const float a = (2.0f * fg + (float)(2 * r + u)) * fn;
        u32 x, y;
        pair_cs<true>(cosr(a), sinr(a), x, y); k.a4re[u][0][r] = x; k.a4im[u][0][r] = y;
      }
      k.a4re[u][1] = k.a4re[u][0] ^ lm; k.a4im[u][1] = k.a4im[u][0] ^ lm;
      PIN(k.a4re[u][0]); PIN(k.a4re[u][1]); PIN(k.a4im[u][0]); PIN(k.a4im[u][1]);
    }
  }
}

DI void transpose8x4(const u32x2 (&v)[8], u32x4 (&o)[4]) {
#pragma unroll
  for (int jj = 0; jj < 4; ++jj) {
    o[0][jj] = __builtin_amdgcn_perm(v[2 * jj + 1].x, v[2 * jj].x, 0x05040100u);
    o[1][jj] = __builtin_amdgcn_perm(v[2 * jj + 1].x, v[2 * jj].x, 0x07060302u);
    o[2][jj] = __builtin_amdgcn_perm(v[2 * jj + 1].y, v[2 * jj].y, 0x05040100u);
    o[3][jj] = __builtin_amdgcn_perm(v[2 * jj + 1].y, v[2 * jj].y, 0x07060302u);
  }
}
template <int H> DI void prep_load1(const Params& P, int wt, int lane, u32x2 (&v)[8]) {
  const u32 vo = (lane < 56 ? lane : 55) * 8, vo2 = vo + 7168;
  const int piece = (wt >> 3) & 63;
  const u8* src = (const u8*)((wt >> 9) ? P.wu : P.wg) + opq(wt & 7) * 448 + opq(4 * piece) * 3584;
  static_for<4>([&](auto J) { constexpr int j = decltype(J)::value; ld2p<(j & 1) * 3584>(v[4 * H + j], j & 2 ? vo2 : vo, src + H * (256 * 3584)); });
}
template <int H> DI void prep_load2(const Params& P, int wt, int lane, u32 (&v)[8]) {
  const int piece = wt >> 2, g = piece & 3, kq = piece >> 2, wv = kq / 14, ks = kq % 14;
  const int f0 = ks < 4 ? 112 * (4 * wv + ks) : ks < 6 ? 112 * (4 * wv + 2 * (ks - 4)) + 32 : ks < 10 ? 112 * (4 * wv + ks - 6) + 48 : 112 * (4 * wv + ks - 10) + 80;
  const int f1 = f0 + ((ks == 4 || ks == 5) ? 112 : 16);
  const u8* s0 = (const u8*)P.wd + (wt & 3) * 256 + (4 * g + (H ? f1 : f0)) * 1024;
  static_for<4>([&](auto J) { constexpr int j = decltype(J)::value; ld1p<j * 1024>(v[4 * H + j], lane * 4, s0); });
}
DI u32 prep_dst1(int wt, int tile) {
  const int piece = (wt >> 3) & 63, mat = wt >> 9, hi = tile >= 7, p = tile - 7 * hi, ks = piece >> 2;
  const u32 frag = (p < 3 ? ks * 6 + 2 * p : p < 5 ? 96 + ks * 4 + 2 * (p - 3) : 160 + ks * 4 + 2 * (p - 5)) + mat;
  return OFF_W1 + ((2 * (wt & 7) + hi) * 224 + frag) * 1024 + (piece & 3) * 256;
}
DI u32 prep_dst2(int wt, int tile) {
  const int piece = wt >> 2, tl = (wt & 3) * 8 + tile, rot = (tl >> 2) & 3, kq = piece >> 2, ks = kq % 14;
  const int slot = ks < 4 ? (ks - rot) & 3 : ks < 6 ? ks ^ (rot & 1) : ks < 10 ? 6 + ((ks - 6 - rot) & 3) : 10 + ((ks - 10 - rot) & 3);
  return OFF_W2 + (((tl >> 2) * 56 + kq - ks + slot) * 4 + (tl & 3)) * 1024 + (piece & 3) * 256;
}

constexpr int NTOK0 = 12, NRES = 40, NEARLY = 26;
constexpr int g1_job(int L, int x) {
  if (((x >= 34 && x < 96) && (x & 3) == 2) || (x >= 98 && x < 114 && !(x & 1))) return 2;
  if ((x < 34 && !(x & 1)) || (x < 64 && (x & 3) == 0)) return 1;
  if (L == 1 && x >= 47 && x < 60 && (x & 3) == 3) return 6;
  if (x >= 164) return ((x - 164) % 7 == 0 || (x - 164) % 7 == 2 || (x - 164) % 7 == 5) ? 4 : 0;
  if (x >= 116 && x % 3 == 2) return 5;
  if (L == 0 && ((x < 34 && x % 4 == 1) || x == 158 || x == 161 || x == 163 || (x >= 48 && x < 98 && (x & 3) == 0) || (x >= 114 && x % 3 == 0) || (x >= 35 && x < 98 && x % 8 == 1)))
    return 3;
  return 0;
}
constexpr int NEPI1 = 123, NEPI2 = 82;
struct G1Sched {
  int job[224], idx[224], epi[225], nst[224], cum[225], pos[7][64], cnt[7];
  constexpr G1Sched(int L) : job{}, idx{}, epi{}, nst{}, cum{}, pos{}, cnt{} {
    cnt[1] = NTOK0;
    for (int i = 0; i < NTOK0; ++i) pos[1][i] = i;
    cum[0] = NTOK0;
    for (int x = 0; x < 224; ++x) {
      int j = g1_job(L, x);
      if (j == 1 && cnt[1] >= 32) j = L == 0 ? 3 : 0;
      if ((j == 3 && cnt[3] >= NRES) || (j == 4 && cnt[4] >= NEARLY)) j = 0;
      job[x] = j; idx[x] = cnt[j];
      if (x == 161) epi[x] = NEPI1;
      const bool on = (x >= 97 && x < 160 && epi[x] < NEPI1) || x >= 161;
      epi[x + 1] = on ? epi[x] + (j ? 2 : 3) : epi[x];
      if (x < 160 && epi[x + 1] > NEPI1) epi[x + 1] = NEPI1;
      for (int n = epi[x]; n < epi[x + 1]; ++n)
        nst[x] += n < NEPI1 ? (n == 40 || n == 81 || n == 122) : (n < NEPI1 + NEPI2 && (n - NEPI1) % 41 == 40);
      if (j) pos[j][cnt[j]++] = cum[x] + nst[x];
      cum[x + 1] = cum[x] + nst[x] + (j ? 1 : 0);
    }
  }
};
constexpr G1Sched G1S[2] = {G1Sched(0), G1Sched(1)};
static_assert(G1S[0].cnt[1] == 32 && G1S[0].cnt[2] == 24 && G1S[0].cnt[3] == NRES && G1S[0].cnt[4] == NEARLY && G1S[0].cnt[5] == 16, "");
static_assert(G1S[1].cnt[6] == 4 && G1S[1].pos[6][3] < G1S[1].pos[2][7], "");
static_assert(G1S[0].idx[66] == 8 && G1S[0].idx[98] == 16 && G1S[0].epi[113] <= 40 && G1S[1].epi[113] <= 40, "");
static_assert(G1S[0].epi[152] == NEPI1 && G1S[0].epi[204] >= NEPI1 + NEPI2, "");
constexpr int clamp63(int n) { return n > 63 ? 63 : n; }
constexpr int g1_wait(int L, int x, int job, int idx) { return clamp63(G1S[L].cum[x] - G1S[L].pos[job][idx] - 1); }

#define FRESH()                                                                                                   \
  int tid = threadIdx.x, b = BMAP(blockIdx.x), w = w_;                                                                   \
  u8* ws = P.ws;                                                                                                   \
  asm volatile("" : "+v"(tid), "+s"(b), "+s"(w), "+s"(ws));                                                        \
  const int lane = tid & 63, m = lane & 15, g = lane >> 4, half2 = b & 1, ct = b >> 5;                             \
  const int G = ((b >> 1) & 3) * 4 + ((b >> 3) & 3), member = half2 * 8 + ct, pos = G * 16 + member;               \
  const u32 l16 = lane * 16, t16 = tid * 16, xo = 32 * m + 8 * g;                                                  \
  auto fill_h = [&](u32 base, int q) { st4(sent4, base + (4 * G + w) * 114688 + member * 7168 + q * 1024 + l16); };          \
  auto fill_rows = [&](u32 base) { st4(sent4, base + pos * 8192 + t16); st4(sent4, base + pos * 8192 + 4096 + t16); }; \
  auto ring4 = [&](int q) {                                   \
    const u32 o = (q >> 1) * 32768 + w * 8192 + (q & 1) * 4096;                                                    \
    dma4(l16, ws + OFF_W1 + member * 229376 + o, ldsbase + o);                                                     \
  };                                                                                                               \
  (void)m; (void)g; (void)half2; (void)ct; (void)l16; (void)t16; (void)xo; (void)fill_h; (void)fill_rows; (void)ring4

__global__ __launch_bounds__(256, 1) void k_all(Params P) {
  extern __shared__ __attribute__((aligned(16))) u8 lds[];
  ST_DECL;
  const int w_ = __builtin_amdgcn_readfirstlane(threadIdx.x >> 6);
  const __amdgpu_buffer_rsrc_t rs = __builtin_amdgcn_make_buffer_rsrc(P.ws, 0, WS_USED, 0x00020000);
  const u32 ldsbase = (u32)(size_t)(__attribute__((address_space(3))) u8*)lds;
  const u32x4 sent4 = {SENT, SENT, SENT, SENT};
  auto st4 = [&](u32x4 v, u32 off) { __builtin_amdgcn_raw_buffer_store_b128(v, rs, off, 0, 16); };
  auto st2 = [&](u32x2 v, u32 off) { __builtin_amdgcn_raw_buffer_store_b64(v, rs, off, 0, 16); };
  u32x2 xl[2], xh[2], pv0[8];
  u32 pv1[8];
  u32 wfr[8], wfi[8], wn[8];
  f32x2 xv[8], xc;
  float xch;
  FwdP fp;
  u32 pd[6];
  u32x2 ep;
  u32 cpf = 0, tw[5];
  RowC rc;
  ColC cc;
  ST(0);
  {
    FRESH();
    const u8* xp = (const u8*)P.x + row_of_pos(pos, w) * 1024;
    ld2p<0>(xl[0], xo, xp); ld2p<512>(xl[1], xo, xp); ld2p<0>(xh[0], xo, xp + 1024 * 1024); ld2p<512>(xh[1], xo, xp + 1024 * 1024);
    { const u32 z = 0; ld2<0>(ep, z, ws); }
    {
      const u8* pc;
      asm volatile("s_getpc_b64 %0" : "=s"(pc));
      const u32 o = (((b >> 3) * 4 + w) * 8 + (lane & 7)) * 128;
      touch(cpf, o < CODE_BYTES ? o : 0, pc);
    }
    {
      const u32 z = 0;
      ld1p<0>(tw[0], z, (const u8*)P.wr); ld1p<0>(tw[1], z, (const u8*)P.wi); ld1p<0>(tw[2], z, (const u8*)(b < 128 ? P.wg : P.wu));
      ld1p<0>(tw[3], z, (const u8*)P.wd); ld1p<0>(tw[4], z, (const u8*)P.out);
    }
    XS(32);
    fill_rows(OFF_A); fill_rows(OFF_WX);
    if (tid < 8) st4(sent4, OFF_WNY + tid * 4096 + pos * 16);
    SB();
    row_consts<false, 0>(rc, m, g);
    row_consts<false, 1>(rc, m, g);
    SB();
    XS(33);
    const int kb = 2 * (16 * ((pos >> 6) & 1) + (pos & 15)) + (pos >> 7) + 256 * ((pos >> 4) & 3);
    static_for<8>([&](auto J) {
      constexpr int j = decltype(J)::value;
      const u32 o = tid * 2 + (kb + 64 * (16 * (j >> 2) + (j & 3))) * 514;
      ld1s(wfr[j], o, (const u8*)P.wr); ld1s(wfi[j], o, (const u8*)P.wi);
    });
    {
      const int k = kb + 64 * (16 * ((lane >> 2) & 1) + (lane & 3)), km = (2048 - k) & 2047;
      const u32 o = k * 514, om = km * 514;
      ld1s(wn[0], o, (const u8*)P.wr); ld1s(wn[1], o, (const u8*)P.wi); ld1s(wn[2], om, (const u8*)P.wr); ld1s(wn[3], om, (const u8*)P.wi);
      ld1s(wn[4], o, (const u8*)P.wr + 512); ld1s(wn[5], o, (const u8*)P.wi + 512); ld1s(wn[6], om, (const u8*)P.wr + 512); ld1s(wn[7], om, (const u8*)P.wi + 512);
    }
    XS(34);
    fwd_pre(fp, tid, w, pos);
#pragma unroll
    for (int x = 0; x < 4; ++x) pd[x] = x < 3 || g < 2 ? prep_dst1(b * 4 + w, 4 * x + g) + m * 16 : WS_USED;
#pragma unroll
    for (int x = 0; x < 2; ++x) pd[4 + x] = b < 224 ? prep_dst2(b * 4 + w, 4 * x + g) + m * 16 : WS_USED;
#pragma unroll
    for (int x = 0; x < 6; ++x) PIN(pd[x]);
    SB();
    XS(35);
    vmwait<1 + 5 + 4 + 24>();
    XS(36);
    PIN(ep); PIN(xl[0]); PIN(xl[1]); PIN(xh[0]); PIN(xh[1]);
    { const u64 e = (((u64)(u32)__builtin_amdgcn_readfirstlane(ep.y) << 32) | (u32)__builtin_amdgcn_readfirstlane(ep.x)) + 1; ep = u32x2{(u32)e, (u32)(e >> 32)}; }
#pragma unroll
    for (int t = 0; t < 2; ++t)
#pragma unroll
      for (int p = 0; p < 2; ++p) {
        xv[4 * t + 2 * p] = f32x2{blo(xl[t][p]), blo(xh[t][p])};
        xv[4 * t + 2 * p + 1] = f32x2{bhi(xl[t][p]), bhi(xh[t][p])};
      }
    xc = f32x2{first_lane(xv[0].x), first_lane(xv[0].y)};
    xch = lane < 32 ? xc.x : xc.y;
#pragma unroll
    for (int i = 0; i < 8; ++i) xv[i] -= xc;
  }
  ST(1);

  u32x4 w2a[10][4];

  auto layer = [&](auto LC) {
    constexpr int L = decltype(LC)::value;
    constexpr int S0 = 2 + 16 * L;
    {
      FRESH();
      const f32x2 S = ((xv[0] + xv[1]) + (xv[2] + xv[3])) + ((xv[4] + xv[5]) + (xv[6] + xv[7]));
      f32x2 q0 = xv[0] * xv[0], q1 = xv[1] * xv[1];
#pragma unroll
      for (int i = 2; i < 8; i += 2) { q0 += xv[i] * xv[i]; q1 += xv[i + 1] * xv[i + 1]; }
      if constexpr (L == 0) { SB(); fill_rows(OFF_U); SB(); }
      f32x2 R, O;
      XF(32);
      ln_coef(S, q0 + q1, xch, R, O);
      XF(33);
      u8* img = lds + LDS_SCR;
      if constexpr (L == 0) {
        SB(); vmwait<26>(); PIN(cpf); PIN(tw[0]); PIN(tw[1]); PIN(tw[2]); PIN(tw[3]); PIN(tw[4]);
        if (lane == 0) st2(ep, OFF_FLAG + (w * 256 + pos) * 8);
        prep_load1<0>(P, b * 4 + w, lane, pv0); SB();
      }
#pragma unroll
      for (int i = 0; i < 8; ++i) {
        const f32x2 v = xv[i] * R + O;
        *(u32*)(img + fp.iw + ((i & 3) * 20 + 8 * (i >> 2)) * 4) = pkh(v.x, v.y);
      }
      const u32x4 a0 = *(const u32x4*)(img + fp.ir), a1 = *(const u32x4*)(img + fp.ir + 1280);
      if constexpr (L == 0) prep_load1<1>(P, b * 4 + w, lane, pv0); else ring4(1);
      const float* p0 = (const float*)(img + fp.ap0);
      const float* p1 = (const float*)(img + fp.ap1);
      const float* r0 = (const float*)(img + fp.ar0);
      const float* r1 = (const float*)(img + fp.ar1);
      const f32x2 cA = {1.0f / 64.0f, 1.0f / 64.0f}, cC = {1.0f / 64.0f, -1.0f / 64.0f}, cB = fp.cB, cD = fp.cD, eA = fp.eA, eB = fp.eB, eC = fp.eC, eD = fp.eD;
      const u32 zo = fp.zo;
      f32x4 dr0, di0, dr1, di1;
      XF(34);
      row_fft<false, 6>(rc, a0, a1, dr0, di0, dr1, di1);
      XF(35);
      SB();
      float* op = (float*)(img + fp.ir);
      *(f32x4*)(op) = dr0; *(f32x4*)(op + 640) = di0;
      SB();
      *(f32x4*)(op + 320) = dr1; *(f32x4*)(op + 960) = di1;
      if constexpr (L == 0) { prep_load2<0>(P, (b < 224 ? b : 0) * 4 + w, lane, pv1); vmwait<13>(); }
      bar();
      XF(36);
      f32x2 pa[4], qa[4], pb[4], qb[4];
#pragma unroll
      for (int j = 0; j < 4; ++j) {
        pa[j] = f32x2{p0[j * 1280], p0[j * 1280 + 640]}; qa[j] = f32x2{r0[j * 1280], r0[j * 1280 + 640]};
      }
#pragma unroll
      for (int j = 0; j < 4; ++j) {
        pb[j] = f32x2{p1[j * 1280], p1[j * 1280 + 640]}; qb[j] = f32x2{r1[j * 1280], r1[j * 1280 + 640]};
      }
      if constexpr (L == 0) prep_load2<1>(P, (b < 224 ? b : 0) * 4 + w, lane, pv1);
      SB();
      u32x4 o;
#pragma unroll
      for (int j = 0; j < 4; ++j) {
        const f32x2 v = eD * swp(qa[j]) + (eC * qa[j] + (eB * swp(pa[j]) + eA * pa[j]));
        o[j] = mark<(L == 0 ? 0x10001u : 0u), (L == 0 ? 0u : 0x10000u)>(pkh(v.x, v.y));
      }
      st4(o, zo);
#pragma unroll
      for (int j = 0; j < 4; ++j) {
        const f32x2 v = cD * swp(qb[j]) + (cC * qb[j] + (cB * swp(pb[j]) + cA * pb[j]));
        o[j] = mark<(L == 0 ? 0x10001u : 0u), (L == 0 ? 0u : 0x10000u)>(pkh(v.x, v.y));
      }
      st4(o, zo + 4096);
      SB();
      if constexpr (L == 1) {
#pragma unroll
        for (int t = 0; t < 2; ++t)
#pragma unroll
          for (int p = 0; p < 2; ++p) {
            const f32x2 v0 = xv[4 * t + 2 * p] + xc, v1 = xv[4 * t + 2 * p + 1] + xc;
            xl[t][p] = pkb(v0.x, v1.x); xh[t][p] = pkb(v0.y, v1.y);
          }
        PIN(xl[0]); PIN(xl[1]); PIN(xh[0]); PIN(xh[1]);
      }
    }
    if constexpr (L == 0) {
      XS(37);
      FRESH();
#pragma unroll
      for (int j = 0; j < 8; ++j) { PIN(wfr[j]); PIN(wfi[j]); PIN(wn[j]); }
      u8* bw = lds + w * 2048;
      u32x4 v0, v1;
#pragma unroll
      for (int j = 0; j < 4; ++j) { v0[j] = wfr[j] | (wfi[j] << 16); v1[j] = wfr[4 + j] | (wfi[4 + j] << 16); }
      *(u32x4*)(bw + lane * 32) = v0; *(u32x4*)(bw + lane * 32 + 16) = v1;
      st4(*(const u32x4*)(bw + l16), OFF_WX + pos * 8192 + w * 2048 + l16);
      st4(*(const u32x4*)(bw + 1024 + l16), OFF_WX + pos * 8192 + w * 2048 + 1024 + l16);
      const float hr = blo(wn[0]) + blo(wn[2]), hi = blo(wn[1]) - blo(wn[3]), nr = blo(wn[4]) + blo(wn[6]), ni = blo(wn[5]) - blo(wn[7]);
      const f32x4 ab = {0.25f * (hr + nr), 0.25f * (hi + ni), 0.25f * (hr - nr), 0.25f * (hi - ni)};
      if (tid < 8) st4(__builtin_bit_cast(u32x4, ab), OFF_WNY + tid * 4096 + pos * 16);
    }
    ST(S0);
    {
      FRESH();
      const int phi = pos, pi_ = w >> 1, kt = w & 1;
      const int k1l = 2 * (16 * kt + m) + pi_;
      auto consts = [&](auto PC) { col_consts<decltype(PC)::value>(cc, m, g, w); SB(); };
      using I0 = std::integral_constant<int, 0>; using I1 = std::integral_constant<int, 1>;
      using I2 = std::integral_constant<int, 2>; using I3 = std::integral_constant<int, 3>;
      u8* const bw = lds + 16384 + w * 8192;
      const u32 zo = (tid >> 1) * 8192 + (tid & 1) * 16;
      const u8* zb0 = ws + OFF_A + phi * 32;
      const u8* zb1 = zb0 + 128 * 8192;
      const u8* wb0 = ws + OFF_WX + phi * 32;
      const u8* wb1 = wb0 + 128 * 8192;
      const u32 wo = zo;
      PINS(zb0); PINS(zb1); PINS(wb0); PINS(wb1);
      if constexpr (L == 0) {
        u32x4 fl0, fl1;
        const u32 o = lane * 32;
        const u8* fb = ws + OFF_FLAG + ((phi >> 5) & 3) * 2048;
        ld4<0>(fl0, o, fb); ld4<16>(fl1, o, fb);
        SB();
        consts(I0{});
        consts(I1{});
        XS(38);
        vmwait<0>(); PIN(fl0); PIN(fl1);
        RETRY(!(fl0.x == ep.x && fl0.y == ep.y && fl0.z == ep.x && fl0.w == ep.y && fl1.x == ep.x && fl1.y == ep.y && fl1.z == ep.x && fl1.w == ep.y), {
          CNT(0);
          ld4r<0>(fl0, o, fb); ld4r<16>(fl1, o, fb);
          vmwait<0>(); PIN(fl0); PIN(fl1);
        });
      } else {
        SB();
        consts(I0{});
        consts(I2{});
      }
      u32x4 z0, z1, wv[2];
      if constexpr (L == 0) XS(39);
      ld4<0>(z0, zo, zb0); ld4<0>(z1, zo, zb1);
      ld4<0>(wv[0], wo, wb0); ld4<0>(wv[1], wo, wb1);
      SB();
      u32x4 wa[8], po[6];
      if constexpr (L == 0) {
        XS(40);
        u32x4 ot[4];
#pragma unroll
        for (int j = 0; j < 8; ++j) { PIN(pv0[j]); PIN(pv1[j]); }
        transpose8x4(pv0, ot);
#pragma unroll
        for (int x = 0; x < 4; ++x) *(u32x4*)(bw + lane * 64 + ((x + lane) & 3) * 16) = ot[x];
#pragma unroll
        for (int jj = 0; jj < 4; ++jj) {
          ot[0][jj] = __builtin_amdgcn_perm(pv1[2 * jj + 1], pv1[2 * jj], 0x05040100u);
          ot[1][jj] = __builtin_amdgcn_perm(pv1[2 * jj + 1], pv1[2 * jj], 0x07060302u);
        }
        *(u32x4*)(bw + 4096 + lane * 32) = ot[0]; *(u32x4*)(bw + 4096 + lane * 32 + 16) = ot[1];
#pragma unroll
        for (int x = 0; x < 4; ++x) {
          const int src = x * 16 + (lane >> 2);
          po[x] = *(const u32x4*)(bw + src * 64 + (((lane & 3) + src) & 3) * 16);
        }
#pragma unroll
        for (int x = 0; x < 2; ++x) {
          po[4 + x] = *(const u32x4*)(bw + 4096 + x * 1024 + (8 * (m >> 2) + 4 * (g & 1) + (m & 3) + 32 * (g >> 1)) * 16);
        }
#pragma unroll
        for (int x = 0; x < 6; ++x) PIN(po[x]);
        st4(po[0], pd[0]); st4(po[1], pd[1]);
        SB();
        XS(41);
        consts(I2{});
        XS(42);
        consts(I3{});
        XS(43);
        st4(po[2], pd[2]); st4(po[3], pd[3]);
      } else {
        consts(I1{});
        ring4(2);
        SB();
        consts(I3{});
      }
      u8* const zi = lds + LDS_SCR;
      u32 zw;
      {
        const int pp = tid >> 1;
        zw = (tid & 1) * 4096 + ((pp >> 6) & 1) * 2048 + (((pp >> 3) & 3) * 16 + 2 * (pp & 7)) * 16 + ((pp >> 5) & 1) * 8;
        PIN(zw);
      }
      u32 uo = OFF_U + phi * 8192 + w * 2048 + l16;
      PIN(uo);
      SB();
      bar_nw();
      ST(S0 + 1);
      vmwait<4>();
      PIN(z0); PIN(z1); PIN(wv[0]); PIN(wv[1]);
      if constexpr (L == 0) {
        RETRY((stale<false, 1u>(red8<false>(z0, z1)) || max4(max4(0, wv[0]), wv[1]) == SENT), {
          CNT(1);
          if (bad_) { ld4r<0>(z0, zo, zb0); ld4r<0>(z1, zo, zb1); ld4r<0>(wv[0], wo, wb0); ld4r<0>(wv[1], wo, wb1); }
          vmwait<0>(); PIN(z0); PIN(z1); PIN(wv[0]); PIN(wv[1]);
        });
      } else {
        RETRY((stale<true, 0x10000u>(red8<true>(z0, z1))), {
          CNT(2);
          if (bad_) { ld4r<0>(z0, zo, zb0); ld4r<0>(z1, zo, zb1); }
          vmwait<0>(); PIN(z0); PIN(z1);
        });
      }
      ST(S0 + 2);
      *(u32x2*)(zi + zw) = u32x2{z0.x, z0.y}; *(u32x2*)(zi + zw + 16) = u32x2{z0.z, z0.w};
      *(u32x2*)(zi + zw + 1024) = u32x2{z1.x, z1.y}; *(u32x2*)(zi + zw + 1024 + 16) = u32x2{z1.z, z1.w};
      *(u32x4*)(zi + 8192 + t16) = wv[0]; *(u32x4*)(zi + 12288 + t16) = wv[1];
      bar();
      XF(37);
      const u8* zr = zi + pi_ * 4096 + l16;
      const u32x4 f00 = *(const u32x4*)(zr), f01 = *(const u32x4*)(zr + 1024), f10 = *(const u32x4*)(zr + 2048), f11 = *(const u32x4*)(zr + 3072);
      wv[0] = *(const u32x4*)(zi + 8192 + tid * 32); wv[1] = *(const u32x4*)(zi + 8192 + tid * 32 + 16);
      if (phi == 0) static_for<8>([&](auto J) { constexpr int j = decltype(J)::value; ld4<0>(wa[j], t16, ws + OFF_WNY + j * 4096); });
      if constexpr (L == 1) ring4(3); else { st4(po[4], pd[4]); st4(po[5], pd[5]); }
      f32x4 d0, d1, d2, d3;
      mm8<false, 3>(d0, d1, d2, d3, f00, cc.b1re[0], f01, cc.b1re[1], f00, cc.b1im[0], f01, cc.b1im[1], f10, cc.b1re[0], f11, cc.b1re[1], f10, cc.b1im[0], f11, cc.b1im[1]);
      XF(38);
      f32x4 e0, e1, e2, e3;
      u32x4 b20 = twiddle<false>(d0, d1, cc.t1c[0], cc.t1s[0]);
      PIN(b20);
      const u32x4 b21 = cplx_under<false, true>(d2, d3, cc.t1c[1], cc.t1s[1], [&](auto I) {
        constexpr int i = decltype(I)::value;
        mf0<false, 0>(i == 0 ? e0 : i == 1 ? e1 : i == 2 ? e2 : e3, i == 0 ? cc.a2re[0][0] : i == 1 ? cc.a2im[0][0] : i == 2 ? cc.a2re[1][0] : cc.a2im[1][0], b20);
      });
      f32x2 wr[2][2], wi[2][2];
      auto unpack_w = [&]() {
#pragma unroll
        for (int t = 0; t < 2; ++t)
#pragma unroll
          for (int h = 0; h < 2; ++h) {
            wr[t][h] = f32x2{blo(wv[t][2 * h]), blo(wv[t][2 * h + 1])}; wi[t][h] = f32x2{bhi(wv[t][2 * h]), bhi(wv[t][2 * h + 1])};
            PIN(wr[t][h]); PIN(wi[t][h]);
          }
      };
      mf4<false, 0, 0>(e0, e1, e2, e3, cc.a2re[0][1], b21, cc.a2im[0][1], b21, cc.a2re[1][1], b21, cc.a2im[1][1], b21);
      SB();
      unpack_w();
      SB();
      if constexpr (L == 0) { fill_h(OFF_H, 0); fill_h(OFF_H, 1); }
      XF(39);
      u32x4 a30, a31;
      auto step3 = [&](auto I) {
        constexpr int i = decltype(I)::value;
        mf0<true, 0>(i == 0 ? d0 : i == 1 ? d1 : i == 2 ? d2 : d3, a30, i == 0 ? cc.b3re[0][0] : i == 1 ? cc.b3im[0][0] : i == 2 ? cc.b3re[1][0] : cc.b3im[1][0]);
      };
      if (phi != 0) {
        a30 = cmul_b(e0, e1, wr[0], wi[0]);
        PIN(a30);
        a31 = cplx_under<true, false>(e2, e3, wr[1], wi[1], step3);
      } else {
        u32* pl = (u32*)(lds + LDS_STASH);
        const u32x4 h0 = {pkh(e0[0], e1[0]), pkh(e0[1], e1[1]), pkh(e0[2], e1[2]), pkh(e0[3], e1[3])};
        const u32x4 h1 = {pkh(e2[0], e3[0]), pkh(e2[1], e3[1]), pkh(e2[2], e3[2]), pkh(e2[3], e3[3])};
        if (k1l != 0) {
          *(u32x4*)(pl + k1l * 32 + ((g ^ (k1l >> 1)) & 7) * 4) = h0; *(u32x4*)(pl + k1l * 32 + (((4 + g) ^ (k1l >> 1)) & 7) * 4) = h1;
        } else {
#pragma unroll
          for (int r = 0; r < 4; ++r) { pl[(4 * g + r - 1) & 31] = h0[r]; pl[16 + 4 * g + r - 1] = h1[r]; }
        }
        bar();
        vmwait<4>();
#pragma unroll
        for (int j = 0; j < 8; ++j) PIN(wa[j]);
        if constexpr (L == 0) {
          RETRY(max4(max4(max4(max4(max4(max4(max4(max4(0, wa[0]), wa[1]), wa[2]), wa[3]), wa[4]), wa[5]), wa[6]), wa[7]) == SENT, {
            static_for<8>([&](auto J) { constexpr int j = decltype(J)::value; ld4r<0>(wa[j], t16, ws + OFF_WNY + j * 4096); });
            vmwait<0>();
            _Pragma("unroll") for (int j = 0; j < 8; ++j) PIN(wa[j]);
          });
        }
        const int kr = (64 - k1l) & 63;
        const u32x4 q0 = *(const u32x4*)(pl + kr * 32 + (((7 - g) ^ (kr >> 1)) & 7) * 4), q1 = *(const u32x4*)(pl + kr * 32 + (((3 - g) ^ (kr >> 1)) & 7) * 4);
#pragma unroll
        for (int r = 0; r < 4; ++r) {
          const f32x4 c0 = __builtin_bit_cast(f32x4, wa[r]), c1 = __builtin_bit_cast(f32x4, wa[4 + r]);
          const u32 u0 = q0[3 - r], u1 = q1[3 - r];
          const f16x2 v0 = __builtin_bit_cast(f16x2, u0), v1 = __builtin_bit_cast(f16x2, u1);
          const float qr0 = (float)v0.x, qi0 = (float)v0.y, qr1 = (float)v1.x, qi1 = (float)v1.y;
          a30[r] = pkb(e0[r] * c0[0] - e1[r] * c0[1] + qr0 * c0[2] + qi0 * c0[3], e0[r] * c0[1] + e1[r] * c0[0] + qr0 * c0[3] - qi0 * c0[2]);
          a31[r] = pkb(e2[r] * c1[0] - e3[r] * c1[1] + qr1 * c1[2] + qi1 * c1[3], e2[r] * c1[1] + e3[r] * c1[0] + qr1 * c1[3] - qi1 * c1[2]);
        }
        PIN(a30); PIN(a31);
        asm volatile("s_nop 1");
        static_for<4>(step3);
      }
      u8* ex = lds + LDS_SCR + 16384;
      mf4<true, 0, 5>(d0, d1, d2, d3, a31, cc.b3re[0][1], a31, cc.b3im[0][1], a31, cc.b3re[1][1], a31, cc.b3im[1][1]);
      XF(40);
      *(u32x4*)(ex + w * 1024 + l16) = twiddle<true>(d0, d1, cc.t2c[0], cc.t2s[0]);
      SB();
      *(u32x4*)(ex + (4 + w) * 1024 + l16) = twiddle<true>(d2, d3, cc.t2c[1], cc.t2s[1]);
      if constexpr (L == 0) vmwait<2>();
      bar();
      XF(41);
      const u8* er = ex + (w & 1) * 4096 + l16;
      const u32x4 g0 = *(const u32x4*)(er), g1 = *(const u32x4*)(er + 1024), g2 = *(const u32x4*)(er + 2048), g3 = *(const u32x4*)(er + 3072);
      mm8<true, 7>(d0, d1, d2, d3, cc.a4re[0][0], g0, cc.a4re[0][1], g1, cc.a4im[0][0], g0, cc.a4im[0][1], g1, cc.a4re[1][0], g2, cc.a4re[1][1], g3, cc.a4im[1][0], g2,
                   cc.a4im[1][1], g3);
      {
        XF(42);
        const f32x2 lr0 = lo2(d0) + lo2(d2), li0 = lo2(d1) + lo2(d3), hr0 = lo2(d0) - lo2(d2), hi0 = lo2(d1) - lo2(d3);
        const f32x2 lr1 = hi2(d0) + hi2(d2), li1 = hi2(d1) + hi2(d3), hr1 = hi2(d0) - hi2(d2), hi1 = hi2(d1) - hi2(d3);
        if (phi != 0) {
          const f32x2 pr0 = lr0 - hi0, pi0 = li0 + hr0, qr0 = lr0 + hi0, qi0 = hr0 - li0;
          st4(mark4<1u - L, L>(u32x4{pkb(pr0.x, pi0.x), pkb(qr0.x, qi0.x), pkb(pr0.y, pi0.y), pkb(qr0.y, qi0.y)}), uo);
          const f32x2 pr1 = lr1 - hi1, pi1 = li1 + hr1, qr1 = lr1 + hi1, qi1 = hr1 - li1;
          st4(mark4<1u - L, L>(u32x4{pkb(pr1.x, pi1.x), pkb(qr1.x, qi1.x), pkb(pr1.y, pi1.y), pkb(qr1.y, qi1.y)}), uo + 1024);
          MEMFENCE();
        } else {
          st4(mark4<1u - L, L>(u32x4{pkb(lr0.x, hr0.x), pkb(li0.x, hi0.x), pkb(lr0.y, hr0.y), pkb(li0.y, hi0.y)}), uo);
          st4(mark4<1u - L, L>(u32x4{pkb(lr1.x, hr1.x), pkb(li1.x, hi1.x), pkb(lr1.y, hr1.y), pkb(li1.y, hi1.y)}), uo + 1024);
        }
      }
    }
    ST(S0 + 3);
    {
      FRESH();
      SB();
      row_consts<true, 0>(rc, m, g);
      f32x2 xa[4], xb[4];
#pragma unroll
      for (int i = 0; i < 4; ++i) {
        xa[i] = f32x2{blo(xl[i >> 1][i & 1]), bhi(xl[i >> 1][i & 1])}; xb[i] = f32x2{blo(xh[i >> 1][i & 1]), bhi(xh[i >> 1][i & 1])};
      }
      const float ca = first_lane(xa[0].x), cb = first_lane(xb[0].x);
      float ch = lane < 32 ? ca : cb;
      PIN(ch);
#pragma unroll
      for (int i = 0; i < 4; ++i) { xa[i] -= ca; xb[i] -= cb; PIN(xa[i]); PIN(xb[i]); }
      SB();
      u32x4 c0, c1;
      const u32 uo = (tid >> 1) * 8192 + (tid & 1) * 16;
      const u8* ub0 = ws + OFF_U + pos * 32;
      const u8* ub1 = ub0 + 128 * 8192;
      row_consts<true, 1>(rc, m, g);
      SB();
      __builtin_amdgcn_s_sleep(1);
      ld4<0>(c0, uo, ub0); ld4<0>(c1, uo, ub1);
      if constexpr (L == 0) { fill_h(OFF_H, 2); fill_h(OFF_H, 3); fill_h(OFF_H, 4); }
      SB();
      if constexpr (L == 1) ring4(4); else { fill_h(OFF_H, 5); fill_h(OFF_H, 6); }
      u32* img = (u32*)(lds + LDS_SCR);
      u32 ip0, iq0, iq1, xno, tw0, tw1, trd;
      {
        const int ph = tid >> 1, fq0 = ph ? 512 - ph : 256, fq1 = 384 - ph;
        ip0 = ((tid & 1) * 1280 + (ph & 31) * 20 + (ph >> 5)) * 4;
        iq0 = ((tid & 1) * 1280 + (fq0 & 31) * 20 + (fq0 >> 5)) * 4; iq1 = ((tid & 1) * 1280 + (fq1 & 31) * 20 + (fq1 >> 5)) * 4;
        xno = OFF_A + (pos >> 1) * 16384 + (tid >> 3) * 256 + (pos & 1) * 128 + (tid & 7) * 16;
        tw0 = (4 * m + g) * 128 + ((2 * w) ^ (m & 7)) * 16; tw1 = (4 * m + g) * 128 + ((2 * w + 1) ^ (m & 7)) * 16;
        trd = (tid >> 3) * 128 + ((tid & 7) ^ ((tid >> 5) & 7)) * 16;
        PIN(ip0); PIN(iq0); PIN(iq1); PIN(xno); PIN(tw0); PIN(tw1); PIN(trd);
      }
      SB();
      ST(S0 + 4);
      vmwait<(L == 0 ? 5 : 4)>(); PIN(c0); PIN(c1);
      {
        RETRY((stale<L == 1, 1u>(red8<L == 1>(c0, c1))), {
          CNT(3);
          if (bad_) { ld4r<0>(c0, uo, ub0); ld4r<0>(c1, uo, ub1); }
          vmwait<0>(); PIN(c0); PIN(c1);
        });
      }
      ST(S0 + 5);
      {
        u8* d = (u8*)img;
        *(u32*)(d + ip0) = c0.x; *(u32*)(d + iq0) = c0.y; *(u32*)(d + 2560 + ip0) = c0.z; *(u32*)(d + 2560 + iq0) = c0.w;
        *(u32*)(d + ip0 + 16) = c1.x; *(u32*)(d + iq1) = c1.y; *(u32*)(d + 2560 + ip0 + 16) = c1.z; *(u32*)(d + 2560 + iq1) = c1.w;
      }
      bar();
      XF(43);
      const u32x4 a0 = *(const u32x4*)(img + w * 640 + m * 20 + 4 * g), a1 = *(const u32x4*)(img + w * 640 + (16 + m) * 20 + 4 * g);
      if constexpr (L == 1) ring4(5);
      if constexpr (L == 0) ring4(0);
      f32x4 dr0, di0, dr1, di1;
      row_fft<true, 7>(rc, a0, a1, dr0, di0, dr1, di1);
      if constexpr (L == 0) ring4(1);
      XF(44);
      const float ys = 1.0f / 32768.0f;
      xa[0] += lo2(dr0) * ys; xa[1] += hi2(dr0) * ys; xa[2] += lo2(dr1) * ys; xa[3] += hi2(dr1) * ys;
      xb[0] += lo2(di0) * ys; xb[1] += hi2(di0) * ys; xb[2] += lo2(di1) * ys; xb[3] += hi2(di1) * ys;
      const f32x2 sa = (xa[0] + xa[1]) + (xa[2] + xa[3]), sb = (xb[0] + xb[1]) + (xb[2] + xb[3]);
      f32x2 qa = xa[0] * xa[0], qb = xb[0] * xb[0];
#pragma unroll
      for (int i = 1; i < 4; ++i) { qa += xa[i] * xa[i]; qb += xb[i] * xb[i]; }
      if constexpr (L == 0) { SB(); ring4(2); SB(); }
      f32x2 R, O;
      XF(45);
      ln_coef(f32x2{sa.x + sa.y, sb.x + sb.y}, f32x2{qa.x + qa.y, qb.x + qb.y}, ch, R, O);
      XF(46);
      u32x4 na, nb;
#pragma unroll
      for (int i = 0; i < 4; ++i) {
        const f32x2 va = xa[i] * R.x + O.x, vb = xb[i] * R.y + O.y;
        na[i] = mark<0x10000u, 1u - L>(pkb(va.x, va.y)); nb[i] = mark<0x10000u, 1u - L>(pkb(vb.x, vb.y));
      }
      u8* tr = lds + LDS_SCR + 12288;
      *(u32x4*)(tr + tw0) = na; *(u32x4*)(tr + tw1) = nb;
      if constexpr (L == 0) vmwait<12>(); else vmwait<0>();
      bar();
      XF(47);
      st4(*(const u32x4*)(tr + trd), xno);
      st4(*(const u32x4*)(tr + 4096 + trd), xno + 8192);
      SB();
#pragma unroll
      for (int i = 0; i < 4; ++i) { xa[i] += ca; xb[i] += cb; }
      const u32x4 rl = {pkb(xa[0].x, xa[0].y), pkb(xa[1].x, xa[1].y), pkb(xa[2].x, xa[2].y), pkb(xa[3].x, xa[3].y)};
      const u32x4 rh = {pkb(xb[0].x, xb[0].y), pkb(xb[1].x, xb[1].y), pkb(xb[2].x, xb[2].y), pkb(xb[3].x, xb[3].y)};
      if constexpr (L == 0) {
        *(u32x4*)(lds + LDS_STASH + t16) = rl; *(u32x4*)(lds + LDS_STASH + 4096 + t16) = rh;
        XS(44);
        ring4(3); ring4(4); ring4(5); ring4(6); ring4(7);
      } else {
        xl[0] = u32x2{rl.x, rl.y}; xl[1] = u32x2{rl.z, rl.w}; xh[0] = u32x2{rh.x, rh.y}; xh[1] = u32x2{rh.z, rh.w};
        PIN(xl[0]); PIN(xl[1]); PIN(xh[0]); PIN(xh[1]);
        __builtin_amdgcn_s_sleep(XSLP);
      }
    }
    ST(S0 + 6);
    {
      FRESH();
      u32x4 tk[32], he[32], hl[16], w2t[4], w2v[3][4];
      u32x4 hh[8], hf[8];
      f32x4 ac1[6][2], ac2[4][2], ac3[4][2];
      const u8* hs0 = ws + OFF_H + (4 * G + 2 * half2) * 114688 + (4 * w) * 7168;
      const u8* hs[4];
      const u8* hp[4];
#pragma unroll
      for (int j = 0; j < 4; ++j) {
        hs[j] = hs0 + opq((j + ct) & 3) * 7168; hp[j] = hs0 + opq(j ^ (2 * (ct & 1))) * 7168 + 6144;
        PINS(hs[j]); PINS(hp[j]);
      }
      const u8* w2src = ws + OFF_W2 + (ct * 4 + w) * 57344;
      const u8* w2x = w2src + 40960 + 4096;
      u32 lx8 = l16 + 8192;
      PINS(w2x); PIN(lx8);
      u32 hv[4], hw[2];
#pragma unroll
      for (int u = 0; u < 4; ++u) { hv[u] = l16 + ((u ^ w) >> 1) * 114688 + ((u ^ w) & 1) * 3072; PIN(hv[u]); }
#pragma unroll
      for (int q = 0; q < 2; ++q) { hw[q] = l16 + (q ^ (w >> 1)) * 114688; PIN(hw[q]); }
      auto hload = [&](auto RE, auto KC, auto UC) {
        constexpr int k = decltype(KC)::value, u = decltype(UC)::value;
        constexpr bool re = decltype(RE)::value;
        if constexpr (k == 4 || k == 5) {
          if constexpr (re) ld4r<0>(hh[4 * (k - 4) + u], hw[u >> 1], hp[2 * (k - 4) + (u & 1)]); else ld4<0>(hh[4 * (k - 4) + u], hw[u >> 1], hp[2 * (k - 4) + (u & 1)]);
        } else {
          constexpr int j = k < 4 ? k : k < 10 ? k - 6 : k - 10, o = k < 4 ? 0 : k < 10 ? 1024 : 2048;
          u32x4& d = k < 10 ? he[(k < 4 ? 0 : 16) + 4 * j + u] : hl[4 * j + u];
          if constexpr (re) ld4r<o>(d, hv[u], hs[j]); else ld4<o>(d, hv[u], hs[j]);
        }
      };
      auto hseq = [&](auto IC) {
        constexpr int i = decltype(IC)::value;
        hload(std::false_type{}, std::integral_constant<int, (i >> 2)>{}, std::integral_constant<int, i & 3>{});
      };
      u64 bm0 = 0;
      {
        const u8* w1b = ws + OFF_W1 + member * 229376 + 131072 + w * 8192;
        const u8* tk0 = ws + OFF_A + (8 * G + 2 * w) * 16384 + 4096;
        const u8* tk1 = tk0 + 8192;
        u32 tv1 = l16 + 16384, lhi = l16 + 65536;
        u32 hd0 = OFF_H + (4 * G + w) * 114688 + member * 7168 + l16, hd1 = hd0 + 3072;
        PIN(tv1); PIN(lhi); PIN(hd0); PIN(hd1); PINS(tk0); PINS(tk1);
        u32 x3o = OFF_X3 + (8 * pos + 2 * w) * 1024 + xo;
        if constexpr (L == 1) PIN(x3o);
        auto tok = [&](auto RE, auto QC) {
          constexpr int q = decltype(QC)::value, ks = q >> 1;
          if constexpr (decltype(RE)::value) ld4r<(ks & 7) * 1024 - 4096>(tk[q], q & 1 ? tv1 : l16, ks >> 3 ? tk1 : tk0);
          else ld4<(ks & 7) * 1024 - 4096>(tk[q], q & 1 ? tv1 : l16, ks >> 3 ? tk1 : tk0);
        };
        constexpr bool TONE = L == 0;
        constexpr u32 TBIT = L == 0 ? 1u : 0x10000u;
        u32 mx0 = 0, mx1 = 0;
        auto mx0_fin = [&]() { return red2<TONE>(mx0, mx1) & TBIT; };
        auto fix = [&](auto QC, u64 bad) {
          constexpr int q0 = decltype(QC)::value;
          if (__builtin_expect(bad != 0, 0)) {
            _Pragma("nounroll") for (int sp = 0; sp < SPIN_CAP; ++sp) {
              CNT(4);
              static_for<4>([&](auto I) { tok(std::true_type{}, std::integral_constant<int, q0 + decltype(I)::value>{}); });
              vmwait<0>();
#pragma unroll
              for (int q = q0; q < q0 + 4; ++q) PIN(tk[q]);
              if (!__any(stale<TONE, TBIT>(red2<TONE>(red8<TONE>(tk[q0], tk[q0 + 1]), red8<TONE>(tk[q0 + 2], tk[q0 + 3]))))) break;
            }
          }
        };
        constexpr int PD = 8;
        u32x4 af[PD];
        auto frag = [&](int x) {
          const int o = (x & 127) * 1024;
          return o < 65536 ? *(const u32x4*)(lds + l16 + o) : *(const u32x4*)(lds + lhi + (o - 65536));
        };
        if constexpr (L == 1) {
#pragma unroll
          for (int x = 0; x < PD - 1; ++x) af[x] = frag(x);
          SB();
        }
        XF(48);
        static_for<NTOK0>([&](auto Q) {
          tok(std::false_type{}, Q);
          if constexpr (decltype(Q)::value == 3) bar_nw();
        });
        XF(49);
        if constexpr (L == 0) {
          vmwait<NTOK0>();
          bar_nw();
#pragma unroll
          for (int x = 0; x < PD - 1; ++x) af[x] = frag(x);
        }
        vmwait<NTOK0 - 4>();
        ST(S0 + 7);
#pragma unroll
        for (int q = 0; q < 4; ++q) PIN(tk[q]);
        mx0 = red8<TONE>(tk[0], tk[1]); mx1 = red8<TONE>(tk[2], tk[3]);
        fix(std::integral_constant<int, 0>{}, __ballot(stale<TONE, TBIT>(red2<TONE>(mx0, mx1))));
        SB();
        ST(S0 + 8);
        float eg[2][2], eu[2][2];
        u32 ev[2];
        u64 bm = 0;
        u32x4 eo[1];
        auto epi = [&](auto NC) {
          constexpr int n = decltype(NC)::value;
          if constexpr (n < NEPI1 + NEPI2) {
            constexpr bool p1 = n < NEPI1;
            constexpr int i = p1 ? n - (n > 40) - (n > 81) : (n - NEPI1) - (n - NEPI1) / 41;
            if constexpr (p1 && (n == 40 || n == 81)) st4(eo[0], n == 40 ? hd0 : hd1);
            else if constexpr (p1 && n == 122) st4(eo[0], hd1 + 3072);
            else if constexpr (!p1 && (n - NEPI1) % 41 == 40) st4(eo[0], ((n - NEPI1) / 41 ? hd1 : hd0) + 1024);
            else {
              constexpr int s = i & 1, st = (i % 20) >> 1, blk = i / 20;
              constexpr int u = p1 ? (blk < 4 ? blk >> 1 : blk - 4) : blk >> 1, pr = p1 ? (blk < 4 ? blk & 1 : 2) : blk & 1;
              constexpr int ei = 0, d = 2 * (pr == 2 ? u : pr) + s;
              const f32x4& ga = p1 ? ac1[2 * pr][u] : ac2[2 * (pr & 1)][u];
              const f32x4& up = p1 ? ac1[2 * pr + 1][u] : ac2[2 * (pr & 1) + 1][u];
              if constexpr (st < 2) { eg[s][st] = ga[2 * s + st]; PIN(eg[s][st]); }
              else if constexpr (st < 4) { eu[s][st - 2] = up[2 * s + st - 2]; PIN(eu[s][st - 2]); }
              else if constexpr (st < 6) { eg[s][st - 4] = relu(eg[s][st - 4]); PIN(eg[s][st - 4]); }
              else if constexpr (st < 8) { eg[s][st - 6] *= eu[s][st - 6]; PIN(eg[s][st - 6]); }
              else if constexpr (st == 8) { ev[s] = pkb(eg[s][0], eg[s][1]); PIN(ev[s]); }
              else { u32 v = mark<1u - L, L>(ev[s]); PIN(v); eo[ei][d] = v; }
            }
            SB();
          }
        };
        static_for<224>([&](auto XC) {
          constexpr int x = decltype(XC)::value, job = G1S[L].job[x], ji = G1S[L].idx[x], e0 = G1S[L].epi[x], e1 = G1S[L].epi[x + 1];
          if constexpr (x == 32 || x == 64) bar_nw();
          if constexpr (x == 97) { vmwait<g1_wait(L, x, 2, 7)>(); bar_nw(); }
          if constexpr (x == 150) { vmwait<g1_wait(L, x, 2, 23)>(); bar_nw(); }
          constexpr int cj = (x + 12) / 12, co = x + 12 - 12 * cj;
          constexpr bool chk = x < 84 && co >= 5;
          constexpr int ks = x < 96 ? x / 6 : x < 160 ? (x - 96) >> 2 : (x - 160) >> 2, t = x < 96 ? x % 6 : x & 3;
          auto mm = [&](int u) {
            f32x4& a = x < 96 ? ac1[x < 96 ? t : 0][u] : x < 160 ? ac2[x < 96 ? 0 : t][u] : ac3[x < 96 ? 0 : t][u];
            if constexpr (ks == 0) a = mfma_b(af[x % PD], tk[u], f32x4{0.f, 0.f, 0.f, 0.f}); else a = mfma_b(af[x % PD], tk[2 * ks + u], a);
          };
          SB();
          mm(0);
          SB();
          if constexpr (x < 223) __builtin_amdgcn_s_waitcnt(0xC07F | ((x + PD - 2 < 224 ? PD - 3 : 222 - x) << 8));
          if constexpr (chk && co == 5) {
            vmwait<g1_wait(L, x, 1, 4 * cj + 3)>();
            PIN(tk[4 * cj]); PIN(tk[4 * cj + 1]); PIN(tk[4 * cj + 2]); PIN(tk[4 * cj + 3]);
          }
          SB();
          if constexpr (chk && co >= 6 && co < 10) {
            const u32x4 v = tk[4 * cj + co - 6];
            mx0 = co == 6 ? red2<TONE>(v.x, v.y) : red3<TONE>(mx0, v.x, v.y); PIN(mx0); SB();
            mx1 = co == 6 ? red2<TONE>(v.z, v.w) : red3<TONE>(mx1, v.z, v.w); PIN(mx1);
          }
          if constexpr (chk && co == 10) { mx0 = mx0_fin(); PIN(mx0); SB(); bm = __ballot(TONE ? mx0 == 0 : mx0 != 0); PINS(bm); }
          if constexpr (x >= 212 && x < 218) {
            if constexpr (x == 212) { vmwait<g1_wait(L, x, 4, 3)>(); PIN(he[0]); PIN(he[1]); PIN(he[2]); PIN(he[3]); }
            else if constexpr (x < 217) {
              const u32x4 v = he[x - 213];
              mx0 = x == 213 ? red2<L == 1>(v.x, v.y) : red3<L == 1>(mx0, v.x, v.y); PIN(mx0); SB();
              mx1 = x == 213 ? red2<L == 1>(v.z, v.w) : red3<L == 1>(mx1, v.z, v.w); PIN(mx1);
            } else { mx0 = red2<L == 1>(mx0, mx1) & 1u; PIN(mx0); SB(); bm0 = __ballot(L == 1 ? mx0 == 0 : mx0 != 0); PINS(bm0); }
          }
          if constexpr (e0 < e1) epi(std::integral_constant<int, e0>{});
          if constexpr (e0 + 1 < e1) epi(std::integral_constant<int, e0 + 1>{});
          SB();
          mm(1);
          SB();
          if constexpr (chk && co == 11) fix(std::integral_constant<int, 4 * cj>{}, bm);
          if constexpr (job == 2 && (ji & 3) == 0) set_m0(ldsbase + (ji >> 3) * 32768 + w * 8192 + (ji & 4) * 1024);
          SB();
          if constexpr (x + PD - 1 < 224) af[(x + PD - 1) % PD] = frag(x + PD - 1);
          SB();
          if constexpr (job == 1) tok(std::false_type{}, std::integral_constant<int, ji>{});
          if constexpr (job == 2) dma1<(ji & 3) * 1024>(l16, w1b + (ji >> 3) * 32768 + (ji & 4) * 1024);
          if constexpr (job == 3) ld4a<(ji & 7) * 1024 - 4096>(w2a[ji >> 2][ji & 3], l16, w2src + (ji >> 3) * 8192 + 4096);
          if constexpr (job == 5) {
            if constexpr (ji >= 12) ld4a<(ji - 12) * 1024 - 4096>(w2t[ji - 12], l16, w2x);
            else ld4<((ji + 4) & 7) * 1024 - 4096>(w2v[ji >> 2][ji & 3], ji < 4 ? l16 : lx8, w2x);
          }
          if constexpr (job == 6) st2(ji & 2 ? xh[ji & 1] : xl[ji & 1], x3o + ji * 512);
          if constexpr (job == 4) hseq(std::integral_constant<int, ji>{});
          if constexpr (e0 + 2 < e1) epi(std::integral_constant<int, e0 + 2>{});
          SB();
        });
        ST(S0 + 9);
#pragma unroll
        for (int u = 0; u < 2; ++u) {
          u32x4 o;
#pragma unroll
          for (int p = 0; p < 2; ++p) {
            const f32x4 ga = ac3[2 * p][u], up = ac3[2 * p + 1][u];
            const f32x2 a = f32x2{relu(ga[0]), relu(ga[1])} * lo2(up), c = f32x2{relu(ga[2]), relu(ga[3])} * hi2(up);
            o[2 * p] = mark<1u - L, L>(pkb(a.x, a.y)); o[2 * p + 1] = mark<1u - L, L>(pkb(c.x, c.y));
          }
          st4(o, (u ? hd1 : hd0) + 2048);
        }
        SB();
      }
      ST(S0 + 10);
      {
        f32x4 acc[4][4];
        u32x4 x3a, x3b;
        u32 eo = 0, oo = 0, rw[3], ta = 0, tb = 0;
        auto addr = [&](auto IC) {
          constexpr int i = decltype(IC)::value;
          const u32 sb = (128 * G + 64 * half2 + 16 * w) * 1024 + ct * 128;
          if constexpr (i < 3) { rw[i] = l16 + (((i + 1) ^ w) * 3 + i) * 4096; PIN(rw[i]); }
          if constexpr (i == 3) { ta = (l16 >> 4) & 0x30; PIN(ta); }
          if constexpr (i == 4) { tb = l16 & (L == 1 ? 0xf0 : 0xe0); PIN(tb); }
          if constexpr (i == 5) { eo = tb * 64 + sb; PIN(eo); }
          if constexpr (i == 6) { tb = l16 & 0x10; PIN(tb); }
          if constexpr (i == 7) { tb = tb * 4 + ta; PIN(tb); }
          if constexpr (i == 8) { eo += L == 1 ? ta : tb; PIN(eo); }
          if constexpr (L == 1) {
            if constexpr (i == 9) { ta = l16 & 0x20; PIN(ta); }
            if constexpr (i == 10) { oo = ta * 1024 + tb; PIN(oo); }
            if constexpr (i == 11) { ta = l16 & 0x80; PIN(ta); }
            if constexpr (i == 12) { oo += ta * 16; PIN(oo); }
            if constexpr (i == 13) { ta = l16 & 0x40; PIN(ta); }
            if constexpr (i == 14) { oo += ta * 16; PIN(oo); }
            if constexpr (i == 15) { oo += row_of_pos(16 * G + 8 * half2 + 2 * w, 0) * 1024 + ct * 128; PIN(oo); }
          }
        };
        const u8* x3s = ws + OFF_X3;
        u64 bm = 0;
        PINS(x3s);
        constexpr int LB = 40 - NEARLY;
        auto cum = [](int n) constexpr { const int c = n - n / 8; return c < LB + 16 ? c : LB + 16; };
        auto young = [cum](int k, int n) constexpr {
          const int i = 4 * k + 3;
          if (k < 10 && i < NEARLY) return cum(n) + 2 + G1S[L].cum[224] - G1S[L].pos[4][i] - 1;
          if (k < 10) return cum(n) - (i - NEARLY + 1);
          return cum(n) - (LB + 4 + 3 * (k - 10));
        };
        constexpr bool HONE = L == 1;
        u32 mx0 = 0, mx1 = 0;
        auto mx0_fin = [&]() { return red2<HONE>(mx0, mx1) & 1u; };
        auto pin_k = [&](auto KC) {
          constexpr int k = decltype(KC)::value;
#pragma unroll
          for (int u = 0; u < 4; ++u) {
            if constexpr (k < 4) PIN(he[4 * k + u]);
            else if constexpr (k < 6) PIN(hh[4 * (k - 4) + u]);
            else if constexpr (k < 10) PIN(he[16 + 4 * (k - 6) + u]);
            else PIN(hl[4 * (k - 10) + u]);
          }
        };
        auto opnd = [&](auto KC, int u) {
          constexpr int k = decltype(KC)::value;
          if constexpr (k < 4) return he[4 * k + u];
          else if constexpr (k < 6) return hh[4 * (k - 4) + u];
          else if constexpr (k < 10) return he[16 + 4 * (k - 6) + u];
          else return hl[4 * (k - 10) + u];
        };
        u32 wodd = (w & 1) ? ~0u : 0u;
        PIN(wodd);
        auto mkhalf = [&](auto KC, auto IC) {
          constexpr int k = decltype(KC)::value, i = decltype(IC)::value, u = i >> 2, d = i & 3;
          const u32x4& src = hh[4 * (k - 4) + (u & 2) + (d >> 1)];
          u32 v = (src[2 * ((u & 1) ^ 1) + (d & 1)] & wodd) | (src[2 * (u & 1) + (d & 1)] & ~wodd);
          PIN(v);
          hf[4 * (k - 4) + u][d] = v;
        };
        auto fixk = [&](auto KC, u64 bad) {
          constexpr int k = decltype(KC)::value;
          if (__builtin_expect(bad != 0, 0)) {
            _Pragma("nounroll") for (int sp = 0; sp < SPIN_CAP; ++sp) {
              CNT(k < 10 ? 5 : 6);
              static_for<4>([&](auto U) { hload(std::true_type{}, KC, U); });
              vmwait<0>();
              pin_k(KC);
              if (!__any(stale<HONE, 1u>(red2<HONE>(red8<HONE>(opnd(KC, 0), opnd(KC, 1)), red8<HONE>(opnd(KC, 2), opnd(KC, 3)))))) break;
            }
            if constexpr (k == 4 || k == 5) static_for<16>([&](auto I) { mkhalf(KC, I); });
          }
        };
        if constexpr (L == 0) {
#pragma unroll
          for (int k = 0; k < 10; ++k) { PINA(w2a[k][0]); PINA(w2a[k][1]); PINA(w2a[k][2]); PINA(w2a[k][3]); }
        }
#pragma unroll
        for (int c = 0; c < 4; ++c) { PINA(w2t[c]); PIN(w2v[0][c]); PIN(w2v[1][c]); PIN(w2v[2][c]); }
        fixk(std::integral_constant<int, 0>{}, bm0);
        auto qchk = [&](auto QC, auto JC) {
          constexpr int qd = decltype(QC)::value, j = decltype(JC)::value, u = (qd + 1) & 3;
          if constexpr (j == 0) { PIN(hl[4 + u]); PIN(hl[8 + u]); PIN(hl[12 + u]); }
          else if constexpr (j < 4) {
            const u32x4 v = hl[4 * j + u];
            mx0 = j == 1 ? red2<HONE>(v.x, v.y) : red3<HONE>(mx0, v.x, v.y); PIN(mx0); SB();
            mx1 = j == 1 ? red2<HONE>(v.z, v.w) : red3<HONE>(mx1, v.z, v.w); PIN(mx1);
          } else if constexpr (j == 4) { mx0 = mx0_fin(); PIN(mx0); SB(); bm = __ballot(HONE ? mx0 == 0 : mx0 != 0); PINS(bm); }
          else if (__builtin_expect(bm != 0, 0)) {
            _Pragma("nounroll") for (int sp = 0; sp < SPIN_CAP; ++sp) {
              CNT(6);
              static_for<3>([&](auto K) { hload(std::true_type{}, std::integral_constant<int, 11 + decltype(K)::value>{}, std::integral_constant<int, u>{}); });
              vmwait<0>();
              PIN(hl[4 + u]); PIN(hl[8 + u]); PIN(hl[12 + u]);
              if (!__any(stale<HONE, 1u>(red4<HONE>(red8<HONE>(hl[4 + u], hl[8 + u]), hl[12 + u])))) break;
            }
          }
        };
        ST(S0 + 11);
        auto wfr = [&](auto KC, int c) {
          constexpr int k = decltype(KC)::value;
          if constexpr (k < 10) return w2a[k][c]; else if constexpr (k == 10) return w2t[c]; else return w2v[k - 11][c];
        };
        static_for<44>([&](auto NC) {
          constexpr int n = decltype(NC)::value, k = n >> 2, c = n & 3;
          using KC = std::integral_constant<int, k>;
          using KN = std::integral_constant<int, k + 1>;
          static_for<4>([&](auto UC) {
            constexpr int u = decltype(UC)::value;
            SB();
            if constexpr (k == 0) acc[c][u] = mfma_b(wfr(KC{}, c), opnd(KC{}, u), f32x4{0.f, 0.f, 0.f, 0.f});
            else if constexpr (k == 4 || k == 5) acc[c][u] = mfma_b(wfr(KC{}, c), hf[4 * (k - 4) + u], acc[c][u]);
            else acc[c][u] = mfma_b(wfr(KC{}, c), opnd(KC{}, u), acc[c][u]);
            SB();
            constexpr bool hk = k == 3 || k == 4;
            constexpr int j = u == 3 ? -9 : 3 * c + u - (hk ? 1 : 5);
            if constexpr (k == 10) {
              if constexpr (j == -1) vmwait<clamp63(young(11, n))>();
              if constexpr (j >= -1 && j < 4) qchk(std::integral_constant<int, 0>{}, std::integral_constant<int, j + 1>{});
            }
            if constexpr (k < 10 && j == -1) { vmwait<clamp63(young(k + 1, n))>(); pin_k(KN{}); }
            if constexpr (k < 10 && j >= 0 && j < 4) {
              const u32x4 v = opnd(KN{}, j);
              mx0 = j == 0 ? red2<HONE>(v.x, v.y) : red3<HONE>(mx0, v.x, v.y); PIN(mx0); SB();
              mx1 = j == 0 ? red2<HONE>(v.z, v.w) : red3<HONE>(mx1, v.z, v.w); PIN(mx1);
            }
            if constexpr (k >= 6 && k < 10 && c == 0 && u < 2) {
              addr(std::integral_constant<int, 4 * (k - 6) + 2 * u>{}); SB(); addr(std::integral_constant<int, 4 * (k - 6) + 2 * u + 1>{});
            }
            if constexpr (hk && j >= 4 && j != 5) {
              constexpr int i0 = j == 4 ? 0 : 2 + 3 * (j - 6);
              mkhalf(KN{}, std::integral_constant<int, i0>{}); SB(); mkhalf(KN{}, std::integral_constant<int, i0 + 1>{});
              if constexpr (j >= 6 && i0 + 2 < 16) { SB(); mkhalf(KN{}, std::integral_constant<int, i0 + 2>{}); }
            }
            if constexpr (k < 10 && j == 5) { mx0 = mx0_fin(); PIN(mx0); SB(); bm = __ballot(HONE ? mx0 == 0 : mx0 != 0); PINS(bm); }
            if constexpr (u == 3) {
              constexpr int q = cum(n);
              if constexpr (cum(n + 1) == q) {}
              else if constexpr (q < LB) hseq(std::integral_constant<int, NEARLY + q>{});
              else if constexpr (q < LB + 4) hload(std::false_type{}, std::integral_constant<int, 10>{}, std::integral_constant<int, q - LB>{});
              else hload(std::false_type{}, std::integral_constant<int, 11 + (q - LB - 4) % 3>{}, std::integral_constant<int, ((q - LB - 4) / 3 + 1) & 3>{});
              if constexpr (n == 23) {
                bar_nw();
              }
              if constexpr (c == 3) {
                if constexpr (k < 10) fixk(KN{}, bm); else qchk(std::integral_constant<int, 0>{}, std::integral_constant<int, 5>{});
              }
            }
          });
        });
        if constexpr (L == 1) { ld4<0>(x3a, eo, x3s); ld4<64>(x3b, eo, x3s); }
        static_for<16>([&](auto TC) {
          constexpr int t = decltype(TC)::value, u = ((t >> 2) + 1) & 3, c = t & 3;
          using QN = std::integral_constant<int, (t >> 2) + 1>;
          SB();
          acc[c][u] = mfma_b(w2v[0][c], hl[4 + u], acc[c][u]);
          SB();
          if constexpr (t < 12) {
            if constexpr (c == 0) { vmwait<young(12 + (t >> 2), 44) + (L == 1 ? 2 : 0)>(); qchk(QN{}, std::integral_constant<int, 0>{}); }
            else qchk(QN{}, std::integral_constant<int, c>{});
          }
          SB();
          acc[c][u] = mfma_b(w2v[1][c], hl[8 + u], acc[c][u]);
          SB();
          if constexpr (t >= 3 && t < 15) *(f32x4*)(lds + rw[(t - 3) >> 2] + ((t - 3) & 3) * 1024) = acc[(t - 3) & 3][((t - 3) >> 2) + 1];
          SB();
          acc[c][u] = mfma_b(w2v[2][c], hl[12 + u], acc[c][u]);
          SB();
          if constexpr (t < 12 && c == 3) { qchk(QN{}, std::integral_constant<int, 4>{}); qchk(QN{}, std::integral_constant<int, 5>{}); }
        });
        SB();
        ST(S0 + 12);
        bar();
        f32x4 s[3][4];
#pragma unroll
        for (int c = 0; c < 4; ++c)
#pragma unroll
          for (int p = 0; p < 3; ++p) s[p][c] = *(const f32x4*)(lds + w * 12288 + p * 4096 + c * 1024 + l16);
        SB();
        if constexpr (L == 1) { vmwait<0>(); PIN(x3a); PIN(x3b); }
        u32x4 y[2];
#pragma unroll
        for (int a = 0; a < 2; ++a) {
          const f32x4 s0 = (acc[2 * a][0] + s[0][2 * a]) + (s[1][2 * a] + s[2][2 * a]);
          const f32x4 s1 = (acc[2 * a + 1][0] + s[0][2 * a + 1]) + (s[1][2 * a + 1] + s[2][2 * a + 1]);
          y[a] = u32x4{pkb(s0[0], s0[1]), pkb(s0[2], s0[3]), pkb(s1[0], s1[1]), pkb(s1[2], s1[3])};
          if constexpr (L == 1) {
            const u32x4 xr = a ? x3b : x3a;
#pragma unroll
            for (int p = 0; p < 4; ++p) y[a][p] = pkb(blo(xr[p]) + blo(y[a][p]), bhi(xr[p]) + bhi(y[a][p]));
          }
        }
        {
          const bool odd = m & 1;
          u32x4 z0, z1;
#pragma unroll
          for (int p = 0; p < 4; ++p) {
            const u32 t = (u32)__builtin_amdgcn_mov_dpp((int)(odd ? y[0][p] : y[1][p]), 0xB1, 0xf, 0xf, true);
            z0[p] = odd ? t : y[0][p]; z1[p] = odd ? y[1][p] : t;
          }
          if constexpr (L == 0) { st4(mark4<0x10001u, 0u>(z0), OFF_A + eo); st4(mark4<0x10001u, 0u>(z1), OFF_A + eo + 1024); }
          else {
            const __amdgpu_buffer_rsrc_t ro = __builtin_amdgcn_make_buffer_rsrc(P.out, 0, 2048 * 1024, 0x00020000);
            __builtin_amdgcn_raw_buffer_store_b128(z0, ro, oo, 0, 16); __builtin_amdgcn_raw_buffer_store_b128(z1, ro, oo + 1024 * 1024, 0, 16);
          }
        }
      }
    }
    ST(S0 + 13);
    if constexpr (L == 0) {
      FRESH();
      SB();
      row_consts<false, 0>(rc, m, g);
      SB();
      u32x2 d0, d1, d2, d3;
      const u8* db = ws + OFF_A + (8 * pos + 2 * w) * 1024;
      const u32x4 sl = *(const u32x4*)(lds + LDS_STASH + t16), sh = *(const u32x4*)(lds + LDS_STASH + 4096 + t16);
#pragma unroll
      for (int i = 0; i < 4; ++i) {
        xv[2 * i] = f32x2{blo(sl[i]), blo(sh[i])}; xv[2 * i + 1] = f32x2{bhi(sl[i]), bhi(sh[i])};
      }
      xc = f32x2{first_lane(xv[0].x), first_lane(xv[0].y)};
      xch = lane < 32 ? xc.x : xc.y;
      PIN(xch);
#pragma unroll
      for (int i = 0; i < 8; ++i) { xv[i] -= xc; PIN(xv[i]); }
      SB();
      bar_nw();
      ld2<0>(d0, xo, db); ld2<512>(d1, xo, db); ld2<1024>(d2, xo, db); ld2<1536>(d3, xo, db);
      ring4(0);
      SB();
      row_consts<false, 1>(rc, m, g);
      SB();
      fwd_pre(fp, tid, w, pos);
      SB();
      ST(S0 + 14);
      vmwait<4>(); PIN(d0); PIN(d1); PIN(d2); PIN(d3);
      RETRY((stale<false, 1u>(red8<false>(u32x4{d0.x, d0.y, d1.x, d1.y}, u32x4{d2.x, d2.y, d3.x, d3.y}))), {
        CNT(7);
        if (bad_) { ld2r<0>(d0, xo, db); ld2r<512>(d1, xo, db); ld2r<1024>(d2, xo, db); ld2r<1536>(d3, xo, db); }
        vmwait<0>(); PIN(d0); PIN(d1); PIN(d2); PIN(d3);
      });
      xv[0] += f32x2{blo(d0.x), blo(d2.x)}; xv[1] += f32x2{bhi(d0.x), bhi(d2.x)}; xv[2] += f32x2{blo(d0.y), blo(d2.y)}; xv[3] += f32x2{bhi(d0.y), bhi(d2.y)};
      xv[4] += f32x2{blo(d1.x), blo(d3.x)}; xv[5] += f32x2{bhi(d1.x), bhi(d3.x)}; xv[6] += f32x2{blo(d1.y), blo(d3.y)}; xv[7] += f32x2{bhi(d1.y), bhi(d3.y)};
      ST(S0 + 15);
    }
  };
  layer(std::integral_constant<int, 0>{});
  layer(std::integral_constant<int, 1>{});
  if (blockIdx.x == 0 && threadIdx.x == 0) st2(ep, OFF_E);
  ST_OUT();
}

extern "C" void kernel_launch(void* const* d_in, const int* in_sizes, int n_in, void* d_out, int out_size, void* d_ws, size_t ws_size,
                              hipStream_t stream) {
  Params p;
  p.x = (const u16*)d_in[0]; p.wr = (const u16*)d_in[1]; p.wi = (const u16*)d_in[2];
  p.wg = (const u16*)d_in[4]; p.wu = (const u16*)d_in[5]; p.wd = (const u16*)d_in[6];
  p.out = (u16*)d_out; p.ws = (u8*)d_ws;
  (void)hipFuncSetAttribute((const void*)k_all, hipFuncAttributeMaxDynamicSharedMemorySize, LDS_TOTAL);
  k_all<<<256, 256, LDS_TOTAL, stream>>>(p);
}
```

```cpp
#include <hip/hip_runtime.h>
#include <hip/hip_bf16.h>
#include <stdint.h>
#include <type_traits>
#include <utility>

typedef unsigned char u8;
typedef unsigned short u16;
typedef unsigned int u32;
typedef unsigned long long u64;
typedef u32 u32x2 __attribute__((ext_vector_type(2)));
typedef u32 u32x4 __attribute__((ext_vector_type(4)));
typedef float f32x2 __attribute__((ext_vector_type(2)));
typedef float f32x4 __attribute__((ext_vector_type(4)));
typedef _Float16 f16x2 __attribute__((ext_vector_type(2)));
typedef __bf16 bf16x2 __attribute__((ext_vector_type(2)));
typedef __bf16 bf16x8 __attribute__((ext_vector_type(8)));
#define DI __device__ __forceinline__
#define SB() __builtin_amdgcn_sched_barrier(0)
#define MEMFENCE() asm volatile("" ::: "memory")

#define XSLP 4
#define BMAP(x) (x)
#define XE0 168
#define XE1 157
constexpr u32 MB2 = 2u << 20;
constexpr u32 OFF_E = 0, OFF_FLAG = 8192, OFF_WNY = 65536;
constexpr u32 OFF_A = MB2;
constexpr u32 OFF_U = OFF_A + MB2;
constexpr u32 OFF_WX = OFF_U + MB2;
constexpr u32 OFF_X3 = OFF_WX + MB2;
constexpr u32 OFF_H = OFF_X3 + MB2;
constexpr u32 OFF_W1 = OFF_H + 7340032;
constexpr u32 OFF_W2 = OFF_W1 + 3670016;
constexpr u32 WS_USED = OFF_W2 + 1835008;
constexpr u32 SENT = 0xFFFFFFFFu;
constexpr int SPIN_CAP = 1 << 15;

constexpr int LDS_SCR = 131072, LDS_STASH = LDS_SCR + 24576, LDS_TOTAL = 163840;

struct Params {
  const u16* x; const u16* wr; const u16* wi; const u16* wg; const u16* wu; const u16* wd; u16* out; u8* ws;
};

#define ST(i)
#define ST_DECL
#define CNT(i)
#define ST_OUT()

#define XS(i)
#define XF(i)
DI u32 pkh(float a, float b) { f32x2 v = {a, b}; return __builtin_bit_cast(u32, __builtin_convertvector(v, f16x2)); }
DI u32 pkb(float a, float b) { f32x2 v = {a, b}; return __builtin_bit_cast(u32, __builtin_convertvector(v, bf16x2)); }
template <bool BF> DI u32 pkc(float a, float b) { return BF ? pkb(a, b) : pkh(a, b); }
DI u32 pkh_n0(float a, float b, u32 after) { u32 r; asm("v_cvt_pk_f16_f32 %0, -%1, %2" : "=v"(r) : "v"(a), "v"(b), "v"(after)); return r; }
DI u32 pkb_n1(float a, float b, u32 after) { u32 r; asm("v_cvt_pk_bf16_f32 %0, %1, -%2" : "=v"(r) : "v"(a), "v"(b), "v"(after)); return r; }
DI float blo(u32 u) { return __uint_as_float(u << 16); }
DI float bhi(u32 u) { return __uint_as_float(u & 0xffff0000u); }
DI float bfr(float a) { return blo(pkb(a, a)); }
DI float relu(float a) { const int i = __float_as_int(a); return __int_as_float(i > 0 ? i : 0); }
DI u32 umax(u32 a, u32 b) { return a > b ? a : b; }
DI u32 max3(u32 a, u32 b, u32 c) { return umax(umax(a, b), c); }
DI u32 max4(u32 acc, u32x4 v) { return max3(max3(acc, v.x, v.y), v.z, v.w); }
template <bool ONE> DI u32 red3(u32 a, u32 b, u32 c) { return ONE ? (a & b & c) : (a | b | c); }
template <bool ONE> DI u32 red2(u32 a, u32 b) { return ONE ? (a & b) : (a | b); }
template <bool ONE> DI u32 red4(u32 acc, u32x4 v) { return red3<ONE>(red3<ONE>(acc, v.x, v.y), v.z, v.w); }
template <bool ONE> DI u32 red8(u32x4 a, u32x4 b) { return red4<ONE>(red3<ONE>(red2<ONE>(a.x, a.y), a.z, a.w), b); }
template <bool ONE, u32 BIT> DI bool stale(u32 r) { return ONE ? (r & BIT) == 0 : (r & BIT) != 0; }
template <u32 CLR, u32 SET> DI u32 mark(u32 v) { return (v & ~CLR) | SET; }
template <u32 CLR, u32 SET> DI u32x4 mark4(u32x4 v) { return u32x4{mark<CLR, SET>(v.x), mark<CLR, SET>(v.y), mark<CLR, SET>(v.z), mark<CLR, SET>(v.w)}; }
DI f32x2 lo2(f32x4 v) { return __builtin_shufflevector(v, v, 0, 1); }
DI f32x2 hi2(f32x4 v) { return __builtin_shufflevector(v, v, 2, 3); }
DI f32x2 swp(f32x2 v) { return __builtin_shufflevector(v, v, 1, 0); }
DI f32x4 mfma_b(u32x4 a, u32x4 b, f32x4 c) {
  return __builtin_amdgcn_mfma_f32_16x16x32_bf16(__builtin_bit_cast(bf16x8, a), __builtin_bit_cast(bf16x8, b), c, 0, 0, 0);
}
template <class T> DI void PIN(T& x) { asm volatile("" : "+v"(x)); }
template <class T> DI void PINS(T& x) { asm volatile("" : "+s"(x)); }
DI int opq(int x) { asm("" : "+s"(x)); return x; }
template <class T> DI void PINA(T& x) { asm volatile("" : "+a"(x)); }
template <class F, int... I> DI void static_for_(F&& f, std::integer_sequence<int, I...>) { (f(std::integral_constant<int, I>{}), ...); }
template <int N, class F> DI void static_for(F&& f) { static_for_(f, std::make_integer_sequence<int, N>{}); }

#define MM_(T) "v_mfma_f32_16x16x32_" T
template <bool BF> DI void mm4(f32x4& d0, f32x4& d1, f32x4& d2, f32x4& d3, u32x4 a0, u32x4 a1, u32x4 b0, u32x4 b1) {
#define MM4_(T) asm volatile("s_nop 1\n\t" MM_(T) " %0, %4, %6, 0\n\t" MM_(T) " %1, %4, %7, 0\n\t" MM_(T) " %2, %5, %6, 0\n\t" MM_(T) " %3, %5, %7, 0\n\ts_nop 5" \
               : "=&v"(d0), "=&v"(d1), "=&v"(d2), "=&v"(d3) : "v"(a0), "v"(a1), "v"(b0), "v"(b1))
  if constexpr (BF) MM4_("bf16"); else MM4_("f16");
}
template <bool BF, int TAIL, int AG = 0> DI void mm8(f32x4& d0, f32x4& d1, f32x4& d2, f32x4& d3, u32x4 a00, u32x4 b00, u32x4 a01, u32x4 b01, u32x4 a10,
                               u32x4 b10, u32x4 a11, u32x4 b11, u32x4 a20, u32x4 b20, u32x4 a21, u32x4 b21, u32x4 a30, u32x4 b30, u32x4 a31, u32x4 b31) {
#define MM8_(T, A, B) asm volatile("s_nop 1\n\t" MM_(T) " %0, %4, %5, 0\n\t" MM_(T) " %1, %8, %9, 0\n\t" MM_(T) " %0, %6, %7, %0\n\t" MM_(T) " %1, %10, %11, %1\n\t" \
               MM_(T) " %2, %12, %13, 0\n\t" MM_(T) " %3, %16, %17, 0\n\t" MM_(T) " %2, %14, %15, %2\n\t" MM_(T) " %3, %18, %19, %3\n\ts_nop %20"         \
               : "=&v"(d0), "=&v"(d1), "=&v"(d2), "=&v"(d3)                                                                                              \
               : A(a00), B(b00), A(a01), B(b01), A(a10), B(b10), A(a11), B(b11), A(a20), B(b20), A(a21), B(b21), A(a30), B(b30), A(a31), B(b31), "i"(TAIL))
  if constexpr (!BF) MM8_("f16", "v", "v");
  else if constexpr (AG == 0) MM8_("bf16", "v", "v");
  else if constexpr (AG == 1) MM8_("bf16", "a", "v");
  else MM8_("bf16", "v", "a");
}
template <bool BF, int AG> DI void mf0(f32x4& d, u32x4 a, u32x4 b) {
  if constexpr (!BF) asm volatile(MM_("f16") " %0, %1, %2, 0" : "=&v"(d) : "v"(a), "v"(b));
  else if constexpr (AG == 0) asm volatile(MM_("bf16") " %0, %1, %2, 0" : "=&v"(d) : "v"(a), "v"(b));
  else asm volatile(MM_("bf16") " %0, %1, %2, 0" : "=&v"(d) : "v"(a), "a"(b));
}
template <bool FIRST> DI void mmw(f32x4& d, const u32x4& a, const u32x4& b) {
  if constexpr (FIRST) asm volatile(MM_("bf16") " %0, %1, %2, 0" : "=&v"(d) : "a"(a), "v"(b));
  else asm volatile(MM_("bf16") " %0, %1, %2, %0" : "+v"(d) : "a"(a), "v"(b));
}
template <int O> DI void lda(u32x4& d, u32 addr) { asm volatile("ds_read_b128 %0, %1 offset:%2" : "=a"(d) : "v"(addr), "i"(O) : "memory"); }
template <int N> DI void lgwait() { asm volatile("s_waitcnt lgkmcnt(%0)" ::"i"(N) : "memory"); }
template <bool BF, int AG, int TAIL> DI void mf4(f32x4& d0, f32x4& d1, f32x4& d2, f32x4& d3, u32x4 a0, u32x4 b0, u32x4 a1, u32x4 b1, u32x4 a2,
                                               u32x4 b2, u32x4 a3, u32x4 b3) {
#define MF4_(T, B) asm volatile("s_nop 1\n\t" MM_(T) " %0, %4, %5, %0\n\t" MM_(T) " %1, %6, %7, %1\n\t" MM_(T) " %2, %8, %9, %2\n\t" MM_(T) " %3, %10, %11, %3\n\ts_nop %12" \
               : "+v"(d0), "+v"(d1), "+v"(d2), "+v"(d3) : "v"(a0), B(b0), "v"(a1), B(b1), "v"(a2), B(b2), "v"(a3), B(b3), "i"(TAIL))
  if constexpr (!BF) MF4_("f16", "v"); else if constexpr (AG == 0) MF4_("bf16", "v"); else MF4_("bf16", "a");
}
template <bool BF, bool CONJ, class MM> DI u32x4 cplx_under(f32x4 r, f32x4 i, const f32x2 (&c)[2], const f32x2 (&s)[2], MM&& mm) {
  asm volatile("s_nop 1");
  SB(); mm(std::integral_constant<int, 0>{}); SB();
  f32x2 a0 = lo2(i) * s[0], b0 = lo2(r) * s[0], a1 = hi2(i) * s[1];
  PIN(a0); PIN(b0); PIN(a1);
  SB(); mm(std::integral_constant<int, 1>{}); SB();
  f32x2 b1 = hi2(r) * s[1], re0 = CONJ ? lo2(r) * c[0] + a0 : lo2(r) * c[0] - a0, im0 = CONJ ? lo2(i) * c[0] - b0 : lo2(i) * c[0] + b0;
  PIN(b1); PIN(re0); PIN(im0);
  SB(); mm(std::integral_constant<int, 2>{}); SB();
  f32x2 re1 = CONJ ? hi2(r) * c[1] + a1 : hi2(r) * c[1] - a1, im1 = CONJ ? hi2(i) * c[1] - b1 : hi2(i) * c[1] + b1;
  u32 o0 = pkc<BF>(re0.x, im0.x);
  PIN(re1); PIN(im1); PIN(o0);
  SB(); mm(std::integral_constant<int, 3>{}); SB();
  return u32x4{o0, pkc<BF>(re0.y, im0.y), pkc<BF>(re1.x, im1.x), pkc<BF>(re1.y, im1.y)};
}
template <bool BF> DI u32x4 twiddle(f32x4 r, f32x4 i, const f32x2 (&c)[2], const f32x2 (&s)[2]) {
  const f32x2 re0 = lo2(r) * c[0] + lo2(i) * s[0], im0 = lo2(i) * c[0] - lo2(r) * s[0];
  const f32x2 re1 = hi2(r) * c[1] + hi2(i) * s[1], im1 = hi2(i) * c[1] - hi2(r) * s[1];
  return u32x4{pkc<BF>(re0.x, im0.x), pkc<BF>(re0.y, im0.y), pkc<BF>(re1.x, im1.x), pkc<BF>(re1.y, im1.y)};
}
DI u32x4 cmul_b(f32x4 r, f32x4 i, const f32x2 (&c)[2], const f32x2 (&s)[2]) {
  const f32x2 re0 = lo2(r) * c[0] - lo2(i) * s[0], im0 = lo2(i) * c[0] + lo2(r) * s[0];
  const f32x2 re1 = hi2(r) * c[1] - hi2(i) * s[1], im1 = hi2(i) * c[1] + hi2(r) * s[1];
  return u32x4{pkb(re0.x, im0.x), pkb(re0.y, im0.y), pkb(re1.x, im1.x), pkb(re1.y, im1.y)};
}
DI float cosr(float a) { return __builtin_amdgcn_cosf(a); }
DI float sinr(float a) { return __builtin_amdgcn_sinf(a); }
template <int CTRL> DI float dppf(float x) {
  return __builtin_bit_cast(float, __builtin_amdgcn_mov_dpp(__builtin_bit_cast(int, x), CTRL, 0xf, 0xf, true));
}
DI f32x2 bcast2(float x) {
  auto t = __builtin_amdgcn_permlane32_swap(__float_as_uint(x), __float_as_uint(x), false, false);
  return f32x2{__uint_as_float(t[0]), __uint_as_float(t[1])};
}
DI void ln_coef(f32x2 S, f32x2 Q, float ch, f32x2& R, f32x2& O) {
  auto t = __builtin_amdgcn_permlane32_swap(__float_as_uint(S.x), __float_as_uint(S.y), false, false);
  auto u = __builtin_amdgcn_permlane32_swap(__float_as_uint(Q.x), __float_as_uint(Q.y), false, false);
  const float s = __uint_as_float(t[0]) + __uint_as_float(t[1]), q = __uint_as_float(u[0]) + __uint_as_float(u[1]);
  auto z = __builtin_amdgcn_permlane16_swap(__float_as_uint(s), __float_as_uint(q), false, false);
  float x = __uint_as_float(z[0]) + __uint_as_float(z[1]);
  x += dppf<0xB1>(x);
  x += dppf<0x4E>(x);
  x += dppf<0x141>(x);
  x += dppf<0x140>(x);
  auto y = __builtin_amdgcn_permlane16_swap(__float_as_uint(x), __float_as_uint(x), false, false);
  const float md = __uint_as_float(y[0]) * (1.0f / 512.0f);
  const float var = relu(__uint_as_float(y[1]) * (1.0f / 512.0f) - md * md);
  const float r = __builtin_amdgcn_rcpf(bfr(__builtin_amdgcn_sqrtf(bfr(bfr(var) + 1e-5f))));
  const float o = (ch - bfr(ch + md)) * r;
  R = bcast2(r); O = bcast2(o);
}
DI float first_lane(float x) { return __uint_as_float(__builtin_amdgcn_readfirstlane(__float_as_uint(x))); }
DI int row_of_pos(int pos, int j) {
  const int c = 8 * (pos >> 7) + 2 * ((pos >> 3) & 3) + ((pos >> 5) & 1), q = 8 * ((pos >> 6) & 1) + (pos & 7);
  return 32 * (2 * c + (j & 1)) + 2 * q + (j >> 1);
}

template <int I> DI void ld4(u32x4& d, u32 v, const u8* s) { asm volatile("global_load_dwordx4 %0, %1, %2 offset:%3 sc1" : "=v"(d) : "v"(v), "s"(s), "i"(I) : "memory"); }
template <int I> DI void ld4a(u32x4& d, u32 v, const u8* s) { asm volatile("global_load_dwordx4 %0, %1, %2 offset:%3 sc1" : "=a"(d) : "v"(v), "s"(s), "i"(I) : "memory"); }
template <int I> DI void ld4r(u32x4& d, u32 v, const u8* s) { asm volatile("global_load_dwordx4 %0, %1, %2 offset:%3 sc1" : "+v"(d) : "v"(v), "s"(s), "i"(I) : "memory"); }
template <int I> DI void ld2(u32x2& d, u32 v, const u8* s) { asm volatile("global_load_dwordx2 %0, %1, %2 offset:%3 sc1" : "=v"(d) : "v"(v), "s"(s), "i"(I) : "memory"); }
template <int I> DI void ld2r(u32x2& d, u32 v, const u8* s) { asm volatile("global_load_dwordx2 %0, %1, %2 offset:%3 sc1" : "+v"(d) : "v"(v), "s"(s), "i"(I) : "memory"); }
template <int I> DI void ld2p(u32x2& d, u32 v, const u8* s) { asm volatile("global_load_dwordx2 %0, %1, %2 offset:%3" : "=v"(d) : "v"(v), "s"(s), "i"(I) : "memory"); }
DI void touch(u32& d, u32 v, const u8* s) { asm volatile("global_load_dword %0, %1, %2 sc1" : "+v"(d) : "v"(v), "s"(s) : "memory"); }
template <int I> DI void ld1p(u32& d, u32 v, const u8* s) { asm volatile("global_load_dword %0, %1, %2 offset:%3" : "=v"(d) : "v"(v), "s"(s), "i"(I) : "memory"); }
DI void ld1s(u32& d, u32 v, const u8* s) { asm volatile("global_load_ushort %0, %1, %2" : "=v"(d) : "v"(v), "s"(s) : "memory"); }
template <int N> DI void vmwait() { static_assert(N >= 0 && N < 64, ""); asm volatile("s_waitcnt vmcnt(%0)" ::"i"(N) : "memory"); }
DI void set_m0(u32 ldsaddr) { asm volatile("s_mov_b32 m0, %0" ::"s"(ldsaddr) : "memory"); }
template <int I> DI void dma1(u32 voff, const u8* src) {
  asm volatile("global_load_lds_dwordx4 %0, %1 offset:%2 sc1" ::"v"(voff), "s"(src), "i"(I) : "memory");
}
DI void dma4(u32 voff, const u8* src, u32 ldsaddr) {
  asm volatile("s_mov_b32 m0, %2\n\ts_nop 0\n\tglobal_load_lds_dwordx4 %0, %1 sc1\n\tglobal_load_lds_dwordx4 %0, %1 offset:1024 sc1\n\t"
               "global_load_lds_dwordx4 %0, %1 offset:2048 sc1\n\tglobal_load_lds_dwordx4 %0, %1 offset:3072 sc1" ::"v"(voff), "s"(src), "s"(ldsaddr) : "memory");
}
#define RETRY(COND, ...)                                                      \
  do {                                                                        \
    bool bad_ = (COND);                                                       \
    if (__builtin_expect(__any(bad_), 0)) {                                   \
      _Pragma("nounroll") for (int sp_ = 0; sp_ < SPIN_CAP; ++sp_) {          \
        __VA_ARGS__                                                           \
        bad_ = (COND);                                                        \
        if (!__any(bad_)) break;                                              \
      }                                                                       \
    }                                                                         \
  } while (0)
DI void bar() { asm volatile("s_waitcnt lgkmcnt(0)\n\ts_barrier" ::: "memory"); }
DI void bar_nw() { asm volatile("s_barrier" ::: "memory"); }

struct RowC { u32x4 b1re, b1im, b2re[2][2], b2im[2][2]; f32x2 twc[2][2], tws[2][2]; };
template <bool INV> DI void pair_cs(float c, float s, u32& re, u32& im) {
  if constexpr (INV) { im = pkb(s, c); re = pkb_n1(c, s, im); } else { re = pkh(c, s); im = pkh_n0(s, c, re); }
}
template <bool AG = false> DI void sign4(u32x4 base, u32 lm, u32x4& f00, u32x4& f01, u32x4& f10, u32x4& f11) {
  f00 = base;
  f01 = base ^ lm;
  f10 = base ^ u32x4{0u, 0x80008000u, 0u, 0x80008000u};
  f11 = f10 ^ lm;
  if constexpr (AG) { PINA(f00); PINA(f01); PINA(f10); PINA(f11); } else { PIN(f00); PIN(f01); PIN(f10); PIN(f11); }
}
template <bool INV, int PART> DI void row_consts(RowC& k, int m, int g) {
  const float fm = (float)m, fg = (float)(4 * g);
  if constexpr (PART == 0) {
    u32x4 br, bi;
#pragma unroll
    for (int i = 0; i < 4; ++i) {
      const float a = (fg + (float)i) * (fm * (1.0f / 16.0f));
      u32 x, y;
      pair_cs<INV>(cosr(a), sinr(a), x, y); k.b1re[i] = x; k.b1im[i] = y;
      const float a2 = (fg + (float)i) * (fm * (1.0f / 32.0f));
      pair_cs<INV>(cosr(a2), sinr(a2), x, y); br[i] = x; bi[i] = y;
    }
    PIN(k.b1re); PIN(k.b1im);
    const u32 lm = (m & 1) ? 0x80008000u : 0u;
    sign4(br, lm, k.b2re[0][0], k.b2re[0][1], k.b2re[1][0], k.b2re[1][1]);
    sign4(bi, lm, k.b2im[0][0], k.b2im[0][1], k.b2im[1][0], k.b2im[1][1]);
  } else {
#pragma unroll
    for (int e = 0; e < 2; ++e)
#pragma unroll
      for (int i = 0; i < 4; ++i) {
        const float a = (fg + (float)(16 * e + i)) * (fm * (INV ? -1.0f / 512.0f : 1.0f / 512.0f));
        k.twc[e][i >> 1][i & 1] = cosr(a); k.tws[e][i >> 1][i & 1] = sinr(a);
      }
#pragma unroll
    for (int e = 0; e < 2; ++e) { PIN(k.twc[e][0]); PIN(k.twc[e][1]); PIN(k.tws[e][0]); PIN(k.tws[e][1]); }
  }
}
template <bool INV, int TAIL> DI void row_fft(const RowC& k, u32x4 a0, u32x4 a1, f32x4& dr0, f32x4& di0, f32x4& dr1, f32x4& di1) {
  f32x4 tr0, ti0, tr1, ti1;
  mm4<INV>(tr0, ti0, tr1, ti1, a0, a1, k.b1re, k.b1im);
  u32x4 p0 = twiddle<INV>(tr0, ti0, k.twc[0], k.tws[0]);
  PIN(p0);
  const u32x4 p1 = cplx_under<INV, true>(tr1, ti1, k.twc[1], k.tws[1], [&](auto I) {
    constexpr int i = decltype(I)::value;
    mf0<INV, 0>(i == 0 ? dr0 : i == 1 ? di0 : i == 2 ? dr1 : di1, p0, i == 0 ? k.b2re[0][0] : i == 1 ? k.b2im[0][0] : i == 2 ? k.b2re[1][0] : k.b2im[1][0]);
  });
  mf4<INV, 0, TAIL>(dr0, di0, dr1, di1, p1, k.b2re[0][1], p1, k.b2im[0][1], p1, k.b2re[1][1], p1, k.b2im[1][1]);
}

struct FwdP { u32 iw, ir, ow, ap0, ap1, ar0, ar1, zo; f32x2 cB, cD, eA, eB, eC, eD; };
DI void fwd_pre(FwdP& p, int tid, int w, int pos) {
  const int m = tid & 15, g = (tid >> 4) & 3, ph = tid >> 1, fq = ph ? 512 - ph : 256, fq1 = 384 - ph;
  p.iw = w * 5120 + ((16 * (m & 1) + 4 * g) * 20 + (m >> 1)) * 4; p.ir = w * 5120 + (m * 20 + 4 * g) * 4;
  p.ap0 = (ph + (ph >> 4) * 4) * 4; p.ap1 = p.ap0 + 640; p.ar0 = (fq + (fq >> 4) * 4) * 4; p.ar1 = (fq1 + (fq1 >> 4) * 4) * 4;
  p.zo = OFF_A + pos * 8192 + tid * 16;
  const float sc = 1.0f / 64.0f, sg = (tid & 1) ? -sc : sc;
  p.cB = f32x2{sg, -sg}; p.cD = f32x2{sg, sg};
  p.eA = ph ? f32x2{sc, sc} : f32x2{2 * sc, 0.f}; p.eB = ph ? p.cB : f32x2{2 * sg, 0.f};
  p.eC = ph ? f32x2{sc, -sc} : f32x2{0.f, 2 * sg}; p.eD = ph ? p.cD : f32x2{0.f, 2 * sc};
  PIN(p.iw); PIN(p.ir); PIN(p.ap0); PIN(p.ap1); PIN(p.ar0); PIN(p.ar1); PIN(p.zo);
  PIN(p.cB); PIN(p.cD); PIN(p.eA); PIN(p.eB); PIN(p.eC); PIN(p.eD);
}
struct ColC {
  u32x4 b1re[2], b1im[2], a2re[2][2], a2im[2][2], b3re[2][2], b3im[2][2], a4re[2][2], a4im[2][2];
  f32x2 t1c[2][2], t1s[2][2], t2c[2][2], t2s[2][2];
};
template <int PART> DI void col_consts(ColC& k, int m, int g, int w) {
  const int pi_ = w >> 1, kt = w & 1;
  const float fm = (float)m, fg = (float)(4 * g);
  const u32 lm = (m & 1) ? 0x80008000u : 0u;
  if constexpr (PART == 0) {
    const float fk = (float)(2 * (16 * kt + m) + pi_);
#pragma unroll
    for (int u = 0; u < 2; ++u) {
#pragma unroll
      for (int r = 0; r < 4; ++r) {
        const float n = fg + (float)(16 * u + r), a = n * (fk * (1.0f / 64.0f)), a2 = n * (fk * (1.0f / 2048.0f));
        u32 x, y;
        pair_cs<false>(cosr(a), sinr(a), x, y); k.b1re[u][r] = x; k.b1im[u][r] = y;
        k.t1c[u][r >> 1][r & 1] = cosr(a2); k.t1s[u][r >> 1][r & 1] = sinr(a2);
      }
      PIN(k.b1re[u]); PIN(k.b1im[u]); PIN(k.t1c[u][0]); PIN(k.t1c[u][1]); PIN(k.t1s[u][0]); PIN(k.t1s[u][1]);
    }
  } else if constexpr (PART == 1) {
    u32x4 fr, fi, ir, ii;
#pragma unroll
    for (int r = 0; r < 4; ++r) {
      const float a = (fg + (float)r) * (fm * (1.0f / 32.0f)), c = cosr(a), s = sinr(a);
      u32 x, y;
      pair_cs<false>(c, s, x, y); fr[r] = x; fi[r] = y;
      pair_cs<true>(c, s, x, y); ir[r] = x; ii[r] = y;
    }
    sign4(fr, lm, k.a2re[0][0], k.a2re[0][1], k.a2re[1][0], k.a2re[1][1]);
    sign4(fi, lm, k.a2im[0][0], k.a2im[0][1], k.a2im[1][0], k.a2im[1][1]);
    sign4(ir, lm, k.b3re[0][0], k.b3re[0][1], k.b3re[1][0], k.b3re[1][1]);
    sign4(ii, lm, k.b3im[0][0], k.b3im[0][1], k.b3im[1][0], k.b3im[1][1]);
  } else if constexpr (PART == 2) {
    const float kb = (float)(32 * kt + pi_) + 2.0f * fg;
#pragma unroll
    for (int u = 0; u < 2; ++u) {
#pragma unroll
      for (int r = 0; r < 4; ++r) {
        const float a = (kb + (float)(2 * r)) * ((fm + (float)(16 * u)) * (-1.0f / 2048.0f));
        k.t2c[u][r >> 1][r & 1] = cosr(a); k.t2s[u][r >> 1][r & 1] = sinr(a);
      }
      PIN(k.t2c[u][0]); PIN(k.t2c[u][1]); PIN(k.t2s[u][0]); PIN(k.t2s[u][1]);
    }
  } else {
    const float fn = (fm + (float)(16 * (w >> 1))) * (1.0f / 64.0f);
#pragma unroll
    for (int u = 0; u < 2; ++u) {
#pragma unroll
      for (int r = 0; r < 4; ++r) {
        const float a = (2.0f * fg + (float)(2 * r + u)) * fn;
        u32 x, y;
        pair_cs<true>(cosr(a), sinr(a), x, y); k.a4re[u][0][r] = x; k.a4im[u][0][r] = y;
      }
      k.a4re[u][1] = k.a4re[u][0] ^ lm; k.a4im[u][1] = k.a4im[u][0] ^ lm;
      PIN(k.a4re[u][0]); PIN(k.a4re[u][1]); PIN(k.a4im[u][0]); PIN(k.a4im[u][1]);
    }
  }
}

DI void transpose8x4(const u32x2 (&v)[8], u32x4 (&o)[4]) {
#pragma unroll
  for (int jj = 0; jj < 4; ++jj) {
    o[0][jj] = __builtin_amdgcn_perm(v[2 * jj + 1].x, v[2 * jj].x, 0x05040100u);
    o[1][jj] = __builtin_amdgcn_perm(v[2 * jj + 1].x, v[2 * jj].x, 0x07060302u);
    o[2][jj] = __builtin_amdgcn_perm(v[2 * jj + 1].y, v[2 * jj].y, 0x05040100u);
    o[3][jj] = __builtin_amdgcn_perm(v[2 * jj + 1].y, v[2 * jj].y, 0x07060302u);
  }
}
template <int H> DI void prep_load1(const Params& P, int wt, int lane, u32x2 (&v)[8]) {
  const u32 vo = (lane < 56 ? lane : 55) * 8, vo2 = vo + 7168;
  const int piece = (wt >> 3) & 63;
  const u8* src = (const u8*)((wt >> 9) ? P.wu : P.wg) + opq(wt & 7) * 448 + opq(4 * piece) * 3584;
  static_for<4>([&](auto J) { constexpr int j = decltype(J)::value; ld2p<(j & 1) * 3584>(v[4 * H + j], j & 2 ? vo2 : vo, src + H * (256 * 3584)); });
}
template <int H> DI void prep_load2(const Params& P, int wt, int lane, u32 (&v)[8]) {
  const int piece = wt >> 2, g = piece & 3, kq = piece >> 2, wv = kq / 14, ks = kq % 14;
  const int f0 = ks < 4 ? 112 * (4 * wv + ks) : ks < 6 ? 112 * (4 * wv + 2 * (ks - 4)) + 32 : ks < 10 ? 112 * (4 * wv + ks - 6) + 48 : 112 * (4 * wv + ks - 10) + 80;
  const int f1 = f0 + ((ks == 4 || ks == 5) ? 112 : 16);
  const u8* s0 = (const u8*)P.wd + (wt & 3) * 256 + (4 * g + (H ? f1 : f0)) * 1024;
  static_for<4>([&](auto J) { constexpr int j = decltype(J)::value; ld1p<j * 1024>(v[4 * H + j], lane * 4, s0); });
}
DI u32 prep_dst1(int wt, int tile) {
  const int piece = (wt >> 3) & 63, mat = wt >> 9, hi = tile >= 7, p = tile - 7 * hi, ks = piece >> 2;
  const u32 frag = (p < 3 ? ks * 6 + 2 * p : p < 5 ? 96 + ks * 4 + 2 * (p - 3) : 160 + ks * 4 + 2 * (p - 5)) + mat;
  return OFF_W1 + ((2 * (wt & 7) + hi) * 224 + frag) * 1024 + (piece & 3) * 256;
}
DI u32 prep_dst2(int wt, int tile) {
  const int piece = wt >> 2, tl = (wt & 3) * 8 + tile, rot = (tl >> 2) & 3, kq = piece >> 2, ks = kq % 14;
  const int slot = ks < 4 ? (ks - rot) & 3 : ks < 6 ? ks ^ (rot & 1) : ks < 10 ? 6 + ((ks - 6 - rot) & 3) : 10 + ((ks - 10 - rot) & 3);
  return OFF_W2 + (((tl >> 2) * 56 + kq - ks + slot) * 4 + (tl & 3)) * 1024 + (piece & 3) * 256;
}

constexpr int NTOK0 = 12, NRES = 56;
constexpr int g1_xe(int L) { return L == 0 ? XE0 : XE1; }
constexpr int g1_ne(int L) { return (224 - g1_xe(L)) / 7 * 3 + ((224 - g1_xe(L)) % 7 > 0) + ((224 - g1_xe(L)) % 7 > 2) + ((224 - g1_xe(L)) % 7 > 5); }
constexpr int g1_job(int L, int x) {
  if (((x >= 34 && x < 96) && (x & 3) == 2) || (x >= 98 && x < 114 && !(x & 1))) return 2;
  if ((x < 34 && !(x & 1)) || (x < 64 && (x & 3) == 0)) return 1;
  if (L == 1 && x >= 47 && x < 60 && (x & 3) == 3) return 6;
  if (x >= g1_xe(L)) return ((x - g1_xe(L)) % 7 == 0 || (x - g1_xe(L)) % 7 == 2 || (x - g1_xe(L)) % 7 == 5) ? 4 : 0;
  if (L == 0 && ((x < 34 && x % 4 == 1) || (x >= 48 && x < 98 && (x & 3) == 0) || (x >= 114 && x % 5 != 1 && x % 5 != 3) || (x >= 35 && x < 98 && x % 8 == 1))) return 3;
  return 0;
}
constexpr int NEPI1 = 75, NEPI2 = 50, EPI1_X = 99;
struct G1Sched {
  int job[224], idx[224], epi[225], nst[224], cum[225], pos[7][64], cnt[7];
  constexpr G1Sched(int L) : job{}, idx{}, epi{}, nst{}, cum{}, pos{}, cnt{} {
    cnt[1] = NTOK0;
    for (int i = 0; i < NTOK0; ++i) pos[1][i] = i;
    cum[0] = NTOK0;
    for (int x = 0; x < 224; ++x) {
      int j = g1_job(L, x);
      if (j == 1 && cnt[1] >= 32) j = L == 0 ? 3 : 0;
      if ((j == 3 && cnt[3] >= NRES)) j = 0;
      job[x] = j; idx[x] = cnt[j];
      if (x == 162) epi[x] = NEPI1;
      const bool on = (x >= EPI1_X && x < 160 && epi[x] < NEPI1) || x >= 162;
      epi[x + 1] = on ? epi[x] + (x < 114 ? 2 - (x & 1 ? 0 : 1) : 2) : epi[x];
      if (x < 160 && epi[x + 1] > NEPI1) epi[x + 1] = NEPI1;
      for (int n = epi[x]; n < epi[x + 1]; ++n)
        nst[x] += n < NEPI1 ? (n == 24 || n == 49 || n == 74) : (n < NEPI1 + NEPI2 && (n - NEPI1) % 25 == 24);
      if (j) pos[j][cnt[j]++] = cum[x] + nst[x];
      cum[x + 1] = cum[x] + nst[x] + (j ? 1 : 0);
    }
  }
};
constexpr G1Sched G1S[2] = {G1Sched(0), G1Sched(1)};
static_assert(G1S[0].cnt[1] == 32 && G1S[0].cnt[2] == 24 && G1S[0].cnt[3] == NRES && G1S[0].cnt[4] == g1_ne(0) && G1S[1].cnt[4] == g1_ne(1), "");
static_assert(G1S[1].cnt[6] == 4 && G1S[1].pos[6][3] < G1S[1].pos[2][7], "");
static_assert(G1S[0].idx[66] == 8 && G1S[0].idx[98] == 16 && G1S[0].epi[113] <= 24 && G1S[1].epi[113] <= 24, "");
static_assert(G1S[0].epi[152] == NEPI1 && G1S[0].epi[204] >= NEPI1 + NEPI2, "");
constexpr int clamp63(int n) { return n > 63 ? 63 : n; }
constexpr int g1_wait(int L, int x, int job, int idx) { return clamp63(G1S[L].cum[x] - G1S[L].pos[job][idx] - 1); }

#define FRESH()                                                                                                   \
  int tid = threadIdx.x, b = BMAP(blockIdx.x), w = w_;                                                                   \
  u8* ws = P.ws;                                                                                                   \
  asm volatile("" : "+v"(tid), "+s"(b), "+s"(w), "+s"(ws));                                                        \
  const int lane = tid & 63, m = lane & 15, g = lane >> 4, half2 = b & 1, ct = b >> 5;                             \
  const int G = ((b >> 1) & 3) * 4 + ((b >> 3) & 3), member = half2 * 8 + ct, pos = G * 16 + member;               \
  const u32 l16 = lane * 16, t16 = tid * 16, xo = 32 * m + 8 * g;                                                  \
  auto fill_h = [&](u32 base, int q) { st4(sent4, base + (4 * G + w) * 114688 + member * 7168 + q * 1024 + l16); };          \
  auto fill_rows = [&](u32 base) { st4(sent4, base + pos * 8192 + t16); st4(sent4, base + pos * 8192 + 4096 + t16); }; \
  auto ring4 = [&](int q) {                                   \
    const u32 o = (q >> 1) * 32768 + w * 8192 + (q & 1) * 4096;                                                    \
    dma4(l16, ws + OFF_W1 + member * 229376 + o, ldsbase + o);                                                     \
  };                                                                                                               \
  (void)m; (void)g; (void)half2; (void)ct; (void)l16; (void)t16; (void)xo; (void)fill_h; (void)fill_rows; (void)ring4

__global__ __launch_bounds__(256, 1) void k_all(Params P) {
  extern __shared__ __attribute__((aligned(16))) u8 lds[];
  ST_DECL;
  const int w_ = __builtin_amdgcn_readfirstlane(threadIdx.x >> 6);
  const __amdgpu_buffer_rsrc_t rs = __builtin_amdgcn_make_buffer_rsrc(P.ws, 0, WS_USED, 0x00020000);
  const u32 ldsbase = (u32)(size_t)(__attribute__((address_space(3))) u8*)lds;
  const u32x4 sent4 = {SENT, SENT, SENT, SENT};
  auto st4 = [&](u32x4 v, u32 off) { __builtin_amdgcn_raw_buffer_store_b128(v, rs, off, 0, 16); };
  auto st2 = [&](u32x2 v, u32 off) { __builtin_amdgcn_raw_buffer_store_b64(v, rs, off, 0, 16); };
  u32x2 xl[2], xh[2], pv0[8];
  u32 pv1[8];
  u32 wfr[8], wfi[8], wn[8];
  f32x2 xv[8], xc;
  float xch;
  FwdP fp;
  u32 pd[6];
  u32x2 ep;
  u32 cpf = 0, tw[4];
  RowC rc;
  ColC cc;
  ST(0);
  {
    FRESH();
    const u8* xp = (const u8*)P.x + row_of_pos(pos, w) * 1024;
    ld2p<0>(xl[0], xo, xp); ld2p<512>(xl[1], xo, xp); ld2p<0>(xh[0], xo, xp + 1024 * 1024); ld2p<512>(xh[1], xo, xp + 1024 * 1024);
    { const u32 z = 0; ld2<0>(ep, z, ws); }
    {
      const u8* pc;
      u32 len;
      asm volatile("s_getpc_b64 %0\n.Lgf_begin:\n\ts_mov_b32 %1, .Lgf_end-.Lgf_begin" : "=s"(pc), "=s"(len));
      const u32 o = (((b >> 3) * 4 + w) * 8 + (lane & 7)) * 128;
      touch(cpf, o < len ? o : 0, pc);
    }
    {
      const u32 z = 0;
      ld1p<0>(tw[0], z, (const u8*)P.wr); ld1p<0>(tw[1], z, (const u8*)P.wi); ld1p<0>(tw[2], z, (const u8*)(b < 128 ? P.wg : P.wu));
      ld1p<0>(tw[3], z, (const u8*)P.wd);
    }
    XS(32);
    fill_rows(OFF_A); fill_rows(OFF_WX);
    if (tid < 8) st4(sent4, OFF_WNY + tid * 4096 + pos * 16);
    SB();
    row_consts<false, 0>(rc, m, g);
    row_consts<false, 1>(rc, m, g);
    SB();
    XS(33);
    const int kb = 2 * (16 * ((pos >> 6) & 1) + (pos & 15)) + (pos >> 7) + 256 * ((pos >> 4) & 3);
    static_for<8>([&](auto J) {
      constexpr int j = decltype(J)::value;
      const u32 o = tid * 2 + (kb + 64 * (16 * (j >> 2) + (j & 3))) * 514;
      ld1s(wfr[j], o, (const u8*)P.wr); ld1s(wfi[j], o, (const u8*)P.wi);
    });
    {
      const int k = kb + 64 * (16 * ((lane >> 2) & 1) + (lane & 3)), km = (2048 - k) & 2047;
      const u32 o = k * 514, om = km * 514;
      ld1s(wn[0], o, (const u8*)P.wr); ld1s(wn[1], o, (const u8*)P.wi); ld1s(wn[2], om, (const u8*)P.wr); ld1s(wn[3], om, (const u8*)P.wi);
      ld1s(wn[4], o, (const u8*)P.wr + 512); ld1s(wn[5], o, (const u8*)P.wi + 512); ld1s(wn[6], om, (const u8*)P.wr + 512); ld1s(wn[7], om, (const u8*)P.wi + 512);
    }
    XS(34);
    fwd_pre(fp, tid, w, pos);
#pragma unroll
    for (int x = 0; x < 4; ++x) pd[x] = x < 3 || g < 2 ? prep_dst1(b * 4 + w, 4 * x + g) + m * 16 : WS_USED;
#pragma unroll
    for (int x = 0; x < 2; ++x) pd[4 + x] = b < 224 ? prep_dst2(b * 4 + w, 4 * x + g) + m * 16 : WS_USED;
#pragma unroll
    for (int x = 0; x < 6; ++x) PIN(pd[x]);
    SB();
    XS(35);
    vmwait<1 + 4 + 4 + 24>();
    XS(36);
    PIN(ep); PIN(xl[0]); PIN(xl[1]); PIN(xh[0]); PIN(xh[1]);
    { const u64 e = (((u64)(u32)__builtin_amdgcn_readfirstlane(ep.y) << 32) | (u32)__builtin_amdgcn_readfirstlane(ep.x)) + 1; ep = u32x2{(u32)e, (u32)(e >> 32)}; }
#pragma unroll
    for (int t = 0; t < 2; ++t)
#pragma unroll
      for (int p = 0; p < 2; ++p) {
        xv[4 * t + 2 * p] = f32x2{blo(xl[t][p]), blo(xh[t][p])};
        xv[4 * t + 2 * p + 1] = f32x2{bhi(xl[t][p]), bhi(xh[t][p])};
      }
    xc = f32x2{first_lane(xv[0].x), first_lane(xv[0].y)};
    xch = lane < 32 ? xc.x : xc.y;
#pragma unroll
    for (int i = 0; i < 8; ++i) xv[i] -= xc;
  }
  ST(1);

  u32x4 w2a[14][4];

  auto layer = [&](auto LC) {
    constexpr int L = decltype(LC)::value;
    constexpr int S0 = 2 + 16 * L;
    {
      FRESH();
      const f32x2 S = ((xv[0] + xv[1]) + (xv[2] + xv[3])) + ((xv[4] + xv[5]) + (xv[6] + xv[7]));
      f32x2 q0 = xv[0] * xv[0], q1 = xv[1] * xv[1];
#pragma unroll
      for (int i = 2; i < 8; i += 2) { q0 += xv[i] * xv[i]; q1 += xv[i + 1] * xv[i + 1]; }
      if constexpr (L == 0) { SB(); fill_rows(OFF_U); SB(); }
      f32x2 R, O;
      XF(32);
      ln_coef(S, q0 + q1, xch, R, O);
      XF(33);
      u8* img = lds + LDS_SCR;
      if constexpr (L == 0) {
        SB(); vmwait<26>(); PIN(cpf); PIN(tw[0]); PIN(tw[1]); PIN(tw[2]); PIN(tw[3]);
        if (lane == 0) st2(ep, OFF_FLAG + (w * 256 + pos) * 8);
        prep_load1<0>(P, b * 4 + w, lane, pv0); SB();
      }
#pragma unroll
      for (int i = 0; i < 8; ++i) {
        const f32x2 v = xv[i] * R + O;
        *(u32*)(img + fp.iw + ((i & 3) * 20 + 8 * (i >> 2)) * 4) = pkh(v.x, v.y);
      }
      const u32x4 a0 = *(const u32x4*)(img + fp.ir), a1 = *(const u32x4*)(img + fp.ir + 1280);
      if constexpr (L == 0) prep_load1<1>(P, b * 4 + w, lane, pv0); else ring4(1);
      const float* p0 = (const float*)(img + fp.ap0);
      const float* p1 = (const float*)(img + fp.ap1);
      const float* r0 = (const float*)(img + fp.ar0);
      const float* r1 = (const float*)(img + fp.ar1);
      const f32x2 cA = {1.0f / 64.0f, 1.0f / 64.0f}, cC = {1.0f / 64.0f, -1.0f / 64.0f}, cB = fp.cB, cD = fp.cD, eA = fp.eA, eB = fp.eB, eC = fp.eC, eD = fp.eD;
      const u32 zo = fp.zo;
      f32x4 dr0, di0, dr1, di1;
      XF(34);
      row_fft<false, 6>(rc, a0, a1, dr0, di0, dr1, di1);
      XF(35);
      SB();
      float* op = (float*)(img + fp.ir);
      *(f32x4*)(op) = dr0; *(f32x4*)(op + 640) = di0;
      SB();
      *(f32x4*)(op + 320) = dr1; *(f32x4*)(op + 960) = di1;
      if constexpr (L == 0) { prep_load2<0>(P, (b < 224 ? b : 0) * 4 + w, lane, pv1); vmwait<13>(); }
      bar();
      XF(36);
      f32x2 pa[4], qa[4], pb[4], qb[4];
#pragma unroll
      for (int j = 0; j < 4; ++j) {
        pa[j] = f32x2{p0[j * 1280], p0[j * 1280 + 640]}; qa[j] = f32x2{r0[j * 1280], r0[j * 1280 + 640]};
      }
#pragma unroll
      for (int j = 0; j < 4; ++j) {
        pb[j] = f32x2{p1[j * 1280], p1[j * 1280 + 640]}; qb[j] = f32x2{r1[j * 1280], r1[j * 1280 + 640]};
      }
      if constexpr (L == 0) prep_load2<1>(P, (b < 224 ? b : 0) * 4 + w, lane, pv1);
      SB();
      u32x4 o;
#pragma unroll
      for (int j = 0; j < 4; ++j) {
        const f32x2 v = eD * swp(qa[j]) + (eC * qa[j] + (eB * swp(pa[j]) + eA * pa[j]));
        o[j] = mark<(L == 0 ? 0x10001u : 0u), (L == 0 ? 0u : 0x10000u)>(pkh(v.x, v.y));
      }
      st4(o, zo);
#pragma unroll
      for (int j = 0; j < 4; ++j) {
        const f32x2 v = cD * swp(qb[j]) + (cC * qb[j] + (cB * swp(pb[j]) + cA * pb[j]));
        o[j] = mark<(L == 0 ? 0x10001u : 0u), (L == 0 ? 0u : 0x10000u)>(pkh(v.x, v.y));
      }
      st4(o, zo + 4096);
      SB();
      if constexpr (L == 1) {
#pragma unroll
        for (int t = 0; t < 2; ++t)
#pragma unroll
          for (int p = 0; p < 2; ++p) {
            const f32x2 v0 = xv[4 * t + 2 * p] + xc, v1 = xv[4 * t + 2 * p + 1] + xc;
            xl[t][p] = pkb(v0.x, v1.x); xh[t][p] = pkb(v0.y, v1.y);
          }
        PIN(xl[0]); PIN(xl[1]); PIN(xh[0]); PIN(xh[1]);
      }
    }
    if constexpr (L == 0) {
      XS(37);
      FRESH();
#pragma unroll
      for (int j = 0; j < 8; ++j) { PIN(wfr[j]); PIN(wfi[j]); PIN(wn[j]); }
      u8* bw = lds + w * 2048;
      u32x4 v0, v1;
#pragma unroll
      for (int j = 0; j < 4; ++j) { v0[j] = wfr[j] | (wfi[j] << 16); v1[j] = wfr[4 + j] | (wfi[4 + j] << 16); }
      *(u32x4*)(bw + lane * 32) = v0; *(u32x4*)(bw + lane * 32 + 16) = v1;
      st4(*(const u32x4*)(bw + l16), OFF_WX + pos * 8192 + w * 2048 + l16);
      st4(*(const u32x4*)(bw + 1024 + l16), OFF_WX + pos * 8192 + w * 2048 + 1024 + l16);
      const float hr = blo(wn[0]) + blo(wn[2]), hi = blo(wn[1]) - blo(wn[3]), nr = blo(wn[4]) + blo(wn[6]), ni = blo(wn[5]) - blo(wn[7]);
      const f32x4 ab = {0.25f * (hr + nr), 0.25f * (hi + ni), 0.25f * (hr - nr), 0.25f * (hi - ni)};
      if (tid < 8) st4(__builtin_bit_cast(u32x4, ab), OFF_WNY + tid * 4096 + pos * 16);
    }
    ST(S0);
    {
      FRESH();
      const int phi = pos, pi_ = w >> 1, kt = w & 1;
      const int k1l = 2 * (16 * kt + m) + pi_;
      auto consts = [&](auto PC) { col_consts<decltype(PC)::value>(cc, m, g, w); SB(); };
      using I0 = std::integral_constant<int, 0>; using I1 = std::integral_constant<int, 1>;
      using I2 = std::integral_constant<int, 2>; using I3 = std::integral_constant<int, 3>;
      u8* const bw = lds + 16384 + w * 8192;
      const u32 zo = (tid >> 1) * 8192 + (tid & 1) * 16;
      const u8* zb0 = ws + OFF_A + phi * 32;
      const u8* zb1 = zb0 + 128 * 8192;
      const u8* wb0 = ws + OFF_WX + phi * 32;
      const u8* wb1 = wb0 + 128 * 8192;
      const u32 wo = zo;
      PINS(zb0); PINS(zb1); PINS(wb0); PINS(wb1);
      if constexpr (L == 0) {
        u32x4 fl0, fl1;
        const u32 o = lane * 32;
        const u8* fb = ws + OFF_FLAG + ((phi >> 5) & 3) * 2048;
        ld4<0>(fl0, o, fb); ld4<16>(fl1, o, fb);
        SB();
        consts(I0{});
        consts(I1{});
        XS(38);
        vmwait<0>(); PIN(fl0); PIN(fl1);
        RETRY(!(fl0.x == ep.x && fl0.y == ep.y && fl0.z == ep.x && fl0.w == ep.y && fl1.x == ep.x && fl1.y == ep.y && fl1.z == ep.x && fl1.w == ep.y), {
          CNT(0);
          ld4r<0>(fl0, o, fb); ld4r<16>(fl1, o, fb);
          vmwait<0>(); PIN(fl0); PIN(fl1);
        });
      } else {
        SB();
        consts(I0{});
        consts(I2{});
      }
      u32x4 z0, z1, wv[2];
      if constexpr (L == 0) XS(39);
      ld4<0>(z0, zo, zb0); ld4<0>(z1, zo, zb1);
      ld4<0>(wv[0], wo, wb0); ld4<0>(wv[1], wo, wb1);
      SB();
      u32x4 wa[8], po[6];
      if constexpr (L == 0) {
        XS(40);
        u32x4 ot[4];
#pragma unroll
        for (int j = 0; j < 8; ++j) { PIN(pv0[j]); PIN(pv1[j]); }
        transpose8x4(pv0, ot);
#pragma unroll
        for (int x = 0; x < 4; ++x) *(u32x4*)(bw + lane * 64 + ((x + lane) & 3) * 16) = ot[x];
#pragma unroll
        for (int jj = 0; jj < 4; ++jj) {
          ot[0][jj] = __builtin_amdgcn_perm(pv1[2 * jj + 1], pv1[2 * jj], 0x05040100u);
          ot[1][jj] = __builtin_amdgcn_perm(pv1[2 * jj + 1], pv1[2 * jj], 0x07060302u);
        }
        *(u32x4*)(bw + 4096 + lane * 32) = ot[0]; *(u32x4*)(bw + 4096 + lane * 32 + 16) = ot[1];
#pragma unroll
        for (int x = 0; x < 4; ++x) {
          const int src = x * 16 + (lane >> 2);
          po[x] = *(const u32x4*)(bw + src * 64 + (((lane & 3) + src) & 3) * 16);
        }
#pragma unroll
        for (int x = 0; x < 2; ++x) {
          po[4 + x] = *(const u32x4*)(bw + 4096 + x * 1024 + (8 * (m >> 2) + 4 * (g & 1) + (m & 3) + 32 * (g >> 1)) * 16);
        }
#pragma unroll
        for (int x = 0; x < 6; ++x) PIN(po[x]);
        st4(po[0], pd[0]); st4(po[1], pd[1]);
        SB();
        XS(41);
        consts(I2{});
        XS(42);
        consts(I3{});
        XS(43);
        st4(po[2], pd[2]); st4(po[3], pd[3]);
      } else {
        consts(I1{});
        ring4(2);
        SB();
        consts(I3{});
      }
      u8* const zi = lds + LDS_SCR;
      u32 zw;
      {
        const int pp = tid >> 1;
        zw = (tid & 1) * 4096 + ((pp >> 6) & 1) * 2048 + (((pp >> 3) & 3) * 16 + 2 * (pp & 7)) * 16 + ((pp >> 5) & 1) * 8;
        PIN(zw);
      }
      u32 uo = OFF_U + phi * 8192 + w * 2048 + l16;
      PIN(uo);
      SB();
      bar_nw();
      ST(S0 + 1);
      vmwait<4>();
      PIN(z0); PIN(z1); PIN(wv[0]); PIN(wv[1]);
      if constexpr (L == 0) {
        RETRY((stale<false, 1u>(red8<false>(z0, z1)) || max4(max4(0, wv[0]), wv[1]) == SENT), {
          CNT(1);
          if (bad_) { ld4r<0>(z0, zo, zb0); ld4r<0>(z1, zo, zb1); ld4r<0>(wv[0], wo, wb0); ld4r<0>(wv[1], wo, wb1); }
          vmwait<0>(); PIN(z0); PIN(z1); PIN(wv[0]); PIN(wv[1]);
        });
      } else {
        RETRY((stale<true, 0x10000u>(red8<true>(z0, z1))), {
          CNT(2);
          if (bad_) { ld4r<0>(z0, zo, zb0); ld4r<0>(z1, zo, zb1); }
          vmwait<0>(); PIN(z0); PIN(z1);
        });
      }
      ST(S0 + 2);
      *(u32x2*)(zi + zw) = u32x2{z0.x, z0.y}; *(u32x2*)(zi + zw + 16) = u32x2{z0.z, z0.w};
      *(u32x2*)(zi + zw + 1024) = u32x2{z1.x, z1.y}; *(u32x2*)(zi + zw + 1024 + 16) = u32x2{z1.z, z1.w};
      *(u32x4*)(zi + 8192 + t16) = wv[0]; *(u32x4*)(zi + 12288 + t16) = wv[1];
      bar();
      XF(37);
      const u8* zr = zi + pi_ * 4096 + l16;
      const u32x4 f00 = *(const u32x4*)(zr), f01 = *(const u32x4*)(zr + 1024), f10 = *(const u32x4*)(zr + 2048), f11 = *(const u32x4*)(zr + 3072);
      wv[0] = *(const u32x4*)(zi + 8192 + tid * 32); wv[1] = *(const u32x4*)(zi + 8192 + tid * 32 + 16);
      if (phi == 0) static_for<8>([&](auto J) { constexpr int j = decltype(J)::value; ld4<0>(wa[j], t16, ws + OFF_WNY + j * 4096); });
      if constexpr (L == 1) ring4(3); else { st4(po[4], pd[4]); st4(po[5], pd[5]); }
      f32x4 d0, d1, d2, d3;
      mm8<false, 3>(d0, d1, d2, d3, f00, cc.b1re[0], f01, cc.b1re[1], f00, cc.b1im[0], f01, cc.b1im[1], f10, cc.b1re[0], f11, cc.b1re[1], f10, cc.b1im[0], f11, cc.b1im[1]);
      XF(38);
      f32x4 e0, e1, e2, e3;
      u32x4 b20 = twiddle<false>(d0, d1, cc.t1c[0], cc.t1s[0]);
      PIN(b20);
      const u32x4 b21 = cplx_under<false, true>(d2, d3, cc.t1c[1], cc.t1s[1], [&](auto I) {
        constexpr int i = decltype(I)::value;
        mf0<false, 0>(i == 0 ? e0 : i == 1 ? e1 : i == 2 ? e2 : e3, i == 0 ? cc.a2re[0][0] : i == 1 ? cc.a2im[0][0] : i == 2 ? cc.a2re[1][0] : cc.a2im[1][0], b20);
      });
      f32x2 wr[2][2], wi[2][2];
      auto unpack_w = [&]() {
#pragma unroll
        for (int t = 0; t < 2; ++t)
#pragma unroll
          for (int h = 0; h < 2; ++h) {
            wr[t][h] = f32x2{blo(wv[t][2 * h]), blo(wv[t][2 * h + 1])}; wi[t][h] = f32x2{bhi(wv[t][2 * h]), bhi(wv[t][2 * h + 1])};
            PIN(wr[t][h]); PIN(wi[t][h]);
          }
      };
      mf4<false, 0, 0>(e0, e1, e2, e3, cc.a2re[0][1], b21, cc.a2im[0][1], b21, cc.a2re[1][1], b21, cc.a2im[1][1], b21);
      SB();
      unpack_w();
      SB();
      if constexpr (L == 0) { fill_h(OFF_H, 0); fill_h(OFF_H, 1); }
      XF(39);
      u32x4 a30, a31;
      auto step3 = [&](auto I) {
        constexpr int i = decltype(I)::value;
        mf0<true, 0>(i == 0 ? d0 : i == 1 ? d1 : i == 2 ? d2 : d3, a30, i == 0 ? cc.b3re[0][0] : i == 1 ? cc.b3im[0][0] : i == 2 ? cc.b3re[1][0] : cc.b3im[1][0]);
      };
      if (phi != 0) {
        a30 = cmul_b(e0, e1, wr[0], wi[0]);
        PIN(a30);
        a31 = cplx_under<true, false>(e2, e3, wr[1], wi[1], step3);
      } else {
        u32* pl = (u32*)(lds + LDS_STASH);
        const u32x4 h0 = {pkh(e0[0], e1[0]), pkh(e0[1], e1[1]), pkh(e0[2], e1[2]), pkh(e0[3], e1[3])};
        const u32x4 h1 = {pkh(e2[0], e3[0]), pkh(e2[1], e3[1]), pkh(e2[2], e3[2]), pkh(e2[3], e3[3])};
        if (k1l != 0) {
          *(u32x4*)(pl + k1l * 32 + ((g ^ (k1l >> 1)) & 7) * 4) = h0; *(u32x4*)(pl + k1l * 32 + (((4 + g) ^ (k1l >> 1)) & 7) * 4) = h1;
        } else {
#pragma unroll
          for (int r = 0; r < 4; ++r) { pl[(4 * g + r - 1) & 31] = h0[r]; pl[16 + 4 * g + r - 1] = h1[r]; }
        }
        bar();
        vmwait<4>();
#pragma unroll
        for (int j = 0; j < 8; ++j) PIN(wa[j]);
        if constexpr (L == 0) {
          RETRY(max4(max4(max4(max4(max4(max4(max4(max4(0, wa[0]), wa[1]), wa[2]), wa[3]), wa[4]), wa[5]), wa[6]), wa[7]) == SENT, {
            static_for<8>([&](auto J) { constexpr int j = decltype(J)::value; ld4r<0>(wa[j], t16, ws + OFF_WNY + j * 4096); });
            vmwait<0>();
            _Pragma("unroll") for (int j = 0; j < 8; ++j) PIN(wa[j]);
          });
        }
        const int kr = (64 - k1l) & 63;
        const u32x4 q0 = *(const u32x4*)(pl + kr * 32 + (((7 - g) ^ (kr >> 1)) & 7) * 4), q1 = *(const u32x4*)(pl + kr * 32 + (((3 - g) ^ (kr >> 1)) & 7) * 4);
#pragma unroll
        for (int r = 0; r < 4; ++r) {
          const f32x4 c0 = __builtin_bit_cast(f32x4, wa[r]), c1 = __builtin_bit_cast(f32x4, wa[4 + r]);
          const u32 u0 = q0[3 - r], u1 = q1[3 - r];
          const f16x2 v0 = __builtin_bit_cast(f16x2, u0), v1 = __builtin_bit_cast(f16x2, u1);
          const float qr0 = (float)v0.x, qi0 = (float)v0.y, qr1 = (float)v1.x, qi1 = (float)v1.y;
          a30[r] = pkb(e0[r] * c0[0] - e1[r] * c0[1] + qr0 * c0[2] + qi0 * c0[3], e0[r] * c0[1] + e1[r] * c0[0] + qr0 * c0[3] - qi0 * c0[2]);
          a31[r] = pkb(e2[r] * c1[0] - e3[r] * c1[1] + qr1 * c1[2] + qi1 * c1[3], e2[r] * c1[1] + e3[r] * c1[0] + qr1 * c1[3] - qi1 * c1[2]);
        }
        PIN(a30); PIN(a31);
        asm volatile("s_nop 1");
        static_for<4>(step3);
      }
      u8* ex = lds + LDS_SCR + 16384;
      mf4<true, 0, 5>(d0, d1, d2, d3, a31, cc.b3re[0][1], a31, cc.b3im[0][1], a31, cc.b3re[1][1], a31, cc.b3im[1][1]);
      XF(40);
      *(u32x4*)(ex + w * 1024 + l16) = twiddle<true>(d0, d1, cc.t2c[0], cc.t2s[0]);
      SB();
      *(u32x4*)(ex + (4 + w) * 1024 + l16) = twiddle<true>(d2, d3, cc.t2c[1], cc.t2s[1]);
      if constexpr (L == 0) vmwait<2>();
      bar();
      XF(41);
      const u8* er = ex + (w & 1) * 4096 + l16;
      const u32x4 g0 = *(const u32x4*)(er), g1 = *(const u32x4*)(er + 1024), g2 = *(const u32x4*)(er + 2048), g3 = *(const u32x4*)(er + 3072);
      mm8<true, 7>(d0, d1, d2, d3, cc.a4re[0][0], g0, cc.a4re[0][1], g1, cc.a4im[0][0], g0, cc.a4im[0][1], g1, cc.a4re[1][0], g2, cc.a4re[1][1], g3, cc.a4im[1][0], g2,
                   cc.a4im[1][1], g3);
      {
        XF(42);
        const f32x2 lr0 = lo2(d0) + lo2(d2), li0 = lo2(d1) + lo2(d3), hr0 = lo2(d0) - lo2(d2), hi0 = lo2(d1) - lo2(d3);
        const f32x2 lr1 = hi2(d0) + hi2(d2), li1 = hi2(d1) + hi2(d3), hr1 = hi2(d0) - hi2(d2), hi1 = hi2(d1) - hi2(d3);
        if (phi != 0) {
          const f32x2 pr0 = lr0 - hi0, pi0 = li0 + hr0, qr0 = lr0 + hi0, qi0 = hr0 - li0;
          st4(mark4<1u - L, L>(u32x4{pkb(pr0.x, pi0.x), pkb(qr0.x, qi0.x), pkb(pr0.y, pi0.y), pkb(qr0.y, qi0.y)}), uo);
          const f32x2 pr1 = lr1 - hi1, pi1 = li1 + hr1, qr1 = lr1 + hi1, qi1 = hr1 - li1;
          st4(mark4<1u - L, L>(u32x4{pkb(pr1.x, pi1.x), pkb(qr1.x, qi1.x), pkb(pr1.y, pi1.y), pkb(qr1.y, qi1.y)}), uo + 1024);
          MEMFENCE();
        } else {
          st4(mark4<1u - L, L>(u32x4{pkb(lr0.x, hr0.x), pkb(li0.x, hi0.x), pkb(lr0.y, hr0.y), pkb(li0.y, hi0.y)}), uo);
          st4(mark4<1u - L, L>(u32x4{pkb(lr1.x, hr1.x), pkb(li1.x, hi1.x), pkb(lr1.y, hr1.y), pkb(li1.y, hi1.y)}), uo + 1024);
        }
      }
    }
    ST(S0 + 3);
    {
      FRESH();
      SB();
      row_consts<true, 0>(rc, m, g);
      f32x2 xa[4], xb[4];
#pragma unroll
      for (int i = 0; i < 4; ++i) {
        xa[i] = f32x2{blo(xl[i >> 1][i & 1]), bhi(xl[i >> 1][i & 1])}; xb[i] = f32x2{blo(xh[i >> 1][i & 1]), bhi(xh[i >> 1][i & 1])};
      }
      const float ca = first_lane(xa[0].x), cb = first_lane(xb[0].x);
      float ch = lane < 32 ? ca : cb;
      PIN(ch);
#pragma unroll
      for (int i = 0; i < 4; ++i) { xa[i] -= ca; xb[i] -= cb; PIN(xa[i]); PIN(xb[i]); }
      SB();
      u32x4 c0, c1;
      const u32 uo = (tid >> 1) * 8192 + (tid & 1) * 16;
      const u8* ub0 = ws + OFF_U + pos * 32;
      const u8* ub1 = ub0 + 128 * 8192;
      row_consts<true, 1>(rc, m, g);
      SB();
      __builtin_amdgcn_s_sleep(1);
      ld4<0>(c0, uo, ub0); ld4<0>(c1, uo, ub1);
      if constexpr (L == 0) { fill_h(OFF_H, 2); fill_h(OFF_H, 3); fill_h(OFF_H, 4); }
      SB();
      if constexpr (L == 1) ring4(4); else { fill_h(OFF_H, 5); fill_h(OFF_H, 6); }
      u32* img = (u32*)(lds + LDS_SCR);
      u32 ip0, iq0, iq1, xno, tw0, tw1, trd;
      {
        const int ph = tid >> 1, fq0 = ph ? 512 - ph : 256, fq1 = 384 - ph;
        ip0 = ((tid & 1) * 1280 + (ph & 31) * 20 + (ph >> 5)) * 4;
        iq0 = ((tid & 1) * 1280 + (fq0 & 31) * 20 + (fq0 >> 5)) * 4; iq1 = ((tid & 1) * 1280 + (fq1 & 31) * 20 + (fq1 >> 5)) * 4;
        xno = OFF_A + (pos >> 1) * 16384 + (tid >> 3) * 256 + (pos & 1) * 128 + (tid & 7) * 16;
        tw0 = (4 * m + g) * 128 + ((2 * w) ^ (m & 7)) * 16; tw1 = (4 * m + g) * 128 + ((2 * w + 1) ^ (m & 7)) * 16;
        trd = (tid >> 3) * 128 + ((tid & 7) ^ ((tid >> 5) & 7)) * 16;
        PIN(ip0); PIN(iq0); PIN(iq1); PIN(xno); PIN(tw0); PIN(tw1); PIN(trd);
      }
      SB();
      ST(S0 + 4);
      vmwait<(L == 0 ? 5 : 4)>(); PIN(c0); PIN(c1);
      {
        RETRY((stale<L == 1, 1u>(red8<L == 1>(c0, c1))), {
          CNT(3);
          if (bad_) { ld4r<0>(c0, uo, ub0); ld4r<0>(c1, uo, ub1); }
          vmwait<0>(); PIN(c0); PIN(c1);
        });
      }
      ST(S0 + 5);
      {
        u8* d = (u8*)img;
        *(u32*)(d + ip0) = c0.x; *(u32*)(d + iq0) = c0.y; *(u32*)(d + 2560 + ip0) = c0.z; *(u32*)(d + 2560 + iq0) = c0.w;
        *(u32*)(d + ip0 + 16) = c1.x; *(u32*)(d + iq1) = c1.y; *(u32*)(d + 2560 + ip0 + 16) = c1.z; *(u32*)(d + 2560 + iq1) = c1.w;
      }
      bar();
      XF(43);
      const u32x4 a0 = *(const u32x4*)(img + w * 640 + m * 20 + 4 * g), a1 = *(const u32x4*)(img + w * 640 + (16 + m) * 20 + 4 * g);
      if constexpr (L == 1) ring4(5);
      if constexpr (L == 0) ring4(0);
      f32x4 dr0, di0, dr1, di1;
      row_fft<true, 7>(rc, a0, a1, dr0, di0, dr1, di1);
      if constexpr (L == 0) ring4(1);
      XF(44);
      const float ys = 1.0f / 32768.0f;
      xa[0] += lo2(dr0) * ys; xa[1] += hi2(dr0) * ys; xa[2] += lo2(dr1) * ys; xa[3] += hi2(dr1) * ys;
      xb[0] += lo2(di0) * ys; xb[1] += hi2(di0) * ys; xb[2] += lo2(di1) * ys; xb[3] += hi2(di1) * ys;
      const f32x2 sa = (xa[0] + xa[1]) + (xa[2] + xa[3]), sb = (xb[0] + xb[1]) + (xb[2] + xb[3]);
      f32x2 qa = xa[0] * xa[0], qb = xb[0] * xb[0];
#pragma unroll
      for (int i = 1; i < 4; ++i) { qa += xa[i] * xa[i]; qb += xb[i] * xb[i]; }
      if constexpr (L == 0) { SB(); ring4(2); SB(); }
      f32x2 R, O;
      XF(45);
      ln_coef(f32x2{sa.x + sa.y, sb.x + sb.y}, f32x2{qa.x + qa.y, qb.x + qb.y}, ch, R, O);
      XF(46);
      u32x4 na, nb;
#pragma unroll
      for (int i = 0; i < 4; ++i) {
        const f32x2 va = xa[i] * R.x + O.x, vb = xb[i] * R.y + O.y;
        na[i] = mark<0x10000u, 1u - L>(pkb(va.x, va.y)); nb[i] = mark<0x10000u, 1u - L>(pkb(vb.x, vb.y));
      }
      u8* tr = lds + LDS_SCR + 12288;
      *(u32x4*)(tr + tw0) = na; *(u32x4*)(tr + tw1) = nb;
      if constexpr (L == 0) vmwait<12>(); else vmwait<0>();
      bar();
      XF(47);
      st4(*(const u32x4*)(tr + trd), xno);
      st4(*(const u32x4*)(tr + 4096 + trd), xno + 8192);
      SB();
#pragma unroll
      for (int i = 0; i < 4; ++i) { xa[i] += ca; xb[i] += cb; }
      const u32x4 rl = {pkb(xa[0].x, xa[0].y), pkb(xa[1].x, xa[1].y), pkb(xa[2].x, xa[2].y), pkb(xa[3].x, xa[3].y)};
      const u32x4 rh = {pkb(xb[0].x, xb[0].y), pkb(xb[1].x, xb[1].y), pkb(xb[2].x, xb[2].y), pkb(xb[3].x, xb[3].y)};
      if constexpr (L == 0) {
        *(u32x4*)(lds + LDS_STASH + t16) = rl; *(u32x4*)(lds + LDS_STASH + 4096 + t16) = rh;
        XS(44);
        ring4(3); ring4(4); ring4(5); ring4(6); ring4(7);
      } else {
        xl[0] = u32x2{rl.x, rl.y}; xl[1] = u32x2{rl.z, rl.w}; xh[0] = u32x2{rh.x, rh.y}; xh[1] = u32x2{rh.z, rh.w};
        PIN(xl[0]); PIN(xl[1]); PIN(xh[0]); PIN(xh[1]);
        __builtin_amdgcn_s_sleep(XSLP);
      }
    }
    ST(S0 + 6);
    {
      FRESH();
      u32x4 tk[32], he[32], hl[16];
      u32x4 hh[8], hf[8];
      f32x4 ac1[6][2], ac2[4][2], ac3[4][2];
      const u8* hs0 = ws + OFF_H + (4 * G + 2 * half2) * 114688 + (4 * w) * 7168;
      const u8* hs[4];
      const u8* hp[4];
#pragma unroll
      for (int j = 0; j < 4; ++j) {
        hs[j] = hs0 + opq((j + ct) & 3) * 7168; hp[j] = hs0 + opq(j ^ (2 * (ct & 1))) * 7168 + 6144;
        PINS(hs[j]); PINS(hp[j]);
      }
      const u8* w2src = ws + OFF_W2 + (ct * 4 + w) * 57344;
      u32 hv[4], hw[2];
#pragma unroll
      for (int u = 0; u < 4; ++u) { hv[u] = l16 + ((u ^ w) >> 1) * 114688 + ((u ^ w) & 1) * 3072; PIN(hv[u]); }
#pragma unroll
      for (int q = 0; q < 2; ++q) { hw[q] = l16 + (q ^ (w >> 1)) * 114688; PIN(hw[q]); }
      auto hload = [&](auto RE, auto KC, auto UC) {
        constexpr int k = decltype(KC)::value, u = decltype(UC)::value;
        constexpr bool re = decltype(RE)::value;
        if constexpr (k == 4 || k == 5) {
          if constexpr (re) ld4r<0>(hh[4 * (k - 4) + u], hw[u >> 1], hp[2 * (k - 4) + (u & 1)]); else ld4<0>(hh[4 * (k - 4) + u], hw[u >> 1], hp[2 * (k - 4) + (u & 1)]);
        } else {
          constexpr int j = k < 4 ? k : k < 10 ? k - 6 : k - 10, o = k < 4 ? 0 : k < 10 ? 1024 : 2048;
          u32x4& d = k < 10 ? he[(k < 4 ? 0 : 16) + 4 * j + u] : hl[4 * j + u];
          if constexpr (re) ld4r<o>(d, hv[u], hs[j]); else ld4<o>(d, hv[u], hs[j]);
        }
      };
      auto hseq = [&](auto IC) {
        constexpr int i = decltype(IC)::value;
        hload(std::false_type{}, std::integral_constant<int, (i >> 2)>{}, std::integral_constant<int, i & 3>{});
      };
      u64 bm0 = 0;
      {
        const u8* w1b = ws + OFF_W1 + member * 229376 + 131072 + w * 8192;
        const u8* tk0 = ws + OFF_A + (8 * G + 2 * w) * 16384 + 4096;
        const u8* tk1 = tk0 + 8192;
        u32 tv1 = l16 + 16384, llo = ldsbase + l16, lhi = llo + 65536;
        u32 hd0 = OFF_H + (4 * G + w) * 114688 + member * 7168 + l16, hd1 = hd0 + 3072;
        PIN(tv1); PIN(llo); PIN(lhi); PIN(hd0); PIN(hd1); PINS(tk0); PINS(tk1);
        u32 x3o = OFF_X3 + (8 * pos + 2 * w) * 1024 + xo;
        if constexpr (L == 1) PIN(x3o);
        auto tok = [&](auto RE, auto QC) {
          constexpr int q = decltype(QC)::value, ks = q >> 1;
          if constexpr (decltype(RE)::value) ld4r<(ks & 7) * 1024 - 4096>(tk[q], q & 1 ? tv1 : l16, ks >> 3 ? tk1 : tk0);
          else ld4<(ks & 7) * 1024 - 4096>(tk[q], q & 1 ? tv1 : l16, ks >> 3 ? tk1 : tk0);
        };
        constexpr bool TONE = L == 0;
        constexpr u32 TBIT = L == 0 ? 1u : 0x10000u;
        u32 mx0 = 0, mx1 = 0;
        auto mx0_fin = [&]() { return red2<TONE>(mx0, mx1) & TBIT; };
        auto fix = [&](auto QC, u64 bad) {
          constexpr int q0 = decltype(QC)::value;
          if (__builtin_expect(bad != 0, 0)) {
            _Pragma("nounroll") for (int sp = 0; sp < SPIN_CAP; ++sp) {
              CNT(4);
              static_for<4>([&](auto I) { tok(std::true_type{}, std::integral_constant<int, q0 + decltype(I)::value>{}); });
              vmwait<0>();
#pragma unroll
              for (int q = q0; q < q0 + 4; ++q) PIN(tk[q]);
              if (!__any(stale<TONE, TBIT>(red2<TONE>(red8<TONE>(tk[q0], tk[q0 + 1]), red8<TONE>(tk[q0 + 2], tk[q0 + 3]))))) break;
            }
          }
        };
        constexpr int PD = 6;
        u32x4 af[PD];
        auto frag = [&](auto XC) {
          constexpr int x = decltype(XC)::value, o = (x & 127) * 1024;
          lda<o & 65535>(af[x % PD], o < 65536 ? llo : lhi);
        };
        if constexpr (L == 1) static_for<PD - 1>(frag);
        XF(48);
        static_for<NTOK0>([&](auto Q) {
          tok(std::false_type{}, Q);
          if constexpr (decltype(Q)::value == 3) bar_nw();
        });
        XF(49);
        if constexpr (L == 0) {
          vmwait<NTOK0>();
          bar_nw();
          static_for<PD - 1>(frag);
        }
        vmwait<NTOK0 - 4>();
        ST(S0 + 7);
#pragma unroll
        for (int q = 0; q < 4; ++q) PIN(tk[q]);
        mx0 = red8<TONE>(tk[0], tk[1]); mx1 = red8<TONE>(tk[2], tk[3]);
        fix(std::integral_constant<int, 0>{}, __ballot(stale<TONE, TBIT>(red2<TONE>(mx0, mx1))));
        lgwait<PD - 2>();
        SB();
        ST(S0 + 8);
        float eg[2][2];
        u32 ev[2];
        u64 bm = 0;
        u32x4 eo[1];
        auto epi = [&](auto NC) {
          constexpr int n = decltype(NC)::value;
          if constexpr (n < NEPI1 + NEPI2) {
            constexpr bool p1 = n < NEPI1;
            constexpr int n0 = p1 ? n : n - NEPI1, i = n0 - n0 / 25;
            if constexpr (n0 % 25 == 24) st4(eo[0], p1 ? (n == 24 ? hd0 : n == 49 ? hd1 : hd1 + 3072) : (n0 == 24 ? hd0 : hd1) + 1024);
            else {
              constexpr int s = i & 1, st = (i % 12) >> 1, blk = i / 12;
              constexpr int u = p1 ? (blk < 4 ? blk >> 1 : blk - 4) : blk >> 1, pr = p1 ? (blk < 4 ? blk & 1 : 2) : blk & 1;
              constexpr int d = 2 * (pr == 2 ? u : pr) + s;
              const f32x4& ga = p1 ? ac1[2 * pr][u] : ac2[2 * (pr & 1)][u];
              const f32x4& up = p1 ? ac1[2 * pr + 1][u] : ac2[2 * (pr & 1) + 1][u];
              if constexpr (st < 2) { eg[s][st] = relu(ga[2 * s + st]); PIN(eg[s][st]); }
              else if constexpr (st < 4) { eg[s][st - 2] *= up[2 * s + st - 2]; PIN(eg[s][st - 2]); }
              else if constexpr (st == 4) { ev[s] = pkb(eg[s][0], eg[s][1]); PIN(ev[s]); }
              else { u32 v = mark<1u - L, L>(ev[s]); PIN(v); eo[0][d] = v; }
            }
            SB();
          }
        };
        static_for<224>([&](auto XC) {
          constexpr int x = decltype(XC)::value, job = G1S[L].job[x], ji = G1S[L].idx[x], e0 = G1S[L].epi[x], e1 = G1S[L].epi[x + 1];
          if constexpr (x == 32 || x == 64) bar_nw();
          if constexpr (x == 97) { vmwait<g1_wait(L, x, 2, 7)>(); bar_nw(); }
          if constexpr (x == 150) { vmwait<g1_wait(L, x, 2, 23)>(); bar_nw(); }
          constexpr int cj = (x + 12) / 12, co = x + 12 - 12 * cj;
          constexpr bool chk = x < 84 && co >= 5;
          constexpr int ks = x < 96 ? x / 6 : x < 160 ? (x - 96) >> 2 : (x - 160) >> 2, t = x < 96 ? x % 6 : x & 3;
          auto mm = [&](int u) {
            f32x4& a = x < 96 ? ac1[x < 96 ? t : 0][u] : x < 160 ? ac2[x < 96 ? 0 : t][u] : ac3[x < 96 ? 0 : t][u];
            mmw<ks == 0>(a, af[x % PD], tk[2 * ks + u]);
          };
          SB();
          mm(0);
          SB();
          if constexpr (x < 223) lgwait<(x + PD - 2 < 224 ? PD - 3 : 222 - x)>();
          if constexpr (chk && co == 5) {
            vmwait<g1_wait(L, x, 1, 4 * cj + 3)>();
            PIN(tk[4 * cj]); PIN(tk[4 * cj + 1]); PIN(tk[4 * cj + 2]); PIN(tk[4 * cj + 3]);
          }
          SB();
          if constexpr (chk && co >= 6 && co < 10) {
            const u32x4 v = tk[4 * cj + co - 6];
            mx0 = co == 6 ? red2<TONE>(v.x, v.y) : red3<TONE>(mx0, v.x, v.y); PIN(mx0); SB();
            mx1 = co == 6 ? red2<TONE>(v.z, v.w) : red3<TONE>(mx1, v.z, v.w); PIN(mx1);
          }
          if constexpr (chk && co == 10) { mx0 = mx0_fin(); PIN(mx0); SB(); bm = __ballot(TONE ? mx0 == 0 : mx0 != 0); PINS(bm); }
          if constexpr (x >= 212 && x < 218) {
            if constexpr (x == 212) { vmwait<g1_wait(L, x, 4, 3)>(); PIN(he[0]); PIN(he[1]); PIN(he[2]); PIN(he[3]); }
            else if constexpr (x < 217) {
              const u32x4 v = he[x - 213];
              mx0 = x == 213 ? red2<L == 1>(v.x, v.y) : red3<L == 1>(mx0, v.x, v.y); PIN(mx0); SB();
              mx1 = x == 213 ? red2<L == 1>(v.z, v.w) : red3<L == 1>(mx1, v.z, v.w); PIN(mx1);
            } else { mx0 = red2<L == 1>(mx0, mx1) & 1u; PIN(mx0); SB(); bm0 = __ballot(L == 1 ? mx0 == 0 : mx0 != 0); PINS(bm0); }
          }
          if constexpr (e0 < e1) epi(std::integral_constant<int, e0>{});
          if constexpr (e0 + 1 < e1) epi(std::integral_constant<int, e0 + 1>{});
          SB();
          mm(1);
          SB();
          if constexpr (chk && co == 11) fix(std::integral_constant<int, 4 * cj>{}, bm);
          if constexpr (job == 2 && (ji & 3) == 0) set_m0(ldsbase + (ji >> 3) * 32768 + w * 8192 + (ji & 4) * 1024);
          SB();
          if constexpr (x + PD - 1 < 224) frag(std::integral_constant<int, x + PD - 1>{});
          SB();
          if constexpr (job == 1) tok(std::false_type{}, std::integral_constant<int, ji>{});
          if constexpr (job == 2) dma1<(ji & 3) * 1024>(l16, w1b + (ji >> 3) * 32768 + (ji & 4) * 1024);
          if constexpr (job == 3) ld4a<(ji & 7) * 1024 - 4096>(w2a[ji >> 2][ji & 3], l16, w2src + (ji >> 3) * 8192 + 4096);
          if constexpr (job == 6) st2(ji & 2 ? xh[ji & 1] : xl[ji & 1], x3o + ji * 512);
          if constexpr (job == 4) hseq(std::integral_constant<int, ji>{});
          if constexpr (e0 + 2 < e1) epi(std::integral_constant<int, e0 + 2>{});
          SB();
        });
        ST(S0 + 9);
        asm volatile("s_nop 7");
        SB();
#pragma unroll
        for (int u = 0; u < 2; ++u) {
          u32x4 o;
#pragma unroll
          for (int p = 0; p < 2; ++p) {
            const f32x4 ga = ac3[2 * p][u], up = ac3[2 * p + 1][u];
            const f32x2 a = f32x2{relu(ga[0]), relu(ga[1])} * lo2(up), c = f32x2{relu(ga[2]), relu(ga[3])} * hi2(up);
            o[2 * p] = mark<1u - L, L>(pkb(a.x, a.y)); o[2 * p + 1] = mark<1u - L, L>(pkb(c.x, c.y));
          }
          st4(o, (u ? hd1 : hd0) + 2048);
        }
        SB();
      }
      ST(S0 + 10);
      {
        f32x4 acc[4][4];
        u32x4 x3a, x3b;
        u32 eo = 0, oo = 0, rw[3], ta = 0, tb = 0;
        auto addr = [&](auto IC) {
          constexpr int i = decltype(IC)::value;
          const u32 sb = (128 * G + 64 * half2 + 16 * w) * 1024 + ct * 128;
          if constexpr (i < 3) { rw[i] = l16 + (((i + 1) ^ w) * 3 + i) * 4096; PIN(rw[i]); }
          if constexpr (i == 3) { ta = (l16 >> 4) & 0x30; PIN(ta); }
          if constexpr (i == 4) { tb = l16 & (L == 1 ? 0xf0 : 0xe0); PIN(tb); }
          if constexpr (i == 5) { eo = tb * 64 + sb; PIN(eo); }
          if constexpr (i == 6) { tb = l16 & 0x10; PIN(tb); }
          if constexpr (i == 7) { tb = tb * 4 + ta; PIN(tb); }
          if constexpr (i == 8) { eo += L == 1 ? ta : tb; PIN(eo); }
          if constexpr (L == 1) {
            if constexpr (i == 9) { ta = l16 & 0x20; PIN(ta); }
            if constexpr (i == 10) { oo = ta * 1024 + tb; PIN(oo); }
            if constexpr (i == 11) { ta = l16 & 0x80; PIN(ta); }
            if constexpr (i == 12) { oo += ta * 16; PIN(oo); }
            if constexpr (i == 13) { ta = l16 & 0x40; PIN(ta); }
            if constexpr (i == 14) { oo += ta * 16; PIN(oo); }
            if constexpr (i == 15) { oo += row_of_pos(16 * G + 8 * half2 + 2 * w, 0) * 1024 + ct * 128; PIN(oo); }
          }
        };
        const u8* x3s = ws + OFF_X3;
        u64 bm = 0;
        PINS(x3s);
        constexpr int NEARLY = g1_ne(L), LB = 40 - NEARLY, XG = 31;
        auto cum = [](int n) constexpr { const int c = n - n / 8; return c < LB + 16 ? c : LB + 16; };
        auto young = [cum](int k, int n) constexpr {
          const int i = 4 * k + 3;
          const int done = k < 10 ? i - NEARLY + 1 : LB + 4 + 3 * (k - 10);
          const int xr = (L == 1 && n > XG && done <= cum(XG)) ? 2 : 0;
          if (k < 10 && i < NEARLY) return cum(n) + 2 + G1S[L].cum[224] - G1S[L].pos[4][i] - 1 + xr;
          return cum(n) - done + xr;
        };
        constexpr bool HONE = L == 1;
        u32 mx0 = 0, mx1 = 0;
        auto mx0_fin = [&]() { return red2<HONE>(mx0, mx1) & 1u; };
        auto pin_k = [&](auto KC) {
          constexpr int k = decltype(KC)::value;
#pragma unroll
          for (int u = 0; u < 4; ++u) {
            if constexpr (k < 4) PIN(he[4 * k + u]);
            else if constexpr (k < 6) PIN(hh[4 * (k - 4) + u]);
            else if constexpr (k < 10) PIN(he[16 + 4 * (k - 6) + u]);
            else PIN(hl[4 * (k - 10) + u]);
          }
        };
        auto opnd = [&](auto KC, int u) {
          constexpr int k = decltype(KC)::value;
          if constexpr (k < 4) return he[4 * k + u];
          else if constexpr (k < 6) return hh[4 * (k - 4) + u];
          else if constexpr (k < 10) return he[16 + 4 * (k - 6) + u];
          else return hl[4 * (k - 10) + u];
        };
        u32 wodd = (w & 1) ? ~0u : 0u;
        PIN(wodd);
        auto mkhalf = [&](auto KC, auto IC) {
          constexpr int k = decltype(KC)::value, i = decltype(IC)::value, u = i >> 2, d = i & 3;
          const u32x4& src = hh[4 * (k - 4) + (u & 2) + (d >> 1)];
          u32 v = (src[2 * ((u & 1) ^ 1) + (d & 1)] & wodd) | (src[2 * (u & 1) + (d & 1)] & ~wodd);
          PIN(v);
          hf[4 * (k - 4) + u][d] = v;
        };
        auto fixk = [&](auto KC, u64 bad) {
          constexpr int k = decltype(KC)::value;
          if (__builtin_expect(bad != 0, 0)) {
            _Pragma("nounroll") for (int sp = 0; sp < SPIN_CAP; ++sp) {
              CNT(k < 10 ? 5 : 6);
              static_for<4>([&](auto U) { hload(std::true_type{}, KC, U); });
              vmwait<0>();
              pin_k(KC);
              if (!__any(stale<HONE, 1u>(red2<HONE>(red8<HONE>(opnd(KC, 0), opnd(KC, 1)), red8<HONE>(opnd(KC, 2), opnd(KC, 3)))))) break;
            }
            if constexpr (k == 4 || k == 5) static_for<16>([&](auto I) { mkhalf(KC, I); });
          }
        };
        if constexpr (L == 0) {
#pragma unroll
          for (int k = 0; k < 14; ++k) { PINA(w2a[k][0]); PINA(w2a[k][1]); PINA(w2a[k][2]); PINA(w2a[k][3]); }
        }
        fixk(std::integral_constant<int, 0>{}, bm0);
        auto qchk = [&](auto QC, auto JC) {
          constexpr int qd = decltype(QC)::value, j = decltype(JC)::value, u = (qd + 1) & 3;
          if constexpr (j == 0) { PIN(hl[4 + u]); PIN(hl[8 + u]); PIN(hl[12 + u]); }
          else if constexpr (j < 4) {
            const u32x4 v = hl[4 * j + u];
            mx0 = j == 1 ? red2<HONE>(v.x, v.y) : red3<HONE>(mx0, v.x, v.y); PIN(mx0); SB();
            mx1 = j == 1 ? red2<HONE>(v.z, v.w) : red3<HONE>(mx1, v.z, v.w); PIN(mx1);
          } else if constexpr (j == 4) { mx0 = mx0_fin(); PIN(mx0); SB(); bm = __ballot(HONE ? mx0 == 0 : mx0 != 0); PINS(bm); }
          else if (__builtin_expect(bm != 0, 0)) {
            _Pragma("nounroll") for (int sp = 0; sp < SPIN_CAP; ++sp) {
              CNT(6);
              static_for<3>([&](auto K) { hload(std::true_type{}, std::integral_constant<int, 11 + decltype(K)::value>{}, std::integral_constant<int, u>{}); });
              vmwait<0>();
              PIN(hl[4 + u]); PIN(hl[8 + u]); PIN(hl[12 + u]);
              if (!__any(stale<HONE, 1u>(red4<HONE>(red8<HONE>(hl[4 + u], hl[8 + u]), hl[12 + u])))) break;
            }
          }
        };
        ST(S0 + 11);
        static_for<44>([&](auto NC) {
          constexpr int n = decltype(NC)::value, k = n >> 2, c = n & 3;
          using KC = std::integral_constant<int, k>;
          using KN = std::integral_constant<int, k + 1>;
          static_for<4>([&](auto UC) {
            constexpr int u = decltype(UC)::value;
            SB();
            if constexpr (k == 4 || k == 5) mmw<false>(acc[c][u], w2a[k][c], hf[4 * (k - 4) + u]);
            else mmw<k == 0>(acc[c][u], w2a[k][c], opnd(KC{}, u));
            SB();
            constexpr bool hk = k == 3 || k == 4;
            constexpr int j = u == 3 ? -9 : 3 * c + u - (hk ? 1 : 5);
            if constexpr (k == 10) {
              if constexpr (j == -1) vmwait<clamp63(young(11, n))>();
              if constexpr (j >= -1 && j < 4) qchk(std::integral_constant<int, 0>{}, std::integral_constant<int, j + 1>{});
            }
            if constexpr (k < 10 && j == -1) { vmwait<clamp63(young(k + 1, n))>(); pin_k(KN{}); }
            if constexpr (k < 10 && j >= 0 && j < 4) {
              const u32x4 v = opnd(KN{}, j);
              mx0 = j == 0 ? red2<HONE>(v.x, v.y) : red3<HONE>(mx0, v.x, v.y); PIN(mx0); SB();
              mx1 = j == 0 ? red2<HONE>(v.z, v.w) : red3<HONE>(mx1, v.z, v.w); PIN(mx1);
            }
            if constexpr ((k < 3 || k == 5) && c == 0 && u < 2) {
              constexpr int i0 = 4 * (k < 3 ? k : 3) + 2 * u;
              addr(std::integral_constant<int, i0>{}); SB(); addr(std::integral_constant<int, i0 + 1>{});
            }
            if constexpr (hk && j >= 4 && j != 5) {
              constexpr int i0 = j == 4 ? 0 : 2 + 3 * (j - 6);
              mkhalf(KN{}, std::integral_constant<int, i0>{}); SB(); mkhalf(KN{}, std::integral_constant<int, i0 + 1>{});
              if constexpr (j >= 6 && i0 + 2 < 16) { SB(); mkhalf(KN{}, std::integral_constant<int, i0 + 2>{}); }
            }
            if constexpr (k < 10 && j == 5) { mx0 = mx0_fin(); PIN(mx0); SB(); bm = __ballot(HONE ? mx0 == 0 : mx0 != 0); PINS(bm); }
            if constexpr (u == 3) {
              constexpr int q = cum(n);
              if constexpr (cum(n + 1) == q) {}
              else if constexpr (q < LB) hseq(std::integral_constant<int, NEARLY + q>{});
              else if constexpr (q < LB + 4) hload(std::false_type{}, std::integral_constant<int, 10>{}, std::integral_constant<int, q - LB>{});
              else hload(std::false_type{}, std::integral_constant<int, 11 + (q - LB - 4) % 3>{}, std::integral_constant<int, ((q - LB - 4) / 3 + 1) & 3>{});
              if constexpr (n == 23) {
                bar_nw();
              }
              if constexpr (L == 1 && n == XG) { ld4<0>(x3a, eo, x3s); ld4<64>(x3b, eo, x3s); }
              if constexpr (c == 3) {
                if constexpr (k < 10) fixk(KN{}, bm); else qchk(std::integral_constant<int, 0>{}, std::integral_constant<int, 5>{});
              }
            }
          });
        });
        static_for<16>([&](auto TC) {
          constexpr int t = decltype(TC)::value, u = ((t >> 2) + 1) & 3, c = t & 3;
          using QN = std::integral_constant<int, (t >> 2) + 1>;
          SB();
          mmw<false>(acc[c][u], w2a[11][c], hl[4 + u]);
          SB();
          if constexpr (t < 12) {
            if constexpr (c == 0) { vmwait<young(12 + (t >> 2), 44)>(); qchk(QN{}, std::integral_constant<int, 0>{}); }
            else qchk(QN{}, std::integral_constant<int, c>{});
          }
          SB();
          mmw<false>(acc[c][u], w2a[12][c], hl[8 + u]);
          SB();
          if constexpr (t >= 3 && t < 15) *(f32x4*)(lds + rw[(t - 3) >> 2] + ((t - 3) & 3) * 1024) = acc[(t - 3) & 3][((t - 3) >> 2) + 1];
          SB();
          mmw<false>(acc[c][u], w2a[13][c], hl[12 + u]);
          SB();
          if constexpr (t < 12 && c == 3) { qchk(QN{}, std::integral_constant<int, 4>{}); qchk(QN{}, std::integral_constant<int, 5>{}); }
        });
        SB();
        ST(S0 + 12);
        bar();
        f32x4 s[3][4];
#pragma unroll
        for (int c = 0; c < 4; ++c)
#pragma unroll
          for (int p = 0; p < 3; ++p) s[p][c] = *(const f32x4*)(lds + w * 12288 + p * 4096 + c * 1024 + l16);
        SB();
        if constexpr (L == 1) { vmwait<0>(); PIN(x3a); PIN(x3b); }
        u32x4 y[2];
#pragma unroll
        for (int a = 0; a < 2; ++a) {
          const f32x4 s0 = (acc[2 * a][0] + s[0][2 * a]) + (s[1][2 * a] + s[2][2 * a]);
          const f32x4 s1 = (acc[2 * a + 1][0] + s[0][2 * a + 1]) + (s[1][2 * a + 1] + s[2][2 * a + 1]);
          y[a] = u32x4{pkb(s0[0], s0[1]), pkb(s0[2], s0[3]), pkb(s1[0], s1[1]), pkb(s1[2], s1[3])};
          if constexpr (L == 1) {
            const u32x4 xr = a ? x3b : x3a;
#pragma unroll
            for (int p = 0; p < 4; ++p) y[a][p] = pkb(blo(xr[p]) + blo(y[a][p]), bhi(xr[p]) + bhi(y[a][p]));
          }
        }
        {
          const bool odd = m & 1;
          u32x4 z0, z1;
#pragma unroll
          for (int p = 0; p < 4; ++p) {
            const u32 t = (u32)__builtin_amdgcn_mov_dpp((int)(odd ? y[0][p] : y[1][p]), 0xB1, 0xf, 0xf, true);
            z0[p] = odd ? t : y[0][p]; z1[p] = odd ? y[1][p] : t;
          }
          if constexpr (L == 0) { st4(mark4<0x10001u, 0u>(z0), OFF_A + eo); st4(mark4<0x10001u, 0u>(z1), OFF_A + eo + 1024); }
          else {
            const __amdgpu_buffer_rsrc_t ro = __builtin_amdgcn_make_buffer_rsrc(P.out, 0, 2048 * 1024, 0x00020000);
            __builtin_amdgcn_raw_buffer_store_b128(z0, ro, oo, 0, 16); __builtin_amdgcn_raw_buffer_store_b128(z1, ro, oo + 1024 * 1024, 0, 16);
          }
        }
      }
    }
    ST(S0 + 13);
    if constexpr (L == 0) {
      FRESH();
      SB();
      row_consts<false, 0>(rc, m, g);
      SB();
      u32x2 d0, d1, d2, d3;
      const u8* db = ws + OFF_A + (8 * pos + 2 * w) * 1024;
      const u32x4 sl = *(const u32x4*)(lds + LDS_STASH + t16), sh = *(const u32x4*)(lds + LDS_STASH + 4096 + t16);
#pragma unroll
      for (int i = 0; i < 4; ++i) {
        xv[2 * i] = f32x2{blo(sl[i]), blo(sh[i])}; xv[2 * i + 1] = f32x2{bhi(sl[i]), bhi(sh[i])};
      }
      xc = f32x2{first_lane(xv[0].x), first_lane(xv[0].y)};
      xch = lane < 32 ? xc.x : xc.y;
      PIN(xch);
#pragma unroll
      for (int i = 0; i < 8; ++i) { xv[i] -= xc; PIN(xv[i]); }
      SB();
      bar_nw();
      ld2<0>(d0, xo, db); ld2<512>(d1, xo, db); ld2<1024>(d2, xo, db); ld2<1536>(d3, xo, db);
      ring4(0);
      SB();
      row_consts<false, 1>(rc, m, g);
      SB();
      fwd_pre(fp, tid, w, pos);
      SB();
      ST(S0 + 14);
      vmwait<4>(); PIN(d0); PIN(d1); PIN(d2); PIN(d3);
      RETRY((stale<false, 1u>(red8<false>(u32x4{d0.x, d0.y, d1.x, d1.y}, u32x4{d2.x, d2.y, d3.x, d3.y}))), {
        CNT(7);
        if (bad_) { ld2r<0>(d0, xo, db); ld2r<512>(d1, xo, db); ld2r<1024>(d2, xo, db); ld2r<1536>(d3, xo, db); }
        vmwait<0>(); PIN(d0); PIN(d1); PIN(d2); PIN(d3);
      });
      xv[0] += f32x2{blo(d0.x), blo(d2.x)}; xv[1] += f32x2{bhi(d0.x), bhi(d2.x)}; xv[2] += f32x2{blo(d0.y), blo(d2.y)}; xv[3] += f32x2{bhi(d0.y), bhi(d2.y)};
      xv[4] += f32x2{blo(d1.x), blo(d3.x)}; xv[5] += f32x2{bhi(d1.x), bhi(d3.x)}; xv[6] += f32x2{blo(d1.y), blo(d3.y)}; xv[7] += f32x2{bhi(d1.y), bhi(d3.y)};
      ST(S0 + 15);
    }
  };
  layer(std::integral_constant<int, 0>{});
  layer(std::integral_constant<int, 1>{});
  if (blockIdx.x == 0 && threadIdx.x == 0) st2(ep, OFF_E);
  asm volatile(".Lgf_end:");
  ST_OUT();
}

extern "C" void kernel_launch(void* const* d_in, const int* in_sizes, int n_in, void* d_out, int out_size, void* d_ws, size_t ws_size,
                              hipStream_t stream) {
  Params p;
  p.x = (const u16*)d_in[0]; p.wr = (const u16*)d_in[1]; p.wi = (const u16*)d_in[2];
  p.wg = (const u16*)d_in[4]; p.wu = (const u16*)d_in[5]; p.wd = (const u16*)d_in[6];
  p.out = (u16*)d_out; p.ws = (u8*)d_ws;
  (void)hipFuncSetAttribute((const void*)k_all, hipFuncAttributeMaxDynamicSharedMemorySize, LDS_TOTAL);
  k_all<<<256, 256, LDS_TOTAL, stream>>>(p);
}
```

```cpp
#include <hip/hip_runtime.h>
#include <hip/hip_bf16.h>
#include <stdint.h>
#include <type_traits>
#include <utility>

typedef unsigned char u8;
typedef unsigned short u16;
typedef unsigned int u32;
typedef unsigned long long u64;
typedef u32 u32x2 __attribute__((ext_vector_type(2)));
typedef u32 u32x4 __attribute__((ext_vector_type(4)));
typedef float f32x2 __attribute__((ext_vector_type(2)));
typedef float f32x4 __attribute__((ext_vector_type(4)));
typedef _Float16 f16x2 __attribute__((ext_vector_type(2)));
typedef __bf16 bf16x2 __attribute__((ext_vector_type(2)));
typedef __bf16 bf16x8 __attribute__((ext_vector_type(8)));
#define DI __device__ __forceinline__
#define SB() __builtin_amdgcn_sched_barrier(0)
#define MEMFENCE() asm volatile("" ::: "memory")

#define XSLP 4
#define BMAP(x) (x)
#define XE0 164
#define XE1 157
constexpr u32 MB2 = 2u << 20;
constexpr u32 OFF_E = 0, OFF_FLAG = 8192, OFF_WNY = 65536;
constexpr u32 OFF_A = MB2;
constexpr u32 OFF_U = OFF_A + MB2;
constexpr u32 OFF_WX = OFF_U + MB2;
constexpr u32 OFF_X3 = OFF_WX + MB2;
constexpr u32 OFF_H = OFF_X3 + MB2;
constexpr u32 OFF_W1 = OFF_H + 7340032;
constexpr u32 OFF_W2 = OFF_W1 + 3670016;
constexpr u32 WS_USED = OFF_W2 + 1048576;
constexpr u32 SENT = 0xFFFFFFFFu;
constexpr int SPIN_CAP = 1 << 15;

constexpr int LDS_SCR = 131072, LDS_STASH = LDS_SCR + 24576, LDS_TOTAL = 163840;

struct Params {
  const u16* x; const u16* wr; const u16* wi; const u16* wg; const u16* wu; const u16* wd; u16* out; u8* ws;
};

#define ST(i)
#define ST_DECL
#define CNT(i)
#define ST_OUT()

#define XS(i)
#define XF(i)
DI u32 pkh(float a, float b) { f32x2 v = {a, b}; return __builtin_bit_cast(u32, __builtin_convertvector(v, f16x2)); }
DI u32 pkb(float a, float b) { f32x2 v = {a, b}; return __builtin_bit_cast(u32, __builtin_convertvector(v, bf16x2)); }
template <bool BF> DI u32 pkc(float a, float b) { return BF ? pkb(a, b) : pkh(a, b); }
DI u32 pkh_n0(float a, float b, u32 after) { u32 r; asm("v_cvt_pk_f16_f32 %0, -%1, %2" : "=v"(r) : "v"(a), "v"(b), "v"(after)); return r; }
DI u32 pkb_n1(float a, float b, u32 after) { u32 r; asm("v_cvt_pk_bf16_f32 %0, %1, -%2" : "=v"(r) : "v"(a), "v"(b), "v"(after)); return r; }
DI float blo(u32 u) { return __uint_as_float(u << 16); }
DI float bhi(u32 u) { return __uint_as_float(u & 0xffff0000u); }
DI float bfr(float a) { return blo(pkb(a, a)); }
DI float relu(float a) { const int i = __float_as_int(a); return __int_as_float(i > 0 ? i : 0); }
DI u32 umax(u32 a, u32 b) { return a > b ? a : b; }
DI u32 max3(u32 a, u32 b, u32 c) { return umax(umax(a, b), c); }
DI u32 max4(u32 acc, u32x4 v) { return max3(max3(acc, v.x, v.y), v.z, v.w); }
template <bool ONE> DI u32 red3(u32 a, u32 b, u32 c) { return ONE ? (a & b & c) : (a | b | c); }
template <bool ONE> DI u32 red2(u32 a, u32 b) { return ONE ? (a & b) : (a | b); }
template <bool ONE> DI u32 red4(u32 acc, u32x4 v) { return red3<ONE>(red3<ONE>(acc, v.x, v.y), v.z, v.w); }
template <bool ONE> DI u32 red8(u32x4 a, u32x4 b) { return red4<ONE>(red3<ONE>(red2<ONE>(a.x, a.y), a.z, a.w), b); }
template <bool ONE, u32 BIT> DI bool stale(u32 r) { return ONE ? (r & BIT) == 0 : (r & BIT) != 0; }
template <u32 CLR, u32 SET> DI u32 mark(u32 v) { return (v & ~CLR) | SET; }
template <u32 CLR, u32 SET> DI u32x4 mark4(u32x4 v) { return u32x4{mark<CLR, SET>(v.x), mark<CLR, SET>(v.y), mark<CLR, SET>(v.z), mark<CLR, SET>(v.w)}; }
DI f32x2 lo2(f32x4 v) { return __builtin_shufflevector(v, v, 0, 1); }
DI f32x2 hi2(f32x4 v) { return __builtin_shufflevector(v, v, 2, 3); }
DI f32x2 swp(f32x2 v) { return __builtin_shufflevector(v, v, 1, 0); }
DI f32x4 mfma_b(u32x4 a, u32x4 b, f32x4 c) {
  return __builtin_amdgcn_mfma_f32_16x16x32_bf16(__builtin_bit_cast(bf16x8, a), __builtin_bit_cast(bf16x8, b), c, 0, 0, 0);
}
template <class T> DI void PIN(T& x) { asm volatile("" : "+v"(x)); }
template <class T> DI void PINS(T& x) { asm volatile("" : "+s"(x)); }
DI int opq(int x) { asm("" : "+s"(x)); return x; }
template <class T> DI void PINA(T& x) { asm volatile("" : "+a"(x)); }
template <class F, int... I> DI void static_for_(F&& f, std::integer_sequence<int, I...>) { (f(std::integral_constant<int, I>{}), ...); }
template <int N, class F> DI void static_for(F&& f) { static_for_(f, std::make_integer_sequence<int, N>{}); }

#define MM_(T) "v_mfma_f32_16x16x32_" T
template <bool BF> DI void mm4(f32x4& d0, f32x4& d1, f32x4& d2, f32x4& d3, u32x4 a0, u32x4 a1, u32x4 b0, u32x4 b1) {
#define MM4_(T) asm volatile("s_nop 1\n\t" MM_(T) " %0, %4, %6, 0\n\t" MM_(T) " %1, %4, %7, 0\n\t" MM_(T) " %2, %5, %6, 0\n\t" MM_(T) " %3, %5, %7, 0\n\ts_nop 5" \
               : "=&v"(d0), "=&v"(d1), "=&v"(d2), "=&v"(d3) : "v"(a0), "v"(a1), "v"(b0), "v"(b1))
  if constexpr (BF) MM4_("bf16"); else MM4_("f16");
}
template <bool BF, int TAIL, int AG = 0> DI void mm8(f32x4& d0, f32x4& d1, f32x4& d2, f32x4& d3, u32x4 a00, u32x4 b00, u32x4 a01, u32x4 b01, u32x4 a10,
                               u32x4 b10, u32x4 a11, u32x4 b11, u32x4 a20, u32x4 b20, u32x4 a21, u32x4 b21, u32x4 a30, u32x4 b30, u32x4 a31, u32x4 b31) {
#define MM8_(T, A, B) asm volatile("s_nop 1\n\t" MM_(T) " %0, %4, %5, 0\n\t" MM_(T) " %1, %8, %9, 0\n\t" MM_(T) " %0, %6, %7, %0\n\t" MM_(T) " %1, %10, %11, %1\n\t" \
               MM_(T) " %2, %12, %13, 0\n\t" MM_(T) " %3, %16, %17, 0\n\t" MM_(T) " %2, %14, %15, %2\n\t" MM_(T) " %3, %18, %19, %3\n\ts_nop %20"         \
               : "=&v"(d0), "=&v"(d1), "=&v"(d2), "=&v"(d3)                                                                                              \
               : A(a00), B(b00), A(a01), B(b01), A(a10), B(b10), A(a11), B(b11), A(a20), B(b20), A(a21), B(b21), A(a30), B(b30), A(a31), B(b31), "i"(TAIL))
  if constexpr (!BF) MM8_("f16", "v", "v");
  else if constexpr (AG == 0) MM8_("bf16", "v", "v");
  else if constexpr (AG == 1) MM8_("bf16", "a", "v");
  else MM8_("bf16", "v", "a");
}
template <bool BF, int AG> DI void mf0(f32x4& d, u32x4 a, u32x4 b) {
  if constexpr (!BF) asm volatile(MM_("f16") " %0, %1, %2, 0" : "=&v"(d) : "v"(a), "v"(b));
  else if constexpr (AG == 0) asm volatile(MM_("bf16") " %0, %1, %2, 0" : "=&v"(d) : "v"(a), "v"(b));
  else asm volatile(MM_("bf16") " %0, %1, %2, 0" : "=&v"(d) : "v"(a), "a"(b));
}
template <bool FIRST> DI void mmw(f32x4& d, const u32x4& a, const u32x4& b) {
  if constexpr (FIRST) asm volatile(MM_("bf16") " %0, %1, %2, 0" : "=&v"(d) : "a"(a), "v"(b));
  else asm volatile(MM_("bf16") " %0, %1, %2, %0" : "+v"(d) : "a"(a), "v"(b));
}
template <int O> DI void lda(u32x4& d, u32 addr) { asm volatile("ds_read_b128 %0, %1 offset:%2" : "=a"(d) : "v"(addr), "i"(O) : "memory"); }
template <int N> DI void lgwait() { asm volatile("s_waitcnt lgkmcnt(%0)" ::"i"(N) : "memory"); }
template <bool BF, int AG, int TAIL> DI void mf4(f32x4& d0, f32x4& d1, f32x4& d2, f32x4& d3, u32x4 a0, u32x4 b0, u32x4 a1, u32x4 b1, u32x4 a2,
                                               u32x4 b2, u32x4 a3, u32x4 b3) {
#define MF4_(T, B) asm volatile("s_nop 1\n\t" MM_(T) " %0, %4, %5, %0\n\t" MM_(T) " %1, %6, %7, %1\n\t" MM_(T) " %2, %8, %9, %2\n\t" MM_(T) " %3, %10, %11, %3\n\ts_nop %12" \
               : "+v"(d0), "+v"(d1), "+v"(d2), "+v"(d3) : "v"(a0), B(b0), "v"(a1), B(b1), "v"(a2), B(b2), "v"(a3), B(b3), "i"(TAIL))
  if constexpr (!BF) MF4_("f16", "v"); else if constexpr (AG == 0) MF4_("bf16", "v"); else MF4_("bf16", "a");
}
template <bool BF, bool CONJ, class MM> DI u32x4 cplx_under(f32x4 r, f32x4 i, const f32x2 (&c)[2], const f32x2 (&s)[2], MM&& mm) {
  asm volatile("s_nop 1");
  SB(); mm(std::integral_constant<int, 0>{}); SB();
  f32x2 a0 = lo2(i) * s[0], b0 = lo2(r) * s[0], a1 = hi2(i) * s[1];
  PIN(a0); PIN(b0); PIN(a1);
  SB(); mm(std::integral_constant<int, 1>{}); SB();
  f32x2 b1 = hi2(r) * s[1], re0 = CONJ ? lo2(r) * c[0] + a0 : lo2(r) * c[0] - a0, im0 = CONJ ? lo2(i) * c[0] - b0 : lo2(i) * c[0] + b0;
  PIN(b1); PIN(re0); PIN(im0);
  SB(); mm(std::integral_constant<int, 2>{}); SB();
  f32x2 re1 = CONJ ? hi2(r) * c[1] + a1 : hi2(r) * c[1] - a1, im1 = CONJ ? hi2(i) * c[1] - b1 : hi2(i) * c[1] + b1;
  u32 o0 = pkc<BF>(re0.x, im0.x);
  PIN(re1); PIN(im1); PIN(o0);
  SB(); mm(std::integral_constant<int, 3>{}); SB();
  return u32x4{o0, pkc<BF>(re0.y, im0.y), pkc<BF>(re1.x, im1.x), pkc<BF>(re1.y, im1.y)};
}
template <bool BF> DI u32x4 twiddle(f32x4 r, f32x4 i, const f32x2 (&c)[2], const f32x2 (&s)[2]) {
  const f32x2 re0 = lo2(r) * c[0] + lo2(i) * s[0], im0 = lo2(i) * c[0] - lo2(r) * s[0];
  const f32x2 re1 = hi2(r) * c[1] + hi2(i) * s[1], im1 = hi2(i) * c[1] - hi2(r) * s[1];
  return u32x4{pkc<BF>(re0.x, im0.x), pkc<BF>(re0.y, im0.y), pkc<BF>(re1.x, im1.x), pkc<BF>(re1.y, im1.y)};
}
DI u32x4 cmul_b(f32x4 r, f32x4 i, const f32x2 (&c)[2], const f32x2 (&s)[2]) {
  const f32x2 re0 = lo2(r) * c[0] - lo2(i) * s[0], im0 = lo2(i) * c[0] + lo2(r) * s[0];
  const f32x2 re1 = hi2(r) * c[1] - hi2(i) * s[1], im1 = hi2(i) * c[1] + hi2(r) * s[1];
  return u32x4{pkb(re0.x, im0.x), pkb(re0.y, im0.y), pkb(re1.x, im1.x), pkb(re1.y, im1.y)};
}
DI float cosr(float a) { return __builtin_amdgcn_cosf(a); }
DI float sinr(float a) { return __builtin_amdgcn_sinf(a); }
template <int CTRL> DI float dppf(float x) {
  return __builtin_bit_cast(float, __builtin_amdgcn_mov_dpp(__builtin_bit_cast(int, x), CTRL, 0xf, 0xf, true));
}
DI f32x2 bcast2(float x) {
  auto t = __builtin_amdgcn_permlane32_swap(__float_as_uint(x), __float_as_uint(x), false, false);
  return f32x2{__uint_as_float(t[0]), __uint_as_float(t[1])};
}
DI void ln_coef(f32x2 S, f32x2 Q, float ch, f32x2& R, f32x2& O) {
  auto t = __builtin_amdgcn_permlane32_swap(__float_as_uint(S.x), __float_as_uint(S.y), false, false);
  auto u = __builtin_amdgcn_permlane32_swap(__float_as_uint(Q.x), __float_as_uint(Q.y), false, false);
  const float s = __uint_as_float(t[0]) + __uint_as_float(t[1]), q = __uint_as_float(u[0]) + __uint_as_float(u[1]);
  auto z = __builtin_amdgcn_permlane16_swap(__float_as_uint(s), __float_as_uint(q), false, false);
  float x = __uint_as_float(z[0]) + __uint_as_float(z[1]);
  x += dppf<0xB1>(x);
  x += dppf<0x4E>(x);
  x += dppf<0x141>(x);
  x += dppf<0x140>(x);
  auto y = __builtin_amdgcn_permlane16_swap(__float_as_uint(x), __float_as_uint(x), false, false);
  const float md = __uint_as_float(y[0]) * (1.0f / 512.0f);
  const float var = relu(__uint_as_float(y[1]) * (1.0f / 512.0f) - md * md);
  const float r = __builtin_amdgcn_rcpf(bfr(__builtin_amdgcn_sqrtf(bfr(bfr(var) + 1e-5f))));
  const float o = (ch - bfr(ch + md)) * r;
  R = bcast2(r); O = bcast2(o);
}
DI float first_lane(float x) { return __uint_as_float(__builtin_amdgcn_readfirstlane(__float_as_uint(x))); }
DI int row_of_pos(int pos, int j) {
  const int c = 8 * (pos >> 7) + 2 * ((pos >> 3) & 3) + ((pos >> 5) & 1), q = 8 * ((pos >> 6) & 1) + (pos & 7);
  return 32 * (2 * c + (j & 1)) + 2 * q + (j >> 1);
}

template <int I> DI void ld4(u32x4& d, u32 v, const u8* s) { asm volatile("global_load_dwordx4 %0, %1, %2 offset:%3 sc1" : "=v"(d) : "v"(v), "s"(s), "i"(I) : "memory"); }
template <int I> DI void ld4a(u32x4& d, u32 v, const u8* s) { asm volatile("global_load_dwordx4 %0, %1, %2 offset:%3 sc1" : "=a"(d) : "v"(v), "s"(s), "i"(I) : "memory"); }
template <int I> DI void ld4r(u32x4& d, u32 v, const u8* s) { asm volatile("global_load_dwordx4 %0, %1, %2 offset:%3 sc1" : "+v"(d) : "v"(v), "s"(s), "i"(I) : "memory"); }
template <int I> DI void ld2(u32x2& d, u32 v, const u8* s) { asm volatile("global_load_dwordx2 %0, %1, %2 offset:%3 sc1" : "=v"(d) : "v"(v), "s"(s), "i"(I) : "memory"); }
template <int I> DI void ld2r(u32x2& d, u32 v, const u8* s) { asm volatile("global_load_dwordx2 %0, %1, %2 offset:%3 sc1" : "+v"(d) : "v"(v), "s"(s), "i"(I) : "memory"); }
template <int I> DI void ld2p(u32x2& d, u32 v, const u8* s) { asm volatile("global_load_dwordx2 %0, %1, %2 offset:%3" : "=v"(d) : "v"(v), "s"(s), "i"(I) : "memory"); }
DI void touch(u32& d, u32 v, const u8* s) { asm volatile("global_load_dword %0, %1, %2 sc1" : "+v"(d) : "v"(v), "s"(s) : "memory"); }
template <int I> DI void ld1p(u32& d, u32 v, const u8* s) { asm volatile("global_load_dword %0, %1, %2 offset:%3" : "=v"(d) : "v"(v), "s"(s), "i"(I) : "memory"); }
DI void ld1s(u32& d, u32 v, const u8* s) { asm volatile("global_load_ushort %0, %1, %2" : "=v"(d) : "v"(v), "s"(s) : "memory"); }
template <int N> DI void vmwait() { static_assert(N >= 0 && N < 64, ""); asm volatile("s_waitcnt vmcnt(%0)" ::"i"(N) : "memory"); }
DI void set_m0(u32 ldsaddr) { asm volatile("s_mov_b32 m0, %0" ::"s"(ldsaddr) : "memory"); }
template <int I> DI void dma1(u32 voff, const u8* src) {
  asm volatile("global_load_lds_dwordx4 %0, %1 offset:%2 sc1" ::"v"(voff), "s"(src), "i"(I) : "memory");
}
DI void dma4(u32 voff, const u8* src, u32 ldsaddr) {
  asm volatile("s_mov_b32 m0, %2\n\ts_nop 0\n\tglobal_load_lds_dwordx4 %0, %1 sc1\n\tglobal_load_lds_dwordx4 %0, %1 offset:1024 sc1\n\t"
               "global_load_lds_dwordx4 %0, %1 offset:2048 sc1\n\tglobal_load_lds_dwordx4 %0, %1 offset:3072 sc1" ::"v"(voff), "s"(src), "s"(ldsaddr) : "memory");
}
#define RETRY(COND, ...)                                                      \
  do {                                                                        \
    bool bad_ = (COND);                                                       \
    if (__builtin_expect(__any(bad_), 0)) {                                   \
      _Pragma("nounroll") for (int sp_ = 0; sp_ < SPIN_CAP; ++sp_) {          \
        __VA_ARGS__                                                           \
        bad_ = (COND);                                                        \
        if (!__any(bad_)) break;                                              \
      }                                                                       \
    }                                                                         \
  } while (0)
DI void bar() { asm volatile("s_waitcnt lgkmcnt(0)\n\ts_barrier" ::: "memory"); }
DI void bar_nw() { asm volatile("s_barrier" ::: "memory"); }

struct RowC { u32x4 b1re, b1im, b2re[2][2], b2im[2][2]; f32x2 twc[2][2], tws[2][2]; };
template <bool INV> DI void pair_cs(float c, float s, u32& re, u32& im) {
  if constexpr (INV) { im = pkb(s, c); re = pkb_n1(c, s, im); } else { re = pkh(c, s); im = pkh_n0(s, c, re); }
}
template <bool AG = false> DI void sign4(u32x4 base, u32 lm, u32x4& f00, u32x4& f01, u32x4& f10, u32x4& f11) {
  f00 = base;
  f01 = base ^ lm;
  f10 = base ^ u32x4{0u, 0x80008000u, 0u, 0x80008000u};
  f11 = f10 ^ lm;
  if constexpr (AG) { PINA(f00); PINA(f01); PINA(f10); PINA(f11); } else { PIN(f00); PIN(f01); PIN(f10); PIN(f11); }
}
template <bool INV, int PART> DI void row_consts(RowC& k, int m, int g) {
  const float fm = (float)m, fg = (float)(4 * g);
  if constexpr (PART == 0) {
    u32x4 br, bi;
#pragma unroll
    for (int i = 0; i < 4; ++i) {
      const float a = (fg + (float)i) * (fm * (1.0f / 16.0f));
      u32 x, y;
      pair_cs<INV>(cosr(a), sinr(a), x, y); k.b1re[i] = x; k.b1im[i] = y;
      const float a2 = (fg + (float)i) * (fm * (1.0f / 32.0f));
      pair_cs<INV>(cosr(a2), sinr(a2), x, y); br[i] = x; bi[i] = y;
    }
    PIN(k.b1re); PIN(k.b1im);
    const u32 lm = (m & 1) ? 0x80008000u : 0u;
    sign4(br, lm, k.b2re[0][0], k.b2re[0][1], k.b2re[1][0], k.b2re[1][1]);
    sign4(bi, lm, k.b2im[0][0], k.b2im[0][1], k.b2im[1][0], k.b2im[1][1]);
  } else {
#pragma unroll
    for (int e = 0; e < 2; ++e)
#pragma unroll
      for (int i = 0; i < 4; ++i) {
        const float a = (fg + (float)(16 * e + i)) * (fm * (INV ? -1.0f / 512.0f : 1.0f / 512.0f));
        k.twc[e][i >> 1][i & 1] = cosr(a); k.tws[e][i >> 1][i & 1] = sinr(a);
      }
#pragma unroll
    for (int e = 0; e < 2; ++e) { PIN(k.twc[e][0]); PIN(k.twc[e][1]); PIN(k.tws[e][0]); PIN(k.tws[e][1]); }
  }
}
template <bool INV, int TAIL> DI void row_fft(const RowC& k, u32x4 a0, u32x4 a1, f32x4& dr0, f32x4& di0, f32x4& dr1, f32x4& di1) {
  f32x4 tr0, ti0, tr1, ti1;
  mm4<INV>(tr0, ti0, tr1, ti1, a0, a1, k.b1re, k.b1im);
  u32x4 p0 = twiddle<INV>(tr0, ti0, k.twc[0], k.tws[0]);
  PIN(p0);
  const u32x4 p1 = cplx_under<INV, true>(tr1, ti1, k.twc[1], k.tws[1], [&](auto I) {
    constexpr int i = decltype(I)::value;
    mf0<INV, 0>(i == 0 ? dr0 : i == 1 ? di0 : i == 2 ? dr1 : di1, p0, i == 0 ? k.b2re[0][0] : i == 1 ? k.b2im[0][0] : i == 2 ? k.b2re[1][0] : k.b2im[1][0]);
  });
  mf4<INV, 0, TAIL>(dr0, di0, dr1, di1, p1, k.b2re[0][1], p1, k.b2im[0][1], p1, k.b2re[1][1], p1, k.b2im[1][1]);
}

struct FwdP { u32 iw, ir, ow, ap0, ap1, ar0, ar1, zo; f32x2 cB, cD, eA, eB, eC, eD; };
DI void fwd_pre(FwdP& p, int tid, int w, int pos) {
  const int m = tid & 15, g = (tid >> 4) & 3, ph = tid >> 1, fq = ph ? 512 - ph : 256, fq1 = 384 - ph;
  p.iw = w * 5120 + ((16 * (m & 1) + 4 * g) * 20 + (m >> 1)) * 4; p.ir = w * 5120 + (m * 20 + 4 * g) * 4;
  p.ap0 = (ph + (ph >> 4) * 4) * 4; p.ap1 = p.ap0 + 640; p.ar0 = (fq + (fq >> 4) * 4) * 4; p.ar1 = (fq1 + (fq1 >> 4) * 4) * 4;
  p.zo = OFF_A + pos * 8192 + tid * 16;
  const float sc = 1.0f / 64.0f, sg = (tid & 1) ? -sc : sc;
  p.cB = f32x2{sg, -sg}; p.cD = f32x2{sg, sg};
  p.eA = ph ? f32x2{sc, sc} : f32x2{2 * sc, 0.f}; p.eB = ph ? p.cB : f32x2{2 * sg, 0.f};
  p.eC = ph ? f32x2{sc, -sc} : f32x2{0.f, 2 * sg}; p.eD = ph ? p.cD : f32x2{0.f, 2 * sc};
  PIN(p.iw); PIN(p.ir); PIN(p.ap0); PIN(p.ap1); PIN(p.ar0); PIN(p.ar1); PIN(p.zo);
  PIN(p.cB); PIN(p.cD); PIN(p.eA); PIN(p.eB); PIN(p.eC); PIN(p.eD);
}
struct ColC {
  u32x4 b1re[2], b1im[2], a2re[2][2], a2im[2][2], b3re[2][2], b3im[2][2], a4re[2][2], a4im[2][2];
  f32x2 t1c[2][2], t1s[2][2], t2c[2][2], t2s[2][2];
};
template <int PART> DI void col_consts(ColC& k, int m, int g, int w) {
  const int pi_ = w >> 1, kt = w & 1;
  const float fm = (float)m, fg = (float)(4 * g);
  const u32 lm = (m & 1) ? 0x80008000u : 0u;
  if constexpr (PART == 0) {
    const float fk = (float)(2 * (16 * kt + m) + pi_);
#pragma unroll
    for (int u = 0; u < 2; ++u) {
#pragma unroll
      for (int r = 0; r < 4; ++r) {
        const float n = fg + (float)(16 * u + r), a = n * (fk * (1.0f / 64.0f)), a2 = n * (fk * (1.0f / 2048.0f));
        u32 x, y;
        pair_cs<false>(cosr(a), sinr(a), x, y); k.b1re[u][r] = x; k.b1im[u][r] = y;
        k.t1c[u][r >> 1][r & 1] = cosr(a2); k.t1s[u][r >> 1][r & 1] = sinr(a2);
      }
      PIN(k.b1re[u]); PIN(k.b1im[u]); PIN(k.t1c[u][0]); PIN(k.t1c[u][1]); PIN(k.t1s[u][0]); PIN(k.t1s[u][1]);
    }
  } else if constexpr (PART == 1) {
    u32x4 fr, fi, ir, ii;
#pragma unroll
    for (int r = 0; r < 4; ++r) {
      const float a = (fg + (float)r) * (fm * (1.0f / 32.0f)), c = cosr(a), s = sinr(a);
      u32 x, y;
      pair_cs<false>(c, s, x, y); fr[r] = x; fi[r] = y;
      pair_cs<true>(c, s, x, y); ir[r] = x; ii[r] = y;
    }
    sign4(fr, lm, k.a2re[0][0], k.a2re[0][1], k.a2re[1][0], k.a2re[1][1]);
    sign4(fi, lm, k.a2im[0][0], k.a2im[0][1], k.a2im[1][0], k.a2im[1][1]);
    sign4(ir, lm, k.b3re[0][0], k.b3re[0][1], k.b3re[1][0], k.b3re[1][1]);
    sign4(ii, lm, k.b3im[0][0], k.b3im[0][1], k.b3im[1][0], k.b3im[1][1]);
  } else if constexpr (PART == 2) {
    const float kb = (float)(32 * kt + pi_) + 2.0f * fg;
#pragma unroll
    for (int u = 0; u < 2; ++u) {
#pragma unroll
      for (int r = 0; r < 4; ++r) {
        const float a = (kb + (float)(2 * r)) * ((fm + (float)(16 * u)) * (-1.0f / 2048.0f));
        k.t2c[u][r >> 1][r & 1] = cosr(a); k.t2s[u][r >> 1][r & 1] = sinr(a);
      }
      PIN(k.t2c[u][0]); PIN(k.t2c[u][1]); PIN(k.t2s[u][0]); PIN(k.t2s[u][1]);
    }
  } else {
    const float fn = (fm + (float)(16 * (w >> 1))) * (1.0f / 64.0f);
#pragma unroll
    for (int u = 0; u < 2; ++u) {
#pragma unroll
      for (int r = 0; r < 4; ++r) {
        const float a = (2.0f * fg + (float)(2 * r + u)) * fn;
        u32 x, y;
        pair_cs<true>(cosr(a), sinr(a), x, y); k.a4re[u][0][r] = x; k.a4im[u][0][r] = y;
      }
      k.a4re[u][1] = k.a4re[u][0] ^ lm; k.a4im[u][1] = k.a4im[u][0] ^ lm;
      PIN(k.a4re[u][0]); PIN(k.a4re[u][1]); PIN(k.a4im[u][0]); PIN(k.a4im[u][1]);
    }
  }
}

DI void transpose8x4(const u32x2 (&v)[8], u32x4 (&o)[4]) {
#pragma unroll
  for (int jj = 0; jj < 4; ++jj) {
    o[0][jj] = __builtin_amdgcn_perm(v[2 * jj + 1].x, v[2 * jj].x, 0x05040100u);
    o[1][jj] = __builtin_amdgcn_perm(v[2 * jj + 1].x, v[2 * jj].x, 0x07060302u);
    o[2][jj] = __builtin_amdgcn_perm(v[2 * jj + 1].y, v[2 * jj].y, 0x05040100u);
    o[3][jj] = __builtin_amdgcn_perm(v[2 * jj + 1].y, v[2 * jj].y, 0x07060302u);
  }
}
template <int H> DI void prep_load1(const Params& P, int wt, int lane, u32x2 (&v)[8]) {
  const u32 vo = (lane < 56 ? lane : 55) * 8, vo2 = vo + 7168;
  const int piece = (wt >> 3) & 63;
  const u8* src = (const u8*)((wt >> 9) ? P.wu : P.wg) + opq(wt & 7) * 448 + opq(4 * piece) * 3584;
  static_for<4>([&](auto J) { constexpr int j = decltype(J)::value; ld2p<(j & 1) * 3584>(v[4 * H + j], j & 2 ? vo2 : vo, src + H * (256 * 3584)); });
}
DI u32 prep_dst1(int wt, int tile) {
  const int piece = (wt >> 3) & 63, mat = wt >> 9, hi = tile >= 7, p = tile - 7 * hi, ks = piece >> 2;
  const u32 frag = (p < 3 ? ks * 6 + 2 * p : p < 5 ? 96 + ks * 4 + 2 * (p - 3) : 160 + ks * 4 + 2 * (p - 5)) + mat;
  return OFF_W1 + ((2 * (wt & 7) + hi) * 224 + frag) * 1024 + (piece & 3) * 256;
}
DI int w2_row(int wv, int slot, int rot, int h) {
  const int f0 = slot < 4 ? 112 * (4 * wv + ((slot + rot) & 3)) : slot < 6 ? 112 * (4 * wv + 2 * ((slot ^ rot) & 1)) + 32
               : slot < 10 ? 112 * (4 * wv + ((slot - 6 + rot) & 3)) + 48 : 112 * (4 * wv + ((slot - 10 + rot) & 3)) + 80;
  return f0 + h * ((slot == 4 || slot == 5) ? 112 : 16);
}
DI void dma_rows(u32 voff, const u8* h0, const u8* h1, u32 ldsaddr) {
  const u8* h0b = h0 + 8192 - 1024;
  const u8* h1a = h1 - 2048;
  const u8* h1b = h1 + 8192 - 3072;
  asm volatile("s_mov_b32 m0, %5\n\ts_nop 0\n\tglobal_load_lds_dwordx4 %0, %1\n\tglobal_load_lds_dwordx4 %0, %2 offset:1024\n\t"
               "global_load_lds_dwordx4 %0, %3 offset:2048\n\tglobal_load_lds_dwordx4 %0, %4 offset:3072"
               ::"v"(voff), "s"(h0), "s"(h0b), "s"(h1a), "s"(h1b), "s"(ldsaddr) : "memory");
}
template <int O> DI void tr_read(u32x2& lo, u32x2& hi, u32 addr) {
  asm volatile("ds_read_b64_tr_b16 %0, %2 offset:%3\n\tds_read_b64_tr_b16 %1, %2 offset:%4" : "=&v"(lo), "=&v"(hi) : "v"(addr), "i"(O), "i"(O + 2048) : "memory");
}
DI u32 tr_addr(int lane, int c) { return (lane >> 2) * 128 + (((lane & 3) + 4 * (c >> 1)) ^ (2 * ((lane >> 2) & 1) + 4 * ((lane >> 3) & 1))) * 16 + (c & 1) * 8; }

constexpr int NTOK0 = 12, NRES = 32;
constexpr int g1_xe(int L) { return L == 0 ? XE0 : XE1; }
constexpr int g1_ne(int L) { return (224 - g1_xe(L)) / 7 * 3 + ((224 - g1_xe(L)) % 7 > 0) + ((224 - g1_xe(L)) % 7 > 2) + ((224 - g1_xe(L)) % 7 > 5); }
constexpr int g1_job(int L, int x) {
  if (((x >= 34 && x < 96) && (x & 3) == 2) || (x >= 98 && x < 114 && !(x & 1))) return 2;
  if ((x < 34 && !(x & 1)) || (x < 64 && (x & 3) == 0)) return 1;
  if (L == 1 && x >= 47 && x < 60 && (x & 3) == 3) return 6;
  if (x >= g1_xe(L)) return ((x - g1_xe(L)) % 7 == 0 || (x - g1_xe(L)) % 7 == 2 || (x - g1_xe(L)) % 7 == 5) ? 4 : 0;
  if (L == 0 && ((x >= 48 && x < 98 && (x & 3) == 0) || (x >= 115 && x % 3 == 0) || (x < 34 && x % 8 == 1))) return 3;
  return 0;
}
constexpr int NEPI1 = 75, NEPI2 = 50, EPI1_X = 99;
struct G1Sched {
  int job[224], idx[224], epi[225], nst[224], cum[225], pos[7][64], cnt[7];
  constexpr G1Sched(int L) : job{}, idx{}, epi{}, nst{}, cum{}, pos{}, cnt{} {
    cnt[1] = NTOK0;
    for (int i = 0; i < NTOK0; ++i) pos[1][i] = i;
    cum[0] = NTOK0;
    for (int x = 0; x < 224; ++x) {
      int j = g1_job(L, x);
      if (j == 1 && cnt[1] >= 32) j = L == 0 ? 3 : 0;
      if ((j == 3 && cnt[3] >= NRES)) j = 0;
      job[x] = j; idx[x] = cnt[j];
      if (x == 162) epi[x] = NEPI1;
      const bool on = (x >= EPI1_X && x < 160 && epi[x] < NEPI1) || x >= 162;
      epi[x + 1] = on ? epi[x] + (x < 114 ? 2 - (x & 1 ? 0 : 1) : 2) : epi[x];
      if (x < 160 && epi[x + 1] > NEPI1) epi[x + 1] = NEPI1;
      for (int n = epi[x]; n < epi[x + 1]; ++n)
        nst[x] += n < NEPI1 ? (n == 24 || n == 49 || n == 74) : (n < NEPI1 + NEPI2 && (n - NEPI1) % 25 == 24);
      if (j) pos[j][cnt[j]++] = cum[x] + nst[x];
      cum[x + 1] = cum[x] + nst[x] + (j ? 1 : 0);
    }
  }
};
constexpr G1Sched G1S[2] = {G1Sched(0), G1Sched(1)};
static_assert(G1S[0].cnt[1] == 32 && G1S[0].cnt[2] == 24 && G1S[0].cnt[3] == NRES && G1S[0].cnt[4] == g1_ne(0) && G1S[1].cnt[4] == g1_ne(1), "");
static_assert(G1S[1].cnt[6] == 4 && G1S[1].pos[6][3] < G1S[1].pos[2][7], "");
static_assert(G1S[0].idx[66] == 8 && G1S[0].idx[98] == 16 && G1S[0].epi[113] <= 24 && G1S[1].epi[113] <= 24, "");
static_assert(G1S[0].epi[152] == NEPI1 && G1S[0].epi[204] >= NEPI1 + NEPI2, "");
constexpr int clamp63(int n) { return n > 63 ? 63 : n; }
constexpr int g1_wait(int L, int x, int job, int idx) { return clamp63(G1S[L].cum[x] - G1S[L].pos[job][idx] - 1); }

#define FRESH()                                                                                                   \
  int tid = threadIdx.x, b = BMAP(blockIdx.x), w = w_;                                                                   \
  u8* ws = P.ws;                                                                                                   \
  asm volatile("" : "+v"(tid), "+s"(b), "+s"(w), "+s"(ws));                                                        \
  const int lane = tid & 63, m = lane & 15, g = lane >> 4, half2 = b & 1, ct = b >> 5;                             \
  const int G = ((b >> 1) & 3) * 4 + ((b >> 3) & 3), member = half2 * 8 + ct, pos = G * 16 + member;               \
  const u32 l16 = lane * 16, t16 = tid * 16, xo = 32 * m + 8 * g;                                                  \
  auto fill_h = [&](u32 base, int q) { st4(sent4, base + (4 * G + w) * 114688 + member * 7168 + q * 1024 + l16); };          \
  auto fill_rows = [&](u32 base) { st4(sent4, base + pos * 8192 + t16); st4(sent4, base + pos * 8192 + 4096 + t16); }; \
  auto ring4 = [&](int q) {                                   \
    const u32 o = (q >> 1) * 32768 + w * 8192 + (q & 1) * 4096;                                                    \
    dma4(l16, ws + OFF_W1 + member * 229376 + o, ldsbase + o);                                                     \
  };                                                                                                               \
      \
  auto w2dma = [&](int slot, int unit) {                                                                           \
    const u8* wd = (const u8*)P.wd + ct * 128;                                                                     \
    dma_rows((lane >> 3) * 1024 + ((lane & 7) ^ (2 * ((lane >> 3) & 1) + 4 * ((lane >> 4) & 1))) * 16,              \
             wd + opq(w2_row(w, slot, ct, 0)) * 1024, wd + opq(w2_row(w, slot, ct, 1)) * 1024, ldsbase + w * 32768 + unit * 4096); \
  };                                                                                                               \
  (void)w2dma;                                                                                                     \
  (void)m; (void)g; (void)half2; (void)ct; (void)l16; (void)t16; (void)xo; (void)fill_h; (void)fill_rows; (void)ring4

__global__ __launch_bounds__(256, 1) void k_all(Params P) {
  extern __shared__ __attribute__((aligned(16))) u8 lds[];
  ST_DECL;
  const int w_ = __builtin_amdgcn_readfirstlane(threadIdx.x >> 6);
  const __amdgpu_buffer_rsrc_t rs = __builtin_amdgcn_make_buffer_rsrc(P.ws, 0, WS_USED, 0x00020000);
  const u32 ldsbase = (u32)(size_t)(__attribute__((address_space(3))) u8*)lds;
  const u32x4 sent4 = {SENT, SENT, SENT, SENT};
  auto st4 = [&](u32x4 v, u32 off) { __builtin_amdgcn_raw_buffer_store_b128(v, rs, off, 0, 16); };
  auto st2 = [&](u32x2 v, u32 off) { __builtin_amdgcn_raw_buffer_store_b64(v, rs, off, 0, 16); };
  u32x2 xl[2], xh[2], pv0[8];
  u32 wfr[8], wfi[8], wn[8];
  f32x2 xv[8], xc;
  float xch;
  FwdP fp;
  u32 pd[4];
  u32x2 ep;
  u32 cpf = 0, tw[4];
  u32x4 w2a[14][4];
  RowC rc;
  ColC cc;
  ST(0);
  {
    FRESH();
    const u8* xp = (const u8*)P.x + row_of_pos(pos, w) * 1024;
    ld2p<0>(xl[0], xo, xp); ld2p<512>(xl[1], xo, xp); ld2p<0>(xh[0], xo, xp + 1024 * 1024); ld2p<512>(xh[1], xo, xp + 1024 * 1024);
    { const u32 z = 0; ld2<0>(ep, z, ws); }
    {
      const u8* pc;
      u32 len;
      asm volatile("s_getpc_b64 %0\n.Lgf_begin:\n\ts_mov_b32 %1, .Lgf_end-.Lgf_begin" : "=s"(pc), "=s"(len));
      const u32 o = (((b >> 3) * 4 + w) * 8 + (lane & 7)) * 128;
      touch(cpf, o < len ? o : 0, pc);
    }
    {
      const u32 z = 0;
      ld1p<0>(tw[0], z, (const u8*)P.wr); ld1p<0>(tw[1], z, (const u8*)P.wi);
      const int wt = b * 4 + w, r8 = lane >> 3;
      ld1p<0>(tw[2], z, (const u8*)((wt >> 9) ? P.wu : P.wg)); (void)r8;
      ld1p<0>(tw[3], (lane < 14 ? lane : 0) * 128, (const u8*)P.wd + opq(wt) * 1792);
    }
    XS(32);
    fill_rows(OFF_A); fill_rows(OFF_WX);
    if (tid < 8) st4(sent4, OFF_WNY + tid * 4096 + pos * 16);
    SB();
    row_consts<false, 0>(rc, m, g);
    row_consts<false, 1>(rc, m, g);
    SB();
    XS(33);
    const int kb = 2 * (16 * ((pos >> 6) & 1) + (pos & 15)) + (pos >> 7) + 256 * ((pos >> 4) & 3);
    static_for<8>([&](auto J) {
      constexpr int j = decltype(J)::value;
      const u32 o = tid * 2 + (kb + 64 * (16 * (j >> 2) + (j & 3))) * 514;
      ld1s(wfr[j], o, (const u8*)P.wr); ld1s(wfi[j], o, (const u8*)P.wi);
    });
    {
      const int k = kb + 64 * (16 * ((lane >> 2) & 1) + (lane & 3)), km = (2048 - k) & 2047;
      const u32 o = k * 514, om = km * 514;
      ld1s(wn[0], o, (const u8*)P.wr); ld1s(wn[1], o, (const u8*)P.wi); ld1s(wn[2], om, (const u8*)P.wr); ld1s(wn[3], om, (const u8*)P.wi);
      ld1s(wn[4], o, (const u8*)P.wr + 512); ld1s(wn[5], o, (const u8*)P.wi + 512); ld1s(wn[6], om, (const u8*)P.wr + 512); ld1s(wn[7], om, (const u8*)P.wi + 512);
    }
    XS(34);
    fwd_pre(fp, tid, w, pos);
#pragma unroll
    for (int x = 0; x < 4; ++x) pd[x] = x < 3 || g < 2 ? prep_dst1(b * 4 + w, 4 * x + g) + m * 16 : WS_USED;
#pragma unroll
    for (int x = 0; x < 4; ++x) PIN(pd[x]);
    SB();
    XS(35);
    vmwait<1 + 4 + 4 + 24>();
    XS(36);
    PIN(ep); PIN(xl[0]); PIN(xl[1]); PIN(xh[0]); PIN(xh[1]);
    { const u64 e = (((u64)(u32)__builtin_amdgcn_readfirstlane(ep.y) << 32) | (u32)__builtin_amdgcn_readfirstlane(ep.x)) + 1; ep = u32x2{(u32)e, (u32)(e >> 32)}; }
#pragma unroll
    for (int t = 0; t < 2; ++t)
#pragma unroll
      for (int p = 0; p < 2; ++p) {
        xv[4 * t + 2 * p] = f32x2{blo(xl[t][p]), blo(xh[t][p])};
        xv[4 * t + 2 * p + 1] = f32x2{bhi(xl[t][p]), bhi(xh[t][p])};
      }
    xc = f32x2{first_lane(xv[0].x), first_lane(xv[0].y)};
    xch = lane < 32 ? xc.x : xc.y;
#pragma unroll
    for (int i = 0; i < 8; ++i) xv[i] -= xc;
  }
  ST(1);

  auto layer = [&](auto LC) {
    constexpr int L = decltype(LC)::value;
    constexpr int S0 = 2 + 16 * L;
    {
      FRESH();
      const f32x2 S = ((xv[0] + xv[1]) + (xv[2] + xv[3])) + ((xv[4] + xv[5]) + (xv[6] + xv[7]));
      f32x2 q0 = xv[0] * xv[0], q1 = xv[1] * xv[1];
#pragma unroll
      for (int i = 2; i < 8; i += 2) { q0 += xv[i] * xv[i]; q1 += xv[i + 1] * xv[i + 1]; }
      if constexpr (L == 0) { SB(); fill_rows(OFF_U); SB(); }
      f32x2 R, O;
      XF(32);
      ln_coef(S, q0 + q1, xch, R, O);
      XF(33);
      u8* img = lds + LDS_SCR;
      if constexpr (L == 0) {
        SB(); vmwait<26>(); PIN(cpf); PIN(tw[0]); PIN(tw[1]); PIN(tw[2]); PIN(tw[3]);
        if (lane == 0) st2(ep, OFF_FLAG + (w * 256 + pos) * 8);
        prep_load1<0>(P, b * 4 + w, lane, pv0); SB();
      }
#pragma unroll
      for (int i = 0; i < 8; ++i) {
        const f32x2 v = xv[i] * R + O;
        *(u32*)(img + fp.iw + ((i & 3) * 20 + 8 * (i >> 2)) * 4) = pkh(v.x, v.y);
      }
      const u32x4 a0 = *(const u32x4*)(img + fp.ir), a1 = *(const u32x4*)(img + fp.ir + 1280);
      if constexpr (L == 0) prep_load1<1>(P, b * 4 + w, lane, pv0); else ring4(1);
      const float* p0 = (const float*)(img + fp.ap0);
      const float* p1 = (const float*)(img + fp.ap1);
      const float* r0 = (const float*)(img + fp.ar0);
      const float* r1 = (const float*)(img + fp.ar1);
      const f32x2 cA = {1.0f / 64.0f, 1.0f / 64.0f}, cC = {1.0f / 64.0f, -1.0f / 64.0f}, cB = fp.cB, cD = fp.cD, eA = fp.eA, eB = fp.eB, eC = fp.eC, eD = fp.eD;
      const u32 zo = fp.zo;
      f32x4 dr0, di0, dr1, di1;
      XF(34);
      row_fft<false, 6>(rc, a0, a1, dr0, di0, dr1, di1);
      XF(35);
      SB();
      float* op = (float*)(img + fp.ir);
      *(f32x4*)(op) = dr0; *(f32x4*)(op + 640) = di0;
      SB();
      *(f32x4*)(op + 320) = dr1; *(f32x4*)(op + 960) = di1;
      if constexpr (L == 0) { w2dma(6 + half2 * 4 + (G >> 2), 6); vmwait<13>(); }
      bar();
      XF(36);
      f32x2 pa[4], qa[4], pb[4], qb[4];
#pragma unroll
      for (int j = 0; j < 4; ++j) {
        pa[j] = f32x2{p0[j * 1280], p0[j * 1280 + 640]}; qa[j] = f32x2{r0[j * 1280], r0[j * 1280 + 640]};
      }
#pragma unroll
      for (int j = 0; j < 4; ++j) {
        pb[j] = f32x2{p1[j * 1280], p1[j * 1280 + 640]}; qb[j] = f32x2{r1[j * 1280], r1[j * 1280 + 640]};
      }
      if constexpr (L == 0) w2dma(0, 0);
      SB();
      u32x4 o;
#pragma unroll
      for (int j = 0; j < 4; ++j) {
        const f32x2 v = eD * swp(qa[j]) + (eC * qa[j] + (eB * swp(pa[j]) + eA * pa[j]));
        o[j] = mark<(L == 0 ? 0x10001u : 0u), (L == 0 ? 0u : 0x10000u)>(pkh(v.x, v.y));
      }
      st4(o, zo);
#pragma unroll
      for (int j = 0; j < 4; ++j) {
        const f32x2 v = cD * swp(qb[j]) + (cC * qb[j] + (cB * swp(pb[j]) + cA * pb[j]));
        o[j] = mark<(L == 0 ? 0x10001u : 0u), (L == 0 ? 0u : 0x10000u)>(pkh(v.x, v.y));
      }
      st4(o, zo + 4096);
      SB();
      if constexpr (L == 1) {
#pragma unroll
        for (int t = 0; t < 2; ++t)
#pragma unroll
          for (int p = 0; p < 2; ++p) {
            const f32x2 v0 = xv[4 * t + 2 * p] + xc, v1 = xv[4 * t + 2 * p + 1] + xc;
            xl[t][p] = pkb(v0.x, v1.x); xh[t][p] = pkb(v0.y, v1.y);
          }
        PIN(xl[0]); PIN(xl[1]); PIN(xh[0]); PIN(xh[1]);
      }
    }
    if constexpr (L == 0) {
      XS(37);
      FRESH();
#pragma unroll
      for (int j = 0; j < 8; ++j) { PIN(wfr[j]); PIN(wfi[j]); PIN(wn[j]); }
      u8* bw = lds + w * 32768 + 28672;
      u32x4 v0, v1;
#pragma unroll
      for (int j = 0; j < 4; ++j) { v0[j] = wfr[j] | (wfi[j] << 16); v1[j] = wfr[4 + j] | (wfi[4 + j] << 16); }
      *(u32x4*)(bw + lane * 32) = v0; *(u32x4*)(bw + lane * 32 + 16) = v1;
      st4(*(const u32x4*)(bw + l16), OFF_WX + pos * 8192 + w * 2048 + l16);
      st4(*(const u32x4*)(bw + 1024 + l16), OFF_WX + pos * 8192 + w * 2048 + 1024 + l16);
      const float hr = blo(wn[0]) + blo(wn[2]), hi = blo(wn[1]) - blo(wn[3]), nr = blo(wn[4]) + blo(wn[6]), ni = blo(wn[5]) - blo(wn[7]);
      const f32x4 ab = {0.25f * (hr + nr), 0.25f * (hi + ni), 0.25f * (hr - nr), 0.25f * (hi - ni)};
      if (tid < 8) st4(__builtin_bit_cast(u32x4, ab), OFF_WNY + tid * 4096 + pos * 16);
    }
    ST(S0);
    {
      FRESH();
      const int phi = pos, pi_ = w >> 1, kt = w & 1;
      const int k1l = 2 * (16 * kt + m) + pi_;
      auto consts = [&](auto PC) { col_consts<decltype(PC)::value>(cc, m, g, w); SB(); };
      using I0 = std::integral_constant<int, 0>; using I1 = std::integral_constant<int, 1>;
      using I2 = std::integral_constant<int, 2>; using I3 = std::integral_constant<int, 3>;
      u8* const bw = lds + w * 32768 + 28672;
      const u32 zo = (tid >> 1) * 8192 + (tid & 1) * 16;
      const u8* zb0 = ws + OFF_A + phi * 32;
      const u8* zb1 = zb0 + 128 * 8192;
      const u8* wb0 = ws + OFF_WX + phi * 32;
      const u8* wb1 = wb0 + 128 * 8192;
      const u32 wo = zo;
      PINS(zb0); PINS(zb1); PINS(wb0); PINS(wb1);
      if constexpr (L == 0) {
        u32x4 fl0, fl1;
        const u32 o = lane * 32;
        const u8* fb = ws + OFF_FLAG + ((phi >> 5) & 3) * 2048;
        w2dma(1, 1);
        ld4<0>(fl0, o, fb); ld4<16>(fl1, o, fb);
        SB();
        consts(I0{});
        consts(I1{});
        XS(38);
        vmwait<0>(); PIN(fl0); PIN(fl1);
        RETRY(!(fl0.x == ep.x && fl0.y == ep.y && fl0.z == ep.x && fl0.w == ep.y && fl1.x == ep.x && fl1.y == ep.y && fl1.z == ep.x && fl1.w == ep.y), {
          CNT(0);
          ld4r<0>(fl0, o, fb); ld4r<16>(fl1, o, fb);
          vmwait<0>(); PIN(fl0); PIN(fl1);
        });
      } else {
        SB();
        consts(I0{});
        consts(I2{});
      }
      u32x4 z0, z1, wv[2];
      if constexpr (L == 0) XS(39);
      ld4<0>(z0, zo, zb0); ld4<0>(z1, zo, zb1);
      ld4<0>(wv[0], wo, wb0); ld4<0>(wv[1], wo, wb1);
      SB();
      u32x4 wa[8], po[5];
      if constexpr (L == 0) {
        XS(40);
        u32x4 ot[4];
#pragma unroll
        for (int j = 0; j < 8; ++j) PIN(pv0[j]);
        transpose8x4(pv0, ot);
#pragma unroll
        for (int x = 0; x < 4; ++x) *(u32x4*)(bw + lane * 64 + ((x + lane) & 3) * 16) = ot[x];
#pragma unroll
        for (int x = 0; x < 4; ++x) {
          const int src = x * 16 + (lane >> 2);
          po[x] = *(const u32x4*)(bw + src * 64 + (((lane & 3) + src) & 3) * 16);
        }
        {
          u32x2 lo, hi;
          tr_read<6 * 4096>(lo, hi, ldsbase + w * 32768 + tr_addr(lane, G & 3));
          lgwait<0>();
          PIN(lo); PIN(hi);
          po[4] = u32x4{lo.x, lo.y, hi.x, hi.y};
        }
#pragma unroll
        for (int x = 0; x < 5; ++x) PIN(po[x]);
        st4(po[0], pd[0]); st4(po[1], pd[1]);
        SB();
        XS(41);
        consts(I2{});
        XS(42);
        consts(I3{});
        XS(43);
        st4(po[2], pd[2]); st4(po[3], pd[3]);
        w2dma(2, 2);
      } else {
        consts(I1{});
        ring4(2);
        SB();
        consts(I3{});
      }
      u8* const zi = lds + LDS_SCR;
      u32 zw;
      {
        const int pp = tid >> 1;
        zw = (tid & 1) * 4096 + ((pp >> 6) & 1) * 2048 + (((pp >> 3) & 3) * 16 + 2 * (pp & 7)) * 16 + ((pp >> 5) & 1) * 8;
        PIN(zw);
      }
      u32 uo = OFF_U + phi * 8192 + w * 2048 + l16;
      PIN(uo);
      SB();
      bar_nw();
      ST(S0 + 1);
      vmwait<(L == 0 ? 8 : 4)>();
      PIN(z0); PIN(z1); PIN(wv[0]); PIN(wv[1]);
      if constexpr (L == 0) {
        RETRY((stale<false, 1u>(red8<false>(z0, z1)) || max4(max4(0, wv[0]), wv[1]) == SENT), {
          CNT(1);
          if (bad_) { ld4r<0>(z0, zo, zb0); ld4r<0>(z1, zo, zb1); ld4r<0>(wv[0], wo, wb0); ld4r<0>(wv[1], wo, wb1); }
          vmwait<0>(); PIN(z0); PIN(z1); PIN(wv[0]); PIN(wv[1]);
        });
      } else {
        RETRY((stale<true, 0x10000u>(red8<true>(z0, z1))), {
          CNT(2);
          if (bad_) { ld4r<0>(z0, zo, zb0); ld4r<0>(z1, zo, zb1); }
          vmwait<0>(); PIN(z0); PIN(z1);
        });
      }
      ST(S0 + 2);
      *(u32x2*)(zi + zw) = u32x2{z0.x, z0.y}; *(u32x2*)(zi + zw + 16) = u32x2{z0.z, z0.w};
      *(u32x2*)(zi + zw + 1024) = u32x2{z1.x, z1.y}; *(u32x2*)(zi + zw + 1024 + 16) = u32x2{z1.z, z1.w};
      *(u32x4*)(zi + 8192 + t16) = wv[0]; *(u32x4*)(zi + 12288 + t16) = wv[1];
      bar();
      XF(37);
      const u8* zr = zi + pi_ * 4096 + l16;
      const u32x4 f00 = *(const u32x4*)(zr), f01 = *(const u32x4*)(zr + 1024), f10 = *(const u32x4*)(zr + 2048), f11 = *(const u32x4*)(zr + 3072);
      wv[0] = *(const u32x4*)(zi + 8192 + tid * 32); wv[1] = *(const u32x4*)(zi + 8192 + tid * 32 + 16);
      if (phi == 0) static_for<8>([&](auto J) { constexpr int j = decltype(J)::value; ld4<0>(wa[j], t16, ws + OFF_WNY + j * 4096); });
      if constexpr (L == 1) ring4(3); else { st4(po[4], OFF_W2 + (ct * 4 + w) * 32768 + (half2 * 16 + G) * 1024 + l16); }
      f32x4 d0, d1, d2, d3;
      mm8<false, 3>(d0, d1, d2, d3, f00, cc.b1re[0], f01, cc.b1re[1], f00, cc.b1im[0], f01, cc.b1im[1], f10, cc.b1re[0], f11, cc.b1re[1], f10, cc.b1im[0], f11, cc.b1im[1]);
      XF(38);
      f32x4 e0, e1, e2, e3;
      u32x4 b20 = twiddle<false>(d0, d1, cc.t1c[0], cc.t1s[0]);
      PIN(b20);
      const u32x4 b21 = cplx_under<false, true>(d2, d3, cc.t1c[1], cc.t1s[1], [&](auto I) {
        constexpr int i = decltype(I)::value;
        mf0<false, 0>(i == 0 ? e0 : i == 1 ? e1 : i == 2 ? e2 : e3, i == 0 ? cc.a2re[0][0] : i == 1 ? cc.a2im[0][0] : i == 2 ? cc.a2re[1][0] : cc.a2im[1][0], b20);
      });
      if constexpr (L == 0) w2dma(3, 3);
      f32x2 wr[2][2], wi[2][2];
      auto unpack_w = [&]() {
#pragma unroll
        for (int t = 0; t < 2; ++t)
#pragma unroll
          for (int h = 0; h < 2; ++h) {
            wr[t][h] = f32x2{blo(wv[t][2 * h]), blo(wv[t][2 * h + 1])}; wi[t][h] = f32x2{bhi(wv[t][2 * h]), bhi(wv[t][2 * h + 1])};
            PIN(wr[t][h]); PIN(wi[t][h]);
          }
      };
      mf4<false, 0, 0>(e0, e1, e2, e3, cc.a2re[0][1], b21, cc.a2im[0][1], b21, cc.a2re[1][1], b21, cc.a2im[1][1], b21);
      SB();
      unpack_w();
      SB();
      if constexpr (L == 0) { fill_h(OFF_H, 0); fill_h(OFF_H, 1); }
      XF(39);
      u32x4 a30, a31;
      auto step3 = [&](auto I) {
        constexpr int i = decltype(I)::value;
        mf0<true, 0>(i == 0 ? d0 : i == 1 ? d1 : i == 2 ? d2 : d3, a30, i == 0 ? cc.b3re[0][0] : i == 1 ? cc.b3im[0][0] : i == 2 ? cc.b3re[1][0] : cc.b3im[1][0]);
      };
      if (phi != 0) {
        a30 = cmul_b(e0, e1, wr[0], wi[0]);
        PIN(a30);
        a31 = cplx_under<true, false>(e2, e3, wr[1], wi[1], step3);
      } else {
        u32* pl = (u32*)(lds + LDS_STASH);
        const u32x4 h0 = {pkh(e0[0], e1[0]), pkh(e0[1], e1[1]), pkh(e0[2], e1[2]), pkh(e0[3], e1[3])};
        const u32x4 h1 = {pkh(e2[0], e3[0]), pkh(e2[1], e3[1]), pkh(e2[2], e3[2]), pkh(e2[3], e3[3])};
        if (k1l != 0) {
          *(u32x4*)(pl + k1l * 32 + ((g ^ (k1l >> 1)) & 7) * 4) = h0; *(u32x4*)(pl + k1l * 32 + (((4 + g) ^ (k1l >> 1)) & 7) * 4) = h1;
        } else {
#pragma unroll
          for (int r = 0; r < 4; ++r) { pl[(4 * g + r - 1) & 31] = h0[r]; pl[16 + 4 * g + r - 1] = h1[r]; }
        }
        bar();
        vmwait<(L == 0 ? 7 : 4)>();
#pragma unroll
        for (int j = 0; j < 8; ++j) PIN(wa[j]);
        if constexpr (L == 0) {
          RETRY(max4(max4(max4(max4(max4(max4(max4(max4(0, wa[0]), wa[1]), wa[2]), wa[3]), wa[4]), wa[5]), wa[6]), wa[7]) == SENT, {
            static_for<8>([&](auto J) { constexpr int j = decltype(J)::value; ld4r<0>(wa[j], t16, ws + OFF_WNY + j * 4096); });
            vmwait<0>();
            _Pragma("unroll") for (int j = 0; j < 8; ++j) PIN(wa[j]);
          });
        }
        const int kr = (64 - k1l) & 63;
        const u32x4 q0 = *(const u32x4*)(pl + kr * 32 + (((7 - g) ^ (kr >> 1)) & 7) * 4), q1 = *(const u32x4*)(pl + kr * 32 + (((3 - g) ^ (kr >> 1)) & 7) * 4);
#pragma unroll
        for (int r = 0; r < 4; ++r) {
          const f32x4 c0 = __builtin_bit_cast(f32x4, wa[r]), c1 = __builtin_bit_cast(f32x4, wa[4 + r]);
          const u32 u0 = q0[3 - r], u1 = q1[3 - r];
          const f16x2 v0 = __builtin_bit_cast(f16x2, u0), v1 = __builtin_bit_cast(f16x2, u1);
          const float qr0 = (float)v0.x, qi0 = (float)v0.y, qr1 = (float)v1.x, qi1 = (float)v1.y;
          a30[r] = pkb(e0[r] * c0[0] - e1[r] * c0[1] + qr0 * c0[2] + qi0 * c0[3], e0[r] * c0[1] + e1[r] * c0[0] + qr0 * c0[3] - qi0 * c0[2]);
          a31[r] = pkb(e2[r] * c1[0] - e3[r] * c1[1] + qr1 * c1[2] + qi1 * c1[3], e2[r] * c1[1] + e3[r] * c1[0] + qr1 * c1[3] - qi1 * c1[2]);
        }
        PIN(a30); PIN(a31);
        asm volatile("s_nop 1");
        static_for<4>(step3);
      }
      u8* ex = lds + LDS_SCR + 16384;
      mf4<true, 0, 5>(d0, d1, d2, d3, a31, cc.b3re[0][1], a31, cc.b3im[0][1], a31, cc.b3re[1][1], a31, cc.b3im[1][1]);
      XF(40);
      *(u32x4*)(ex + w * 1024 + l16) = twiddle<true>(d0, d1, cc.t2c[0], cc.t2s[0]);
      SB();
      *(u32x4*)(ex + (4 + w) * 1024 + l16) = twiddle<true>(d2, d3, cc.t2c[1], cc.t2s[1]);
      if constexpr (L == 0) vmwait<6>();
      bar();
      XF(41);
      const u8* er = ex + (w & 1) * 4096 + l16;
      const u32x4 g0 = *(const u32x4*)(er), g1 = *(const u32x4*)(er + 1024), g2 = *(const u32x4*)(er + 2048), g3 = *(const u32x4*)(er + 3072);
      mm8<true, 7>(d0, d1, d2, d3, cc.a4re[0][0], g0, cc.a4re[0][1], g1, cc.a4im[0][0], g0, cc.a4im[0][1], g1, cc.a4re[1][0], g2, cc.a4re[1][1], g3, cc.a4im[1][0], g2,
                   cc.a4im[1][1], g3);
      {
        XF(42);
        const f32x2 lr0 = lo2(d0) + lo2(d2), li0 = lo2(d1) + lo2(d3), hr0 = lo2(d0) - lo2(d2), hi0 = lo2(d1) - lo2(d3);
        const f32x2 lr1 = hi2(d0) + hi2(d2), li1 = hi2(d1) + hi2(d3), hr1 = hi2(d0) - hi2(d2), hi1 = hi2(d1) - hi2(d3);
        if (phi != 0) {
          const f32x2 pr0 = lr0 - hi0, pi0 = li0 + hr0, qr0 = lr0 + hi0, qi0 = hr0 - li0;
          st4(mark4<1u - L, L>(u32x4{pkb(pr0.x, pi0.x), pkb(qr0.x, qi0.x), pkb(pr0.y, pi0.y), pkb(qr0.y, qi0.y)}), uo);
          const f32x2 pr1 = lr1 - hi1, pi1 = li1 + hr1, qr1 = lr1 + hi1, qi1 = hr1 - li1;
          st4(mark4<1u - L, L>(u32x4{pkb(pr1.x, pi1.x), pkb(qr1.x, qi1.x), pkb(pr1.y, pi1.y), pkb(qr1.y, qi1.y)}), uo + 1024);
          MEMFENCE();
        } else {
          st4(mark4<1u - L, L>(u32x4{pkb(lr0.x, hr0.x), pkb(li0.x, hi0.x), pkb(lr0.y, hr0.y), pkb(li0.y, hi0.y)}), uo);
          st4(mark4<1u - L, L>(u32x4{pkb(lr1.x, hr1.x), pkb(li1.x, hi1.x), pkb(lr1.y, hr1.y), pkb(li1.y, hi1.y)}), uo + 1024);
        }
      }
    }
    ST(S0 + 3);
    {
      FRESH();
      auto w2take = [&](auto KC) {
        constexpr int k = decltype(KC)::value;
        u32x2 lo[4], hi[4];
        static_for<4>([&](auto C) { constexpr int c = decltype(C)::value; tr_read<k * 4096>(lo[c], hi[c], ldsbase + w * 32768 + tr_addr(lane, c)); });
        lgwait<0>();
#pragma unroll
        for (int c = 0; c < 4; ++c) {
          PIN(lo[c]); PIN(hi[c]);
          w2a[k][c] = u32x4{lo[c].x, lo[c].y, hi[c].x, hi[c].y};
          PINA(w2a[k][c]);
        }
      };
      if constexpr (L == 0) { w2dma(4, 4); w2dma(5, 5); vmwait<12>(); static_for<4>(w2take); }
      SB();
      row_consts<true, 0>(rc, m, g);
      f32x2 xa[4], xb[4];
#pragma unroll
      for (int i = 0; i < 4; ++i) {
        xa[i] = f32x2{blo(xl[i >> 1][i & 1]), bhi(xl[i >> 1][i & 1])}; xb[i] = f32x2{blo(xh[i >> 1][i & 1]), bhi(xh[i >> 1][i & 1])};
      }
      const float ca = first_lane(xa[0].x), cb = first_lane(xb[0].x);
      float ch = lane < 32 ? ca : cb;
      PIN(ch);
#pragma unroll
      for (int i = 0; i < 4; ++i) { xa[i] -= ca; xb[i] -= cb; PIN(xa[i]); PIN(xb[i]); }
      SB();
      u32x4 c0, c1;
      const u32 uo = (tid >> 1) * 8192 + (tid & 1) * 16;
      const u8* ub0 = ws + OFF_U + pos * 32;
      const u8* ub1 = ub0 + 128 * 8192;
      row_consts<true, 1>(rc, m, g);
      SB();
      __builtin_amdgcn_s_sleep(1);
      ld4<0>(c0, uo, ub0); ld4<0>(c1, uo, ub1);
      if constexpr (L == 0) {
        fill_h(OFF_H, 2); fill_h(OFF_H, 3); fill_h(OFF_H, 4);
        vmwait<5>();
        w2take(std::integral_constant<int, 4>{}); w2take(std::integral_constant<int, 5>{});
      }
      SB();
      if constexpr (L == 1) ring4(4); else { fill_h(OFF_H, 5); fill_h(OFF_H, 6); }
      u32* img = (u32*)(lds + LDS_SCR);
      u32 ip0, iq0, iq1, xno, tw0, tw1, trd;
      {
        const int ph = tid >> 1, fq0 = ph ? 512 - ph : 256, fq1 = 384 - ph;
        ip0 = ((tid & 1) * 1280 + (ph & 31) * 20 + (ph >> 5)) * 4;
        iq0 = ((tid & 1) * 1280 + (fq0 & 31) * 20 + (fq0 >> 5)) * 4; iq1 = ((tid & 1) * 1280 + (fq1 & 31) * 20 + (fq1 >> 5)) * 4;
        xno = OFF_A + (pos >> 1) * 16384 + (tid >> 3) * 256 + (pos & 1) * 128 + (tid & 7) * 16;
        tw0 = (4 * m + g) * 128 + ((2 * w) ^ (m & 7)) * 16; tw1 = (4 * m + g) * 128 + ((2 * w + 1) ^ (m & 7)) * 16;
        trd = (tid >> 3) * 128 + ((tid & 7) ^ ((tid >> 5) & 7)) * 16;
        PIN(ip0); PIN(iq0); PIN(iq1); PIN(xno); PIN(tw0); PIN(tw1); PIN(trd);
      }
      SB();
      ST(S0 + 4);
      vmwait<(L == 0 ? 5 : 4)>(); PIN(c0); PIN(c1);
      {
        RETRY((stale<L == 1, 1u>(red8<L == 1>(c0, c1))), {
          CNT(3);
          if (bad_) { ld4r<0>(c0, uo, ub0); ld4r<0>(c1, uo, ub1); }
          vmwait<0>(); PIN(c0); PIN(c1);
        });
      }
      ST(S0 + 5);
      {
        u8* d = (u8*)img;
        *(u32*)(d + ip0) = c0.x; *(u32*)(d + iq0) = c0.y; *(u32*)(d + 2560 + ip0) = c0.z; *(u32*)(d + 2560 + iq0) = c0.w;
        *(u32*)(d + ip0 + 16) = c1.x; *(u32*)(d + iq1) = c1.y; *(u32*)(d + 2560 + ip0 + 16) = c1.z; *(u32*)(d + 2560 + iq1) = c1.w;
      }
      bar();
      XF(43);
      const u32x4 a0 = *(const u32x4*)(img + w * 640 + m * 20 + 4 * g), a1 = *(const u32x4*)(img + w * 640 + (16 + m) * 20 + 4 * g);
      if constexpr (L == 1) ring4(5);
      if constexpr (L == 0) ring4(0);
      f32x4 dr0, di0, dr1, di1;
      row_fft<true, 7>(rc, a0, a1, dr0, di0, dr1, di1);
      if constexpr (L == 0) ring4(1);
      XF(44);
      const float ys = 1.0f / 32768.0f;
      xa[0] += lo2(dr0) * ys; xa[1] += hi2(dr0) * ys; xa[2] += lo2(dr1) * ys; xa[3] += hi2(dr1) * ys;
      xb[0] += lo2(di0) * ys; xb[1] += hi2(di0) * ys; xb[2] += lo2(di1) * ys; xb[3] += hi2(di1) * ys;
      const f32x2 sa = (xa[0] + xa[1]) + (xa[2] + xa[3]), sb = (xb[0] + xb[1]) + (xb[2] + xb[3]);
      f32x2 qa = xa[0] * xa[0], qb = xb[0] * xb[0];
#pragma unroll
      for (int i = 1; i < 4; ++i) { qa += xa[i] * xa[i]; qb += xb[i] * xb[i]; }
      if constexpr (L == 0) { SB(); ring4(2); SB(); }
      f32x2 R, O;
      XF(45);
      ln_coef(f32x2{sa.x + sa.y, sb.x + sb.y}, f32x2{qa.x + qa.y, qb.x + qb.y}, ch, R, O);
      XF(46);
      u32x4 na, nb;
#pragma unroll
      for (int i = 0; i < 4; ++i) {
        const f32x2 va = xa[i] * R.x + O.x, vb = xb[i] * R.y + O.y;
        na[i] = mark<0x10000u, 1u - L>(pkb(va.x, va.y)); nb[i] = mark<0x10000u, 1u - L>(pkb(vb.x, vb.y));
      }
      u8* tr = lds + LDS_SCR + 12288;
      *(u32x4*)(tr + tw0) = na; *(u32x4*)(tr + tw1) = nb;
      if constexpr (L == 0) vmwait<12>(); else vmwait<0>();
      bar();
      XF(47);
      st4(*(const u32x4*)(tr + trd), xno);
      st4(*(const u32x4*)(tr + 4096 + trd), xno + 8192);
      SB();
#pragma unroll
      for (int i = 0; i < 4; ++i) { xa[i] += ca; xb[i] += cb; }
      const u32x4 rl = {pkb(xa[0].x, xa[0].y), pkb(xa[1].x, xa[1].y), pkb(xa[2].x, xa[2].y), pkb(xa[3].x, xa[3].y)};
      const u32x4 rh = {pkb(xb[0].x, xb[0].y), pkb(xb[1].x, xb[1].y), pkb(xb[2].x, xb[2].y), pkb(xb[3].x, xb[3].y)};
      if constexpr (L == 0) {
        *(u32x4*)(lds + LDS_STASH + t16) = rl; *(u32x4*)(lds + LDS_STASH + 4096 + t16) = rh;
        XS(44);
        ring4(3); ring4(4); ring4(5); ring4(6); ring4(7);
      } else {
        xl[0] = u32x2{rl.x, rl.y}; xl[1] = u32x2{rl.z, rl.w}; xh[0] = u32x2{rh.x, rh.y}; xh[1] = u32x2{rh.z, rh.w};
        PIN(xl[0]); PIN(xl[1]); PIN(xh[0]); PIN(xh[1]);
        __builtin_amdgcn_s_sleep(XSLP);
      }
    }
    ST(S0 + 6);
    {
      FRESH();
      u32x4 tk[32], he[32], hl[16];
      u32x4 hh[8], hf[8];
      f32x4 ac1[6][2], ac2[4][2], ac3[4][2];
      const u8* hs0 = ws + OFF_H + (4 * G + 2 * half2) * 114688 + (4 * w) * 7168;
      const u8* hs[4];
      const u8* hp[4];
#pragma unroll
      for (int j = 0; j < 4; ++j) {
        hs[j] = hs0 + opq((j + ct) & 3) * 7168; hp[j] = hs0 + opq(j ^ (2 * (ct & 1))) * 7168 + 6144;
        PINS(hs[j]); PINS(hp[j]);
      }
      const u8* w2src = ws + OFF_W2 + (ct * 4 + w) * 32768;
      u32 hv[4], hw[2];
#pragma unroll
      for (int u = 0; u < 4; ++u) { hv[u] = l16 + ((u ^ w) >> 1) * 114688 + ((u ^ w) & 1) * 3072; PIN(hv[u]); }
#pragma unroll
      for (int q = 0; q < 2; ++q) { hw[q] = l16 + (q ^ (w >> 1)) * 114688; PIN(hw[q]); }
      auto hload = [&](auto RE, auto KC, auto UC) {
        constexpr int k = decltype(KC)::value, u = decltype(UC)::value;
        constexpr bool re = decltype(RE)::value;
        if constexpr (k == 4 || k == 5) {
          if constexpr (re) ld4r<0>(hh[4 * (k - 4) + u], hw[u >> 1], hp[2 * (k - 4) + (u & 1)]); else ld4<0>(hh[4 * (k - 4) + u], hw[u >> 1], hp[2 * (k - 4) + (u & 1)]);
        } else {
          constexpr int j = k < 4 ? k : k < 10 ? k - 6 : k - 10, o = k < 4 ? 0 : k < 10 ? 1024 : 2048;
          u32x4& d = k < 10 ? he[(k < 4 ? 0 : 16) + 4 * j + u] : hl[4 * j + u];
          if constexpr (re) ld4r<o>(d, hv[u], hs[j]); else ld4<o>(d, hv[u], hs[j]);
        }
      };
      auto hseq = [&](auto IC) {
        constexpr int i = decltype(IC)::value;
        hload(std::false_type{}, std::integral_constant<int, (i >> 2)>{}, std::integral_constant<int, i & 3>{});
      };
      u64 bm0 = 0;
      {
        const u8* w1b = ws + OFF_W1 + member * 229376 + 131072 + w * 8192;
        const u8* tk0 = ws + OFF_A + (8 * G + 2 * w) * 16384 + 4096;
        const u8* tk1 = tk0 + 8192;
        u32 tv1 = l16 + 16384, llo = ldsbase + l16, lhi = llo + 65536;
        u32 hd0 = OFF_H + (4 * G + w) * 114688 + member * 7168 + l16, hd1 = hd0 + 3072;
        PIN(tv1); PIN(llo); PIN(lhi); PIN(hd0); PIN(hd1); PINS(tk0); PINS(tk1);
        u32 x3o = OFF_X3 + (8 * pos + 2 * w) * 1024 + xo;
        if constexpr (L == 1) PIN(x3o);
        auto tok = [&](auto RE, auto QC) {
          constexpr int q = decltype(QC)::value, ks = q >> 1;
          if constexpr (decltype(RE)::value) ld4r<(ks & 7) * 1024 - 4096>(tk[q], q & 1 ? tv1 : l16, ks >> 3 ? tk1 : tk0);
          else ld4<(ks & 7) * 1024 - 4096>(tk[q], q & 1 ? tv1 : l16, ks >> 3 ? tk1 : tk0);
        };
        constexpr bool TONE = L == 0;
        constexpr u32 TBIT = L == 0 ? 1u : 0x10000u;
        u32 mx0 = 0, mx1 = 0;
        auto mx0_fin = [&]() { return red2<TONE>(mx0, mx1) & TBIT; };
        auto fix = [&](auto QC, u64 bad) {
          constexpr int q0 = decltype(QC)::value;
          if (__builtin_expect(bad != 0, 0)) {
            _Pragma("nounroll") for (int sp = 0; sp < SPIN_CAP; ++sp) {
              CNT(4);
              static_for<4>([&](auto I) { tok(std::true_type{}, std::integral_constant<int, q0 + decltype(I)::value>{}); });
              vmwait<0>();
#pragma unroll
              for (int q = q0; q < q0 + 4; ++q) PIN(tk[q]);
              if (!__any(stale<TONE, TBIT>(red2<TONE>(red8<TONE>(tk[q0], tk[q0 + 1]), red8<TONE>(tk[q0 + 2], tk[q0 + 3]))))) break;
            }
          }
        };
        constexpr int PD = 6;
        u32x4 af[PD];
        auto frag = [&](auto XC) {
          constexpr int x = decltype(XC)::value, o = (x & 127) * 1024;
          lda<o & 65535>(af[x % PD], o < 65536 ? llo : lhi);
        };
        if constexpr (L == 1) static_for<PD - 1>(frag);
        XF(48);
        static_for<NTOK0>([&](auto Q) {
          tok(std::false_type{}, Q);
          if constexpr (decltype(Q)::value == 3) bar_nw();
        });
        XF(49);
        if constexpr (L == 0) {
          vmwait<NTOK0>();
          bar_nw();
          static_for<PD - 1>(frag);
        }
        vmwait<NTOK0 - 4>();
        ST(S0 + 7);
#pragma unroll
        for (int q = 0; q < 4; ++q) PIN(tk[q]);
        mx0 = red8<TONE>(tk[0], tk[1]); mx1 = red8<TONE>(tk[2], tk[3]);
        fix(std::integral_constant<int, 0>{}, __ballot(stale<TONE, TBIT>(red2<TONE>(mx0, mx1))));
        lgwait<PD - 2>();
        SB();
        ST(S0 + 8);
        float eg[2][2];
        u32 ev[2];
        u64 bm = 0;
        u32x4 eo[1];
        auto epi = [&](auto NC) {
          constexpr int n = decltype(NC)::value;
          if constexpr (n < NEPI1 + NEPI2) {
            constexpr bool p1 = n < NEPI1;
            constexpr int n0 = p1 ? n : n - NEPI1, i = n0 - n0 / 25;
            if constexpr (n0 % 25 == 24) st4(eo[0], p1 ? (n == 24 ? hd0 : n == 49 ? hd1 : hd1 + 3072) : (n0 == 24 ? hd0 : hd1) + 1024);
            else {
              constexpr int s = i & 1, st = (i % 12) >> 1, blk = i / 12;
              constexpr int u = p1 ? (blk < 4 ? blk >> 1 : blk - 4) : blk >> 1, pr = p1 ? (blk < 4 ? blk & 1 : 2) : blk & 1;
              constexpr int d = 2 * (pr == 2 ? u : pr) + s;
              const f32x4& ga = p1 ? ac1[2 * pr][u] : ac2[2 * (pr & 1)][u];
              const f32x4& up = p1 ? ac1[2 * pr + 1][u] : ac2[2 * (pr & 1) + 1][u];
              if constexpr (st < 2) { eg[s][st] = relu(ga[2 * s + st]); PIN(eg[s][st]); }
              else if constexpr (st < 4) { eg[s][st - 2] *= up[2 * s + st - 2]; PIN(eg[s][st - 2]); }
              else if constexpr (st == 4) { ev[s] = pkb(eg[s][0], eg[s][1]); PIN(ev[s]); }
              else { u32 v = mark<1u - L, L>(ev[s]); PIN(v); eo[0][d] = v; }
            }
            SB();
          }
        };
        static_for<224>([&](auto XC) {
          constexpr int x = decltype(XC)::value, job = G1S[L].job[x], ji = G1S[L].idx[x], e0 = G1S[L].epi[x], e1 = G1S[L].epi[x + 1];
          if constexpr (x == 32 || x == 64) bar_nw();
          if constexpr (x == 97) { vmwait<g1_wait(L, x, 2, 7)>(); bar_nw(); }
          if constexpr (x == 150) { vmwait<g1_wait(L, x, 2, 23)>(); bar_nw(); }
          constexpr int cj = (x + 12) / 12, co = x + 12 - 12 * cj;
          constexpr bool chk = x < 84 && co >= 5;
          constexpr int ks = x < 96 ? x / 6 : x < 160 ? (x - 96) >> 2 : (x - 160) >> 2, t = x < 96 ? x % 6 : x & 3;
          auto mm = [&](int u) {
            f32x4& a = x < 96 ? ac1[x < 96 ? t : 0][u] : x < 160 ? ac2[x < 96 ? 0 : t][u] : ac3[x < 96 ? 0 : t][u];
            mmw<ks == 0>(a, af[x % PD], tk[2 * ks + u]);
          };
          SB();
          mm(0);
          SB();
          if constexpr (x < 223) lgwait<(x + PD - 2 < 224 ? PD - 3 : 222 - x)>();
          if constexpr (chk && co == 5) {
            vmwait<g1_wait(L, x, 1, 4 * cj + 3)>();
            PIN(tk[4 * cj]); PIN(tk[4 * cj + 1]); PIN(tk[4 * cj + 2]); PIN(tk[4 * cj + 3]);
          }
          SB();
          if constexpr (chk && co >= 6 && co < 10) {
            const u32x4 v = tk[4 * cj + co - 6];
            mx0 = co == 6 ? red2<TONE>(v.x, v.y) : red3<TONE>(mx0, v.x, v.y); PIN(mx0); SB();
            mx1 = co == 6 ? red2<TONE>(v.z, v.w) : red3<TONE>(mx1, v.z, v.w); PIN(mx1);
          }
          if constexpr (chk && co == 10) { mx0 = mx0_fin(); PIN(mx0); SB(); bm = __ballot(TONE ? mx0 == 0 : mx0 != 0); PINS(bm); }
          if constexpr (x >= 212 && x < 218) {
            if constexpr (x == 212) { vmwait<g1_wait(L, x, 4, 3)>(); PIN(he[0]); PIN(he[1]); PIN(he[2]); PIN(he[3]); }
            else if constexpr (x < 217) {
              const u32x4 v = he[x - 213];
              mx0 = x == 213 ? red2<L == 1>(v.x, v.y) : red3<L == 1>(mx0, v.x, v.y); PIN(mx0); SB();
              mx1 = x == 213 ? red2<L == 1>(v.z, v.w) : red3<L == 1>(mx1, v.z, v.w); PIN(mx1);
            } else { mx0 = red2<L == 1>(mx0, mx1) & 1u; PIN(mx0); SB(); bm0 = __ballot(L == 1 ? mx0 == 0 : mx0 != 0); PINS(bm0); }
          }
          if constexpr (e0 < e1) epi(std::integral_constant<int, e0>{});
          if constexpr (e0 + 1 < e1) epi(std::integral_constant<int, e0 + 1>{});
          SB();
          mm(1);
          SB();
          if constexpr (chk && co == 11) fix(std::integral_constant<int, 4 * cj>{}, bm);
          if constexpr (job == 2 && (ji & 3) == 0) set_m0(ldsbase + (ji >> 3) * 32768 + w * 8192 + (ji & 4) * 1024);
          SB();
          if constexpr (x + PD - 1 < 224) frag(std::integral_constant<int, x + PD - 1>{});
          SB();
          if constexpr (job == 1) tok(std::false_type{}, std::integral_constant<int, ji>{});
          if constexpr (job == 2) dma1<(ji & 3) * 1024>(l16, w1b + (ji >> 3) * 32768 + (ji & 4) * 1024);
          if constexpr (job == 3) ld4a<(ji & 7) * 1024 - 4096>(w2a[6 + (ji >> 2)][ji & 3], l16, w2src + (ji >> 3) * 8192 + 4096);
          if constexpr (job == 6) st2(ji & 2 ? xh[ji & 1] : xl[ji & 1], x3o + ji * 512);
          if constexpr (job == 4) hseq(std::integral_constant<int, ji>{});
          if constexpr (e0 + 2 < e1) epi(std::integral_constant<int, e0 + 2>{});
          SB();
        });
        ST(S0 + 9);
        asm volatile("s_nop 7");
        SB();
#pragma unroll
        for (int u = 0; u < 2; ++u) {
          u32x4 o;
#pragma unroll
          for (int p = 0; p < 2; ++p) {
            const f32x4 ga = ac3[2 * p][u], up = ac3[2 * p + 1][u];
            const f32x2 a = f32x2{relu(ga[0]), relu(ga[1])} * lo2(up), c = f32x2{relu(ga[2]), relu(ga[3])} * hi2(up);
            o[2 * p] = mark<1u - L, L>(pkb(a.x, a.y)); o[2 * p + 1] = mark<1u - L, L>(pkb(c.x, c.y));
          }
          st4(o, (u ? hd1 : hd0) + 2048);
        }
        SB();
      }
      ST(S0 + 10);
      {
        f32x4 acc[4][4];
        u32x4 x3a, x3b;
        u32 eo = 0, oo = 0, rw[3], ta = 0, tb = 0;
        auto addr = [&](auto IC) {
          constexpr int i = decltype(IC)::value;
          const u32 sb = (128 * G + 64 * half2 + 16 * w) * 1024 + ct * 128;
          if constexpr (i < 3) { rw[i] = l16 + (((i + 1) ^ w) * 3 + i) * 4096; PIN(rw[i]); }
          if constexpr (i == 3) { ta = (l16 >> 4) & 0x30; PIN(ta); }
          if constexpr (i == 4) { tb = l16 & (L == 1 ? 0xf0 : 0xe0); PIN(tb); }
          if constexpr (i == 5) { eo = tb * 64 + sb; PIN(eo); }
          if constexpr (i == 6) { tb = l16 & 0x10; PIN(tb); }
          if constexpr (i == 7) { tb = tb * 4 + ta; PIN(tb); }
          if constexpr (i == 8) { eo += L == 1 ? ta : tb; PIN(eo); }
          if constexpr (L == 1) {
            if constexpr (i == 9) { ta = l16 & 0x20; PIN(ta); }
            if constexpr (i == 10) { oo = ta * 1024 + tb; PIN(oo); }
            if constexpr (i == 11) { ta = l16 & 0x80; PIN(ta); }
            if constexpr (i == 12) { oo += ta * 16; PIN(oo); }
            if constexpr (i == 13) { ta = l16 & 0x40; PIN(ta); }
            if constexpr (i == 14) { oo += ta * 16; PIN(oo); }
            if constexpr (i == 15) { oo += row_of_pos(16 * G + 8 * half2 + 2 * w, 0) * 1024 + ct * 128; PIN(oo); }
          }
        };
        const u8* x3s = ws + OFF_X3;
        u64 bm = 0;
        PINS(x3s);
        constexpr int NEARLY = g1_ne(L), LB = 40 - NEARLY, XG = 31;
        auto cum = [](int n) constexpr { const int c = n - n / 8; return c < LB + 16 ? c : LB + 16; };
        auto young = [cum](int k, int n) constexpr {
          const int i = 4 * k + 3;
          const int done = k < 10 ? i - NEARLY + 1 : LB + 4 + 3 * (k - 10);
          const int xr = (L == 1 && n > XG && done <= cum(XG)) ? 2 : 0;
          if (k < 10 && i < NEARLY) return cum(n) + 2 + G1S[L].cum[224] - G1S[L].pos[4][i] - 1 + xr;
          return cum(n) - done + xr;
        };
        constexpr bool HONE = L == 1;
        u32 mx0 = 0, mx1 = 0;
        auto mx0_fin = [&]() { return red2<HONE>(mx0, mx1) & 1u; };
        auto pin_k = [&](auto KC) {
          constexpr int k = decltype(KC)::value;
#pragma unroll
          for (int u = 0; u < 4; ++u) {
            if constexpr (k < 4) PIN(he[4 * k + u]);
            else if constexpr (k < 6) PIN(hh[4 * (k - 4) + u]);
            else if constexpr (k < 10) PIN(he[16 + 4 * (k - 6) + u]);
            else PIN(hl[4 * (k - 10) + u]);
          }
        };
        auto opnd = [&](auto KC, int u) {
          constexpr int k = decltype(KC)::value;
          if constexpr (k < 4) return he[4 * k + u];
          else if constexpr (k < 6) return hh[4 * (k - 4) + u];
          else if constexpr (k < 10) return he[16 + 4 * (k - 6) + u];
          else return hl[4 * (k - 10) + u];
        };
        u32 wodd = (w & 1) ? ~0u : 0u;
        PIN(wodd);
        auto mkhalf = [&](auto KC, auto IC) {
          constexpr int k = decltype(KC)::value, i = decltype(IC)::value, u = i >> 2, d = i & 3;
          const u32x4& src = hh[4 * (k - 4) + (u & 2) + (d >> 1)];
          u32 v = (src[2 * ((u & 1) ^ 1) + (d & 1)] & wodd) | (src[2 * (u & 1) + (d & 1)] & ~wodd);
          PIN(v);
          hf[4 * (k - 4) + u][d] = v;
        };
        auto fixk = [&](auto KC, u64 bad) {
          constexpr int k = decltype(KC)::value;
          if (__builtin_expect(bad != 0, 0)) {
            _Pragma("nounroll") for (int sp = 0; sp < SPIN_CAP; ++sp) {
              CNT(k < 10 ? 5 : 6);
              static_for<4>([&](auto U) { hload(std::true_type{}, KC, U); });
              vmwait<0>();
              pin_k(KC);
              if (!__any(stale<HONE, 1u>(red2<HONE>(red8<HONE>(opnd(KC, 0), opnd(KC, 1)), red8<HONE>(opnd(KC, 2), opnd(KC, 3)))))) break;
            }
            if constexpr (k == 4 || k == 5) static_for<16>([&](auto I) { mkhalf(KC, I); });
          }
        };
        if constexpr (L == 0) {
#pragma unroll
          for (int k = 6; k < 14; ++k) { PINA(w2a[k][0]); PINA(w2a[k][1]); PINA(w2a[k][2]); PINA(w2a[k][3]); }
        }
        fixk(std::integral_constant<int, 0>{}, bm0);
        auto qchk = [&](auto QC, auto JC) {
          constexpr int qd = decltype(QC)::value, j = decltype(JC)::value, u = (qd + 1) & 3;
          if constexpr (j == 0) { PIN(hl[4 + u]); PIN(hl[8 + u]); PIN(hl[12 + u]); }
          else if constexpr (j < 4) {
            const u32x4 v = hl[4 * j + u];
            mx0 = j == 1 ? red2<HONE>(v.x, v.y) : red3<HONE>(mx0, v.x, v.y); PIN(mx0); SB();
            mx1 = j == 1 ? red2<HONE>(v.z, v.w) : red3<HONE>(mx1, v.z, v.w); PIN(mx1);
          } else if constexpr (j == 4) { mx0 = mx0_fin(); PIN(mx0); SB(); bm = __ballot(HONE ? mx0 == 0 : mx0 != 0); PINS(bm); }
          else if (__builtin_expect(bm != 0, 0)) {
            _Pragma("nounroll") for (int sp = 0; sp < SPIN_CAP; ++sp) {
              CNT(6);
              static_for<3>([&](auto K) { hload(std::true_type{}, std::integral_constant<int, 11 + decltype(K)::value>{}, std::integral_constant<int, u>{}); });
              vmwait<0>();
              PIN(hl[4 + u]); PIN(hl[8 + u]); PIN(hl[12 + u]);
              if (!__any(stale<HONE, 1u>(red4<HONE>(red8<HONE>(hl[4 + u], hl[8 + u]), hl[12 + u])))) break;
            }
          }
        };
        ST(S0 + 11);
        static_for<44>([&](auto NC) {
          constexpr int n = decltype(NC)::value, k = n >> 2, c = n & 3;
          using KC = std::integral_constant<int, k>;
          using KN = std::integral_constant<int, k + 1>;
          static_for<4>([&](auto UC) {
            constexpr int u = decltype(UC)::value;
            SB();
            if constexpr (k == 4 || k == 5) mmw<false>(acc[c][u], w2a[k][c], hf[4 * (k - 4) + u]);
            else mmw<k == 0>(acc[c][u], w2a[k][c], opnd(KC{}, u));
            SB();
            constexpr bool hk = k == 3 || k == 4;
            constexpr int j = u == 3 ? -9 : 3 * c + u - (hk ? 1 : 5);
            if constexpr (k == 10) {
              if constexpr (j == -1) vmwait<clamp63(young(11, n))>();
              if constexpr (j >= -1 && j < 4) qchk(std::integral_constant<int, 0>{}, std::integral_constant<int, j + 1>{});
            }
            if constexpr (k < 10 && j == -1) { vmwait<clamp63(young(k + 1, n))>(); pin_k(KN{}); }
            if constexpr (k < 10 && j >= 0 && j < 4) {
              const u32x4 v = opnd(KN{}, j);
              mx0 = j == 0 ? red2<HONE>(v.x, v.y) : red3<HONE>(mx0, v.x, v.y); PIN(mx0); SB();
              mx1 = j == 0 ? red2<HONE>(v.z, v.w) : red3<HONE>(mx1, v.z, v.w); PIN(mx1);
            }
            if constexpr ((k < 3 || k == 5) && c == 0 && u < 2) {
              constexpr int i0 = 4 * (k < 3 ? k : 3) + 2 * u;
              addr(std::integral_constant<int, i0>{}); SB(); addr(std::integral_constant<int, i0 + 1>{});
            }
            if constexpr (hk && j >= 4 && j != 5) {
              constexpr int i0 = j == 4 ? 0 : 2 + 3 * (j - 6);
              mkhalf(KN{}, std::integral_constant<int, i0>{}); SB(); mkhalf(KN{}, std::integral_constant<int, i0 + 1>{});
              if constexpr (j >= 6 && i0 + 2 < 16) { SB(); mkhalf(KN{}, std::integral_constant<int, i0 + 2>{}); }
            }
            if constexpr (k < 10 && j == 5) { mx0 = mx0_fin(); PIN(mx0); SB(); bm = __ballot(HONE ? mx0 == 0 : mx0 != 0); PINS(bm); }
            if constexpr (u == 3) {
              constexpr int q = cum(n);
              if constexpr (cum(n + 1) == q) {}
              else if constexpr (q < LB) hseq(std::integral_constant<int, NEARLY + q>{});
              else if constexpr (q < LB + 4) hload(std::false_type{}, std::integral_constant<int, 10>{}, std::integral_constant<int, q - LB>{});
              else hload(std::false_type{}, std::integral_constant<int, 11 + (q - LB - 4) % 3>{}, std::integral_constant<int, ((q - LB - 4) / 3 + 1) & 3>{});
              if constexpr (n == 23) {
                bar_nw();
              }
              if constexpr (L == 1 && n == XG) { ld4<0>(x3a, eo, x3s); ld4<64>(x3b, eo, x3s); }
              if constexpr (c == 3) {
                if constexpr (k < 10) fixk(KN{}, bm); else qchk(std::integral_constant<int, 0>{}, std::integral_constant<int, 5>{});
              }
            }
          });
        });
        static_for<16>([&](auto TC) {
          constexpr int t = decltype(TC)::value, u = ((t >> 2) + 1) & 3, c = t & 3;
          using QN = std::integral_constant<int, (t >> 2) + 1>;
          SB();
          mmw<false>(acc[c][u], w2a[11][c], hl[4 + u]);
          SB();
          if constexpr (t < 12) {
            if constexpr (c == 0) { vmwait<young(12 + (t >> 2), 44)>(); qchk(QN{}, std::integral_constant<int, 0>{}); }
            else qchk(QN{}, std::integral_constant<int, c>{});
          }
          SB();
          mmw<false>(acc[c][u], w2a[12][c], hl[8 + u]);
          SB();
          if constexpr (t >= 3 && t < 15) *(f32x4*)(lds + rw[(t - 3) >> 2] + ((t - 3) & 3) * 1024) = acc[(t - 3) & 3][((t - 3) >> 2) + 1];
          SB();
          mmw<false>(acc[c][u], w2a[13][c], hl[12 + u]);
          SB();
          if constexpr (t < 12 && c == 3) { qchk(QN{}, std::integral_constant<int, 4>{}); qchk(QN{}, std::integral_constant<int, 5>{}); }
        });
        SB();
        ST(S0 + 12);
        bar();
        f32x4 s[3][4];
#pragma unroll
        for (int c = 0; c < 4; ++c)
#pragma unroll
          for (int p = 0; p < 3; ++p) s[p][c] = *(const f32x4*)(lds + w * 12288 + p * 4096 + c * 1024 + l16);
        SB();
        if constexpr (L == 1) { vmwait<0>(); PIN(x3a); PIN(x3b); }
        u32x4 y[2];
#pragma unroll
        for (int a = 0; a < 2; ++a) {
          const f32x4 s0 = (acc[2 * a][0] + s[0][2 * a]) + (s[1][2 * a] + s[2][2 * a]);
          const f32x4 s1 = (acc[2 * a + 1][0] + s[0][2 * a + 1]) + (s[1][2 * a + 1] + s[2][2 * a + 1]);
          y[a] = u32x4{pkb(s0[0], s0[1]), pkb(s0[2], s0[3]), pkb(s1[0], s1[1]), pkb(s1[2], s1[3])};
          if constexpr (L == 1) {
            const u32x4 xr = a ? x3b : x3a;
#pragma unroll
            for (int p = 0; p < 4; ++p) y[a][p] = pkb(blo(xr[p]) + blo(y[a][p]), bhi(xr[p]) + bhi(y[a][p]));
          }
        }
        {
          const bool odd = m & 1;
          u32x4 z0, z1;
#pragma unroll
          for (int p = 0; p < 4; ++p) {
            const u32 t = (u32)__builtin_amdgcn_mov_dpp((int)(odd ? y[0][p] : y[1][p]), 0xB1, 0xf, 0xf, true);
            z0[p] = odd ? t : y[0][p]; z1[p] = odd ? y[1][p] : t;
          }
          if constexpr (L == 0) { st4(mark4<0x10001u, 0u>(z0), OFF_A + eo); st4(mark4<0x10001u, 0u>(z1), OFF_A + eo + 1024); }
          else {
            const __amdgpu_buffer_rsrc_t ro = __builtin_amdgcn_make_buffer_rsrc(P.out, 0, 2048 * 1024, 0x00020000);
            __builtin_amdgcn_raw_buffer_store_b128(z0, ro, oo, 0, 16); __builtin_amdgcn_raw_buffer_store_b128(z1, ro, oo + 1024 * 1024, 0, 16);
          }
        }
      }
    }
    ST(S0 + 13);
    if constexpr (L == 0) {
      FRESH();
      SB();
      row_consts<false, 0>(rc, m, g);
      SB();
      u32x2 d0, d1, d2, d3;
      const u8* db = ws + OFF_A + (8 * pos + 2 * w) * 1024;
      const u32x4 sl = *(const u32x4*)(lds + LDS_STASH + t16), sh = *(const u32x4*)(lds + LDS_STASH + 4096 + t16);
#pragma unroll
      for (int i = 0; i < 4; ++i) {
        xv[2 * i] = f32x2{blo(sl[i]), blo(sh[i])}; xv[2 * i + 1] = f32x2{bhi(sl[i]), bhi(sh[i])};
      }
      xc = f32x2{first_lane(xv[0].x), first_lane(xv[0].y)};
      xch = lane < 32 ? xc.x : xc.y;
      PIN(xch);
#pragma unroll
      for (int i = 0; i < 8; ++i) { xv[i] -= xc; PIN(xv[i]); }
      SB();
      bar_nw();
      ld2<0>(d0, xo, db); ld2<512>(d1, xo, db); ld2<1024>(d2, xo, db); ld2<1536>(d3, xo, db);
      ring4(0);
      SB();
      row_consts<false, 1>(rc, m, g);
      SB();
      fwd_pre(fp, tid, w, pos);
      SB();
      ST(S0 + 14);
      vmwait<4>(); PIN(d0); PIN(d1); PIN(d2); PIN(d3);
      RETRY((stale<false, 1u>(red8<false>(u32x4{d0.x, d0.y, d1.x, d1.y}, u32x4{d2.x, d2.y, d3.x, d3.y}))), {
        CNT(7);
        if (bad_) { ld2r<0>(d0, xo, db); ld2r<512>(d1, xo, db); ld2r<1024>(d2, xo, db); ld2r<1536>(d3, xo, db); }
        vmwait<0>(); PIN(d0); PIN(d1); PIN(d2); PIN(d3);
      });
      xv[0] += f32x2{blo(d0.x), blo(d2.x)}; xv[1] += f32x2{bhi(d0.x), bhi(d2.x)}; xv[2] += f32x2{blo(d0.y), blo(d2.y)}; xv[3] += f32x2{bhi(d0.y), bhi(d2.y)};
      xv[4] += f32x2{blo(d1.x), blo(d3.x)}; xv[5] += f32x2{bhi(d1.x), bhi(d3.x)}; xv[6] += f32x2{blo(d1.y), blo(d3.y)}; xv[7] += f32x2{bhi(d1.y), bhi(d3.y)};
      ST(S0 + 15);
    }
  };
  layer(std::integral_constant<int, 0>{});
  layer(std::integral_constant<int, 1>{});
  if (blockIdx.x == 0 && threadIdx.x == 0) st2(ep, OFF_E);
  asm volatile(".Lgf_end:");
  ST_OUT();
}

extern "C" void kernel_launch(void* const* d_in, const int* in_sizes, int n_in, void* d_out, int out_size, void* d_ws, size_t ws_size,
                              hipStream_t stream) {
  Params p;
  p.x = (const u16*)d_in[0]; p.wr = (const u16*)d_in[1]; p.wi = (const u16*)d_in[2];
  p.wg = (const u16*)d_in[4]; p.wu = (const u16*)d_in[5]; p.wd = (const u16*)d_in[6];
  p.out = (u16*)d_out; p.ws = (u8*)d_ws;
  (void)hipFuncSetAttribute((const void*)k_all, hipFuncAttributeMaxDynamicSharedMemorySize, LDS_TOTAL);
  k_all<<<256, 256, LDS_TOTAL, stream>>>(p);
}
```

```cpp
#include <hip/hip_runtime.h>
#include <hip/hip_bf16.h>
#include <stdint.h>
#include <type_traits>
#include <utility>

typedef unsigned char u8;
typedef unsigned short u16;
typedef unsigned int u32;
typedef unsigned long long u64;
typedef u32 u32x2 __attribute__((ext_vector_type(2)));
typedef u32 u32x4 __attribute__((ext_vector_type(4)));
typedef float f32x2 __attribute__((ext_vector_type(2)));
typedef float f32x4 __attribute__((ext_vector_type(4)));
typedef _Float16 f16x2 __attribute__((ext_vector_type(2)));
typedef __bf16 bf16x2 __attribute__((ext_vector_type(2)));
typedef __bf16 bf16x8 __attribute__((ext_vector_type(8)));
#define DI __device__ __forceinline__
#define SB() __builtin_amdgcn_sched_barrier(0)
#define MEMFENCE() asm volatile("" ::: "memory")

#define XSLP 4
#define BMAP(x) (x)
#define XE0 164
#define XE1 157
constexpr u32 MB2 = 2u << 20;
constexpr u32 OFF_E = 0, OFF_FLAG = 8192, OFF_WNY = 65536;
constexpr u32 OFF_A = MB2;
constexpr u32 OFF_U = OFF_A + MB2;
constexpr u32 OFF_WX = OFF_U + MB2;
constexpr u32 OFF_X3 = OFF_WX + MB2;
constexpr u32 OFF_H = OFF_X3 + MB2;
constexpr u32 OFF_W1 = OFF_H + 7340032;
constexpr u32 OFF_W2 = OFF_W1 + 3670016;
constexpr u32 WS_USED = OFF_W2 + 1048576;
constexpr u32 SENT = 0xFFFFFFFFu;
constexpr int SPIN_CAP = 1 << 15;

constexpr int LDS_SCR = 131072, LDS_STASH = LDS_SCR + 24576, LDS_TOTAL = 163840;

struct Params {
  const u16* x; const u16* wr; const u16* wi; const u16* wg; const u16* wu; const u16* wd; u16* out; u8* ws;
};

#define ST(i)
#define ST_DECL
#define CNT(i)
#define ST_OUT()

#define XS(i)
#define XF(i)
DI u32 pkh(float a, float b) { f32x2 v = {a, b}; return __builtin_bit_cast(u32, __builtin_convertvector(v, f16x2)); }
DI u32 pkb(float a, float b) { f32x2 v = {a, b}; return __builtin_bit_cast(u32, __builtin_convertvector(v, bf16x2)); }
template <bool BF> DI u32 pkc(float a, float b) { return BF ? pkb(a, b) : pkh(a, b); }
DI u32 pkh_n0(float a, float b, u32 after) { u32 r; asm("v_cvt_pk_f16_f32 %0, -%1, %2" : "=v"(r) : "v"(a), "v"(b), "v"(after)); return r; }
DI u32 pkb_n1(float a, float b, u32 after) { u32 r; asm("v_cvt_pk_bf16_f32 %0, %1, -%2" : "=v"(r) : "v"(a), "v"(b), "v"(after)); return r; }
DI float blo(u32 u) { return __uint_as_float(u << 16); }
DI float bhi(u32 u) { return __uint_as_float(u & 0xffff0000u); }
DI float bfr(float a) { return blo(pkb(a, a)); }
DI float relu(float a) { const int i = __float_as_int(a); return __int_as_float(i > 0 ? i : 0); }
DI u32 umax(u32 a, u32 b) { return a > b ? a : b; }
DI u32 max3(u32 a, u32 b, u32 c) { return umax(umax(a, b), c); }
DI u32 max4(u32 acc, u32x4 v) { return max3(max3(acc, v.x, v.y), v.z, v.w); }
template <bool ONE> DI u32 red3(u32 a, u32 b, u32 c) { return ONE ? (a & b & c) : (a | b | c); }
template <bool ONE> DI u32 red2(u32 a, u32 b) { return ONE ? (a & b) : (a | b); }
template <bool ONE> DI u32 red4(u32 acc, u32x4 v) { return red3<ONE>(red3<ONE>(acc, v.x, v.y), v.z, v.w); }
template <bool ONE> DI u32 red8(u32x4 a, u32x4 b) { return red4<ONE>(red3<ONE>(red2<ONE>(a.x, a.y), a.z, a.w), b); }
template <bool ONE, u32 BIT> DI bool stale(u32 r) { return ONE ? (r & BIT) == 0 : (r & BIT) != 0; }
template <u32 CLR, u32 SET> DI u32 mark(u32 v) { return (v & ~CLR) | SET; }
template <u32 CLR, u32 SET> DI u32x4 mark4(u32x4 v) { return u32x4{mark<CLR, SET>(v.x), mark<CLR, SET>(v.y), mark<CLR, SET>(v.z), mark<CLR, SET>(v.w)}; }
DI f32x2 lo2(f32x4 v) { return __builtin_shufflevector(v, v, 0, 1); }
DI f32x2 hi2(f32x4 v) { return __builtin_shufflevector(v, v, 2, 3); }
DI f32x2 swp(f32x2 v) { return __builtin_shufflevector(v, v, 1, 0); }
DI f32x4 mfma_b(u32x4 a, u32x4 b, f32x4 c) {
  return __builtin_amdgcn_mfma_f32_16x16x32_bf16(__builtin_bit_cast(bf16x8, a), __builtin_bit_cast(bf16x8, b), c, 0, 0, 0);
}
template <class T> DI void PIN(T& x) { asm volatile("" : "+v"(x)); }
template <class T> DI void PINS(T& x) { asm volatile("" : "+s"(x)); }
DI int opq(int x) { asm("" : "+s"(x)); return x; }
template <class T> DI void PINA(T& x) { asm volatile("" : "+a"(x)); }
template <class F, int... I> DI void static_for_(F&& f, std::integer_sequence<int, I...>) { (f(std::integral_constant<int, I>{}), ...); }
template <int N, class F> DI void static_for(F&& f) { static_for_(f, std::make_integer_sequence<int, N>{}); }

#define MM_(T) "v_mfma_f32_16x16x32_" T
template <bool BF> DI void mm4(f32x4& d0, f32x4& d1, f32x4& d2, f32x4& d3, u32x4 a0, u32x4 a1, u32x4 b0, u32x4 b1) {
#define MM4_(T) asm volatile("s_nop 1\n\t" MM_(T) " %0, %4, %6, 0\n\t" MM_(T) " %1, %4, %7, 0\n\t" MM_(T) " %2, %5, %6, 0\n\t" MM_(T) " %3, %5, %7, 0\n\ts_nop 5" \
               : "=&v"(d0), "=&v"(d1), "=&v"(d2), "=&v"(d3) : "v"(a0), "v"(a1), "v"(b0), "v"(b1))
  if constexpr (BF) MM4_("bf16"); else MM4_("f16");
}
template <bool BF, int TAIL, int AG = 0> DI void mm8(f32x4& d0, f32x4& d1, f32x4& d2, f32x4& d3, u32x4 a00, u32x4 b00, u32x4 a01, u32x4 b01, u32x4 a10,
                               u32x4 b10, u32x4 a11, u32x4 b11, u32x4 a20, u32x4 b20, u32x4 a21, u32x4 b21, u32x4 a30, u32x4 b30, u32x4 a31, u32x4 b31) {
#define MM8_(T, A, B) asm volatile("s_nop 1\n\t" MM_(T) " %0, %4, %5, 0\n\t" MM_(T) " %1, %8, %9, 0\n\t" MM_(T) " %0, %6, %7, %0\n\t" MM_(T) " %1, %10, %11, %1\n\t" \
               MM_(T) " %2, %12, %13, 0\n\t" MM_(T) " %3, %16, %17, 0\n\t" MM_(T) " %2, %14, %15, %2\n\t" MM_(T) " %3, %18, %19, %3\n\ts_nop %20"         \
               : "=&v"(d0), "=&v"(d1), "=&v"(d2), "=&v"(d3)                                                                                              \
               : A(a00), B(b00), A(a01), B(b01), A(a10), B(b10), A(a11), B(b11), A(a20), B(b20), A(a21), B(b21), A(a30), B(b30), A(a31), B(b31), "i"(TAIL))
  if constexpr (!BF) MM8_("f16", "v", "v");
  else if constexpr (AG == 0) MM8_("bf16", "v", "v");
  else if constexpr (AG == 1) MM8_("bf16", "a", "v");
  else MM8_("bf16", "v", "a");
}
template <bool BF, int AG> DI void mf0(f32x4& d, u32x4 a, u32x4 b) {
  if constexpr (!BF) asm volatile(MM_("f16") " %0, %1, %2, 0" : "=&v"(d) : "v"(a), "v"(b));
  else if constexpr (AG == 0) asm volatile(MM_("bf16") " %0, %1, %2, 0" : "=&v"(d) : "v"(a), "v"(b));
  else asm volatile(MM_("bf16") " %0, %1, %2, 0" : "=&v"(d) : "v"(a), "a"(b));
}
template <bool FIRST> DI void mmw(f32x4& d, const u32x4& a, const u32x4& b) {
  if constexpr (FIRST) asm volatile(MM_("bf16") " %0, %1, %2, 0" : "=&v"(d) : "a"(a), "v"(b));
  else asm volatile(MM_("bf16") " %0, %1, %2, %0" : "+v"(d) : "a"(a), "v"(b));
}
template <int O> DI void lda(u32x4& d, u32 addr) { asm volatile("ds_read_b128 %0, %1 offset:%2" : "=a"(d) : "v"(addr), "i"(O) : "memory"); }
template <int N> DI void lgwait() { asm volatile("s_waitcnt lgkmcnt(%0)" ::"i"(N) : "memory"); }
template <bool BF, int AG, int TAIL> DI void mf4(f32x4& d0, f32x4& d1, f32x4& d2, f32x4& d3, u32x4 a0, u32x4 b0, u32x4 a1, u32x4 b1, u32x4 a2,
                                               u32x4 b2, u32x4 a3, u32x4 b3) {
#define MF4_(T, B) asm volatile("s_nop 1\n\t" MM_(T) " %0, %4, %5, %0\n\t" MM_(T) " %1, %6, %7, %1\n\t" MM_(T) " %2, %8, %9, %2\n\t" MM_(T) " %3, %10, %11, %3\n\ts_nop %12" \
               : "+v"(d0), "+v"(d1), "+v"(d2), "+v"(d3) : "v"(a0), B(b0), "v"(a1), B(b1), "v"(a2), B(b2), "v"(a3), B(b3), "i"(TAIL))
  if constexpr (!BF) MF4_("f16", "v"); else if constexpr (AG == 0) MF4_("bf16", "v"); else MF4_("bf16", "a");
}
template <bool BF, bool CONJ, class MM> DI u32x4 cplx_under(f32x4 r, f32x4 i, const f32x2 (&c)[2], const f32x2 (&s)[2], MM&& mm) {
  asm volatile("s_nop 1");
  SB(); mm(std::integral_constant<int, 0>{}); SB();
  f32x2 a0 = lo2(i) * s[0], b0 = lo2(r) * s[0], a1 = hi2(i) * s[1];
  PIN(a0); PIN(b0); PIN(a1);
  SB(); mm(std::integral_constant<int, 1>{}); SB();
  f32x2 b1 = hi2(r) * s[1], re0 = CONJ ? lo2(r) * c[0] + a0 : lo2(r) * c[0] - a0, im0 = CONJ ? lo2(i) * c[0] - b0 : lo2(i) * c[0] + b0;
  PIN(b1); PIN(re0); PIN(im0);
  SB(); mm(std::integral_constant<int, 2>{}); SB();
  f32x2 re1 = CONJ ? hi2(r) * c[1] + a1 : hi2(r) * c[1] - a1, im1 = CONJ ? hi2(i) * c[1] - b1 : hi2(i) * c[1] + b1;
  u32 o0 = pkc<BF>(re0.x, im0.x);
  PIN(re1); PIN(im1); PIN(o0);
  SB(); mm(std::integral_constant<int, 3>{}); SB();
  return u32x4{o0, pkc<BF>(re0.y, im0.y), pkc<BF>(re1.x, im1.x), pkc<BF>(re1.y, im1.y)};
}
template <bool BF> DI u32x4 twiddle(f32x4 r, f32x4 i, const f32x2 (&c)[2], const f32x2 (&s)[2]) {
  const f32x2 re0 = lo2(r) * c[0] + lo2(i) * s[0], im0 = lo2(i) * c[0] - lo2(r) * s[0];
  const f32x2 re1 = hi2(r) * c[1] + hi2(i) * s[1], im1 = hi2(i) * c[1] - hi2(r) * s[1];
  return u32x4{pkc<BF>(re0.x, im0.x), pkc<BF>(re0.y, im0.y), pkc<BF>(re1.x, im1.x), pkc<BF>(re1.y, im1.y)};
}
DI u32x4 cmul_b(f32x4 r, f32x4 i, const f32x2 (&c)[2], const f32x2 (&s)[2]) {
  const f32x2 re0 = lo2(r) * c[0] - lo2(i) * s[0], im0 = lo2(i) * c[0] + lo2(r) * s[0];
  const f32x2 re1 = hi2(r) * c[1] - hi2(i) * s[1], im1 = hi2(i) * c[1] + hi2(r) * s[1];
  return u32x4{pkb(re0.x, im0.x), pkb(re0.y, im0.y), pkb(re1.x, im1.x), pkb(re1.y, im1.y)};
}
DI float cosr(float a) { return __builtin_amdgcn_cosf(a); }
DI float sinr(float a) { return __builtin_amdgcn_sinf(a); }
template <int CTRL> DI float dppf(float x) {
  return __builtin_bit_cast(float, __builtin_amdgcn_mov_dpp(__builtin_bit_cast(int, x), CTRL, 0xf, 0xf, true));
}
DI f32x2 bcast2(float x) {
  auto t = __builtin_amdgcn_permlane32_swap(__float_as_uint(x), __float_as_uint(x), false, false);
  return f32x2{__uint_as_float(t[0]), __uint_as_float(t[1])};
}
DI void ln_coef(f32x2 S, f32x2 Q, float ch, f32x2& R, f32x2& O) {
  auto t = __builtin_amdgcn_permlane32_swap(__float_as_uint(S.x), __float_as_uint(S.y), false, false);
  auto u = __builtin_amdgcn_permlane32_swap(__float_as_uint(Q.x), __float_as_uint(Q.y), false, false);
  const float s = __uint_as_float(t[0]) + __uint_as_float(t[1]), q = __uint_as_float(u[0]) + __uint_as_float(u[1]);
  auto z = __builtin_amdgcn_permlane16_swap(__float_as_uint(s), __float_as_uint(q), false, false);
  float x = __uint_as_float(z[0]) + __uint_as_float(z[1]);
  x += dppf<0xB1>(x);
  x += dppf<0x4E>(x);
  x += dppf<0x141>(x);
  x += dppf<0x140>(x);
  auto y = __builtin_amdgcn_permlane16_swap(__float_as_uint(x), __float_as_uint(x), false, false);
  const float md = __uint_as_float(y[0]) * (1.0f / 512.0f);
  const float var = relu(__uint_as_float(y[1]) * (1.0f / 512.0f) - md * md);
  const float r = __builtin_amdgcn_rcpf(bfr(__builtin_amdgcn_sqrtf(bfr(bfr(var) + 1e-5f))));
  const float o = (ch - bfr(ch + md)) * r;
  R = bcast2(r); O = bcast2(o);
}
DI float first_lane(float x) { return __uint_as_float(__builtin_amdgcn_readfirstlane(__float_as_uint(x))); }
DI int row_of_pos(int pos, int j) {
  const int c = 8 * (pos >> 7) + 2 * ((pos >> 3) & 3) + ((pos >> 5) & 1), q = 8 * ((pos >> 6) & 1) + (pos & 7);
  return 32 * (2 * c + (j & 1)) + 2 * q + (j >> 1);
}

template <int I> DI void ld4(u32x4& d, u32 v, const u8* s) { asm volatile("global_load_dwordx4 %0, %1, %2 offset:%3 sc1" : "=v"(d) : "v"(v), "s"(s), "i"(I) : "memory"); }
template <int I> DI void ld4a(u32x4& d, u32 v, const u8* s) { asm volatile("global_load_dwordx4 %0, %1, %2 offset:%3 sc1" : "=a"(d) : "v"(v), "s"(s), "i"(I) : "memory"); }
template <int I> DI void ld4r(u32x4& d, u32 v, const u8* s) { asm volatile("global_load_dwordx4 %0, %1, %2 offset:%3 sc1" : "+v"(d) : "v"(v), "s"(s), "i"(I) : "memory"); }
template <int I> DI void ld2(u32x2& d, u32 v, const u8* s) { asm volatile("global_load_dwordx2 %0, %1, %2 offset:%3 sc1" : "=v"(d) : "v"(v), "s"(s), "i"(I) : "memory"); }
template <int I> DI void ld2r(u32x2& d, u32 v, const u8* s) { asm volatile("global_load_dwordx2 %0, %1, %2 offset:%3 sc1" : "+v"(d) : "v"(v), "s"(s), "i"(I) : "memory"); }
template <int I> DI void ld2p(u32x2& d, u32 v, const u8* s) { asm volatile("global_load_dwordx2 %0, %1, %2 offset:%3" : "=v"(d) : "v"(v), "s"(s), "i"(I) : "memory"); }
DI void touch(u32& d, u32 v, const u8* s) { asm volatile("global_load_dword %0, %1, %2 sc1" : "+v"(d) : "v"(v), "s"(s) : "memory"); }
template <int I> DI void ld1p(u32& d, u32 v, const u8* s) { asm volatile("global_load_dword %0, %1, %2 offset:%3" : "=v"(d) : "v"(v), "s"(s), "i"(I) : "memory"); }
DI void ld1s(u32& d, u32 v, const u8* s) { asm volatile("global_load_ushort %0, %1, %2" : "=v"(d) : "v"(v), "s"(s) : "memory"); }
template <int N> DI void vmwait() { static_assert(N >= 0 && N < 64, ""); asm volatile("s_waitcnt vmcnt(%0)" ::"i"(N) : "memory"); }
DI void set_m0(u32 ldsaddr) { asm volatile("s_mov_b32 m0, %0" ::"s"(ldsaddr) : "memory"); }
template <int I> DI void dma1(u32 voff, const u8* src) {
  asm volatile("global_load_lds_dwordx4 %0, %1 offset:%2 sc1" ::"v"(voff), "s"(src), "i"(I) : "memory");
}
DI void dma4(u32 voff, const u8* src, u32 ldsaddr) {
  asm volatile("s_mov_b32 m0, %2\n\ts_nop 0\n\tglobal_load_lds_dwordx4 %0, %1 sc1\n\tglobal_load_lds_dwordx4 %0, %1 offset:1024 sc1\n\t"
               "global_load_lds_dwordx4 %0, %1 offset:2048 sc1\n\tglobal_load_lds_dwordx4 %0, %1 offset:3072 sc1" ::"v"(voff), "s"(src), "s"(ldsaddr) : "memory");
}
#define RETRY(COND, ...)                                                      \
  do {                                                                        \
    bool bad_ = (COND);                                                       \
    if (__builtin_expect(__any(bad_), 0)) {                                   \
      _Pragma("nounroll") for (int sp_ = 0; sp_ < SPIN_CAP; ++sp_) {          \
        __VA_ARGS__                                                           \
        bad_ = (COND);                                                        \
        if (!__any(bad_)) break;                                              \
      }                                                                       \
    }                                                                         \
  } while (0)
DI void bar() { asm volatile("s_waitcnt lgkmcnt(0)\n\ts_barrier" ::: "memory"); }
DI void bar_nw() { asm volatile("s_barrier" ::: "memory"); }

struct RowC { u32x4 b1re, b1im, b2re[2][2], b2im[2][2]; f32x2 twc[2][2], tws[2][2]; };
template <bool INV> DI void pair_cs(float c, float s, u32& re, u32& im) {
  if constexpr (INV) { im = pkb(s, c); re = pkb_n1(c, s, im); } else { re = pkh(c, s); im = pkh_n0(s, c, re); }
}
template <bool AG = false> DI void sign4(u32x4 base, u32 lm, u32x4& f00, u32x4& f01, u32x4& f10, u32x4& f11) {
  f00 = base;
  f01 = base ^ lm;
  f10 = base ^ u32x4{0u, 0x80008000u, 0u, 0x80008000u};
  f11 = f10 ^ lm;
  if constexpr (AG) { PINA(f00); PINA(f01); PINA(f10); PINA(f11); } else { PIN(f00); PIN(f01); PIN(f10); PIN(f11); }
}
template <bool INV, int PART> DI void row_consts(RowC& k, int m, int g) {
  const float fm = (float)m, fg = (float)(4 * g);
  if constexpr (PART == 0) {
    u32x4 br, bi;
#pragma unroll
    for (int i = 0; i < 4; ++i) {
      const float a = (fg + (float)i) * (fm * (1.0f / 16.0f));
      u32 x, y;
      pair_cs<INV>(cosr(a), sinr(a), x, y); k.b1re[i] = x; k.b1im[i] = y;
      const float a2 = (fg + (float)i) * (fm * (1.0f / 32.0f));
      pair_cs<INV>(cosr(a2), sinr(a2), x, y); br[i] = x; bi[i] = y;
    }
    PIN(k.b1re); PIN(k.b1im);
    const u32 lm = (m & 1) ? 0x80008000u : 0u;
    sign4(br, lm, k.b2re[0][0], k.b2re[0][1], k.b2re[1][0], k.b2re[1][1]);
    sign4(bi, lm, k.b2im[0][0], k.b2im[0][1], k.b2im[1][0], k.b2im[1][1]);
  } else {
#pragma unroll
    for (int e = 0; e < 2; ++e)
#pragma unroll
      for (int i = 0; i < 4; ++i) {
        const float a = (fg + (float)(16 * e + i)) * (fm * (INV ? -1.0f / 512.0f : 1.0f / 512.0f));
        k.twc[e][i >> 1][i & 1] = cosr(a); k.tws[e][i >> 1][i & 1] = sinr(a);
      }
#pragma unroll
    for (int e = 0; e < 2; ++e) { PIN(k.twc[e][0]); PIN(k.twc[e][1]); PIN(k.tws[e][0]); PIN(k.tws[e][1]); }
  }
}
template <bool INV, int TAIL, class H> DI void row_fft(const RowC& k, u32x4 a0, u32x4 a1, f32x4& dr0, f32x4& di0, f32x4& dr1, f32x4& di1, H&& mid) {
  f32x4 tr0, ti0, tr1, ti1;
  mm4<INV>(tr0, ti0, tr1, ti1, a0, a1, k.b1re, k.b1im);
  u32x4 p0 = twiddle<INV>(tr0, ti0, k.twc[0], k.tws[0]);
  PIN(p0);
  mid();
  const u32x4 p1 = cplx_under<INV, true>(tr1, ti1, k.twc[1], k.tws[1], [&](auto I) {
    constexpr int i = decltype(I)::value;
    mf0<INV, 0>(i == 0 ? dr0 : i == 1 ? di0 : i == 2 ? dr1 : di1, p0, i == 0 ? k.b2re[0][0] : i == 1 ? k.b2im[0][0] : i == 2 ? k.b2re[1][0] : k.b2im[1][0]);
  });
  mf4<INV, 0, TAIL>(dr0, di0, dr1, di1, p1, k.b2re[0][1], p1, k.b2im[0][1], p1, k.b2re[1][1], p1, k.b2im[1][1]);
}

struct FwdP { u32 iw, ir, ow, ap0, ap1, ar0, ar1, zo; f32x2 cB, cD, eA, eB, eC, eD; };
DI void fwd_pre(FwdP& p, int tid, int w, int pos) {
  const int m = tid & 15, g = (tid >> 4) & 3, ph = tid >> 1, fq = ph ? 512 - ph : 256, fq1 = 384 - ph;
  p.iw = w * 5120 + ((16 * (m & 1) + 4 * g) * 20 + (m >> 1)) * 4; p.ir = w * 5120 + (m * 20 + 4 * g) * 4;
  p.ap0 = (ph + (ph >> 4) * 4) * 4; p.ap1 = p.ap0 + 640; p.ar0 = (fq + (fq >> 4) * 4) * 4; p.ar1 = (fq1 + (fq1 >> 4) * 4) * 4;
  p.zo = OFF_A + pos * 8192 + tid * 16;
  const float sc = 1.0f / 64.0f, sg = (tid & 1) ? -sc : sc;
  p.cB = f32x2{sg, -sg}; p.cD = f32x2{sg, sg};
  p.eA = ph ? f32x2{sc, sc} : f32x2{2 * sc, 0.f}; p.eB = ph ? p.cB : f32x2{2 * sg, 0.f};
  p.eC = ph ? f32x2{sc, -sc} : f32x2{0.f, 2 * sg}; p.eD = ph ? p.cD : f32x2{0.f, 2 * sc};
  PIN(p.iw); PIN(p.ir); PIN(p.ap0); PIN(p.ap1); PIN(p.ar0); PIN(p.ar1); PIN(p.zo);
  PIN(p.cB); PIN(p.cD); PIN(p.eA); PIN(p.eB); PIN(p.eC); PIN(p.eD);
}
struct ColC {
  u32x4 b1re[2], b1im[2], a2re[2][2], a2im[2][2], b3re[2][2], b3im[2][2], a4re[2][2], a4im[2][2];
  f32x2 t1c[2][2], t1s[2][2], t2c[2][2], t2s[2][2];
};
template <int PART> DI void col_consts(ColC& k, int m, int g, int w) {
  const int pi_ = w >> 1, kt = w & 1;
  const float fm = (float)m, fg = (float)(4 * g);
  const u32 lm = (m & 1) ? 0x80008000u : 0u;
  if constexpr (PART == 0) {
    const float fk = (float)(2 * (16 * kt + m) + pi_);
#pragma unroll
    for (int u = 0; u < 2; ++u) {
#pragma unroll
      for (int r = 0; r < 4; ++r) {
        const float n = fg + (float)(16 * u + r), a = n * (fk * (1.0f / 64.0f)), a2 = n * (fk * (1.0f / 2048.0f));
        u32 x, y;
        pair_cs<false>(cosr(a), sinr(a), x, y); k.b1re[u][r] = x; k.b1im[u][r] = y;
        k.t1c[u][r >> 1][r & 1] = cosr(a2); k.t1s[u][r >> 1][r & 1] = sinr(a2);
      }
      PIN(k.b1re[u]); PIN(k.b1im[u]); PIN(k.t1c[u][0]); PIN(k.t1c[u][1]); PIN(k.t1s[u][0]); PIN(k.t1s[u][1]);
    }
  } else if constexpr (PART == 1) {
    u32x4 fr, fi, ir, ii;
#pragma unroll
    for (int r = 0; r < 4; ++r) {
      const float a = (fg + (float)r) * (fm * (1.0f / 32.0f)), c = cosr(a), s = sinr(a);
      u32 x, y;
      pair_cs<false>(c, s, x, y); fr[r] = x; fi[r] = y;
      pair_cs<true>(c, s, x, y); ir[r] = x; ii[r] = y;
    }
    sign4(fr, lm, k.a2re[0][0], k.a2re[0][1], k.a2re[1][0], k.a2re[1][1]);
    sign4(fi, lm, k.a2im[0][0], k.a2im[0][1], k.a2im[1][0], k.a2im[1][1]);
    sign4(ir, lm, k.b3re[0][0], k.b3re[0][1], k.b3re[1][0], k.b3re[1][1]);
    sign4(ii, lm, k.b3im[0][0], k.b3im[0][1], k.b3im[1][0], k.b3im[1][1]);
  } else if constexpr (PART == 2) {
    const float kb = (float)(32 * kt + pi_) + 2.0f * fg;
#pragma unroll
    for (int u = 0; u < 2; ++u) {
#pragma unroll
      for (int r = 0; r < 4; ++r) {
        const float a = (kb + (float)(2 * r)) * ((fm + (float)(16 * u)) * (-1.0f / 2048.0f));
        k.t2c[u][r >> 1][r & 1] = cosr(a); k.t2s[u][r >> 1][r & 1] = sinr(a);
      }
      PIN(k.t2c[u][0]); PIN(k.t2c[u][1]); PIN(k.t2s[u][0]); PIN(k.t2s[u][1]);
    }
  } else {
    const float fn = (fm + (float)(16 * (w >> 1))) * (1.0f / 64.0f);
#pragma unroll
    for (int u = 0; u < 2; ++u) {
#pragma unroll
      for (int r = 0; r < 4; ++r) {
        const float a = (2.0f * fg + (float)(2 * r + u)) * fn;
        u32 x, y;
        pair_cs<true>(cosr(a), sinr(a), x, y); k.a4re[u][0][r] = x; k.a4im[u][0][r] = y;
      }
      k.a4re[u][1] = k.a4re[u][0] ^ lm; k.a4im[u][1] = k.a4im[u][0] ^ lm;
      PIN(k.a4re[u][0]); PIN(k.a4re[u][1]); PIN(k.a4im[u][0]); PIN(k.a4im[u][1]);
    }
  }
}

DI void transpose8x4(const u32x2 (&v)[8], u32x4 (&o)[4]) {
#pragma unroll
  for (int jj = 0; jj < 4; ++jj) {
    o[0][jj] = __builtin_amdgcn_perm(v[2 * jj + 1].x, v[2 * jj].x, 0x05040100u);
    o[1][jj] = __builtin_amdgcn_perm(v[2 * jj + 1].x, v[2 * jj].x, 0x07060302u);
    o[2][jj] = __builtin_amdgcn_perm(v[2 * jj + 1].y, v[2 * jj].y, 0x05040100u);
    o[3][jj] = __builtin_amdgcn_perm(v[2 * jj + 1].y, v[2 * jj].y, 0x07060302u);
  }
}
template <int H> DI void prep_load1(const Params& P, int wt, int lane, u32x2 (&v)[8]) {
  const u32 vo = (lane < 56 ? lane : 55) * 8, vo2 = vo + 7168;
  const int piece = (wt >> 3) & 63;
  const u8* src = (const u8*)((wt >> 9) ? P.wu : P.wg) + opq(wt & 7) * 448 + opq(4 * piece) * 3584;
  static_for<4>([&](auto J) { constexpr int j = decltype(J)::value; ld2p<(j & 1) * 3584>(v[4 * H + j], j & 2 ? vo2 : vo, src + H * (256 * 3584)); });
}
DI u32 prep_dst1(int wt, int tile) {
  const int piece = (wt >> 3) & 63, mat = wt >> 9, hi = tile >= 7, p = tile - 7 * hi, ks = piece >> 2;
  const u32 frag = (p < 3 ? ks * 6 + 2 * p : p < 5 ? 96 + ks * 4 + 2 * (p - 3) : 160 + ks * 4 + 2 * (p - 5)) + mat;
  return OFF_W1 + ((2 * (wt & 7) + hi) * 224 + frag) * 1024 + (piece & 3) * 256;
}
DI int w2_row(int wv, int slot, int rot, int h) {
  const int f0 = slot < 4 ? 112 * (4 * wv + ((slot + rot) & 3)) : slot < 6 ? 112 * (4 * wv + 2 * ((slot ^ rot) & 1)) + 32
               : slot < 10 ? 112 * (4 * wv + ((slot - 6 + rot) & 3)) + 48 : 112 * (4 * wv + ((slot - 10 + rot) & 3)) + 80;
  return f0 + h * ((slot == 4 || slot == 5) ? 112 : 16);
}
DI void dma_rows(u32 voff, const u8* h0, const u8* h1, u32 ldsaddr) {
  const u8* h0b = h0 + 8192 - 1024;
  const u8* h1a = h1 - 2048;
  const u8* h1b = h1 + 8192 - 3072;
  asm volatile("s_mov_b32 m0, %5\n\ts_nop 0\n\tglobal_load_lds_dwordx4 %0, %1\n\tglobal_load_lds_dwordx4 %0, %2 offset:1024\n\t"
               "global_load_lds_dwordx4 %0, %3 offset:2048\n\tglobal_load_lds_dwordx4 %0, %4 offset:3072"
               ::"v"(voff), "s"(h0), "s"(h0b), "s"(h1a), "s"(h1b), "s"(ldsaddr) : "memory");
}
template <int O> DI void tr_read(u32x2& lo, u32x2& hi, u32 addr) {
  asm volatile("ds_read_b64_tr_b16 %0, %2 offset:%3\n\tds_read_b64_tr_b16 %1, %2 offset:%4" : "=&v"(lo), "=&v"(hi) : "v"(addr), "i"(O), "i"(O + 2048) : "memory");
}
DI u32 tr_addr(int lane, int c) { return (lane >> 2) * 128 + (((lane & 3) + 4 * (c >> 1)) ^ (2 * ((lane >> 2) & 1) + 4 * ((lane >> 3) & 1))) * 16 + (c & 1) * 8; }

constexpr int NTOK0 = 12, NRES = 32;
constexpr int g1_xe(int L) { return L == 0 ? XE0 : XE1; }
constexpr int g1_ne(int L) { return (224 - g1_xe(L)) / 7 * 3 + ((224 - g1_xe(L)) % 7 > 0) + ((224 - g1_xe(L)) % 7 > 2) + ((224 - g1_xe(L)) % 7 > 5); }
constexpr int g1_job(int L, int x) {
  if (((x >= 34 && x < 96) && (x & 3) == 2) || (x >= 98 && x < 114 && !(x & 1))) return 2;
  if ((x < 34 && !(x & 1)) || (x < 64 && (x & 3) == 0)) return 1;
  if (L == 1 && x >= 47 && x < 60 && (x & 3) == 3) return 6;
  if (x >= g1_xe(L)) return ((x - g1_xe(L)) % 7 == 0 || (x - g1_xe(L)) % 7 == 2 || (x - g1_xe(L)) % 7 == 5) ? 4 : 0;
  if (L == 0 && ((x >= 48 && x < 98 && (x & 3) == 0) || (x >= 115 && x % 3 == 0) || (x < 34 && x % 8 == 1))) return 3;
  return 0;
}
constexpr int NEPI1 = 75, NEPI2 = 50, EPI1_X = 99;
struct G1Sched {
  int job[224], idx[224], epi[225], nst[224], cum[225], pos[7][64], cnt[7];
  constexpr G1Sched(int L) : job{}, idx{}, epi{}, nst{}, cum{}, pos{}, cnt{} {
    cnt[1] = NTOK0;
    for (int i = 0; i < NTOK0; ++i) pos[1][i] = i;
    cum[0] = NTOK0;
    for (int x = 0; x < 224; ++x) {
      int j = g1_job(L, x);
      if (j == 1 && cnt[1] >= 32) j = L == 0 ? 3 : 0;
      if ((j == 3 && cnt[3] >= NRES)) j = 0;
      job[x] = j; idx[x] = cnt[j];
      if (x == 162) epi[x] = NEPI1;
      const bool on = (x >= EPI1_X && x < 160 && epi[x] < NEPI1) || x >= 162;
      epi[x + 1] = on ? epi[x] + (x < 114 ? 2 - (x & 1 ? 0 : 1) : 2) : epi[x];
      if (x < 160 && epi[x + 1] > NEPI1) epi[x + 1] = NEPI1;
      for (int n = epi[x]; n < epi[x + 1]; ++n)
        nst[x] += n < NEPI1 ? (n == 24 || n == 49 || n == 74) : (n < NEPI1 + NEPI2 && (n - NEPI1) % 25 == 24);
      if (j) pos[j][cnt[j]++] = cum[x] + nst[x];
      cum[x + 1] = cum[x] + nst[x] + (j ? 1 : 0);
    }
  }
};
constexpr G1Sched G1S[2] = {G1Sched(0), G1Sched(1)};
static_assert(G1S[0].cnt[1] == 32 && G1S[0].cnt[2] == 24 && G1S[0].cnt[3] == NRES && G1S[0].cnt[4] == g1_ne(0) && G1S[1].cnt[4] == g1_ne(1), "");
static_assert(G1S[1].cnt[6] == 4 && G1S[1].pos[6][3] < G1S[1].pos[2][7], "");
static_assert(G1S[0].idx[66] == 8 && G1S[0].idx[98] == 16 && G1S[0].epi[113] <= 24 && G1S[1].epi[113] <= 24, "");
static_assert(G1S[0].epi[152] == NEPI1 && G1S[0].epi[204] >= NEPI1 + NEPI2, "");
constexpr int clamp63(int n) { return n > 63 ? 63 : n; }
constexpr int g1_wait(int L, int x, int job, int idx) { return clamp63(G1S[L].cum[x] - G1S[L].pos[job][idx] - 1); }

#define FRESH()                                                                                                   \
  int tid = threadIdx.x, b = BMAP(blockIdx.x), w = w_;                                                                   \
  u8* ws = P.ws;                                                                                                   \
  asm volatile("" : "+v"(tid), "+s"(b), "+s"(w), "+s"(ws));                                                        \
  const int lane = tid & 63, m = lane & 15, g = lane >> 4, half2 = b & 1, ct = b >> 5;                             \
  const int G = ((b >> 1) & 3) * 4 + ((b >> 3) & 3), member = half2 * 8 + ct, pos = G * 16 + member;               \
  const u32 l16 = lane * 16, t16 = tid * 16, xo = 32 * m + 8 * g;                                                  \
  auto fill_h = [&](u32 base, int q) { st4(sent4, base + (4 * G + w) * 114688 + member * 7168 + q * 1024 + l16); };          \
  auto fill_rows = [&](u32 base) { st4(sent4, base + pos * 8192 + t16); st4(sent4, base + pos * 8192 + 4096 + t16); }; \
  auto ring4 = [&](int q) {                                   \
    const u32 o = (q >> 1) * 32768 + w * 8192 + (q & 1) * 4096;                                                    \
    dma4(l16, ws + OFF_W1 + member * 229376 + o, ldsbase + o);                                                     \
  };                                                                                                               \
      \
  auto w2dma = [&](int slot, int unit) {                                                                           \
    const u8* wd = (const u8*)P.wd + ct * 128;                                                                     \
    dma_rows((lane >> 3) * 1024 + ((lane & 7) ^ (2 * ((lane >> 3) & 1) + 4 * ((lane >> 4) & 1))) * 16,              \
             wd + opq(w2_row(w, slot, ct, 0)) * 1024, wd + opq(w2_row(w, slot, ct, 1)) * 1024, ldsbase + w * 32768 + unit * 4096); \
  };                                                                                                               \
  (void)w2dma;                                                                                                     \
  (void)m; (void)g; (void)half2; (void)ct; (void)l16; (void)t16; (void)xo; (void)fill_h; (void)fill_rows; (void)ring4

__global__ __launch_bounds__(256, 1) void k_all(Params P) {
  extern __shared__ __attribute__((aligned(16))) u8 lds[];
  ST_DECL;
  const int w_ = __builtin_amdgcn_readfirstlane(threadIdx.x >> 6);
  const __amdgpu_buffer_rsrc_t rs = __builtin_amdgcn_make_buffer_rsrc(P.ws, 0, WS_USED, 0x00020000);
  const u32 ldsbase = (u32)(size_t)(__attribute__((address_space(3))) u8*)lds;
  const u32x4 sent4 = {SENT, SENT, SENT, SENT};
  auto st4 = [&](u32x4 v, u32 off) { __builtin_amdgcn_raw_buffer_store_b128(v, rs, off, 0, 16); };
  auto st2 = [&](u32x2 v, u32 off) { __builtin_amdgcn_raw_buffer_store_b64(v, rs, off, 0, 16); };
  u32x2 xl[2], xh[2], pv0[8];
  u32 wfr[8], wfi[8], wn[8];
  f32x2 xv[8], xc;
  float xch;
  FwdP fp;
  u32 pd[4];
  u32x2 ep;
  u32 cpf = 0, tw[4];
  u32x4 w2a[14][4];
  RowC rc;
  ColC cc;
  ST(0);
  {
    FRESH();
    const u8* xp = (const u8*)P.x + row_of_pos(pos, w) * 1024;
    ld2p<0>(xl[0], xo, xp); ld2p<512>(xl[1], xo, xp); ld2p<0>(xh[0], xo, xp + 1024 * 1024); ld2p<512>(xh[1], xo, xp + 1024 * 1024);
    { const u32 z = 0; ld2<0>(ep, z, ws); }
    {
      const u8* pc;
      u32 len;
      asm volatile("s_getpc_b64 %0\n.Lgf_begin:\n\ts_mov_b32 %1, .Lgf_end-.Lgf_begin" : "=s"(pc), "=s"(len));
      const u32 o = (((b >> 3) * 4 + w) * 8 + (lane & 7)) * 128;
      touch(cpf, o < len ? o : 0, pc);
    }
    {
      const u32 z = 0;
      ld1p<0>(tw[0], z, (const u8*)P.wr); ld1p<0>(tw[1], z, (const u8*)P.wi);
      const int wt = b * 4 + w, r8 = lane >> 3;
      ld1p<0>(tw[2], z, (const u8*)((wt >> 9) ? P.wu : P.wg)); (void)r8;
    }
    XS(32);
    fill_rows(OFF_A); fill_rows(OFF_WX);
    if (tid < 8) st4(sent4, OFF_WNY + tid * 4096 + pos * 16);
    SB();
    row_consts<false, 0>(rc, m, g);
    row_consts<false, 1>(rc, m, g);
    SB();
    XS(33);
    const int kb = 2 * (16 * ((pos >> 6) & 1) + (pos & 15)) + (pos >> 7) + 256 * ((pos >> 4) & 3);
    static_for<8>([&](auto J) {
      constexpr int j = decltype(J)::value;
      const u32 o = tid * 2 + (kb + 64 * (16 * (j >> 2) + (j & 3))) * 514;
      ld1s(wfr[j], o, (const u8*)P.wr); ld1s(wfi[j], o, (const u8*)P.wi);
    });
    {
      const int k = kb + 64 * (16 * ((lane >> 2) & 1) + (lane & 3)), km = (2048 - k) & 2047;
      const u32 o = k * 514, om = km * 514;
      ld1s(wn[0], o, (const u8*)P.wr); ld1s(wn[1], o, (const u8*)P.wi); ld1s(wn[2], om, (const u8*)P.wr); ld1s(wn[3], om, (const u8*)P.wi);
      ld1s(wn[4], o, (const u8*)P.wr + 512); ld1s(wn[5], o, (const u8*)P.wi + 512); ld1s(wn[6], om, (const u8*)P.wr + 512); ld1s(wn[7], om, (const u8*)P.wi + 512);
    }
    ld1p<0>(tw[3], (lane < 14 ? lane : 0) * 128, (const u8*)P.wd + opq(b * 4 + w) * 1792);
    XS(34);
    fwd_pre(fp, tid, w, pos);
#pragma unroll
    for (int x = 0; x < 4; ++x) pd[x] = x < 3 || g < 2 ? prep_dst1(b * 4 + w, 4 * x + g) + m * 16 : WS_USED;
#pragma unroll
    for (int x = 0; x < 4; ++x) PIN(pd[x]);
    SB();
    XS(35);
    vmwait<1 + 4 + 4 + 24>();
    XS(36);
    PIN(ep); PIN(xl[0]); PIN(xl[1]); PIN(xh[0]); PIN(xh[1]);
    { const u64 e = (((u64)(u32)__builtin_amdgcn_readfirstlane(ep.y) << 32) | (u32)__builtin_amdgcn_readfirstlane(ep.x)) + 1; ep = u32x2{(u32)e, (u32)(e >> 32)}; }
#pragma unroll
    for (int t = 0; t < 2; ++t)
#pragma unroll
      for (int p = 0; p < 2; ++p) {
        xv[4 * t + 2 * p] = f32x2{blo(xl[t][p]), blo(xh[t][p])};
        xv[4 * t + 2 * p + 1] = f32x2{bhi(xl[t][p]), bhi(xh[t][p])};
      }
    xc = f32x2{first_lane(xv[0].x), first_lane(xv[0].y)};
    xch = lane < 32 ? xc.x : xc.y;
#pragma unroll
    for (int i = 0; i < 8; ++i) xv[i] -= xc;
  }
  ST(1);

  auto layer = [&](auto LC) {
    constexpr int L = decltype(LC)::value;
    constexpr int S0 = 2 + 16 * L;
    {
      FRESH();
      const f32x2 S = ((xv[0] + xv[1]) + (xv[2] + xv[3])) + ((xv[4] + xv[5]) + (xv[6] + xv[7]));
      f32x2 q0 = xv[0] * xv[0], q1 = xv[1] * xv[1];
#pragma unroll
      for (int i = 2; i < 8; i += 2) { q0 += xv[i] * xv[i]; q1 += xv[i + 1] * xv[i + 1]; }
      if constexpr (L == 0) { SB(); fill_rows(OFF_U); SB(); }
      f32x2 R, O;
      XF(32);
      ln_coef(S, q0 + q1, xch, R, O);
      XF(33);
      u8* img = lds + LDS_SCR;
      if constexpr (L == 0) {
        SB(); vmwait<27>(); PIN(cpf); PIN(tw[0]); PIN(tw[1]); PIN(tw[2]);
        if (lane == 0) st2(ep, OFF_FLAG + (w * 256 + pos) * 8);
        prep_load1<0>(P, b * 4 + w, lane, pv0); SB();
      }
#pragma unroll
      for (int i = 0; i < 8; ++i) {
        const f32x2 v = xv[i] * R + O;
        *(u32*)(img + fp.iw + ((i & 3) * 20 + 8 * (i >> 2)) * 4) = pkh(v.x, v.y);
      }
      const u32x4 a0 = *(const u32x4*)(img + fp.ir), a1 = *(const u32x4*)(img + fp.ir + 1280);
      if constexpr (L == 0) prep_load1<1>(P, b * 4 + w, lane, pv0); else ring4(1);
      const float* p0 = (const float*)(img + fp.ap0);
      const float* p1 = (const float*)(img + fp.ap1);
      const float* r0 = (const float*)(img + fp.ar0);
      const float* r1 = (const float*)(img + fp.ar1);
      const f32x2 cA = {1.0f / 64.0f, 1.0f / 64.0f}, cC = {1.0f / 64.0f, -1.0f / 64.0f}, cB = fp.cB, cD = fp.cD, eA = fp.eA, eB = fp.eB, eC = fp.eC, eD = fp.eD;
      const u32 zo = fp.zo;
      f32x4 dr0, di0, dr1, di1;
      XF(34);
      row_fft<false, 6>(rc, a0, a1, dr0, di0, dr1, di1, [] {});
      XF(35);
      SB();
      float* op = (float*)(img + fp.ir);
      *(f32x4*)(op) = dr0; *(f32x4*)(op + 640) = di0;
      SB();
      *(f32x4*)(op + 320) = dr1; *(f32x4*)(op + 960) = di1;
      if constexpr (L == 0) { w2dma(6 + half2 * 4 + (G >> 2), 6); vmwait<13>(); PIN(tw[3]); }
      bar();
      XF(36);
      f32x2 pa[4], qa[4], pb[4], qb[4];
#pragma unroll
      for (int j = 0; j < 4; ++j) {
        pa[j] = f32x2{p0[j * 1280], p0[j * 1280 + 640]}; qa[j] = f32x2{r0[j * 1280], r0[j * 1280 + 640]};
      }
#pragma unroll
      for (int j = 0; j < 4; ++j) {
        pb[j] = f32x2{p1[j * 1280], p1[j * 1280 + 640]}; qb[j] = f32x2{r1[j * 1280], r1[j * 1280 + 640]};
      }
      if constexpr (L == 0) w2dma(0, 0);
      SB();
      u32x4 o;
#pragma unroll
      for (int j = 0; j < 4; ++j) {
        const f32x2 v = eD * swp(qa[j]) + (eC * qa[j] + (eB * swp(pa[j]) + eA * pa[j]));
        o[j] = mark<(L == 0 ? 0x10001u : 0u), (L == 0 ? 0u : 0x10000u)>(pkh(v.x, v.y));
      }
      st4(o, zo);
#pragma unroll
      for (int j = 0; j < 4; ++j) {
        const f32x2 v = cD * swp(qb[j]) + (cC * qb[j] + (cB * swp(pb[j]) + cA * pb[j]));
        o[j] = mark<(L == 0 ? 0x10001u : 0u), (L == 0 ? 0u : 0x10000u)>(pkh(v.x, v.y));
      }
      st4(o, zo + 4096);
      SB();
      if constexpr (L == 1) {
#pragma unroll
        for (int t = 0; t < 2; ++t)
#pragma unroll
          for (int p = 0; p < 2; ++p) {
            const f32x2 v0 = xv[4 * t + 2 * p] + xc, v1 = xv[4 * t + 2 * p + 1] + xc;
            xl[t][p] = pkb(v0.x, v1.x); xh[t][p] = pkb(v0.y, v1.y);
          }
        PIN(xl[0]); PIN(xl[1]); PIN(xh[0]); PIN(xh[1]);
      }
    }
    if constexpr (L == 0) {
      XS(37);
      FRESH();
#pragma unroll
      for (int j = 0; j < 8; ++j) { PIN(wfr[j]); PIN(wfi[j]); PIN(wn[j]); }
      u8* bw = lds + w * 32768 + 28672;
      u32x4 v0, v1;
#pragma unroll
      for (int j = 0; j < 4; ++j) { v0[j] = wfr[j] | (wfi[j] << 16); v1[j] = wfr[4 + j] | (wfi[4 + j] << 16); }
      *(u32x4*)(bw + lane * 32) = v0; *(u32x4*)(bw + lane * 32 + 16) = v1;
      st4(*(const u32x4*)(bw + l16), OFF_WX + pos * 8192 + w * 2048 + l16);
      st4(*(const u32x4*)(bw + 1024 + l16), OFF_WX + pos * 8192 + w * 2048 + 1024 + l16);
      const float hr = blo(wn[0]) + blo(wn[2]), hi = blo(wn[1]) - blo(wn[3]), nr = blo(wn[4]) + blo(wn[6]), ni = blo(wn[5]) - blo(wn[7]);
      const f32x4 ab = {0.25f * (hr + nr), 0.25f * (hi + ni), 0.25f * (hr - nr), 0.25f * (hi - ni)};
      if (tid < 8) st4(__builtin_bit_cast(u32x4, ab), OFF_WNY + tid * 4096 + pos * 16);
    }
    ST(S0);
    {
      FRESH();
      const int phi = pos, pi_ = w >> 1, kt = w & 1;
      const int k1l = 2 * (16 * kt + m) + pi_;
      auto consts = [&](auto PC) { col_consts<decltype(PC)::value>(cc, m, g, w); SB(); };
      using I0 = std::integral_constant<int, 0>; using I1 = std::integral_constant<int, 1>;
      using I2 = std::integral_constant<int, 2>; using I3 = std::integral_constant<int, 3>;
      u8* const bw = lds + w * 32768 + 28672;
      const u32 zo = (tid >> 1) * 8192 + (tid & 1) * 16;
      const u8* zb0 = ws + OFF_A + phi * 32;
      const u8* zb1 = zb0 + 128 * 8192;
      const u8* wb0 = ws + OFF_WX + phi * 32;
      const u8* wb1 = wb0 + 128 * 8192;
      const u32 wo = zo;
      PINS(zb0); PINS(zb1); PINS(wb0); PINS(wb1);
      if constexpr (L == 0) {
        u32x4 fl0, fl1;
        const u32 o = lane * 32;
        const u8* fb = ws + OFF_FLAG + ((phi >> 5) & 3) * 2048;
        w2dma(1, 1);
        ld4<0>(fl0, o, fb); ld4<16>(fl1, o, fb);
        SB();
        consts(I0{});
        consts(I1{});
        XS(38);
        vmwait<0>(); PIN(fl0); PIN(fl1);
        RETRY(!(fl0.x == ep.x && fl0.y == ep.y && fl0.z == ep.x && fl0.w == ep.y && fl1.x == ep.x && fl1.y == ep.y && fl1.z == ep.x && fl1.w == ep.y), {
          CNT(0);
          ld4r<0>(fl0, o, fb); ld4r<16>(fl1, o, fb);
          vmwait<0>(); PIN(fl0); PIN(fl1);
        });
      } else {
        SB();
        consts(I0{});
        consts(I2{});
      }
      u32x4 z0, z1, wv[2];
      if constexpr (L == 0) XS(39);
      ld4<0>(z0, zo, zb0); ld4<0>(z1, zo, zb1);
      ld4<0>(wv[0], wo, wb0); ld4<0>(wv[1], wo, wb1);
      SB();
      u32x4 wa[8], po[5];
      if constexpr (L == 0) {
        XS(40);
        u32x4 ot[4];
#pragma unroll
        for (int j = 0; j < 8; ++j) PIN(pv0[j]);
        transpose8x4(pv0, ot);
#pragma unroll
        for (int x = 0; x < 4; ++x) *(u32x4*)(bw + lane * 64 + ((x + lane) & 3) * 16) = ot[x];
#pragma unroll
        for (int x = 0; x < 4; ++x) {
          const int src = x * 16 + (lane >> 2);
          po[x] = *(const u32x4*)(bw + src * 64 + (((lane & 3) + src) & 3) * 16);
        }
        {
          u32x2 lo, hi;
          tr_read<6 * 4096>(lo, hi, ldsbase + w * 32768 + tr_addr(lane, G & 3));
          lgwait<0>();
          PIN(lo); PIN(hi);
          po[4] = u32x4{lo.x, lo.y, hi.x, hi.y};
        }
#pragma unroll
        for (int x = 0; x < 5; ++x) PIN(po[x]);
        st4(po[0], pd[0]); st4(po[1], pd[1]);
        SB();
        XS(41);
        consts(I2{});
        XS(42);
        consts(I3{});
        XS(43);
        st4(po[2], pd[2]); st4(po[3], pd[3]);
        w2dma(2, 2);
      } else {
        consts(I1{});
        ring4(2);
        SB();
        consts(I3{});
      }
      u8* const zi = lds + LDS_SCR;
      u32 zw;
      {
        const int pp = tid >> 1;
        zw = (tid & 1) * 4096 + ((pp >> 6) & 1) * 2048 + (((pp >> 3) & 3) * 16 + 2 * (pp & 7)) * 16 + ((pp >> 5) & 1) * 8;
        PIN(zw);
      }
      u32 uo = OFF_U + phi * 8192 + w * 2048 + l16;
      PIN(uo);
      SB();
      bar_nw();
      ST(S0 + 1);
      vmwait<(L == 0 ? 8 : 4)>();
      PIN(z0); PIN(z1); PIN(wv[0]); PIN(wv[1]);
      if constexpr (L == 0) {
        RETRY((stale<false, 1u>(red8<false>(z0, z1)) || max4(max4(0, wv[0]), wv[1]) == SENT), {
          CNT(1);
          if (bad_) { ld4r<0>(z0, zo, zb0); ld4r<0>(z1, zo, zb1); ld4r<0>(wv[0], wo, wb0); ld4r<0>(wv[1], wo, wb1); }
          vmwait<0>(); PIN(z0); PIN(z1); PIN(wv[0]); PIN(wv[1]);
        });
      } else {
        RETRY((stale<true, 0x10000u>(red8<true>(z0, z1))), {
          CNT(2);
          if (bad_) { ld4r<0>(z0, zo, zb0); ld4r<0>(z1, zo, zb1); }
          vmwait<0>(); PIN(z0); PIN(z1);
        });
      }
      ST(S0 + 2);
      *(u32x2*)(zi + zw) = u32x2{z0.x, z0.y}; *(u32x2*)(zi + zw + 16) = u32x2{z0.z, z0.w};
      *(u32x2*)(zi + zw + 1024) = u32x2{z1.x, z1.y}; *(u32x2*)(zi + zw + 1024 + 16) = u32x2{z1.z, z1.w};
      *(u32x4*)(zi + 8192 + t16) = wv[0]; *(u32x4*)(zi + 12288 + t16) = wv[1];
      bar();
      XF(37);
      const u8* zr = zi + pi_ * 4096 + l16;
      const u32x4 f00 = *(const u32x4*)(zr), f01 = *(const u32x4*)(zr + 1024), f10 = *(const u32x4*)(zr + 2048), f11 = *(const u32x4*)(zr + 3072);
      wv[0] = *(const u32x4*)(zi + 8192 + tid * 32); wv[1] = *(const u32x4*)(zi + 8192 + tid * 32 + 16);
      if (phi == 0) static_for<8>([&](auto J) { constexpr int j = decltype(J)::value; ld4<0>(wa[j], t16, ws + OFF_WNY + j * 4096); });
      if constexpr (L == 1) ring4(3); else { st4(po[4], OFF_W2 + (ct * 4 + w) * 32768 + (half2 * 16 + G) * 1024 + l16); }
      f32x4 d0, d1, d2, d3;
      mm8<false, 3>(d0, d1, d2, d3, f00, cc.b1re[0], f01, cc.b1re[1], f00, cc.b1im[0], f01, cc.b1im[1], f10, cc.b1re[0], f11, cc.b1re[1], f10, cc.b1im[0], f11, cc.b1im[1]);
      XF(38);
      f32x4 e0, e1, e2, e3;
      u32x4 b20 = twiddle<false>(d0, d1, cc.t1c[0], cc.t1s[0]);
      PIN(b20);
      const u32x4 b21 = cplx_under<false, true>(d2, d3, cc.t1c[1], cc.t1s[1], [&](auto I) {
        constexpr int i = decltype(I)::value;
        mf0<false, 0>(i == 0 ? e0 : i == 1 ? e1 : i == 2 ? e2 : e3, i == 0 ? cc.a2re[0][0] : i == 1 ? cc.a2im[0][0] : i == 2 ? cc.a2re[1][0] : cc.a2im[1][0], b20);
      });
      if constexpr (L == 0) w2dma(3, 3);
      f32x2 wr[2][2], wi[2][2];
      auto unpack_w = [&]() {
#pragma unroll
        for (int t = 0; t < 2; ++t)
#pragma unroll
          for (int h = 0; h < 2; ++h) {
            wr[t][h] = f32x2{blo(wv[t][2 * h]), blo(wv[t][2 * h + 1])}; wi[t][h] = f32x2{bhi(wv[t][2 * h]), bhi(wv[t][2 * h + 1])};
            PIN(wr[t][h]); PIN(wi[t][h]);
          }
      };
      mf4<false, 0, 0>(e0, e1, e2, e3, cc.a2re[0][1], b21, cc.a2im[0][1], b21, cc.a2re[1][1], b21, cc.a2im[1][1], b21);
      SB();
      unpack_w();
      SB();
      if constexpr (L == 0) { fill_h(OFF_H, 0); fill_h(OFF_H, 1); }
      XF(39);
      u32x4 a30, a31;
      auto step3 = [&](auto I) {
        constexpr int i = decltype(I)::value;
        mf0<true, 0>(i == 0 ? d0 : i == 1 ? d1 : i == 2 ? d2 : d3, a30, i == 0 ? cc.b3re[0][0] : i == 1 ? cc.b3im[0][0] : i == 2 ? cc.b3re[1][0] : cc.b3im[1][0]);
      };
      if (phi != 0) {
        a30 = cmul_b(e0, e1, wr[0], wi[0]);
        PIN(a30);
        a31 = cplx_under<true, false>(e2, e3, wr[1], wi[1], step3);
      } else {
        u32* pl = (u32*)(lds + LDS_STASH);
        const u32x4 h0 = {pkh(e0[0], e1[0]), pkh(e0[1], e1[1]), pkh(e0[2], e1[2]), pkh(e0[3], e1[3])};
        const u32x4 h1 = {pkh(e2[0], e3[0]), pkh(e2[1], e3[1]), pkh(e2[2], e3[2]), pkh(e2[3], e3[3])};
        if (k1l != 0) {
          *(u32x4*)(pl + k1l * 32 + ((g ^ (k1l >> 1)) & 7) * 4) = h0; *(u32x4*)(pl + k1l * 32 + (((4 + g) ^ (k1l >> 1)) & 7) * 4) = h1;
        } else {
#pragma unroll
          for (int r = 0; r < 4; ++r) { pl[(4 * g + r - 1) & 31] = h0[r]; pl[16 + 4 * g + r - 1] = h1[r]; }
        }
        bar();
        vmwait<(L == 0 ? 7 : 4)>();
#pragma unroll
        for (int j = 0; j < 8; ++j) PIN(wa[j]);
        if constexpr (L == 0) {
          RETRY(max4(max4(max4(max4(max4(max4(max4(max4(0, wa[0]), wa[1]), wa[2]), wa[3]), wa[4]), wa[5]), wa[6]), wa[7]) == SENT, {
            static_for<8>([&](auto J) { constexpr int j = decltype(J)::value; ld4r<0>(wa[j], t16, ws + OFF_WNY + j * 4096); });
            vmwait<0>();
            _Pragma("unroll") for (int j = 0; j < 8; ++j) PIN(wa[j]);
          });
        }
        const int kr = (64 - k1l) & 63;
        const u32x4 q0 = *(const u32x4*)(pl + kr * 32 + (((7 - g) ^ (kr >> 1)) & 7) * 4), q1 = *(const u32x4*)(pl + kr * 32 + (((3 - g) ^ (kr >> 1)) & 7) * 4);
#pragma unroll
        for (int r = 0; r < 4; ++r) {
          const f32x4 c0 = __builtin_bit_cast(f32x4, wa[r]), c1 = __builtin_bit_cast(f32x4, wa[4 + r]);
          const u32 u0 = q0[3 - r], u1 = q1[3 - r];
          const f16x2 v0 = __builtin_bit_cast(f16x2, u0), v1 = __builtin_bit_cast(f16x2, u1);
          const float qr0 = (float)v0.x, qi0 = (float)v0.y, qr1 = (float)v1.x, qi1 = (float)v1.y;
          a30[r] = pkb(e0[r] * c0[0] - e1[r] * c0[1] + qr0 * c0[2] + qi0 * c0[3], e0[r] * c0[1] + e1[r] * c0[0] + qr0 * c0[3] - qi0 * c0[2]);
          a31[r] = pkb(e2[r] * c1[0] - e3[r] * c1[1] + qr1 * c1[2] + qi1 * c1[3], e2[r] * c1[1] + e3[r] * c1[0] + qr1 * c1[3] - qi1 * c1[2]);
        }
        PIN(a30); PIN(a31);
        asm volatile("s_nop 1");
        static_for<4>(step3);
      }
      u8* ex = lds + LDS_SCR + 16384;
      mf4<true, 0, 5>(d0, d1, d2, d3, a31, cc.b3re[0][1], a31, cc.b3im[0][1], a31, cc.b3re[1][1], a31, cc.b3im[1][1]);
      XF(40);
      *(u32x4*)(ex + w * 1024 + l16) = twiddle<true>(d0, d1, cc.t2c[0], cc.t2s[0]);
      SB();
      *(u32x4*)(ex + (4 + w) * 1024 + l16) = twiddle<true>(d2, d3, cc.t2c[1], cc.t2s[1]);
      if constexpr (L == 0) vmwait<6>();
      bar();
      XF(41);
      const u8* er = ex + (w & 1) * 4096 + l16;
      const u32x4 g0 = *(const u32x4*)(er), g1 = *(const u32x4*)(er + 1024), g2 = *(const u32x4*)(er + 2048), g3 = *(const u32x4*)(er + 3072);
      mm8<true, 7>(d0, d1, d2, d3, cc.a4re[0][0], g0, cc.a4re[0][1], g1, cc.a4im[0][0], g0, cc.a4im[0][1], g1, cc.a4re[1][0], g2, cc.a4re[1][1], g3, cc.a4im[1][0], g2,
                   cc.a4im[1][1], g3);
      {
        XF(42);
        const f32x2 lr0 = lo2(d0) + lo2(d2), li0 = lo2(d1) + lo2(d3), hr0 = lo2(d0) - lo2(d2), hi0 = lo2(d1) - lo2(d3);
        const f32x2 lr1 = hi2(d0) + hi2(d2), li1 = hi2(d1) + hi2(d3), hr1 = hi2(d0) - hi2(d2), hi1 = hi2(d1) - hi2(d3);
        if (phi != 0) {
          const f32x2 pr0 = lr0 - hi0, pi0 = li0 + hr0, qr0 = lr0 + hi0, qi0 = hr0 - li0;
          st4(mark4<1u - L, L>(u32x4{pkb(pr0.x, pi0.x), pkb(qr0.x, qi0.x), pkb(pr0.y, pi0.y), pkb(qr0.y, qi0.y)}), uo);
          const f32x2 pr1 = lr1 - hi1, pi1 = li1 + hr1, qr1 = lr1 + hi1, qi1 = hr1 - li1;
          st4(mark4<1u - L, L>(u32x4{pkb(pr1.x, pi1.x), pkb(qr1.x, qi1.x), pkb(pr1.y, pi1.y), pkb(qr1.y, qi1.y)}), uo + 1024);
          MEMFENCE();
        } else {
          st4(mark4<1u - L, L>(u32x4{pkb(lr0.x, hr0.x), pkb(li0.x, hi0.x), pkb(lr0.y, hr0.y), pkb(li0.y, hi0.y)}), uo);
          st4(mark4<1u - L, L>(u32x4{pkb(lr1.x, hr1.x), pkb(li1.x, hi1.x), pkb(lr1.y, hr1.y), pkb(li1.y, hi1.y)}), uo + 1024);
        }
      }
    }
    ST(S0 + 3);
    {
      FRESH();
      auto w2take = [&](auto KC) {
        constexpr int k = decltype(KC)::value;
        u32x2 lo[4], hi[4];
        static_for<4>([&](auto C) { constexpr int c = decltype(C)::value; tr_read<k * 4096>(lo[c], hi[c], ldsbase + w * 32768 + tr_addr(lane, c)); });
        lgwait<0>();
#pragma unroll
        for (int c = 0; c < 4; ++c) {
          PIN(lo[c]); PIN(hi[c]);
          w2a[k][c] = u32x4{lo[c].x, lo[c].y, hi[c].x, hi[c].y};
          PINA(w2a[k][c]);
        }
      };
      if constexpr (L == 0) { w2dma(4, 4); w2dma(5, 5); vmwait<12>(); static_for<4>(w2take); }
      SB();
      row_consts<true, 0>(rc, m, g);
      f32x2 xa[4], xb[4];
#pragma unroll
      for (int i = 0; i < 4; ++i) {
        xa[i] = f32x2{blo(xl[i >> 1][i & 1]), bhi(xl[i >> 1][i & 1])}; xb[i] = f32x2{blo(xh[i >> 1][i & 1]), bhi(xh[i >> 1][i & 1])};
      }
      const float ca = first_lane(xa[0].x), cb = first_lane(xb[0].x);
      float ch = lane < 32 ? ca : cb;
      PIN(ch);
#pragma unroll
      for (int i = 0; i < 4; ++i) { xa[i] -= ca; xb[i] -= cb; PIN(xa[i]); PIN(xb[i]); }
      SB();
      u32x4 c0, c1;
      const u32 uo = (tid >> 1) * 8192 + (tid & 1) * 16;
      const u8* ub0 = ws + OFF_U + pos * 32;
      const u8* ub1 = ub0 + 128 * 8192;
      row_consts<true, 1>(rc, m, g);
      SB();
      __builtin_amdgcn_s_sleep(1);
      ld4<0>(c0, uo, ub0); ld4<0>(c1, uo, ub1);
      if constexpr (L == 0) {
        fill_h(OFF_H, 2); fill_h(OFF_H, 3); fill_h(OFF_H, 4);
        vmwait<5>();
        w2take(std::integral_constant<int, 4>{}); w2take(std::integral_constant<int, 5>{});
      }
      SB();
      if constexpr (L == 1) ring4(4); else { fill_h(OFF_H, 5); fill_h(OFF_H, 6); }
      u32* img = (u32*)(lds + LDS_SCR);
      u32 ip0, iq0, iq1, xno, tw0, tw1, trd;
      {
        const int ph = tid >> 1, fq0 = ph ? 512 - ph : 256, fq1 = 384 - ph;
        ip0 = ((tid & 1) * 1280 + (ph & 31) * 20 + (ph >> 5)) * 4;
        iq0 = ((tid & 1) * 1280 + (fq0 & 31) * 20 + (fq0 >> 5)) * 4; iq1 = ((tid & 1) * 1280 + (fq1 & 31) * 20 + (fq1 >> 5)) * 4;
        xno = OFF_A + (pos >> 1) * 16384 + (tid >> 3) * 256 + (pos & 1) * 128 + (tid & 7) * 16;
        tw0 = (4 * m + g) * 128 + ((2 * w) ^ (m & 7)) * 16; tw1 = (4 * m + g) * 128 + ((2 * w + 1) ^ (m & 7)) * 16;
        trd = (tid >> 3) * 128 + ((tid & 7) ^ ((tid >> 5) & 7)) * 16;
        PIN(ip0); PIN(iq0); PIN(iq1); PIN(xno); PIN(tw0); PIN(tw1); PIN(trd);
      }
      SB();
      ST(S0 + 4);
      vmwait<(L == 0 ? 5 : 4)>(); PIN(c0); PIN(c1);
      {
        RETRY((stale<L == 1, 1u>(red8<L == 1>(c0, c1))), {
          CNT(3);
          if (bad_) { ld4r<0>(c0, uo, ub0); ld4r<0>(c1, uo, ub1); }
          vmwait<0>(); PIN(c0); PIN(c1);
        });
      }
      ST(S0 + 5);
      {
        u8* d = (u8*)img;
        *(u32*)(d + ip0) = c0.x; *(u32*)(d + iq0) = c0.y; *(u32*)(d + 2560 + ip0) = c0.z; *(u32*)(d + 2560 + iq0) = c0.w;
        *(u32*)(d + ip0 + 16) = c1.x; *(u32*)(d + iq1) = c1.y; *(u32*)(d + 2560 + ip0 + 16) = c1.z; *(u32*)(d + 2560 + iq1) = c1.w;
      }
      bar();
      XF(43);
      const u32x4 a0 = *(const u32x4*)(img + w * 640 + m * 20 + 4 * g), a1 = *(const u32x4*)(img + w * 640 + (16 + m) * 20 + 4 * g);
      if constexpr (L == 1) ring4(5);
      if constexpr (L == 0) ring4(0);
      f32x4 dr0, di0, dr1, di1;
      row_fft<true, 7>(rc, a0, a1, dr0, di0, dr1, di1, [&] { if constexpr (L == 0) ring4(1); });
      if constexpr (L == 0) ring4(2);
      XF(44);
      const float ys = 1.0f / 32768.0f;
      xa[0] += lo2(dr0) * ys; xa[1] += hi2(dr0) * ys; xa[2] += lo2(dr1) * ys; xa[3] += hi2(dr1) * ys;
      xb[0] += lo2(di0) * ys; xb[1] += hi2(di0) * ys; xb[2] += lo2(di1) * ys; xb[3] += hi2(di1) * ys;
      const f32x2 sa = (xa[0] + xa[1]) + (xa[2] + xa[3]), sb = (xb[0] + xb[1]) + (xb[2] + xb[3]);
      f32x2 qa = xa[0] * xa[0], qb = xb[0] * xb[0];
#pragma unroll
      for (int i = 1; i < 4; ++i) { qa += xa[i] * xa[i]; qb += xb[i] * xb[i]; }
      if constexpr (L == 0) { SB(); ring4(3); SB(); }
      f32x2 R, O;
      XF(45);
      ln_coef(f32x2{sa.x + sa.y, sb.x + sb.y}, f32x2{qa.x + qa.y, qb.x + qb.y}, ch, R, O);
      XF(46);
      if constexpr (L == 0) { SB(); ring4(4); SB(); }
      u32x4 na, nb;
#pragma unroll
      for (int i = 0; i < 4; ++i) {
        const f32x2 va = xa[i] * R.x + O.x, vb = xb[i] * R.y + O.y;
        na[i] = mark<0x10000u, 1u - L>(pkb(va.x, va.y)); nb[i] = mark<0x10000u, 1u - L>(pkb(vb.x, vb.y));
      }
      u8* tr = lds + LDS_SCR + 12288;
      *(u32x4*)(tr + tw0) = na; *(u32x4*)(tr + tw1) = nb;
      if constexpr (L == 0) vmwait<20>(); else vmwait<0>();
      bar();
      XF(47);
      st4(*(const u32x4*)(tr + trd), xno);
      st4(*(const u32x4*)(tr + 4096 + trd), xno + 8192);
      SB();
#pragma unroll
      for (int i = 0; i < 4; ++i) { xa[i] += ca; xb[i] += cb; }
      const u32x4 rl = {pkb(xa[0].x, xa[0].y), pkb(xa[1].x, xa[1].y), pkb(xa[2].x, xa[2].y), pkb(xa[3].x, xa[3].y)};
      const u32x4 rh = {pkb(xb[0].x, xb[0].y), pkb(xb[1].x, xb[1].y), pkb(xb[2].x, xb[2].y), pkb(xb[3].x, xb[3].y)};
      if constexpr (L == 0) {
        *(u32x4*)(lds + LDS_STASH + t16) = rl; *(u32x4*)(lds + LDS_STASH + 4096 + t16) = rh;
        XS(44);
        ring4(5); ring4(6); ring4(7);
      } else {
        xl[0] = u32x2{rl.x, rl.y}; xl[1] = u32x2{rl.z, rl.w}; xh[0] = u32x2{rh.x, rh.y}; xh[1] = u32x2{rh.z, rh.w};
        PIN(xl[0]); PIN(xl[1]); PIN(xh[0]); PIN(xh[1]);
        __builtin_amdgcn_s_sleep(XSLP);
      }
    }
    ST(S0 + 6);
    {
      FRESH();
      u32x4 tk[32], he[32], hl[16];
      u32x4 hh[8], hf[8];
      f32x4 ac1[6][2], ac2[4][2], ac3[4][2];
      const u8* hs0 = ws + OFF_H + (4 * G + 2 * half2) * 114688 + (4 * w) * 7168;
      const u8* hs[4];
      const u8* hp[4];
#pragma unroll
      for (int j = 0; j < 4; ++j) {
        hs[j] = hs0 + opq((j + ct) & 3) * 7168; hp[j] = hs0 + opq(j ^ (2 * (ct & 1))) * 7168 + 6144;
        PINS(hs[j]); PINS(hp[j]);
      }
      const u8* w2src = ws + OFF_W2 + (ct * 4 + w) * 32768;
      u32 hv[4], hw[2];
#pragma unroll
      for (int u = 0; u < 4; ++u) { hv[u] = l16 + ((u ^ w) >> 1) * 114688 + ((u ^ w) & 1) * 3072; PIN(hv[u]); }
#pragma unroll
      for (int q = 0; q < 2; ++q) { hw[q] = l16 + (q ^ (w >> 1)) * 114688; PIN(hw[q]); }
      auto hload = [&](auto RE, auto KC, auto UC) {
        constexpr int k = decltype(KC)::value, u = decltype(UC)::value;
        constexpr bool re = decltype(RE)::value;
        if constexpr (k == 4 || k == 5) {
          if constexpr (re) ld4r<0>(hh[4 * (k - 4) + u], hw[u >> 1], hp[2 * (k - 4) + (u & 1)]); else ld4<0>(hh[4 * (k - 4) + u], hw[u >> 1], hp[2 * (k - 4) + (u & 1)]);
        } else {
          constexpr int j = k < 4 ? k : k < 10 ? k - 6 : k - 10, o = k < 4 ? 0 : k < 10 ? 1024 : 2048;
          u32x4& d = k < 10 ? he[(k < 4 ? 0 : 16) + 4 * j + u] : hl[4 * j + u];
          if constexpr (re) ld4r<o>(d, hv[u], hs[j]); else ld4<o>(d, hv[u], hs[j]);
        }
      };
      auto hseq = [&](auto IC) {
        constexpr int i = decltype(IC)::value;
        hload(std::false_type{}, std::integral_constant<int, (i >> 2)>{}, std::integral_constant<int, i & 3>{});
      };
      u64 bm0 = 0;
      {
        const u8* w1b = ws + OFF_W1 + member * 229376 + 131072 + w * 8192;
        const u8* tk0 = ws + OFF_A + (8 * G + 2 * w) * 16384 + 4096;
        const u8* tk1 = tk0 + 8192;
        u32 tv1 = l16 + 16384, llo = ldsbase + l16, lhi = llo + 65536;
        u32 hd0 = OFF_H + (4 * G + w) * 114688 + member * 7168 + l16, hd1 = hd0 + 3072;
        PIN(tv1); PIN(llo); PIN(lhi); PIN(hd0); PIN(hd1); PINS(tk0); PINS(tk1);
        u32 x3o = OFF_X3 + (8 * pos + 2 * w) * 1024 + xo;
        if constexpr (L == 1) PIN(x3o);
        auto tok = [&](auto RE, auto QC) {
          constexpr int q = decltype(QC)::value, ks = q >> 1;
          if constexpr (decltype(RE)::value) ld4r<(ks & 7) * 1024 - 4096>(tk[q], q & 1 ? tv1 : l16, ks >> 3 ? tk1 : tk0);
          else ld4<(ks & 7) * 1024 - 4096>(tk[q], q & 1 ? tv1 : l16, ks >> 3 ? tk1 : tk0);
        };
        constexpr bool TONE = L == 0;
        constexpr u32 TBIT = L == 0 ? 1u : 0x10000u;
        u32 mx0 = 0, mx1 = 0;
        auto mx0_fin = [&]() { return red2<TONE>(mx0, mx1) & TBIT; };
        auto fix = [&](auto QC, u64 bad) {
          constexpr int q0 = decltype(QC)::value;
          if (__builtin_expect(bad != 0, 0)) {
            _Pragma("nounroll") for (int sp = 0; sp < SPIN_CAP; ++sp) {
              CNT(4);
              static_for<4>([&](auto I) { tok(std::true_type{}, std::integral_constant<int, q0 + decltype(I)::value>{}); });
              vmwait<0>();
#pragma unroll
              for (int q = q0; q < q0 + 4; ++q) PIN(tk[q]);
              if (!__any(stale<TONE, TBIT>(red2<TONE>(red8<TONE>(tk[q0], tk[q0 + 1]), red8<TONE>(tk[q0 + 2], tk[q0 + 3]))))) break;
            }
          }
        };
        constexpr int PD = 6;
        u32x4 af[PD];
        auto frag = [&](auto XC) {
          constexpr int x = decltype(XC)::value, o = (x & 127) * 1024;
          lda<o & 65535>(af[x % PD], o < 65536 ? llo : lhi);
        };
        if constexpr (L == 1) static_for<PD - 1>(frag);
        XF(48);
        static_for<NTOK0>([&](auto Q) {
          tok(std::false_type{}, Q);
          if constexpr (decltype(Q)::value == 3) bar_nw();
        });
        XF(49);
        if constexpr (L == 0) {
          vmwait<NTOK0>();
          bar_nw();
          static_for<PD - 1>(frag);
        }
        vmwait<NTOK0 - 4>();
        ST(S0 + 7);
#pragma unroll
        for (int q = 0; q < 4; ++q) PIN(tk[q]);
        mx0 = red8<TONE>(tk[0], tk[1]); mx1 = red8<TONE>(tk[2], tk[3]);
        fix(std::integral_constant<int, 0>{}, __ballot(stale<TONE, TBIT>(red2<TONE>(mx0, mx1))));
        lgwait<PD - 2>();
        SB();
        ST(S0 + 8);
        float eg[2][2];
        u32 ev[2];
        u64 bm = 0;
        u32x4 eo[1];
        auto epi = [&](auto NC) {
          constexpr int n = decltype(NC)::value;
          if constexpr (n < NEPI1 + NEPI2) {
            constexpr bool p1 = n < NEPI1;
            constexpr int n0 = p1 ? n : n - NEPI1, i = n0 - n0 / 25;
            if constexpr (n0 % 25 == 24) st4(eo[0], p1 ? (n == 24 ? hd0 : n == 49 ? hd1 : hd1 + 3072) : (n0 == 24 ? hd0 : hd1) + 1024);
            else {
              constexpr int s = i & 1, st = (i % 12) >> 1, blk = i / 12;
              constexpr int u = p1 ? (blk < 4 ? blk >> 1 : blk - 4) : blk >> 1, pr = p1 ? (blk < 4 ? blk & 1 : 2) : blk & 1;
              constexpr int d = 2 * (pr == 2 ? u : pr) + s;
              const f32x4& ga = p1 ? ac1[2 * pr][u] : ac2[2 * (pr & 1)][u];
              const f32x4& up = p1 ? ac1[2 * pr + 1][u] : ac2[2 * (pr & 1) + 1][u];
              if constexpr (st < 2) { eg[s][st] = relu(ga[2 * s + st]); PIN(eg[s][st]); }
              else if constexpr (st < 4) { eg[s][st - 2] *= up[2 * s + st - 2]; PIN(eg[s][st - 2]); }
              else if constexpr (st == 4) { ev[s] = pkb(eg[s][0], eg[s][1]); PIN(ev[s]); }
              else { u32 v = mark<1u - L, L>(ev[s]); PIN(v); eo[0][d] = v; }
            }
            SB();
          }
        };
        static_for<224>([&](auto XC) {
          constexpr int x = decltype(XC)::value, job = G1S[L].job[x], ji = G1S[L].idx[x], e0 = G1S[L].epi[x], e1 = G1S[L].epi[x + 1];
          if constexpr (x == 32 || x == 64) bar_nw();
          if constexpr (x == 97) { vmwait<g1_wait(L, x, 2, 7)>(); bar_nw(); }
          if constexpr (x == 150) { vmwait<g1_wait(L, x, 2, 23)>(); bar_nw(); }
          constexpr int cj = (x + 12) / 12, co = x + 12 - 12 * cj;
          constexpr bool chk = x < 84 && co >= 5;
          constexpr int ks = x < 96 ? x / 6 : x < 160 ? (x - 96) >> 2 : (x - 160) >> 2, t = x < 96 ? x % 6 : x & 3;
          auto mm = [&](int u) {
            f32x4& a = x < 96 ? ac1[x < 96 ? t : 0][u] : x < 160 ? ac2[x < 96 ? 0 : t][u] : ac3[x < 96 ? 0 : t][u];
            mmw<ks == 0>(a, af[x % PD], tk[2 * ks + u]);
          };
          SB();
          mm(0);
          SB();
          if constexpr (x < 223) lgwait<(x + PD - 2 < 224 ? PD - 3 : 222 - x)>();
          if constexpr (chk && co == 5) {
            vmwait<g1_wait(L, x, 1, 4 * cj + 3)>();
            PIN(tk[4 * cj]); PIN(tk[4 * cj + 1]); PIN(tk[4 * cj + 2]); PIN(tk[4 * cj + 3]);
          }
          SB();
          if constexpr (chk && co >= 6 && co < 10) {
            const u32x4 v = tk[4 * cj + co - 6];
            mx0 = co == 6 ? red2<TONE>(v.x, v.y) : red3<TONE>(mx0, v.x, v.y); PIN(mx0); SB();
            mx1 = co == 6 ? red2<TONE>(v.z, v.w) : red3<TONE>(mx1, v.z, v.w); PIN(mx1);
          }
          if constexpr (chk && co == 10) { mx0 = mx0_fin(); PIN(mx0); SB(); bm = __ballot(TONE ? mx0 == 0 : mx0 != 0); PINS(bm); }
          if constexpr (x >= 212 && x < 218) {
            if constexpr (x == 212) { vmwait<g1_wait(L, x, 4, 3)>(); PIN(he[0]); PIN(he[1]); PIN(he[2]); PIN(he[3]); }
            else if constexpr (x < 217) {
              const u32x4 v = he[x - 213];
              mx0 = x == 213 ? red2<L == 1>(v.x, v.y) : red3<L == 1>(mx0, v.x, v.y); PIN(mx0); SB();
              mx1 = x == 213 ? red2<L == 1>(v.z, v.w) : red3<L == 1>(mx1, v.z, v.w); PIN(mx1);
            } else { mx0 = red2<L == 1>(mx0, mx1) & 1u; PIN(mx0); SB(); bm0 = __ballot(L == 1 ? mx0 == 0 : mx0 != 0); PINS(bm0); }
          }
          if constexpr (e0 < e1) epi(std::integral_constant<int, e0>{});
          if constexpr (e0 + 1 < e1) epi(std::integral_constant<int, e0 + 1>{});
          SB();
          mm(1);
          SB();
          if constexpr (chk && co == 11) fix(std::integral_constant<int, 4 * cj>{}, bm);
          if constexpr (job == 2 && (ji & 3) == 0) set_m0(ldsbase + (ji >> 3) * 32768 + w * 8192 + (ji & 4) * 1024);
          SB();
          if constexpr (x + PD - 1 < 224) frag(std::integral_constant<int, x + PD - 1>{});
          SB();
          if constexpr (job == 1) tok(std::false_type{}, std::integral_constant<int, ji>{});
          if constexpr (job == 2) dma1<(ji & 3) * 1024>(l16, w1b + (ji >> 3) * 32768 + (ji & 4) * 1024);
          if constexpr (job == 3) ld4a<(ji & 7) * 1024 - 4096>(w2a[6 + (ji >> 2)][ji & 3], l16, w2src + (ji >> 3) * 8192 + 4096);
          if constexpr (job == 6) st2(ji & 2 ? xh[ji & 1] : xl[ji & 1], x3o + ji * 512);
          if constexpr (job == 4) hseq(std::integral_constant<int, ji>{});
          if constexpr (e0 + 2 < e1) epi(std::integral_constant<int, e0 + 2>{});
          SB();
        });
        ST(S0 + 9);
        asm volatile("s_nop 7");
        SB();
#pragma unroll
        for (int u = 0; u < 2; ++u) {
          u32x4 o;
#pragma unroll
          for (int p = 0; p < 2; ++p) {
            const f32x4 ga = ac3[2 * p][u], up = ac3[2 * p + 1][u];
            const f32x2 a = f32x2{relu(ga[0]), relu(ga[1])} * lo2(up), c = f32x2{relu(ga[2]), relu(ga[3])} * hi2(up);
            o[2 * p] = mark<1u - L, L>(pkb(a.x, a.y)); o[2 * p + 1] = mark<1u - L, L>(pkb(c.x, c.y));
          }
          st4(o, (u ? hd1 : hd0) + 2048);
        }
        SB();
      }
      ST(S0 + 10);
      {
        f32x4 acc[4][4];
        u32x4 x3a, x3b;
        u32 eo = 0, oo = 0, rw[3], ta = 0, tb = 0;
        auto addr = [&](auto IC) {
          constexpr int i = decltype(IC)::value;
          const u32 sb = (128 * G + 64 * half2 + 16 * w) * 1024 + ct * 128;
          if constexpr (i < 3) { rw[i] = l16 + (((i + 1) ^ w) * 3 + i) * 4096; PIN(rw[i]); }
          if constexpr (i == 3) { ta = (l16 >> 4) & 0x30; PIN(ta); }
          if constexpr (i == 4) { tb = l16 & (L == 1 ? 0xf0 : 0xe0); PIN(tb); }
          if constexpr (i == 5) { eo = tb * 64 + sb; PIN(eo); }
          if constexpr (i == 6) { tb = l16 & 0x10; PIN(tb); }
          if constexpr (i == 7) { tb = tb * 4 + ta; PIN(tb); }
          if constexpr (i == 8) { eo += L == 1 ? ta : tb; PIN(eo); }
          if constexpr (L == 1) {
            if constexpr (i == 9) { ta = l16 & 0x20; PIN(ta); }
            if constexpr (i == 10) { oo = ta * 1024 + tb; PIN(oo); }
            if constexpr (i == 11) { ta = l16 & 0x80; PIN(ta); }
            if constexpr (i == 12) { oo += ta * 16; PIN(oo); }
            if constexpr (i == 13) { ta = l16 & 0x40; PIN(ta); }
            if constexpr (i == 14) { oo += ta * 16; PIN(oo); }
            if constexpr (i == 15) { oo += row_of_pos(16 * G + 8 * half2 + 2 * w, 0) * 1024 + ct * 128; PIN(oo); }
          }
        };
        const u8* x3s = ws + OFF_X3;
        u64 bm = 0;
        PINS(x3s);
        constexpr int NEARLY = g1_ne(L), LB = 40 - NEARLY, XG = 31;
        auto cum = [](int n) constexpr { const int c = n - n / 8; return c < LB + 16 ? c : LB + 16; };
        auto young = [cum](int k, int n) constexpr {
          const int i = 4 * k + 3;
          const int done = k < 10 ? i - NEARLY + 1 : LB + 4 + 3 * (k - 10);
          const int xr = (L == 1 && n > XG && done <= cum(XG)) ? 2 : 0;
          if (k < 10 && i < NEARLY) return cum(n) + 2 + G1S[L].cum[224] - G1S[L].pos[4][i] - 1 + xr;
          return cum(n) - done + xr;
        };
        constexpr bool HONE = L == 1;
        u32 mx0 = 0, mx1 = 0;
        auto mx0_fin = [&]() { return red2<HONE>(mx0, mx1) & 1u; };
        auto pin_k = [&](auto KC) {
          constexpr int k = decltype(KC)::value;
#pragma unroll
          for (int u = 0; u < 4; ++u) {
            if constexpr (k < 4) PIN(he[4 * k + u]);
            else if constexpr (k < 6) PIN(hh[4 * (k - 4) + u]);
            else if constexpr (k < 10) PIN(he[16 + 4 * (k - 6) + u]);
            else PIN(hl[4 * (k - 10) + u]);
          }
        };
        auto opnd = [&](auto KC, int u) {
          constexpr int k = decltype(KC)::value;
          if constexpr (k < 4) return he[4 * k + u];
          else if constexpr (k < 6) return hh[4 * (k - 4) + u];
          else if constexpr (k < 10) return he[16 + 4 * (k - 6) + u];
          else return hl[4 * (k - 10) + u];
        };
        u32 wodd = (w & 1) ? ~0u : 0u;
        PIN(wodd);
        auto mkhalf = [&](auto KC, auto IC) {
          constexpr int k = decltype(KC)::value, i = decltype(IC)::value, u = i >> 2, d = i & 3;
          const u32x4& src = hh[4 * (k - 4) + (u & 2) + (d >> 1)];
          u32 v = (src[2 * ((u & 1) ^ 1) + (d & 1)] & wodd) | (src[2 * (u & 1) + (d & 1)] & ~wodd);
          PIN(v);
          hf[4 * (k - 4) + u][d] = v;
        };
        auto fixk = [&](auto KC, u64 bad) {
          constexpr int k = decltype(KC)::value;
          if (__builtin_expect(bad != 0, 0)) {
            _Pragma("nounroll") for (int sp = 0; sp < SPIN_CAP; ++sp) {
              CNT(k < 10 ? 5 : 6);
              static_for<4>([&](auto U) { hload(std::true_type{}, KC, U); });
              vmwait<0>();
              pin_k(KC);
              if (!__any(stale<HONE, 1u>(red2<HONE>(red8<HONE>(opnd(KC, 0), opnd(KC, 1)), red8<HONE>(opnd(KC, 2), opnd(KC, 3)))))) break;
            }
            if constexpr (k == 4 || k == 5) static_for<16>([&](auto I) { mkhalf(KC, I); });
          }
        };
        if constexpr (L == 0) {
#pragma unroll
          for (int k = 6; k < 14; ++k) { PINA(w2a[k][0]); PINA(w2a[k][1]); PINA(w2a[k][2]); PINA(w2a[k][3]); }
        }
        fixk(std::integral_constant<int, 0>{}, bm0);
        auto qchk = [&](auto QC, auto JC) {
          constexpr int qd = decltype(QC)::value, j = decltype(JC)::value, u = (qd + 1) & 3;
          if constexpr (j == 0) { PIN(hl[4 + u]); PIN(hl[8 + u]); PIN(hl[12 + u]); }
          else if constexpr (j < 4) {
            const u32x4 v = hl[4 * j + u];
            mx0 = j == 1 ? red2<HONE>(v.x, v.y) : red3<HONE>(mx0, v.x, v.y); PIN(mx0); SB();
            mx1 = j == 1 ? red2<HONE>(v.z, v.w) : red3<HONE>(mx1, v.z, v.w); PIN(mx1);
          } else if constexpr (j == 4) { mx0 = mx0_fin(); PIN(mx0); SB(); bm = __ballot(HONE ? mx0 == 0 : mx0 != 0); PINS(bm); }
          else if (__builtin_expect(bm != 0, 0)) {
            _Pragma("nounroll") for (int sp = 0; sp < SPIN_CAP; ++sp) {
              CNT(6);
              static_for<3>([&](auto K) { hload(std::true_type{}, std::integral_constant<int, 11 + decltype(K)::value>{}, std::integral_constant<int, u>{}); });
              vmwait<0>();
              PIN(hl[4 + u]); PIN(hl[8 + u]); PIN(hl[12 + u]);
              if (!__any(stale<HONE, 1u>(red4<HONE>(red8<HONE>(hl[4 + u], hl[8 + u]), hl[12 + u])))) break;
            }
          }
        };
        ST(S0 + 11);
        static_for<44>([&](auto NC) {
          constexpr int n = decltype(NC)::value, k = n >> 2, c = n & 3;
          using KC = std::integral_constant<int, k>;
          using KN = std::integral_constant<int, k + 1>;
          static_for<4>([&](auto UC) {
            constexpr int u = decltype(UC)::value;
            SB();
            if constexpr (k == 4 || k == 5) mmw<false>(acc[c][u], w2a[k][c], hf[4 * (k - 4) + u]);
            else mmw<k == 0>(acc[c][u], w2a[k][c], opnd(KC{}, u));
            SB();
            constexpr bool hk = k == 3 || k == 4;
            constexpr int j = u == 3 ? -9 : 3 * c + u - (hk ? 1 : 5);
            if constexpr (k == 10) {
              if constexpr (j == -1) vmwait<clamp63(young(11, n))>();
              if constexpr (j >= -1 && j < 4) qchk(std::integral_constant<int, 0>{}, std::integral_constant<int, j + 1>{});
            }
            if constexpr (k < 10 && j == -1) { vmwait<clamp63(young(k + 1, n))>(); pin_k(KN{}); }
            if constexpr (k < 10 && j >= 0 && j < 4) {
              const u32x4 v = opnd(KN{}, j);
              mx0 = j == 0 ? red2<HONE>(v.x, v.y) : red3<HONE>(mx0, v.x, v.y); PIN(mx0); SB();
              mx1 = j == 0 ? red2<HONE>(v.z, v.w) : red3<HONE>(mx1, v.z, v.w); PIN(mx1);
            }
            if constexpr ((k < 3 || k == 5) && c == 0 && u < 2) {
              constexpr int i0 = 4 * (k < 3 ? k : 3) + 2 * u;
              addr(std::integral_constant<int, i0>{}); SB(); addr(std::integral_constant<int, i0 + 1>{});
            }
            if constexpr (hk && j >= 4 && j != 5) {
              constexpr int i0 = j == 4 ? 0 : 2 + 3 * (j - 6);
              mkhalf(KN{}, std::integral_constant<int, i0>{}); SB(); mkhalf(KN{}, std::integral_constant<int, i0 + 1>{});
              if constexpr (j >= 6 && i0 + 2 < 16) { SB(); mkhalf(KN{}, std::integral_constant<int, i0 + 2>{}); }
            }
            if constexpr (k < 10 && j == 5) { mx0 = mx0_fin(); PIN(mx0); SB(); bm = __ballot(HONE ? mx0 == 0 : mx0 != 0); PINS(bm); }
            if constexpr (u == 3) {
              constexpr int q = cum(n);
              if constexpr (cum(n + 1) == q) {}
              else if constexpr (q < LB) hseq(std::integral_constant<int, NEARLY + q>{});
              else if constexpr (q < LB + 4) hload(std::false_type{}, std::integral_constant<int, 10>{}, std::integral_constant<int, q - LB>{});
              else hload(std::false_type{}, std::integral_constant<int, 11 + (q - LB - 4) % 3>{}, std::integral_constant<int, ((q - LB - 4) / 3 + 1) & 3>{});
              if constexpr (n == 23) {
                bar_nw();
              }
              if constexpr (L == 1 && n == XG) { ld4<0>(x3a, eo, x3s); ld4<64>(x3b, eo, x3s); }
              if constexpr (c == 3) {
                if constexpr (k < 10) fixk(KN{}, bm); else qchk(std::integral_constant<int, 0>{}, std::integral_constant<int, 5>{});
              }
            }
          });
        });
        static_for<16>([&](auto TC) {
          constexpr int t = decltype(TC)::value, u = ((t >> 2) + 1) & 3, c = t & 3;
          using QN = std::integral_constant<int, (t >> 2) + 1>;
          SB();
          mmw<false>(acc[c][u], w2a[11][c], hl[4 + u]);
          SB();
          if constexpr (t < 12) {
            if constexpr (c == 0) { vmwait<young(12 + (t >> 2), 44)>(); qchk(QN{}, std::integral_constant<int, 0>{}); }
            else qchk(QN{}, std::integral_constant<int, c>{});
          }
          SB();
          mmw<false>(acc[c][u], w2a[12][c], hl[8 + u]);
          SB();
          if constexpr (t >= 3 && t < 15) *(f32x4*)(lds + rw[(t - 3) >> 2] + ((t - 3) & 3) * 1024) = acc[(t - 3) & 3][((t - 3) >> 2) + 1];
          SB();
          mmw<false>(acc[c][u], w2a[13][c], hl[12 + u]);
          SB();
          if constexpr (t < 12 && c == 3) { qchk(QN{}, std::integral_constant<int, 4>{}); qchk(QN{}, std::integral_constant<int, 5>{}); }
        });
        SB();
        ST(S0 + 12);
        bar();
        f32x4 s[3][4];
#pragma unroll
        for (int c = 0; c < 4; ++c)
#pragma unroll
          for (int p = 0; p < 3; ++p) s[p][c] = *(const f32x4*)(lds + w * 12288 + p * 4096 + c * 1024 + l16);
        SB();
        if constexpr (L == 1) { vmwait<0>(); PIN(x3a); PIN(x3b); }
        u32x4 y[2];
#pragma unroll
        for (int a = 0; a < 2; ++a) {
          const f32x4 s0 = (acc[2 * a][0] + s[0][2 * a]) + (s[1][2 * a] + s[2][2 * a]);
          const f32x4 s1 = (acc[2 * a + 1][0] + s[0][2 * a + 1]) + (s[1][2 * a + 1] + s[2][2 * a + 1]);
          y[a] = u32x4{pkb(s0[0], s0[1]), pkb(s0[2], s0[3]), pkb(s1[0], s1[1]), pkb(s1[2], s1[3])};
          if constexpr (L == 1) {
            const u32x4 xr = a ? x3b : x3a;
#pragma unroll
            for (int p = 0; p < 4; ++p) y[a][p] = pkb(blo(xr[p]) + blo(y[a][p]), bhi(xr[p]) + bhi(y[a][p]));
          }
        }
        {
          const bool odd = m & 1;
          u32x4 z0, z1;
#pragma unroll
          for (int p = 0; p < 4; ++p) {
            const u32 t = (u32)__builtin_amdgcn_mov_dpp((int)(odd ? y[0][p] : y[1][p]), 0xB1, 0xf, 0xf, true);
            z0[p] = odd ? t : y[0][p]; z1[p] = odd ? y[1][p] : t;
          }
          if constexpr (L == 0) { st4(mark4<0x10001u, 0u>(z0), OFF_A + eo); st4(mark4<0x10001u, 0u>(z1), OFF_A + eo + 1024); }
          else {
            const __amdgpu_buffer_rsrc_t ro = __builtin_amdgcn_make_buffer_rsrc(P.out, 0, 2048 * 1024, 0x00020000);
            __builtin_amdgcn_raw_buffer_store_b128(z0, ro, oo, 0, 16); __builtin_amdgcn_raw_buffer_store_b128(z1, ro, oo + 1024 * 1024, 0, 16);
          }
        }
      }
    }
    ST(S0 + 13);
    if constexpr (L == 0) {
      FRESH();
      SB();
      row_consts<false, 0>(rc, m, g);
      SB();
      u32x2 d0, d1, d2, d3;
      const u8* db = ws + OFF_A + (8 * pos + 2 * w) * 1024;
      const u32x4 sl = *(const u32x4*)(lds + LDS_STASH + t16), sh = *(const u32x4*)(lds + LDS_STASH + 4096 + t16);
#pragma unroll
      for (int i = 0; i < 4; ++i) {
        xv[2 * i] = f32x2{blo(sl[i]), blo(sh[i])}; xv[2 * i + 1] = f32x2{bhi(sl[i]), bhi(sh[i])};
      }
      xc = f32x2{first_lane(xv[0].x), first_lane(xv[0].y)};
      xch = lane < 32 ? xc.x : xc.y;
      PIN(xch);
#pragma unroll
      for (int i = 0; i < 8; ++i) { xv[i] -= xc; PIN(xv[i]); }
      SB();
      bar_nw();
      ld2<0>(d0, xo, db); ld2<512>(d1, xo, db); ld2<1024>(d2, xo, db); ld2<1536>(d3, xo, db);
      ring4(0);
      SB();
      row_consts<false, 1>(rc, m, g);
      SB();
      fwd_pre(fp, tid, w, pos);
      SB();
      ST(S0 + 14);
      vmwait<4>(); PIN(d0); PIN(d1); PIN(d2); PIN(d3);
      RETRY((stale<false, 1u>(red8<false>(u32x4{d0.x, d0.y, d1.x, d1.y}, u32x4{d2.x, d2.y, d3.x, d3.y}))), {
        CNT(7);
        if (bad_) { ld2r<0>(d0, xo, db); ld2r<512>(d1, xo, db); ld2r<1024>(d2, xo, db); ld2r<1536>(d3, xo, db); }
        vmwait<0>(); PIN(d0); PIN(d1); PIN(d2); PIN(d3);
      });
      xv[0] += f32x2{blo(d0.x), blo(d2.x)}; xv[1] += f32x2{bhi(d0.x), bhi(d2.x)}; xv[2] += f32x2{blo(d0.y), blo(d2.y)}; xv[3] += f32x2{bhi(d0.y), bhi(d2.y)};
      xv[4] += f32x2{blo(d1.x), blo(d3.x)}; xv[5] += f32x2{bhi(d1.x), bhi(d3.x)}; xv[6] += f32x2{blo(d1.y), blo(d3.y)}; xv[7] += f32x2{bhi(d1.y), bhi(d3.y)};
      ST(S0 + 15);
    }
  };
  layer(std::integral_constant<int, 0>{});
  layer(std::integral_constant<int, 1>{});
  if (blockIdx.x == 0 && threadIdx.x == 0) st2(ep, OFF_E);
  asm volatile(".Lgf_end:");
  ST_OUT();
}

extern "C" void kernel_launch(void* const* d_in, const int* in_sizes, int n_in, void* d_out, int out_size, void* d_ws, size_t ws_size,
                              hipStream_t stream) {
  Params p;
  p.x = (const u16*)d_in[0]; p.wr = (const u16*)d_in[1]; p.wi = (const u16*)d_in[2];
  p.wg = (const u16*)d_in[4]; p.wu = (const u16*)d_in[5]; p.wd = (const u16*)d_in[6];
  p.out = (u16*)d_out; p.ws = (u8*)d_ws;
  (void)hipFuncSetAttribute((const void*)k_all, hipFuncAttributeMaxDynamicSharedMemorySize, LDS_TOTAL);
  k_all<<<256, 256, LDS_TOTAL, stream>>>(p);
}
```

```cpp
#include <hip/hip_runtime.h>
#include <hip/hip_bf16.h>
#include <stdint.h>
#include <type_traits>
#include <utility>

typedef unsigned char u8;
typedef unsigned short u16;
typedef unsigned int u32;
typedef unsigned long long u64;
typedef u32 u32x2 __attribute__((ext_vector_type(2)));
typedef u32 u32x4 __attribute__((ext_vector_type(4)));
typedef float f32x2 __attribute__((ext_vector_type(2)));
typedef float f32x4 __attribute__((ext_vector_type(4)));
typedef _Float16 f16x2 __attribute__((ext_vector_type(2)));
typedef __bf16 bf16x2 __attribute__((ext_vector_type(2)));
typedef __bf16 bf16x8 __attribute__((ext_vector_type(8)));
#define DI __device__ __forceinline__
#define SB() __builtin_amdgcn_sched_barrier(0)
#define MEMFENCE() asm volatile("" ::: "memory")

constexpr u32 MB2 = 2u << 20;
constexpr u32 OFF_E = 0, OFF_FLAG = 8192, OFF_WNY = 65536;
constexpr u32 OFF_A = MB2;
constexpr u32 OFF_U = OFF_A + MB2;
constexpr u32 OFF_WX = OFF_U + MB2;
constexpr u32 OFF_X3 = OFF_WX + MB2;
constexpr u32 OFF_H = OFF_X3 + MB2;
constexpr u32 OFF_W1 = OFF_H + 7340032;
constexpr u32 OFF_W2 = OFF_W1 + 3670016;
constexpr u32 WS_USED = OFF_W2 + 1048576;
constexpr u32 SENT = 0xFFFFFFFFu;
constexpr int SPIN_CAP = 1 << 15;

constexpr int LDS_SCR = 131072, LDS_STASH = LDS_SCR + 24576, LDS_TOTAL = 163840;

struct Params {
  const u16* x; const u16* wr; const u16* wi; const u16* wg; const u16* wu; const u16* wd; u16* out; u8* ws;
};

DI u32 pkh(float a, float b) { f32x2 v = {a, b}; return __builtin_bit_cast(u32, __builtin_convertvector(v, f16x2)); }
DI u32 pkb(float a, float b) { f32x2 v = {a, b}; return __builtin_bit_cast(u32, __builtin_convertvector(v, bf16x2)); }
template <bool BF> DI u32 pkc(float a, float b) { return BF ? pkb(a, b) : pkh(a, b); }
DI u32 pkh_n0(float a, float b, u32 after) { u32 r; asm("v_cvt_pk_f16_f32 %0, -%1, %2" : "=v"(r) : "v"(a), "v"(b), "v"(after)); return r; }
DI u32 pkb_n1(float a, float b, u32 after) { u32 r; asm("v_cvt_pk_bf16_f32 %0, %1, -%2" : "=v"(r) : "v"(a), "v"(b), "v"(after)); return r; }
DI float blo(u32 u) { return __uint_as_float(u << 16); }
DI float bhi(u32 u) { return __uint_as_float(u & 0xffff0000u); }
DI float bfr(float a) { return blo(pkb(a, a)); }
DI float relu(float a) { const int i = __float_as_int(a); return __int_as_float(i > 0 ? i : 0); }
DI u32 umax(u32 a, u32 b) { return a > b ? a : b; }
DI u32 max3(u32 a, u32 b, u32 c) { return umax(umax(a, b), c); }
DI u32 max4(u32 acc, u32x4 v) { return max3(max3(acc, v.x, v.y), v.z, v.w); }
template <bool ONE> DI u32 red3(u32 a, u32 b, u32 c) { return ONE ? (a & b & c) : (a | b | c); }
template <bool ONE> DI u32 red2(u32 a, u32 b) { return ONE ? (a & b) : (a | b); }
template <bool ONE> DI u32 red4(u32 acc, u32x4 v) { return red3<ONE>(red3<ONE>(acc, v.x, v.y), v.z, v.w); }
template <bool ONE> DI u32 red8(u32x4 a, u32x4 b) { return red4<ONE>(red3<ONE>(red2<ONE>(a.x, a.y), a.z, a.w), b); }
template <bool ONE, u32 BIT> DI bool stale(u32 r) { return ONE ? (r & BIT) == 0 : (r & BIT) != 0; }
template <u32 CLR, u32 SET> DI u32 mark(u32 v) { return (v & ~CLR) | SET; }
template <u32 CLR, u32 SET> DI u32x4 mark4(u32x4 v) { return u32x4{mark<CLR, SET>(v.x), mark<CLR, SET>(v.y), mark<CLR, SET>(v.z), mark<CLR, SET>(v.w)}; }
DI f32x2 lo2(f32x4 v) { return __builtin_shufflevector(v, v, 0, 1); }
DI f32x2 hi2(f32x4 v) { return __builtin_shufflevector(v, v, 2, 3); }
DI f32x2 swp(f32x2 v) { return __builtin_shufflevector(v, v, 1, 0); }
DI f32x4 mfma_b(u32x4 a, u32x4 b, f32x4 c) {
  return __builtin_amdgcn_mfma_f32_16x16x32_bf16(__builtin_bit_cast(bf16x8, a), __builtin_bit_cast(bf16x8, b), c, 0, 0, 0);
}
template <class T> DI void PIN(T& x) { asm volatile("" : "+v"(x)); }
template <class T> DI void PINS(T& x) { asm volatile("" : "+s"(x)); }
DI int opq(int x) { asm("" : "+s"(x)); return x; }
template <class T> DI void PINA(T& x) { asm volatile("" : "+a"(x)); }
template <class F, int... I> DI void static_for_(F&& f, std::integer_sequence<int, I...>) { (f(std::integral_constant<int, I>{}), ...); }
template <int N, class F> DI void static_for(F&& f) { static_for_(f, std::make_integer_sequence<int, N>{}); }

#define MM_(T) "v_mfma_f32_16x16x32_" T
template <bool BF> DI void mm4(f32x4& d0, f32x4& d1, f32x4& d2, f32x4& d3, u32x4 a0, u32x4 a1, u32x4 b0, u32x4 b1) {
#define MM4_(T) asm volatile("s_nop 1\n\t" MM_(T) " %0, %4, %6, 0\n\t" MM_(T) " %1, %4, %7, 0\n\t" MM_(T) " %2, %5, %6, 0\n\t" MM_(T) " %3, %5, %7, 0\n\ts_nop 5" \
               : "=&v"(d0), "=&v"(d1), "=&v"(d2), "=&v"(d3) : "v"(a0), "v"(a1), "v"(b0), "v"(b1))
  if constexpr (BF) MM4_("bf16"); else MM4_("f16");
}
template <bool BF, int TAIL, int AG = 0> DI void mm8(f32x4& d0, f32x4& d1, f32x4& d2, f32x4& d3, u32x4 a00, u32x4 b00, u32x4 a01, u32x4 b01, u32x4 a10,
                               u32x4 b10, u32x4 a11, u32x4 b11, u32x4 a20, u32x4 b20, u32x4 a21, u32x4 b21, u32x4 a30, u32x4 b30, u32x4 a31, u32x4 b31) {
#define MM8_(T, A, B) asm volatile("s_nop 1\n\t" MM_(T) " %0, %4, %5, 0\n\t" MM_(T) " %1, %8, %9, 0\n\t" MM_(T) " %0, %6, %7, %0\n\t" MM_(T) " %1, %10, %11, %1\n\t" \
               MM_(T) " %2, %12, %13, 0\n\t" MM_(T) " %3, %16, %17, 0\n\t" MM_(T) " %2, %14, %15, %2\n\t" MM_(T) " %3, %18, %19, %3\n\ts_nop %20"         \
               : "=&v"(d0), "=&v"(d1), "=&v"(d2), "=&v"(d3)                                                                                              \
               : A(a00), B(b00), A(a01), B(b01), A(a10), B(b10), A(a11), B(b11), A(a20), B(b20), A(a21), B(b21), A(a30), B(b30), A(a31), B(b31), "i"(TAIL))
  if constexpr (!BF) MM8_("f16", "v", "v");
  else if constexpr (AG == 0) MM8_("bf16", "v", "v");
  else if constexpr (AG == 1) MM8_("bf16", "a", "v");
  else MM8_("bf16", "v", "a");
}
template <bool BF, int AG> DI void mf0(f32x4& d, u32x4 a, u32x4 b) {
  if constexpr (!BF) asm volatile(MM_("f16") " %0, %1, %2, 0" : "=&v"(d) : "v"(a), "v"(b));
  else if constexpr (AG == 0) asm volatile(MM_("bf16") " %0, %1, %2, 0" : "=&v"(d) : "v"(a), "v"(b));
  else asm volatile(MM_("bf16") " %0, %1, %2, 0" : "=&v"(d) : "v"(a), "a"(b));
}
template <bool FIRST> DI void mmw(f32x4& d, const u32x4& a, const u32x4& b) {
  if constexpr (FIRST) asm volatile(MM_("bf16") " %0, %1, %2, 0" : "=&v"(d) : "a"(a), "v"(b));
  else asm volatile(MM_("bf16") " %0, %1, %2, %0" : "+v"(d) : "a"(a), "v"(b));
}
template <int O> DI void lda(u32x4& d, u32 addr) { asm volatile("ds_read_b128 %0, %1 offset:%2" : "=a"(d) : "v"(addr), "i"(O) : "memory"); }
template <int N> DI void lgwait() { asm volatile("s_waitcnt lgkmcnt(%0)" ::"i"(N) : "memory"); }
template <bool BF, int AG, int TAIL> DI void mf4(f32x4& d0, f32x4& d1, f32x4& d2, f32x4& d3, u32x4 a0, u32x4 b0, u32x4 a1, u32x4 b1, u32x4 a2,
                                               u32x4 b2, u32x4 a3, u32x4 b3) {
#define MF4_(T, B) asm volatile("s_nop 1\n\t" MM_(T) " %0, %4, %5, %0\n\t" MM_(T) " %1, %6, %7, %1\n\t" MM_(T) " %2, %8, %9, %2\n\t" MM_(T) " %3, %10, %11, %3\n\ts_nop %12" \
               : "+v"(d0), "+v"(d1), "+v"(d2), "+v"(d3) : "v"(a0), B(b0), "v"(a1), B(b1), "v"(a2), B(b2), "v"(a3), B(b3), "i"(TAIL))
  if constexpr (!BF) MF4_("f16", "v"); else if constexpr (AG == 0) MF4_("bf16", "v"); else MF4_("bf16", "a");
}
template <bool BF, bool CONJ, class MM> DI u32x4 cplx_under(f32x4 r, f32x4 i, const f32x2 (&c)[2], const f32x2 (&s)[2], MM&& mm) {
  asm volatile("s_nop 1");
  SB(); mm(std::integral_constant<int, 0>{}); SB();
  f32x2 a0 = lo2(i) * s[0], b0 = lo2(r) * s[0], a1 = hi2(i) * s[1];
  PIN(a0); PIN(b0); PIN(a1);
  SB(); mm(std::integral_constant<int, 1>{}); SB();
  f32x2 b1 = hi2(r) * s[1], re0 = CONJ ? lo2(r) * c[0] + a0 : lo2(r) * c[0] - a0, im0 = CONJ ? lo2(i) * c[0] - b0 : lo2(i) * c[0] + b0;
  PIN(b1); PIN(re0); PIN(im0);
  SB(); mm(std::integral_constant<int, 2>{}); SB();
  f32x2 re1 = CONJ ? hi2(r) * c[1] + a1 : hi2(r) * c[1] - a1, im1 = CONJ ? hi2(i) * c[1] - b1 : hi2(i) * c[1] + b1;
  u32 o0 = pkc<BF>(re0.x, im0.x);
  PIN(re1); PIN(im1); PIN(o0);
  SB(); mm(std::integral_constant<int, 3>{}); SB();
  return u32x4{o0, pkc<BF>(re0.y, im0.y), pkc<BF>(re1.x, im1.x), pkc<BF>(re1.y, im1.y)};
}
template <bool BF> DI u32x4 twiddle(f32x4 r, f32x4 i, const f32x2 (&c)[2], const f32x2 (&s)[2]) {
  const f32x2 re0 = lo2(r) * c[0] + lo2(i) * s[0], im0 = lo2(i) * c[0] - lo2(r) * s[0];
  const f32x2 re1 = hi2(r) * c[1] + hi2(i) * s[1], im1 = hi2(i) * c[1] - hi2(r) * s[1];
  return u32x4{pkc<BF>(re0.x, im0.x), pkc<BF>(re0.y, im0.y), pkc<BF>(re1.x, im1.x), pkc<BF>(re1.y, im1.y)};
}
DI u32x4 cmul_b(f32x4 r, f32x4 i, const f32x2 (&c)[2], const f32x2 (&s)[2]) {
  const f32x2 re0 = lo2(r) * c[0] - lo2(i) * s[0], im0 = lo2(i) * c[0] + lo2(r) * s[0];
  const f32x2 re1 = hi2(r) * c[1] - hi2(i) * s[1], im1 = hi2(i) * c[1] + hi2(r) * s[1];
  return u32x4{pkb(re0.x, im0.x), pkb(re0.y, im0.y), pkb(re1.x, im1.x), pkb(re1.y, im1.y)};
}
DI float cosr(float a) { return __builtin_amdgcn_cosf(a); }
DI float sinr(float a) { return __builtin_amdgcn_sinf(a); }
template <int CTRL> DI float dppf(float x) {
  return __builtin_bit_cast(float, __builtin_amdgcn_mov_dpp(__builtin_bit_cast(int, x), CTRL, 0xf, 0xf, true));
}
DI f32x2 bcast2(float x) {
  auto t = __builtin_amdgcn_permlane32_swap(__float_as_uint(x), __float_as_uint(x), false, false);
  return f32x2{__uint_as_float(t[0]), __uint_as_float(t[1])};
}
DI void ln_coef(f32x2 S, f32x2 Q, float ch, f32x2& R, f32x2& O) {
  auto t = __builtin_amdgcn_permlane32_swap(__float_as_uint(S.x), __float_as_uint(S.y), false, false);
  auto u = __builtin_amdgcn_permlane32_swap(__float_as_uint(Q.x), __float_as_uint(Q.y), false, false);
  const float s = __uint_as_float(t[0]) + __uint_as_float(t[1]), q = __uint_as_float(u[0]) + __uint_as_float(u[1]);
  auto z = __builtin_amdgcn_permlane16_swap(__float_as_uint(s), __float_as_uint(q), false, false);
  float x = __uint_as_float(z[0]) + __uint_as_float(z[1]);
  x += dppf<0xB1>(x);
  x += dppf<0x4E>(x);
  x += dppf<0x141>(x);
  x += dppf<0x140>(x);
  auto y = __builtin_amdgcn_permlane16_swap(__float_as_uint(x), __float_as_uint(x), false, false);
  const float md = __uint_as_float(y[0]) * (1.0f / 512.0f);
  const float var = relu(__uint_as_float(y[1]) * (1.0f / 512.0f) - md * md);
  const float r = __builtin_amdgcn_rcpf(bfr(__builtin_amdgcn_sqrtf(bfr(bfr(var) + 1e-5f))));
  const float o = (ch - bfr(ch + md)) * r;
  R = bcast2(r); O = bcast2(o);
}
DI float first_lane(float x) { return __uint_as_float(__builtin_amdgcn_readfirstlane(__float_as_uint(x))); }
DI int row_of_pos(int pos, int j) {
  const int c = 8 * (pos >> 7) + 2 * ((pos >> 3) & 3) + ((pos >> 5) & 1), q = 8 * ((pos >> 6) & 1) + (pos & 7);
  return 32 * (2 * c + (j & 1)) + 2 * q + (j >> 1);
}

template <int I> DI void ld4(u32x4& d, u32 v, const u8* s) { asm volatile("global_load_dwordx4 %0, %1, %2 offset:%3 sc1" : "=v"(d) : "v"(v), "s"(s), "i"(I) : "memory"); }
template <int I> DI void ld4a(u32x4& d, u32 v, const u8* s) { asm volatile("global_load_dwordx4 %0, %1, %2 offset:%3 sc1" : "=a"(d) : "v"(v), "s"(s), "i"(I) : "memory"); }
template <int I> DI void ld4r(u32x4& d, u32 v, const u8* s) { asm volatile("global_load_dwordx4 %0, %1, %2 offset:%3 sc1" : "+v"(d) : "v"(v), "s"(s), "i"(I) : "memory"); }
template <int I> DI void ld2(u32x2& d, u32 v, const u8* s) { asm volatile("global_load_dwordx2 %0, %1, %2 offset:%3 sc1" : "=v"(d) : "v"(v), "s"(s), "i"(I) : "memory"); }
template <int I> DI void ld2r(u32x2& d, u32 v, const u8* s) { asm volatile("global_load_dwordx2 %0, %1, %2 offset:%3 sc1" : "+v"(d) : "v"(v), "s"(s), "i"(I) : "memory"); }
template <int I> DI void ld2p(u32x2& d, u32 v, const u8* s) { asm volatile("global_load_dwordx2 %0, %1, %2 offset:%3" : "=v"(d) : "v"(v), "s"(s), "i"(I) : "memory"); }
DI void touch(u32& d, u32 v, const u8* s) { asm volatile("global_load_dword %0, %1, %2 sc1" : "+v"(d) : "v"(v), "s"(s) : "memory"); }
template <int I> DI void ld1p(u32& d, u32 v, const u8* s) { asm volatile("global_load_dword %0, %1, %2 offset:%3" : "=v"(d) : "v"(v), "s"(s), "i"(I) : "memory"); }
DI void ld1s(u32& d, u32 v, const u8* s) { asm volatile("global_load_ushort %0, %1, %2" : "=v"(d) : "v"(v), "s"(s) : "memory"); }
template <int N> DI void vmwait() { static_assert(N >= 0 && N < 64, ""); asm volatile("s_waitcnt vmcnt(%0)" ::"i"(N) : "memory"); }
DI void set_m0(u32 ldsaddr) { asm volatile("s_mov_b32 m0, %0" ::"s"(ldsaddr) : "memory"); }
template <int I> DI void dma1(u32 voff, const u8* src) {
  asm volatile("global_load_lds_dwordx4 %0, %1 offset:%2 sc1" ::"v"(voff), "s"(src), "i"(I) : "memory");
}
DI void dma4(u32 voff, const u8* src, u32 ldsaddr) {
  asm volatile("s_mov_b32 m0, %2\n\ts_nop 0\n\tglobal_load_lds_dwordx4 %0, %1 sc1\n\tglobal_load_lds_dwordx4 %0, %1 offset:1024 sc1\n\t"
               "global_load_lds_dwordx4 %0, %1 offset:2048 sc1\n\tglobal_load_lds_dwordx4 %0, %1 offset:3072 sc1" ::"v"(voff), "s"(src), "s"(ldsaddr) : "memory");
}
#define RETRY(COND, ...)                                                      \
  do {                                                                        \
    bool bad_ = (COND);                                                       \
    if (__builtin_expect(__any(bad_), 0)) {                                   \
      _Pragma("nounroll") for (int sp_ = 0; sp_ < SPIN_CAP; ++sp_) {          \
        __VA_ARGS__                                                           \
        bad_ = (COND);                                                        \
        if (!__any(bad_)) break;                                              \
      }                                                                       \
    }                                                                         \
  } while (0)
DI void bar() { asm volatile("s_waitcnt lgkmcnt(0)\n\ts_barrier" ::: "memory"); }
DI void bar_nw() { asm volatile("s_barrier" ::: "memory"); }

struct RowC { u32x4 b1re, b1im, b2re[2][2], b2im[2][2]; f32x2 twc[2][2], tws[2][2]; };
template <bool INV> DI void pair_cs(float c, float s, u32& re, u32& im) {
  if constexpr (INV) { im = pkb(s, c); re = pkb_n1(c, s, im); } else { re = pkh(c, s); im = pkh_n0(s, c, re); }
}
template <bool AG = false> DI void sign4(u32x4 base, u32 lm, u32x4& f00, u32x4& f01, u32x4& f10, u32x4& f11) {
  f00 = base;
  f01 = base ^ lm;
  f10 = base ^ u32x4{0u, 0x80008000u, 0u, 0x80008000u};
  f11 = f10 ^ lm;
  if constexpr (AG) { PINA(f00); PINA(f01); PINA(f10); PINA(f11); } else { PIN(f00); PIN(f01); PIN(f10); PIN(f11); }
}
template <bool INV, int PART> DI void row_consts(RowC& k, int m, int g) {
  const float fm = (float)m, fg = (float)(4 * g);
  if constexpr (PART == 0) {
    u32x4 br, bi;
#pragma unroll
    for (int i = 0; i < 4; ++i) {
      const float a = (fg + (float)i) * (fm * (1.0f / 16.0f));
      u32 x, y;
      pair_cs<INV>(cosr(a), sinr(a), x, y); k.b1re[i] = x; k.b1im[i] = y;
      const float a2 = (fg + (float)i) * (fm * (1.0f / 32.0f));
      pair_cs<INV>(cosr(a2), sinr(a2), x, y); br[i] = x; bi[i] = y;
    }
    PIN(k.b1re); PIN(k.b1im);
    const u32 lm = (m & 1) ? 0x80008000u : 0u;
    sign4(br, lm, k.b2re[0][0], k.b2re[0][1], k.b2re[1][0], k.b2re[1][1]);
    sign4(bi, lm, k.b2im[0][0], k.b2im[0][1], k.b2im[1][0], k.b2im[1][1]);
  } else {
#pragma unroll
    for (int e = 0; e < 2; ++e)
#pragma unroll
      for (int i = 0; i < 4; ++i) {
        const float a = (fg + (float)(16 * e + i)) * (fm * (INV ? -1.0f / 512.0f : 1.0f / 512.0f));
        k.twc[e][i >> 1][i & 1] = cosr(a); k.tws[e][i >> 1][i & 1] = sinr(a);
      }
#pragma unroll
    for (int e = 0; e < 2; ++e) { PIN(k.twc[e][0]); PIN(k.twc[e][1]); PIN(k.tws[e][0]); PIN(k.tws[e][1]); }
  }
}
template <bool INV, int TAIL, class H> DI void row_fft(const RowC& k, u32x4 a0, u32x4 a1, f32x4& dr0, f32x4& di0, f32x4& dr1, f32x4& di1, H&& mid) {
  f32x4 tr0, ti0, tr1, ti1;
  mm4<INV>(tr0, ti0, tr1, ti1, a0, a1, k.b1re, k.b1im);
  u32x4 p0 = twiddle<INV>(tr0, ti0, k.twc[0], k.tws[0]);
  PIN(p0);
  mid();
  const u32x4 p1 = cplx_under<INV, true>(tr1, ti1, k.twc[1], k.tws[1], [&](auto I) {
    constexpr int i = decltype(I)::value;
    mf0<INV, 0>(i == 0 ? dr0 : i == 1 ? di0 : i == 2 ? dr1 : di1, p0, i == 0 ? k.b2re[0][0] : i == 1 ? k.b2im[0][0] : i == 2 ? k.b2re[1][0] : k.b2im[1][0]);
  });
  mf4<INV, 0, TAIL>(dr0, di0, dr1, di1, p1, k.b2re[0][1], p1, k.b2im[0][1], p1, k.b2re[1][1], p1, k.b2im[1][1]);
}

struct FwdP { u32 iw, ir, ow, ap0, ap1, ar0, ar1, zo; f32x2 cB, cD, eA, eB, eC, eD; };
DI void fwd_pre(FwdP& p, int tid, int w, int pos) {
  const int m = tid & 15, g = (tid >> 4) & 3, ph = tid >> 1, fq = ph ? 512 - ph : 256, fq1 = 384 - ph;
  p.iw = w * 5120 + ((16 * (m & 1) + 4 * g) * 20 + (m >> 1)) * 4; p.ir = w * 5120 + (m * 20 + 4 * g) * 4;
  p.ap0 = (ph + (ph >> 4) * 4) * 4; p.ap1 = p.ap0 + 640; p.ar0 = (fq + (fq >> 4) * 4) * 4; p.ar1 = (fq1 + (fq1 >> 4) * 4) * 4;
  p.zo = OFF_A + pos * 8192 + tid * 16;
  const float sc = 1.0f / 64.0f, sg = (tid & 1) ? -sc : sc;
  p.cB = f32x2{sg, -sg}; p.cD = f32x2{sg, sg};
  p.eA = ph ? f32x2{sc, sc} : f32x2{2 * sc, 0.f}; p.eB = ph ? p.cB : f32x2{2 * sg, 0.f};
  p.eC = ph ? f32x2{sc, -sc} : f32x2{0.f, 2 * sg}; p.eD = ph ? p.cD : f32x2{0.f, 2 * sc};
  PIN(p.iw); PIN(p.ir); PIN(p.ap0); PIN(p.ap1); PIN(p.ar0); PIN(p.ar1); PIN(p.zo);
  PIN(p.cB); PIN(p.cD); PIN(p.eA); PIN(p.eB); PIN(p.eC); PIN(p.eD);
}
struct ColC {
  u32x4 b1re[2], b1im[2], a2re[2][2], a2im[2][2], b3re[2][2], b3im[2][2], a4re[2][2], a4im[2][2];
  f32x2 t1c[2][2], t1s[2][2], t2c[2][2], t2s[2][2];
};
template <int PART> DI void col_consts(ColC& k, int m, int g, int w) {
  const int pi_ = w >> 1, kt = w & 1;
  const float fm = (float)m, fg = (float)(4 * g);
  const u32 lm = (m & 1) ? 0x80008000u : 0u;
  if constexpr (PART == 0) {
    const float fk = (float)(2 * (16 * kt + m) + pi_);
#pragma unroll
    for (int u = 0; u < 2; ++u) {
#pragma unroll
      for (int r = 0; r < 4; ++r) {
        const float n = fg + (float)(16 * u + r), a = n * (fk * (1.0f / 64.0f)), a2 = n * (fk * (1.0f / 2048.0f));
        u32 x, y;
        pair_cs<false>(cosr(a), sinr(a), x, y); k.b1re[u][r] = x; k.b1im[u][r] = y;
        k.t1c[u][r >> 1][r & 1] = cosr(a2); k.t1s[u][r >> 1][r & 1] = sinr(a2);
      }
      PIN(k.b1re[u]); PIN(k.b1im[u]); PIN(k.t1c[u][0]); PIN(k.t1c[u][1]); PIN(k.t1s[u][0]); PIN(k.t1s[u][1]);
    }
  } else if constexpr (PART == 1) {
    u32x4 fr, fi, ir, ii;
#pragma unroll
    for (int r = 0; r < 4; ++r) {
      const float a = (fg + (float)r) * (fm * (1.0f / 32.0f)), c = cosr(a), s = sinr(a);
      u32 x, y;
      pair_cs<false>(c, s, x, y); fr[r] = x; fi[r] = y;
      pair_cs<true>(c, s, x, y); ir[r] = x; ii[r] = y;
    }
    sign4(fr, lm, k.a2re[0][0], k.a2re[0][1], k.a2re[1][0], k.a2re[1][1]);
    sign4(fi, lm, k.a2im[0][0], k.a2im[0][1], k.a2im[1][0], k.a2im[1][1]);
    sign4(ir, lm, k.b3re[0][0], k.b3re[0][1], k.b3re[1][0], k.b3re[1][1]);
    sign4(ii, lm, k.b3im[0][0], k.b3im[0][1], k.b3im[1][0], k.b3im[1][1]);
  } else if constexpr (PART == 2) {
    const float kb = (float)(32 * kt + pi_) + 2.0f * fg;
#pragma unroll
    for (int u = 0; u < 2; ++u) {
#pragma unroll
      for (int r = 0; r < 4; ++r) {
        const float a = (kb + (float)(2 * r)) * ((fm + (float)(16 * u)) * (-1.0f / 2048.0f));
        k.t2c[u][r >> 1][r & 1] = cosr(a); k.t2s[u][r >> 1][r & 1] = sinr(a);
      }
      PIN(k.t2c[u][0]); PIN(k.t2c[u][1]); PIN(k.t2s[u][0]); PIN(k.t2s[u][1]);
    }
  } else {
    const float fn = (fm + (float)(16 * (w >> 1))) * (1.0f / 64.0f);
#pragma unroll
    for (int u = 0; u < 2; ++u) {
#pragma unroll
      for (int r = 0; r < 4; ++r) {
        const float a = (2.0f * fg + (float)(2 * r + u)) * fn;
        u32 x, y;
        pair_cs<true>(cosr(a), sinr(a), x, y); k.a4re[u][0][r] = x; k.a4im[u][0][r] = y;
      }
      k.a4re[u][1] = k.a4re[u][0] ^ lm; k.a4im[u][1] = k.a4im[u][0] ^ lm;
      PIN(k.a4re[u][0]); PIN(k.a4re[u][1]); PIN(k.a4im[u][0]); PIN(k.a4im[u][1]);
    }
  }
}

DI void transpose8x4(const u32x2 (&v)[8], u32x4 (&o)[4]) {
#pragma unroll
  for (int jj = 0; jj < 4; ++jj) {
    o[0][jj] = __builtin_amdgcn_perm(v[2 * jj + 1].x, v[2 * jj].x, 0x05040100u);
    o[1][jj] = __builtin_amdgcn_perm(v[2 * jj + 1].x, v[2 * jj].x, 0x07060302u);
    o[2][jj] = __builtin_amdgcn_perm(v[2 * jj + 1].y, v[2 * jj].y, 0x05040100u);
    o[3][jj] = __builtin_amdgcn_perm(v[2 * jj + 1].y, v[2 * jj].y, 0x07060302u);
  }
}
template <int H> DI void prep_load1(const Params& P, int wt, int lane, u32x2 (&v)[8]) {
  const u32 vo = (lane < 56 ? lane : 55) * 8, vo2 = vo + 7168;
  const int piece = (wt >> 3) & 63;
  const u8* src = (const u8*)((wt >> 9) ? P.wu : P.wg) + opq(wt & 7) * 448 + opq(4 * piece) * 3584;
  static_for<4>([&](auto J) { constexpr int j = decltype(J)::value; ld2p<(j & 1) * 3584>(v[4 * H + j], j & 2 ? vo2 : vo, src + H * (256 * 3584)); });
}
DI u32 prep_dst1(int wt, int tile) {
  const int piece = (wt >> 3) & 63, mat = wt >> 9, hi = tile >= 7, p = tile - 7 * hi, ks = piece >> 2;
  const u32 frag = (p < 3 ? ks * 6 + 2 * p : p < 5 ? 96 + ks * 4 + 2 * (p - 3) : 160 + ks * 4 + 2 * (p - 5)) + mat;
  return OFF_W1 + ((2 * (wt & 7) + hi) * 224 + frag) * 1024 + (piece & 3) * 256;
}
DI int w2_row(int wv, int slot, int rot, int h) {
  const int f0 = slot < 4 ? 112 * (4 * wv + ((slot + rot) & 3)) : slot < 6 ? 112 * (4 * wv + 2 * ((slot ^ rot) & 1)) + 32
               : slot < 10 ? 112 * (4 * wv + ((slot - 6 + rot) & 3)) + 48 : 112 * (4 * wv + ((slot - 10 + rot) & 3)) + 80;
  return f0 + h * ((slot == 4 || slot == 5) ? 112 : 16);
}
DI void dma_rows(u32 voff, const u8* h0, const u8* h1, u32 ldsaddr) {
  const u8* h0b = h0 + 8192 - 1024;
  const u8* h1a = h1 - 2048;
  const u8* h1b = h1 + 8192 - 3072;
  asm volatile("s_mov_b32 m0, %5\n\ts_nop 0\n\tglobal_load_lds_dwordx4 %0, %1\n\tglobal_load_lds_dwordx4 %0, %2 offset:1024\n\t"
               "global_load_lds_dwordx4 %0, %3 offset:2048\n\tglobal_load_lds_dwordx4 %0, %4 offset:3072"
               ::"v"(voff), "s"(h0), "s"(h0b), "s"(h1a), "s"(h1b), "s"(ldsaddr) : "memory");
}
template <int O> DI void tr_read(u32x2& lo, u32x2& hi, u32 addr) {
  asm volatile("ds_read_b64_tr_b16 %0, %2 offset:%3\n\tds_read_b64_tr_b16 %1, %2 offset:%4" : "=&v"(lo), "=&v"(hi) : "v"(addr), "i"(O), "i"(O + 2048) : "memory");
}
DI u32 tr_addr(int lane, int c) { return (lane >> 2) * 128 + (((lane & 3) + 4 * (c >> 1)) ^ (2 * ((lane >> 2) & 1) + 4 * ((lane >> 3) & 1))) * 16 + (c & 1) * 8; }

constexpr int NTOK0 = 12, NRES = 32;
constexpr int g1_xe(int L) { return L == 0 ? 164 : 157; }
constexpr int g1_ne(int L) { return (224 - g1_xe(L)) / 7 * 3 + ((224 - g1_xe(L)) % 7 > 0) + ((224 - g1_xe(L)) % 7 > 2) + ((224 - g1_xe(L)) % 7 > 5); }
constexpr int g1_job(int L, int x) {
  if (((x >= 34 && x < 96) && (x & 3) == 2) || (x >= 98 && x < 114 && !(x & 1))) return 2;
  if ((x < 34 && !(x & 1)) || (x < 64 && (x & 3) == 0)) return 1;
  if (L == 1 && x >= 47 && x < 60 && (x & 3) == 3) return 6;
  if (x >= g1_xe(L)) return ((x - g1_xe(L)) % 7 == 0 || (x - g1_xe(L)) % 7 == 2 || (x - g1_xe(L)) % 7 == 5) ? 4 : 0;
  if (L == 0 && ((x >= 48 && x < 98 && (x & 3) == 0) || (x >= 115 && x % 3 == 0) || (x < 34 && x % 8 == 1))) return 3;
  return 0;
}
constexpr int NEPI1 = 75, NEPI2 = 50, EPI1_X = 99;
struct G1Sched {
  int job[224], idx[224], epi[225], nst[224], cum[225], pos[7][64], cnt[7];
  constexpr G1Sched(int L) : job{}, idx{}, epi{}, nst{}, cum{}, pos{}, cnt{} {
    cnt[1] = NTOK0;
    for (int i = 0; i < NTOK0; ++i) pos[1][i] = i;
    cum[0] = NTOK0;
    for (int x = 0; x < 224; ++x) {
      int j = g1_job(L, x);
      if (j == 1 && cnt[1] >= 32) j = L == 0 ? 3 : 0;
      if ((j == 3 && cnt[3] >= NRES)) j = 0;
      job[x] = j; idx[x] = cnt[j];
      if (x == 162) epi[x] = NEPI1;
      const bool on = (x >= EPI1_X && x < 160 && epi[x] < NEPI1) || x >= 162;
      epi[x + 1] = on ? epi[x] + (x < 114 ? 2 - (x & 1 ? 0 : 1) : 2) : epi[x];
      if (x < 160 && epi[x + 1] > NEPI1) epi[x + 1] = NEPI1;
      for (int n = epi[x]; n < epi[x + 1]; ++n)
        nst[x] += n < NEPI1 ? (n == 24 || n == 49 || n == 74) : (n < NEPI1 + NEPI2 && (n - NEPI1) % 25 == 24);
      if (j) pos[j][cnt[j]++] = cum[x] + nst[x];
      cum[x + 1] = cum[x] + nst[x] + (j ? 1 : 0);
    }
  }
};
constexpr G1Sched G1S[2] = {G1Sched(0), G1Sched(1)};
static_assert(G1S[0].cnt[1] == 32 && G1S[0].cnt[2] == 24 && G1S[0].cnt[3] == NRES && G1S[0].cnt[4] == g1_ne(0) && G1S[1].cnt[4] == g1_ne(1), "");
static_assert(G1S[1].cnt[6] == 4 && G1S[1].pos[6][3] < G1S[1].pos[2][7], "");
static_assert(G1S[0].idx[66] == 8 && G1S[0].idx[98] == 16 && G1S[0].epi[113] <= 24 && G1S[1].epi[113] <= 24, "");
static_assert(G1S[0].epi[152] == NEPI1 && G1S[0].epi[204] >= NEPI1 + NEPI2, "");
constexpr int clamp63(int n) { return n > 63 ? 63 : n; }
constexpr int g1_wait(int L, int x, int job, int idx) { return clamp63(G1S[L].cum[x] - G1S[L].pos[job][idx] - 1); }

#define FRESH()                                                                                                   \
  int tid = threadIdx.x, b = blockIdx.x, w = w_;                                                                   \
  u8* ws = P.ws;                                                                                                   \
  asm volatile("" : "+v"(tid), "+s"(b), "+s"(w), "+s"(ws));                                                        \
  const int lane = tid & 63, m = lane & 15, g = lane >> 4, half2 = b & 1, ct = b >> 5;                             \
  const int G = ((b >> 1) & 3) * 4 + ((b >> 3) & 3), member = half2 * 8 + ct, pos = G * 16 + member;               \
  const u32 l16 = lane * 16, t16 = tid * 16, xo = 32 * m + 8 * g;                                                  \
  auto fill_h = [&](u32 base, int q) { st4(sent4, base + (4 * G + w) * 114688 + member * 7168 + q * 1024 + l16); };          \
  auto fill_rows = [&](u32 base) { st4(sent4, base + pos * 8192 + t16); st4(sent4, base + pos * 8192 + 4096 + t16); }; \
  auto ring4 = [&](int q) {                                   \
    const u32 o = (q >> 1) * 32768 + w * 8192 + (q & 1) * 4096;                                                    \
    dma4(l16, ws + OFF_W1 + member * 229376 + o, ldsbase + o);                                                     \
  };                                                                                                               \
      \
  auto w2dma = [&](int slot, int unit) {                                                                           \
    const u8* wd = (const u8*)P.wd + ct * 128;                                                                     \
    dma_rows((lane >> 3) * 1024 + ((lane & 7) ^ (2 * ((lane >> 3) & 1) + 4 * ((lane >> 4) & 1))) * 16,              \
             wd + opq(w2_row(w, slot, ct, 0)) * 1024, wd + opq(w2_row(w, slot, ct, 1)) * 1024, ldsbase + w * 32768 + unit * 4096); \
  };                                                                                                               \
  (void)w2dma;                                                                                                     \
  (void)m; (void)g; (void)half2; (void)ct; (void)l16; (void)t16; (void)xo; (void)fill_h; (void)fill_rows; (void)ring4

__global__ __launch_bounds__(256, 1) void k_all(Params P) {
  extern __shared__ __attribute__((aligned(16))) u8 lds[];
  const int w_ = __builtin_amdgcn_readfirstlane(threadIdx.x >> 6);
  const __amdgpu_buffer_rsrc_t rs = __builtin_amdgcn_make_buffer_rsrc(P.ws, 0, WS_USED, 0x00020000);
  const u32 ldsbase = (u32)(size_t)(__attribute__((address_space(3))) u8*)lds;
  const u32x4 sent4 = {SENT, SENT, SENT, SENT};
  auto st4 = [&](u32x4 v, u32 off) { __builtin_amdgcn_raw_buffer_store_b128(v, rs, off, 0, 16); };
  auto st2 = [&](u32x2 v, u32 off) { __builtin_amdgcn_raw_buffer_store_b64(v, rs, off, 0, 16); };
  u32x2 xl[2], xh[2], pv0[8];
  u32 wfr[8], wfi[8], wn[8];
  f32x2 xv[8], xc;
  float xch;
  FwdP fp;
  u32 pd[4];
  u32x2 ep;
  u32 cpf = 0, tw[4];
  u32x4 w2a[14][4];
  RowC rc;
  ColC cc;
  {
    FRESH();
    const u8* xp = (const u8*)P.x + row_of_pos(pos, w) * 1024;
    ld2p<0>(xl[0], xo, xp); ld2p<512>(xl[1], xo, xp); ld2p<0>(xh[0], xo, xp + 1024 * 1024); ld2p<512>(xh[1], xo, xp + 1024 * 1024);
    { const u32 z = 0; ld2<0>(ep, z, ws); }
    {
      const u8* pc;
      u32 len;
      asm volatile("s_getpc_b64 %0\n.Lgf_begin:\n\ts_mov_b32 %1, .Lgf_end-.Lgf_begin" : "=s"(pc), "=s"(len));
      const u32 o = (((b >> 3) * 4 + w) * 8 + (lane & 7)) * 128;
      touch(cpf, o < len ? o : 0, pc);
    }
    {
      const u32 z = 0;
      ld1p<0>(tw[0], z, (const u8*)P.wr); ld1p<0>(tw[1], z, (const u8*)P.wi);
      const int wt = b * 4 + w, r8 = lane >> 3;
      ld1p<0>(tw[2], z, (const u8*)((wt >> 9) ? P.wu : P.wg)); (void)r8;
    }
    fill_rows(OFF_A); fill_rows(OFF_WX);
    if (tid < 8) st4(sent4, OFF_WNY + tid * 4096 + pos * 16);
    SB();
    row_consts<false, 0>(rc, m, g);
    row_consts<false, 1>(rc, m, g);
    SB();
    const int kb = 2 * (16 * ((pos >> 6) & 1) + (pos & 15)) + (pos >> 7) + 256 * ((pos >> 4) & 3);
    static_for<8>([&](auto J) {
      constexpr int j = decltype(J)::value;
      const u32 o = tid * 2 + (kb + 64 * (16 * (j >> 2) + (j & 3))) * 514;
      ld1s(wfr[j], o, (const u8*)P.wr); ld1s(wfi[j], o, (const u8*)P.wi);
    });
    {
      const int k = kb + 64 * (16 * ((lane >> 2) & 1) + (lane & 3)), km = (2048 - k) & 2047;
      const u32 o = k * 514, om = km * 514;
      ld1s(wn[0], o, (const u8*)P.wr); ld1s(wn[1], o, (const u8*)P.wi); ld1s(wn[2], om, (const u8*)P.wr); ld1s(wn[3], om, (const u8*)P.wi);
      ld1s(wn[4], o, (const u8*)P.wr + 512); ld1s(wn[5], o, (const u8*)P.wi + 512); ld1s(wn[6], om, (const u8*)P.wr + 512); ld1s(wn[7], om, (const u8*)P.wi + 512);
    }
    ld1p<0>(tw[3], (lane < 14 ? lane : 0) * 128, (const u8*)P.wd + opq(b * 4 + w) * 1792);
    fwd_pre(fp, tid, w, pos);
#pragma unroll
    for (int x = 0; x < 4; ++x) pd[x] = x < 3 || g < 2 ? prep_dst1(b * 4 + w, 4 * x + g) + m * 16 : WS_USED;
#pragma unroll
    for (int x = 0; x < 4; ++x) PIN(pd[x]);
    SB();
    vmwait<1 + 4 + 4 + 24>();
    PIN(ep); PIN(xl[0]); PIN(xl[1]); PIN(xh[0]); PIN(xh[1]);
    { const u64 e = (((u64)(u32)__builtin_amdgcn_readfirstlane(ep.y) << 32) | (u32)__builtin_amdgcn_readfirstlane(ep.x)) + 1; ep = u32x2{(u32)e, (u32)(e >> 32)}; }
#pragma unroll
    for (int t = 0; t < 2; ++t)
#pragma unroll
      for (int p = 0; p < 2; ++p) {
        xv[4 * t + 2 * p] = f32x2{blo(xl[t][p]), blo(xh[t][p])};
        xv[4 * t + 2 * p + 1] = f32x2{bhi(xl[t][p]), bhi(xh[t][p])};
      }
    xc = f32x2{first_lane(xv[0].x), first_lane(xv[0].y)};
    xch = lane < 32 ? xc.x : xc.y;
#pragma unroll
    for (int i = 0; i < 8; ++i) xv[i] -= xc;
  }
  auto layer = [&](auto LC) {
    constexpr int L = decltype(LC)::value;
    {
      FRESH();
      const f32x2 S = ((xv[0] + xv[1]) + (xv[2] + xv[3])) + ((xv[4] + xv[5]) + (xv[6] + xv[7]));
      f32x2 q0 = xv[0] * xv[0], q1 = xv[1] * xv[1];
#pragma unroll
      for (int i = 2; i < 8; i += 2) { q0 += xv[i] * xv[i]; q1 += xv[i + 1] * xv[i + 1]; }
      if constexpr (L == 0) { SB(); fill_rows(OFF_U); SB(); }
      f32x2 R, O;
      ln_coef(S, q0 + q1, xch, R, O);
      u8* img = lds + LDS_SCR;
      if constexpr (L == 0) {
        SB(); vmwait<27>(); PIN(cpf); PIN(tw[0]); PIN(tw[1]); PIN(tw[2]);
        if (lane == 0) st2(ep, OFF_FLAG + (w * 256 + pos) * 8);
        prep_load1<0>(P, b * 4 + w, lane, pv0); SB();
      }
#pragma unroll
      for (int i = 0; i < 8; ++i) {
        const f32x2 v = xv[i] * R + O;
        *(u32*)(img + fp.iw + ((i & 3) * 20 + 8 * (i >> 2)) * 4) = pkh(v.x, v.y);
      }
      const u32x4 a0 = *(const u32x4*)(img + fp.ir), a1 = *(const u32x4*)(img + fp.ir + 1280);
      if constexpr (L == 0) prep_load1<1>(P, b * 4 + w, lane, pv0); else ring4(1);
      const float* p0 = (const float*)(img + fp.ap0);
      const float* p1 = (const float*)(img + fp.ap1);
      const float* r0 = (const float*)(img + fp.ar0);
      const float* r1 = (const float*)(img + fp.ar1);
      const f32x2 cA = {1.0f / 64.0f, 1.0f / 64.0f}, cC = {1.0f / 64.0f, -1.0f / 64.0f}, cB = fp.cB, cD = fp.cD, eA = fp.eA, eB = fp.eB, eC = fp.eC, eD = fp.eD;
      const u32 zo = fp.zo;
      f32x4 dr0, di0, dr1, di1;
      row_fft<false, 6>(rc, a0, a1, dr0, di0, dr1, di1, [] {});
      SB();
      float* op = (float*)(img + fp.ir);
      *(f32x4*)(op) = dr0; *(f32x4*)(op + 640) = di0;
      SB();
      *(f32x4*)(op + 320) = dr1; *(f32x4*)(op + 960) = di1;
      if constexpr (L == 0) { w2dma(6 + half2 * 4 + (G >> 2), 6); vmwait<13>(); PIN(tw[3]); }
      bar();
      f32x2 pa[4], qa[4], pb[4], qb[4];
#pragma unroll
      for (int j = 0; j < 4; ++j) {
        pa[j] = f32x2{p0[j * 1280], p0[j * 1280 + 640]}; qa[j] = f32x2{r0[j * 1280], r0[j * 1280 + 640]};
      }
#pragma unroll
      for (int j = 0; j < 4; ++j) {
        pb[j] = f32x2{p1[j * 1280], p1[j * 1280 + 640]}; qb[j] = f32x2{r1[j * 1280], r1[j * 1280 + 640]};
      }
      if constexpr (L == 0) w2dma(0, 0);
      SB();
      u32x4 o;
#pragma unroll
      for (int j = 0; j < 4; ++j) {
        const f32x2 v = eD * swp(qa[j]) + (eC * qa[j] + (eB * swp(pa[j]) + eA * pa[j]));
        o[j] = mark<(L == 0 ? 0x10001u : 0u), (L == 0 ? 0u : 0x10000u)>(pkh(v.x, v.y));
      }
      st4(o, zo);
#pragma unroll
      for (int j = 0; j < 4; ++j) {
        const f32x2 v = cD * swp(qb[j]) + (cC * qb[j] + (cB * swp(pb[j]) + cA * pb[j]));
        o[j] = mark<(L == 0 ? 0x10001u : 0u), (L == 0 ? 0u : 0x10000u)>(pkh(v.x, v.y));
      }
      st4(o, zo + 4096);
      SB();
      if constexpr (L == 1) {
#pragma unroll
        for (int t = 0; t < 2; ++t)
#pragma unroll
          for (int p = 0; p < 2; ++p) {
            const f32x2 v0 = xv[4 * t + 2 * p] + xc, v1 = xv[4 * t + 2 * p + 1] + xc;
            xl[t][p] = pkb(v0.x, v1.x); xh[t][p] = pkb(v0.y, v1.y);
          }
        PIN(xl[0]); PIN(xl[1]); PIN(xh[0]); PIN(xh[1]);
      }
    }
    if constexpr (L == 0) {
      FRESH();
#pragma unroll
      for (int j = 0; j < 8; ++j) { PIN(wfr[j]); PIN(wfi[j]); PIN(wn[j]); }
      u8* bw = lds + w * 32768 + 28672;
      u32x4 v0, v1;
#pragma unroll
      for (int j = 0; j < 4; ++j) { v0[j] = wfr[j] | (wfi[j] << 16); v1[j] = wfr[4 + j] | (wfi[4 + j] << 16); }
      *(u32x4*)(bw + lane * 32) = v0; *(u32x4*)(bw + lane * 32 + 16) = v1;
      st4(*(const u32x4*)(bw + l16), OFF_WX + pos * 8192 + w * 2048 + l16);
      st4(*(const u32x4*)(bw + 1024 + l16), OFF_WX + pos * 8192 + w * 2048 + 1024 + l16);
      const float hr = blo(wn[0]) + blo(wn[2]), hi = blo(wn[1]) - blo(wn[3]), nr = blo(wn[4]) + blo(wn[6]), ni = blo(wn[5]) - blo(wn[7]);
      const f32x4 ab = {0.25f * (hr + nr), 0.25f * (hi + ni), 0.25f * (hr - nr), 0.25f * (hi - ni)};
      if (tid < 8) st4(__builtin_bit_cast(u32x4, ab), OFF_WNY + tid * 4096 + pos * 16);
    }
    {
      FRESH();
      const int phi = pos, pi_ = w >> 1, kt = w & 1;
      const int k1l = 2 * (16 * kt + m) + pi_;
      auto consts = [&](auto PC) { col_consts<decltype(PC)::value>(cc, m, g, w); SB(); };
      using I0 = std::integral_constant<int, 0>; using I1 = std::integral_constant<int, 1>;
      using I2 = std::integral_constant<int, 2>; using I3 = std::integral_constant<int, 3>;
      u8* const bw = lds + w * 32768 + 28672;
      const u32 zo = (tid >> 1) * 8192 + (tid & 1) * 16;
      const u8* zb0 = ws + OFF_A + phi * 32;
      const u8* zb1 = zb0 + 128 * 8192;
      const u8* wb0 = ws + OFF_WX + phi * 32;
      const u8* wb1 = wb0 + 128 * 8192;
      const u32 wo = zo;
      PINS(zb0); PINS(zb1); PINS(wb0); PINS(wb1);
      if constexpr (L == 0) {
        u32x4 fl0, fl1;
        const u32 o = lane * 32;
        const u8* fb = ws + OFF_FLAG + ((phi >> 5) & 3) * 2048;
        w2dma(1, 1);
        ld4<0>(fl0, o, fb); ld4<16>(fl1, o, fb);
        SB();
        consts(I0{});
        consts(I1{});
        vmwait<0>(); PIN(fl0); PIN(fl1);
        RETRY(!(fl0.x == ep.x && fl0.y == ep.y && fl0.z == ep.x && fl0.w == ep.y && fl1.x == ep.x && fl1.y == ep.y && fl1.z == ep.x && fl1.w == ep.y), {
          ld4r<0>(fl0, o, fb); ld4r<16>(fl1, o, fb);
          vmwait<0>(); PIN(fl0); PIN(fl1);
        });
      } else {
        SB();
        consts(I0{});
        consts(I2{});
      }
      u32x4 z0, z1, wv[2];
      ld4<0>(z0, zo, zb0); ld4<0>(z1, zo, zb1);
      ld4<0>(wv[0], wo, wb0); ld4<0>(wv[1], wo, wb1);
      SB();
      u32x4 wa[8], po[5];
      if constexpr (L == 0) {
        u32x4 ot[4];
#pragma unroll
        for (int j = 0; j < 8; ++j) PIN(pv0[j]);
        transpose8x4(pv0, ot);
#pragma unroll
        for (int x = 0; x < 4; ++x) *(u32x4*)(bw + lane * 64 + ((x + lane) & 3) * 16) = ot[x];
#pragma unroll
        for (int x = 0; x < 4; ++x) {
          const int src = x * 16 + (lane >> 2);
          po[x] = *(const u32x4*)(bw + src * 64 + (((lane & 3) + src) & 3) * 16);
        }
        {
          u32x2 lo, hi;
          tr_read<6 * 4096>(lo, hi, ldsbase + w * 32768 + tr_addr(lane, G & 3));
          lgwait<0>();
          PIN(lo); PIN(hi);
          po[4] = u32x4{lo.x, lo.y, hi.x, hi.y};
        }
#pragma unroll
        for (int x = 0; x < 5; ++x) PIN(po[x]);
        st4(po[0], pd[0]); st4(po[1], pd[1]);
        SB();
        consts(I2{});
        consts(I3{});
        st4(po[2], pd[2]); st4(po[3], pd[3]);
        w2dma(2, 2);
      } else {
        consts(I1{});
        ring4(2);
        SB();
        consts(I3{});
      }
      u8* const zi = lds + LDS_SCR;
      u32 zw;
      {
        const int pp = tid >> 1;
        zw = (tid & 1) * 4096 + ((pp >> 6) & 1) * 2048 + (((pp >> 3) & 3) * 16 + 2 * (pp & 7)) * 16 + ((pp >> 5) & 1) * 8;
        PIN(zw);
      }
      u32 uo = OFF_U + phi * 8192 + w * 2048 + l16;
      PIN(uo);
      SB();
      bar_nw();
      vmwait<(L == 0 ? 8 : 4)>();
      PIN(z0); PIN(z1); PIN(wv[0]); PIN(wv[1]);
      if constexpr (L == 0) {
        RETRY((stale<false, 1u>(red8<false>(z0, z1)) || max4(max4(0, wv[0]), wv[1]) == SENT), {
          if (bad_) { ld4r<0>(z0, zo, zb0); ld4r<0>(z1, zo, zb1); ld4r<0>(wv[0], wo, wb0); ld4r<0>(wv[1], wo, wb1); }
          vmwait<0>(); PIN(z0); PIN(z1); PIN(wv[0]); PIN(wv[1]);
        });
      } else {
        RETRY((stale<true, 0x10000u>(red8<true>(z0, z1))), {
          if (bad_) { ld4r<0>(z0, zo, zb0); ld4r<0>(z1, zo, zb1); }
          vmwait<0>(); PIN(z0); PIN(z1);
        });
      }
      *(u32x2*)(zi + zw) = u32x2{z0.x, z0.y}; *(u32x2*)(zi + zw + 16) = u32x2{z0.z, z0.w};
      *(u32x2*)(zi + zw + 1024) = u32x2{z1.x, z1.y}; *(u32x2*)(zi + zw + 1024 + 16) = u32x2{z1.z, z1.w};
      *(u32x4*)(zi + 8192 + t16) = wv[0]; *(u32x4*)(zi + 12288 + t16) = wv[1];
      bar();
      const u8* zr = zi + pi_ * 4096 + l16;
      const u32x4 f00 = *(const u32x4*)(zr), f01 = *(const u32x4*)(zr + 1024), f10 = *(const u32x4*)(zr + 2048), f11 = *(const u32x4*)(zr + 3072);
      wv[0] = *(const u32x4*)(zi + 8192 + tid * 32); wv[1] = *(const u32x4*)(zi + 8192 + tid * 32 + 16);
      if (phi == 0) static_for<8>([&](auto J) { constexpr int j = decltype(J)::value; ld4<0>(wa[j], t16, ws + OFF_WNY + j * 4096); });
      if constexpr (L == 1) ring4(3); else { st4(po[4], OFF_W2 + (ct * 4 + w) * 32768 + (half2 * 16 + G) * 1024 + l16); }
      f32x4 d0, d1, d2, d3;
      mm8<false, 3>(d0, d1, d2, d3, f00, cc.b1re[0], f01, cc.b1re[1], f00, cc.b1im[0], f01, cc.b1im[1], f10, cc.b1re[0], f11, cc.b1re[1], f10, cc.b1im[0], f11, cc.b1im[1]);
      f32x4 e0, e1, e2, e3;
      u32x4 b20 = twiddle<false>(d0, d1, cc.t1c[0], cc.t1s[0]);
      PIN(b20);
      const u32x4 b21 = cplx_under<false, true>(d2, d3, cc.t1c[1], cc.t1s[1], [&](auto I) {
        constexpr int i = decltype(I)::value;
        mf0<false, 0>(i == 0 ? e0 : i == 1 ? e1 : i == 2 ? e2 : e3, i == 0 ? cc.a2re[0][0] : i == 1 ? cc.a2im[0][0] : i == 2 ? cc.a2re[1][0] : cc.a2im[1][0], b20);
      });
      if constexpr (L == 0) w2dma(3, 3);
      f32x2 wr[2][2], wi[2][2];
      auto unpack_w = [&]() {
#pragma unroll
        for (int t = 0; t < 2; ++t)
#pragma unroll
          for (int h = 0; h < 2; ++h) {
            wr[t][h] = f32x2{blo(wv[t][2 * h]), blo(wv[t][2 * h + 1])}; wi[t][h] = f32x2{bhi(wv[t][2 * h]), bhi(wv[t][2 * h + 1])};
            PIN(wr[t][h]); PIN(wi[t][h]);
          }
      };
      mf4<false, 0, 0>(e0, e1, e2, e3, cc.a2re[0][1], b21, cc.a2im[0][1], b21, cc.a2re[1][1], b21, cc.a2im[1][1], b21);
      SB();
      unpack_w();
      SB();
      if constexpr (L == 0) { fill_h(OFF_H, 0); fill_h(OFF_H, 1); }
      u32x4 a30, a31;
      auto step3 = [&](auto I) {
        constexpr int i = decltype(I)::value;
        mf0<true, 0>(i == 0 ? d0 : i == 1 ? d1 : i == 2 ? d2 : d3, a30, i == 0 ? cc.b3re[0][0] : i == 1 ? cc.b3im[0][0] : i == 2 ? cc.b3re[1][0] : cc.b3im[1][0]);
      };
      if (phi != 0) {
        a30 = cmul_b(e0, e1, wr[0], wi[0]);
        PIN(a30);
        a31 = cplx_under<true, false>(e2, e3, wr[1], wi[1], step3);
      } else {
        u32* pl = (u32*)(lds + LDS_STASH);
        const u32x4 h0 = {pkh(e0[0], e1[0]), pkh(e0[1], e1[1]), pkh(e0[2], e1[2]), pkh(e0[3], e1[3])};
        const u32x4 h1 = {pkh(e2[0], e3[0]), pkh(e2[1], e3[1]), pkh(e2[2], e3[2]), pkh(e2[3], e3[3])};
        if (k1l != 0) {
          *(u32x4*)(pl + k1l * 32 + ((g ^ (k1l >> 1)) & 7) * 4) = h0; *(u32x4*)(pl + k1l * 32 + (((4 + g) ^ (k1l >> 1)) & 7) * 4) = h1;
        } else {
#pragma unroll
          for (int r = 0; r < 4; ++r) { pl[(4 * g + r - 1) & 31] = h0[r]; pl[16 + 4 * g + r - 1] = h1[r]; }
        }
        bar();
        vmwait<(L == 0 ? 7 : 4)>();
#pragma unroll
        for (int j = 0; j < 8; ++j) PIN(wa[j]);
        if constexpr (L == 0) {
          RETRY(max4(max4(max4(max4(max4(max4(max4(max4(0, wa[0]), wa[1]), wa[2]), wa[3]), wa[4]), wa[5]), wa[6]), wa[7]) == SENT, {
            static_for<8>([&](auto J) { constexpr int j = decltype(J)::value; ld4r<0>(wa[j], t16, ws + OFF_WNY + j * 4096); });
            vmwait<0>();
            _Pragma("unroll") for (int j = 0; j < 8; ++j) PIN(wa[j]);
          });
        }
        const int kr = (64 - k1l) & 63;
        const u32x4 q0 = *(const u32x4*)(pl + kr * 32 + (((7 - g) ^ (kr >> 1)) & 7) * 4), q1 = *(const u32x4*)(pl + kr * 32 + (((3 - g) ^ (kr >> 1)) & 7) * 4);
#pragma unroll
        for (int r = 0; r < 4; ++r) {
          const f32x4 c0 = __builtin_bit_cast(f32x4, wa[r]), c1 = __builtin_bit_cast(f32x4, wa[4 + r]);
          const u32 u0 = q0[3 - r], u1 = q1[3 - r];
          const f16x2 v0 = __builtin_bit_cast(f16x2, u0), v1 = __builtin_bit_cast(f16x2, u1);
          const float qr0 = (float)v0.x, qi0 = (float)v0.y, qr1 = (float)v1.x, qi1 = (float)v1.y;
          a30[r] = pkb(e0[r] * c0[0] - e1[r] * c0[1] + qr0 * c0[2] + qi0 * c0[3], e0[r] * c0[1] + e1[r] * c0[0] + qr0 * c0[3] - qi0 * c0[2]);
          a31[r] = pkb(e2[r] * c1[0] - e3[r] * c1[1] + qr1 * c1[2] + qi1 * c1[3], e2[r] * c1[1] + e3[r] * c1[0] + qr1 * c1[3] - qi1 * c1[2]);
        }
        PIN(a30); PIN(a31);
        asm volatile("s_nop 1");
        static_for<4>(step3);
      }
      u8* ex = lds + LDS_SCR + 16384;
      mf4<true, 0, 5>(d0, d1, d2, d3, a31, cc.b3re[0][1], a31, cc.b3im[0][1], a31, cc.b3re[1][1], a31, cc.b3im[1][1]);
      *(u32x4*)(ex + w * 1024 + l16) = twiddle<true>(d0, d1, cc.t2c[0], cc.t2s[0]);
      SB();
      *(u32x4*)(ex + (4 + w) * 1024 + l16) = twiddle<true>(d2, d3, cc.t2c[1], cc.t2s[1]);
      if constexpr (L == 0) vmwait<6>();
      bar();
      const u8* er = ex + (w & 1) * 4096 + l16;
      const u32x4 g0 = *(const u32x4*)(er), g1 = *(const u32x4*)(er + 1024), g2 = *(const u32x4*)(er + 2048), g3 = *(const u32x4*)(er + 3072);
      mm8<true, 7>(d0, d1, d2, d3, cc.a4re[0][0], g0, cc.a4re[0][1], g1, cc.a4im[0][0], g0, cc.a4im[0][1], g1, cc.a4re[1][0], g2, cc.a4re[1][1], g3, cc.a4im[1][0], g2,
                   cc.a4im[1][1], g3);
      {
        const f32x2 lr0 = lo2(d0) + lo2(d2), li0 = lo2(d1) + lo2(d3), hr0 = lo2(d0) - lo2(d2), hi0 = lo2(d1) - lo2(d3);
        const f32x2 lr1 = hi2(d0) + hi2(d2), li1 = hi2(d1) + hi2(d3), hr1 = hi2(d0) - hi2(d2), hi1 = hi2(d1) - hi2(d3);
        if (phi != 0) {
          const f32x2 pr0 = lr0 - hi0, pi0 = li0 + hr0, qr0 = lr0 + hi0, qi0 = hr0 - li0;
          st4(mark4<1u - L, L>(u32x4{pkb(pr0.x, pi0.x), pkb(qr0.x, qi0.x), pkb(pr0.y, pi0.y), pkb(qr0.y, qi0.y)}), uo);
          const f32x2 pr1 = lr1 - hi1, pi1 = li1 + hr1, qr1 = lr1 + hi1, qi1 = hr1 - li1;
          st4(mark4<1u - L, L>(u32x4{pkb(pr1.x, pi1.x), pkb(qr1.x, qi1.x), pkb(pr1.y, pi1.y), pkb(qr1.y, qi1.y)}), uo + 1024);
          MEMFENCE();
        } else {
          st4(mark4<1u - L, L>(u32x4{pkb(lr0.x, hr0.x), pkb(li0.x, hi0.x), pkb(lr0.y, hr0.y), pkb(li0.y, hi0.y)}), uo);
          st4(mark4<1u - L, L>(u32x4{pkb(lr1.x, hr1.x), pkb(li1.x, hi1.x), pkb(lr1.y, hr1.y), pkb(li1.y, hi1.y)}), uo + 1024);
        }
      }
    }
    {
      FRESH();
      auto w2take = [&](auto KC) {
        constexpr int k = decltype(KC)::value;
        u32x2 lo[4], hi[4];
        static_for<4>([&](auto C) { constexpr int c = decltype(C)::value; tr_read<k * 4096>(lo[c], hi[c], ldsbase + w * 32768 + tr_addr(lane, c)); });
        lgwait<0>();
#pragma unroll
        for (int c = 0; c < 4; ++c) {
          PIN(lo[c]); PIN(hi[c]);
          w2a[k][c] = u32x4{lo[c].x, lo[c].y, hi[c].x, hi[c].y};
          PINA(w2a[k][c]);
        }
      };
      if constexpr (L == 0) { w2dma(4, 4); w2dma(5, 5); vmwait<12>(); static_for<4>(w2take); }
      SB();
      row_consts<true, 0>(rc, m, g);
      f32x2 xa[4], xb[4];
#pragma unroll
      for (int i = 0; i < 4; ++i) {
        xa[i] = f32x2{blo(xl[i >> 1][i & 1]), bhi(xl[i >> 1][i & 1])}; xb[i] = f32x2{blo(xh[i >> 1][i & 1]), bhi(xh[i >> 1][i & 1])};
      }
      const float ca = first_lane(xa[0].x), cb = first_lane(xb[0].x);
      float ch = lane < 32 ? ca : cb;
      PIN(ch);
#pragma unroll
      for (int i = 0; i < 4; ++i) { xa[i] -= ca; xb[i] -= cb; PIN(xa[i]); PIN(xb[i]); }
      SB();
      u32x4 c0, c1;
      const u32 uo = (tid >> 1) * 8192 + (tid & 1) * 16;
      const u8* ub0 = ws + OFF_U + pos * 32;
      const u8* ub1 = ub0 + 128 * 8192;
      row_consts<true, 1>(rc, m, g);
      SB();
      __builtin_amdgcn_s_sleep(1);
      ld4<0>(c0, uo, ub0); ld4<0>(c1, uo, ub1);
      if constexpr (L == 0) {
        fill_h(OFF_H, 2); fill_h(OFF_H, 3); fill_h(OFF_H, 4);
        vmwait<5>();
        w2take(std::integral_constant<int, 4>{}); w2take(std::integral_constant<int, 5>{});
      }
      SB();
      if constexpr (L == 1) ring4(4); else { fill_h(OFF_H, 5); fill_h(OFF_H, 6); }
      u32* img = (u32*)(lds + LDS_SCR);
      u32 ip0, iq0, iq1, xno, tw0, tw1, trd;
      {
        const int ph = tid >> 1, fq0 = ph ? 512 - ph : 256, fq1 = 384 - ph;
        ip0 = ((tid & 1) * 1280 + (ph & 31) * 20 + (ph >> 5)) * 4;
        iq0 = ((tid & 1) * 1280 + (fq0 & 31) * 20 + (fq0 >> 5)) * 4; iq1 = ((tid & 1) * 1280 + (fq1 & 31) * 20 + (fq1 >> 5)) * 4;
        xno = OFF_A + (pos >> 1) * 16384 + (tid >> 3) * 256 + (pos & 1) * 128 + (tid & 7) * 16;
        tw0 = (4 * m + g) * 128 + ((2 * w) ^ (m & 7)) * 16; tw1 = (4 * m + g) * 128 + ((2 * w + 1) ^ (m & 7)) * 16;
        trd = (tid >> 3) * 128 + ((tid & 7) ^ ((tid >> 5) & 7)) * 16;
        PIN(ip0); PIN(iq0); PIN(iq1); PIN(xno); PIN(tw0); PIN(tw1); PIN(trd);
      }
      SB();
      vmwait<(L == 0 ? 5 : 4)>(); PIN(c0); PIN(c1);
      {
        RETRY((stale<L == 1, 1u>(red8<L == 1>(c0, c1))), {
          if (bad_) { ld4r<0>(c0, uo, ub0); ld4r<0>(c1, uo, ub1); }
          vmwait<0>(); PIN(c0); PIN(c1);
        });
      }
      {
        u8* d = (u8*)img;
        *(u32*)(d + ip0) = c0.x; *(u32*)(d + iq0) = c0.y; *(u32*)(d + 2560 + ip0) = c0.z; *(u32*)(d + 2560 + iq0) = c0.w;
        *(u32*)(d + ip0 + 16) = c1.x; *(u32*)(d + iq1) = c1.y; *(u32*)(d + 2560 + ip0 + 16) = c1.z; *(u32*)(d + 2560 + iq1) = c1.w;
      }
      bar();
      const u32x4 a0 = *(const u32x4*)(img + w * 640 + m * 20 + 4 * g), a1 = *(const u32x4*)(img + w * 640 + (16 + m) * 20 + 4 * g);
      if constexpr (L == 1) ring4(5);
      if constexpr (L == 0) ring4(0);
      f32x4 dr0, di0, dr1, di1;
      row_fft<true, 7>(rc, a0, a1, dr0, di0, dr1, di1, [&] { if constexpr (L == 0) ring4(1); });
      if constexpr (L == 0) ring4(2);
      const float ys = 1.0f / 32768.0f;
      xa[0] += lo2(dr0) * ys; xa[1] += hi2(dr0) * ys; xa[2] += lo2(dr1) * ys; xa[3] += hi2(dr1) * ys;
      xb[0] += lo2(di0) * ys; xb[1] += hi2(di0) * ys; xb[2] += lo2(di1) * ys; xb[3] += hi2(di1) * ys;
      const f32x2 sa = (xa[0] + xa[1]) + (xa[2] + xa[3]), sb = (xb[0] + xb[1]) + (xb[2] + xb[3]);
      f32x2 qa = xa[0] * xa[0], qb = xb[0] * xb[0];
#pragma unroll
      for (int i = 1; i < 4; ++i) { qa += xa[i] * xa[i]; qb += xb[i] * xb[i]; }
      if constexpr (L == 0) { SB(); ring4(3); SB(); }
      f32x2 R, O;
      ln_coef(f32x2{sa.x + sa.y, sb.x + sb.y}, f32x2{qa.x + qa.y, qb.x + qb.y}, ch, R, O);
      if constexpr (L == 0) { SB(); ring4(4); SB(); }
      u32x4 na, nb;
#pragma unroll
      for (int i = 0; i < 4; ++i) {
        const f32x2 va = xa[i] * R.x + O.x, vb = xb[i] * R.y + O.y;
        na[i] = mark<0x10000u, 1u - L>(pkb(va.x, va.y)); nb[i] = mark<0x10000u, 1u - L>(pkb(vb.x, vb.y));
      }
      u8* tr = lds + LDS_SCR + 12288;
      *(u32x4*)(tr + tw0) = na; *(u32x4*)(tr + tw1) = nb;
      if constexpr (L == 0) vmwait<20>(); else vmwait<0>();
      bar();
      st4(*(const u32x4*)(tr + trd), xno);
      st4(*(const u32x4*)(tr + 4096 + trd), xno + 8192);
      SB();
#pragma unroll
      for (int i = 0; i < 4; ++i) { xa[i] += ca; xb[i] += cb; }
      const u32x4 rl = {pkb(xa[0].x, xa[0].y), pkb(xa[1].x, xa[1].y), pkb(xa[2].x, xa[2].y), pkb(xa[3].x, xa[3].y)};
      const u32x4 rh = {pkb(xb[0].x, xb[0].y), pkb(xb[1].x, xb[1].y), pkb(xb[2].x, xb[2].y), pkb(xb[3].x, xb[3].y)};
      if constexpr (L == 0) {
        *(u32x4*)(lds + LDS_STASH + t16) = rl; *(u32x4*)(lds + LDS_STASH + 4096 + t16) = rh;
        ring4(5); ring4(6); ring4(7);
      } else {
        xl[0] = u32x2{rl.x, rl.y}; xl[1] = u32x2{rl.z, rl.w}; xh[0] = u32x2{rh.x, rh.y}; xh[1] = u32x2{rh.z, rh.w};
        PIN(xl[0]); PIN(xl[1]); PIN(xh[0]); PIN(xh[1]);
        __builtin_amdgcn_s_sleep(4);
      }
    }
    {
      FRESH();
      u32x4 tk[32], he[32], hl[16];
      u32x4 hh[8], hf[8];
      f32x4 ac1[6][2], ac2[4][2], ac3[4][2];
      const u8* hs0 = ws + OFF_H + (4 * G + 2 * half2) * 114688 + (4 * w) * 7168;
      const u8* hs[4];
      const u8* hp[4];
#pragma unroll
      for (int j = 0; j < 4; ++j) {
        hs[j] = hs0 + opq((j + ct) & 3) * 7168; hp[j] = hs0 + opq(j ^ (2 * (ct & 1))) * 7168 + 6144;
        PINS(hs[j]); PINS(hp[j]);
      }
      const u8* w2src = ws + OFF_W2 + (ct * 4 + w) * 32768;
      u32 hv[4], hw[2];
#pragma unroll
      for (int u = 0; u < 4; ++u) { hv[u] = l16 + ((u ^ w) >> 1) * 114688 + ((u ^ w) & 1) * 3072; PIN(hv[u]); }
#pragma unroll
      for (int q = 0; q < 2; ++q) { hw[q] = l16 + (q ^ (w >> 1)) * 114688; PIN(hw[q]); }
      auto hload = [&](auto RE, auto KC, auto UC) {
        constexpr int k = decltype(KC)::value, u = decltype(UC)::value;
        constexpr bool re = decltype(RE)::value;
        if constexpr (k == 4 || k == 5) {
          if constexpr (re) ld4r<0>(hh[4 * (k - 4) + u], hw[u >> 1], hp[2 * (k - 4) + (u & 1)]); else ld4<0>(hh[4 * (k - 4) + u], hw[u >> 1], hp[2 * (k - 4) + (u & 1)]);
        } else {
          constexpr int j = k < 4 ? k : k < 10 ? k - 6 : k - 10, o = k < 4 ? 0 : k < 10 ? 1024 : 2048;
          u32x4& d = k < 10 ? he[(k < 4 ? 0 : 16) + 4 * j + u] : hl[4 * j + u];
          if constexpr (re) ld4r<o>(d, hv[u], hs[j]); else ld4<o>(d, hv[u], hs[j]);
        }
      };
      auto hseq = [&](auto IC) {
        constexpr int i = decltype(IC)::value;
        hload(std::false_type{}, std::integral_constant<int, (i >> 2)>{}, std::integral_constant<int, i & 3>{});
      };
      u64 bm0 = 0;
      {
        const u8* w1b = ws + OFF_W1 + member * 229376 + 131072 + w * 8192;
        const u8* tk0 = ws + OFF_A + (8 * G + 2 * w) * 16384 + 4096;
        const u8* tk1 = tk0 + 8192;
        u32 tv1 = l16 + 16384, llo = ldsbase + l16, lhi = llo + 65536;
        u32 hd0 = OFF_H + (4 * G + w) * 114688 + member * 7168 + l16, hd1 = hd0 + 3072;
        PIN(tv1); PIN(llo); PIN(lhi); PIN(hd0); PIN(hd1); PINS(tk0); PINS(tk1);
        u32 x3o = OFF_X3 + (8 * pos + 2 * w) * 1024 + xo;
        if constexpr (L == 1) PIN(x3o);
        auto tok = [&](auto RE, auto QC) {
          constexpr int q = decltype(QC)::value, ks = q >> 1;
          if constexpr (decltype(RE)::value) ld4r<(ks & 7) * 1024 - 4096>(tk[q], q & 1 ? tv1 : l16, ks >> 3 ? tk1 : tk0);
          else ld4<(ks & 7) * 1024 - 4096>(tk[q], q & 1 ? tv1 : l16, ks >> 3 ? tk1 : tk0);
        };
        constexpr bool TONE = L == 0;
        constexpr u32 TBIT = L == 0 ? 1u : 0x10000u;
        u32 mx0 = 0, mx1 = 0;
        auto mx0_fin = [&]() { return red2<TONE>(mx0, mx1) & TBIT; };
        auto fix = [&](auto QC, u64 bad) {
          constexpr int q0 = decltype(QC)::value;
          if (__builtin_expect(bad != 0, 0)) {
            _Pragma("nounroll") for (int sp = 0; sp < SPIN_CAP; ++sp) {
              static_for<4>([&](auto I) { tok(std::true_type{}, std::integral_constant<int, q0 + decltype(I)::value>{}); });
              vmwait<0>();
#pragma unroll
              for (int q = q0; q < q0 + 4; ++q) PIN(tk[q]);
              if (!__any(stale<TONE, TBIT>(red2<TONE>(red8<TONE>(tk[q0], tk[q0 + 1]), red8<TONE>(tk[q0 + 2], tk[q0 + 3]))))) break;
            }
          }
        };
        constexpr int PD = 6;
        u32x4 af[PD];
        auto frag = [&](auto XC) {
          constexpr int x = decltype(XC)::value, o = (x & 127) * 1024;
          lda<o & 65535>(af[x % PD], o < 65536 ? llo : lhi);
        };
        if constexpr (L == 1) static_for<PD - 1>(frag);
        static_for<NTOK0>([&](auto Q) {
          tok(std::false_type{}, Q);
          if constexpr (decltype(Q)::value == 3) bar_nw();
        });
        if constexpr (L == 0) {
          vmwait<NTOK0>();
          bar_nw();
          static_for<PD - 1>(frag);
        }
        vmwait<NTOK0 - 4>();
#pragma unroll
        for (int q = 0; q < 4; ++q) PIN(tk[q]);
        mx0 = red8<TONE>(tk[0], tk[1]); mx1 = red8<TONE>(tk[2], tk[3]);
        fix(std::integral_constant<int, 0>{}, __ballot(stale<TONE, TBIT>(red2<TONE>(mx0, mx1))));
        lgwait<PD - 2>();
        SB();
        float eg[2][2];
        u32 ev[2];
        u64 bm = 0;
        u32x4 eo[1];
        auto epi = [&](auto NC) {
          constexpr int n = decltype(NC)::value;
          if constexpr (n < NEPI1 + NEPI2) {
            constexpr bool p1 = n < NEPI1;
            constexpr int n0 = p1 ? n : n - NEPI1, i = n0 - n0 / 25;
            if constexpr (n0 % 25 == 24) st4(eo[0], p1 ? (n == 24 ? hd0 : n == 49 ? hd1 : hd1 + 3072) : (n0 == 24 ? hd0 : hd1) + 1024);
            else {
              constexpr int s = i & 1, st = (i % 12) >> 1, blk = i / 12;
              constexpr int u = p1 ? (blk < 4 ? blk >> 1 : blk - 4) : blk >> 1, pr = p1 ? (blk < 4 ? blk & 1 : 2) : blk & 1;
              constexpr int d = 2 * (pr == 2 ? u : pr) + s;
              const f32x4& ga = p1 ? ac1[2 * pr][u] : ac2[2 * (pr & 1)][u];
              const f32x4& up = p1 ? ac1[2 * pr + 1][u] : ac2[2 * (pr & 1) + 1][u];
              if constexpr (st < 2) { eg[s][st] = relu(ga[2 * s + st]); PIN(eg[s][st]); }
              else if constexpr (st < 4) { eg[s][st - 2] *= up[2 * s + st - 2]; PIN(eg[s][st - 2]); }
              else if constexpr (st == 4) { ev[s] = pkb(eg[s][0], eg[s][1]); PIN(ev[s]); }
              else { u32 v = mark<1u - L, L>(ev[s]); PIN(v); eo[0][d] = v; }
            }
            SB();
          }
        };
        static_for<224>([&](auto XC) {
          constexpr int x = decltype(XC)::value, job = G1S[L].job[x], ji = G1S[L].idx[x], e0 = G1S[L].epi[x], e1 = G1S[L].epi[x + 1];
          if constexpr (x == 32 || x == 64) bar_nw();
          if constexpr (x == 97) { vmwait<g1_wait(L, x, 2, 7)>(); bar_nw(); }
          if constexpr (x == 150) { vmwait<g1_wait(L, x, 2, 23)>(); bar_nw(); }
          constexpr int cj = (x + 12) / 12, co = x + 12 - 12 * cj;
          constexpr bool chk = x < 84 && co >= 5;
          constexpr int ks = x < 96 ? x / 6 : x < 160 ? (x - 96) >> 2 : (x - 160) >> 2, t = x < 96 ? x % 6 : x & 3;
          auto mm = [&](int u) {
            f32x4& a = x < 96 ? ac1[x < 96 ? t : 0][u] : x < 160 ? ac2[x < 96 ? 0 : t][u] : ac3[x < 96 ? 0 : t][u];
            mmw<ks == 0>(a, af[x % PD], tk[2 * ks + u]);
          };
          SB();
          mm(0);
          SB();
          if constexpr (x < 223) lgwait<(x + PD - 2 < 224 ? PD - 3 : 222 - x)>();
          if constexpr (chk && co == 5) {
            vmwait<g1_wait(L, x, 1, 4 * cj + 3)>();
            PIN(tk[4 * cj]); PIN(tk[4 * cj + 1]); PIN(tk[4 * cj + 2]); PIN(tk[4 * cj + 3]);
          }
          SB();
          if constexpr (chk && co >= 6 && co < 10) {
            const u32x4 v = tk[4 * cj + co - 6];
            mx0 = co == 6 ? red2<TONE>(v.x, v.y) : red3<TONE>(mx0, v.x, v.y); PIN(mx0); SB();
            mx1 = co == 6 ? red2<TONE>(v.z, v.w) : red3<TONE>(mx1, v.z, v.w); PIN(mx1);
          }
          if constexpr (chk && co == 10) { mx0 = mx0_fin(); PIN(mx0); SB(); bm = __ballot(TONE ? mx0 == 0 : mx0 != 0); PINS(bm); }
          if constexpr (x >= 212 && x < 218) {
            if constexpr (x == 212) { vmwait<g1_wait(L, x, 4, 3)>(); PIN(he[0]); PIN(he[1]); PIN(he[2]); PIN(he[3]); }
            else if constexpr (x < 217) {
              const u32x4 v = he[x - 213];
              mx0 = x == 213 ? red2<L == 1>(v.x, v.y) : red3<L == 1>(mx0, v.x, v.y); PIN(mx0); SB();
              mx1 = x == 213 ? red2<L == 1>(v.z, v.w) : red3<L == 1>(mx1, v.z, v.w); PIN(mx1);
            } else { mx0 = red2<L == 1>(mx0, mx1) & 1u; PIN(mx0); SB(); bm0 = __ballot(L == 1 ? mx0 == 0 : mx0 != 0); PINS(bm0); }
          }
          if constexpr (e0 < e1) epi(std::integral_constant<int, e0>{});
          if constexpr (e0 + 1 < e1) epi(std::integral_constant<int, e0 + 1>{});
          SB();
          mm(1);
          SB();
          if constexpr (chk && co == 11) fix(std::integral_constant<int, 4 * cj>{}, bm);
          if constexpr (job == 2 && (ji & 3) == 0) set_m0(ldsbase + (ji >> 3) * 32768 + w * 8192 + (ji & 4) * 1024);
          SB();
          if constexpr (x + PD - 1 < 224) frag(std::integral_constant<int, x + PD - 1>{});
          SB();
          if constexpr (job == 1) tok(std::false_type{}, std::integral_constant<int, ji>{});
          if constexpr (job == 2) dma1<(ji & 3) * 1024>(l16, w1b + (ji >> 3) * 32768 + (ji & 4) * 1024);
          if constexpr (job == 3) ld4a<(ji & 7) * 1024 - 4096>(w2a[6 + (ji >> 2)][ji & 3], l16, w2src + (ji >> 3) * 8192 + 4096);
          if constexpr (job == 6) st2(ji & 2 ? xh[ji & 1] : xl[ji & 1], x3o + ji * 512);
          if constexpr (job == 4) hseq(std::integral_constant<int, ji>{});
          if constexpr (e0 + 2 < e1) epi(std::integral_constant<int, e0 + 2>{});
          SB();
        });
        asm volatile("s_nop 7");
        SB();
#pragma unroll
        for (int u = 0; u < 2; ++u) {
          u32x4 o;
#pragma unroll
          for (int p = 0; p < 2; ++p) {
            const f32x4 ga = ac3[2 * p][u], up = ac3[2 * p + 1][u];
            const f32x2 a = f32x2{relu(ga[0]), relu(ga[1])} * lo2(up), c = f32x2{relu(ga[2]), relu(ga[3])} * hi2(up);
            o[2 * p] = mark<1u - L, L>(pkb(a.x, a.y)); o[2 * p + 1] = mark<1u - L, L>(pkb(c.x, c.y));
          }
          st4(o, (u ? hd1 : hd0) + 2048);
        }
        SB();
      }
      {
        f32x4 acc[4][4];
        u32x4 x3a, x3b;
        u32 eo = 0, oo = 0, rw[3], ta = 0, tb = 0;
        auto addr = [&](auto IC) {
          constexpr int i = decltype(IC)::value;
          const u32 sb = (128 * G + 64 * half2 + 16 * w) * 1024 + ct * 128;
          if constexpr (i < 3) { rw[i] = l16 + (((i + 1) ^ w) * 3 + i) * 4096; PIN(rw[i]); }
          if constexpr (i == 3) { ta = (l16 >> 4) & 0x30; PIN(ta); }
          if constexpr (i == 4) { tb = l16 & (L == 1 ? 0xf0 : 0xe0); PIN(tb); }
          if constexpr (i == 5) { eo = tb * 64 + sb; PIN(eo); }
          if constexpr (i == 6) { tb = l16 & 0x10; PIN(tb); }
          if constexpr (i == 7) { tb = tb * 4 + ta; PIN(tb); }
          if constexpr (i == 8) { eo += L == 1 ? ta : tb; PIN(eo); }
          if constexpr (L == 1) {
            if constexpr (i == 9) { ta = l16 & 0x20; PIN(ta); }
            if constexpr (i == 10) { oo = ta * 1024 + tb; PIN(oo); }
            if constexpr (i == 11) { ta = l16 & 0x80; PIN(ta); }
            if constexpr (i == 12) { oo += ta * 16; PIN(oo); }
            if constexpr (i == 13) { ta = l16 & 0x40; PIN(ta); }
            if constexpr (i == 14) { oo += ta * 16; PIN(oo); }
            if constexpr (i == 15) { oo += row_of_pos(16 * G + 8 * half2 + 2 * w, 0) * 1024 + ct * 128; PIN(oo); }
          }
        };
        const u8* x3s = ws + OFF_X3;
        u64 bm = 0;
        PINS(x3s);
        constexpr int NEARLY = g1_ne(L), LB = 40 - NEARLY, XG = 31;
        auto cum = [](int n) constexpr { const int c = n - n / 8; return c < LB + 16 ? c : LB + 16; };
        auto young = [cum](int k, int n) constexpr {
          const int i = 4 * k + 3;
          const int done = k < 10 ? i - NEARLY + 1 : LB + 4 + 3 * (k - 10);
          const int xr = (L == 1 && n > XG && done <= cum(XG)) ? 2 : 0;
          if (k < 10 && i < NEARLY) return cum(n) + 2 + G1S[L].cum[224] - G1S[L].pos[4][i] - 1 + xr;
          return cum(n) - done + xr;
        };
        constexpr bool HONE = L == 1;
        u32 mx0 = 0, mx1 = 0;
        auto mx0_fin = [&]() { return red2<HONE>(mx0, mx1) & 1u; };
        auto pin_k = [&](auto KC) {
          constexpr int k = decltype(KC)::value;
#pragma unroll
          for (int u = 0; u < 4; ++u) {
            if constexpr (k < 4) PIN(he[4 * k + u]);
            else if constexpr (k < 6) PIN(hh[4 * (k - 4) + u]);
            else if constexpr (k < 10) PIN(he[16 + 4 * (k - 6) + u]);
            else PIN(hl[4 * (k - 10) + u]);
          }
        };
        auto opnd = [&](auto KC, int u) {
          constexpr int k = decltype(KC)::value;
          if constexpr (k < 4) return he[4 * k + u];
          else if constexpr (k < 6) return hh[4 * (k - 4) + u];
          else if constexpr (k < 10) return he[16 + 4 * (k - 6) + u];
          else return hl[4 * (k - 10) + u];
        };
        u32 wodd = (w & 1) ? ~0u : 0u;
        PIN(wodd);
        auto mkhalf = [&](auto KC, auto IC) {
          constexpr int k = decltype(KC)::value, i = decltype(IC)::value, u = i >> 2, d = i & 3;
          const u32x4& src = hh[4 * (k - 4) + (u & 2) + (d >> 1)];
          u32 v = (src[2 * ((u & 1) ^ 1) + (d & 1)] & wodd) | (src[2 * (u & 1) + (d & 1)] & ~wodd);
          PIN(v);
          hf[4 * (k - 4) + u][d] = v;
        };
        auto fixk = [&](auto KC, u64 bad) {
          constexpr int k = decltype(KC)::value;
          if (__builtin_expect(bad != 0, 0)) {
            _Pragma("nounroll") for (int sp = 0; sp < SPIN_CAP; ++sp) {
              static_for<4>([&](auto U) { hload(std::true_type{}, KC, U); });
              vmwait<0>();
              pin_k(KC);
              if (!__any(stale<HONE, 1u>(red2<HONE>(red8<HONE>(opnd(KC, 0), opnd(KC, 1)), red8<HONE>(opnd(KC, 2), opnd(KC, 3)))))) break;
            }
            if constexpr (k == 4 || k == 5) static_for<16>([&](auto I) { mkhalf(KC, I); });
          }
        };
        if constexpr (L == 0) {
#pragma unroll
          for (int k = 6; k < 14; ++k) { PINA(w2a[k][0]); PINA(w2a[k][1]); PINA(w2a[k][2]); PINA(w2a[k][3]); }
        }
        fixk(std::integral_constant<int, 0>{}, bm0);
        auto qchk = [&](auto QC, auto JC) {
          constexpr int qd = decltype(QC)::value, j = decltype(JC)::value, u = (qd + 1) & 3;
          if constexpr (j == 0) { PIN(hl[4 + u]); PIN(hl[8 + u]); PIN(hl[12 + u]); }
          else if constexpr (j < 4) {
            const u32x4 v = hl[4 * j + u];
            mx0 = j == 1 ? red2<HONE>(v.x, v.y) : red3<HONE>(mx0, v.x, v.y); PIN(mx0); SB();
            mx1 = j == 1 ? red2<HONE>(v.z, v.w) : red3<HONE>(mx1, v.z, v.w); PIN(mx1);
          } else if constexpr (j == 4) { mx0 = mx0_fin(); PIN(mx0); SB(); bm = __ballot(HONE ? mx0 == 0 : mx0 != 0); PINS(bm); }
          else if (__builtin_expect(bm != 0, 0)) {
            _Pragma("nounroll") for (int sp = 0; sp < SPIN_CAP; ++sp) {
              static_for<3>([&](auto K) { hload(std::true_type{}, std::integral_constant<int, 11 + decltype(K)::value>{}, std::integral_constant<int, u>{}); });
              vmwait<0>();
              PIN(hl[4 + u]); PIN(hl[8 + u]); PIN(hl[12 + u]);
              if (!__any(stale<HONE, 1u>(red4<HONE>(red8<HONE>(hl[4 + u], hl[8 + u]), hl[12 + u])))) break;
            }
          }
        };
        static_for<44>([&](auto NC) {
          constexpr int n = decltype(NC)::value, k = n >> 2, c = n & 3;
          using KC = std::integral_constant<int, k>;
          using KN = std::integral_constant<int, k + 1>;
          static_for<4>([&](auto UC) {
            constexpr int u = decltype(UC)::value;
            SB();
            if constexpr (k == 4 || k == 5) mmw<false>(acc[c][u], w2a[k][c], hf[4 * (k - 4) + u]);
            else mmw<k == 0>(acc[c][u], w2a[k][c], opnd(KC{}, u));
            SB();
            constexpr bool hk = k == 3 || k == 4;
            constexpr int j = u == 3 ? -9 : 3 * c + u - (hk ? 1 : 5);
            if constexpr (k == 10) {
              if constexpr (j == -1) vmwait<clamp63(young(11, n))>();
              if constexpr (j >= -1 && j < 4) qchk(std::integral_constant<int, 0>{}, std::integral_constant<int, j + 1>{});
            }
            if constexpr (k < 10 && j == -1) { vmwait<clamp63(young(k + 1, n))>(); pin_k(KN{}); }
            if constexpr (k < 10 && j >= 0 && j < 4) {
              const u32x4 v = opnd(KN{}, j);
              mx0 = j == 0 ? red2<HONE>(v.x, v.y) : red3<HONE>(mx0, v.x, v.y); PIN(mx0); SB();
              mx1 = j == 0 ? red2<HONE>(v.z, v.w) : red3<HONE>(mx1, v.z, v.w); PIN(mx1);
            }
            if constexpr ((k < 3 || k == 5) && c == 0 && u < 2) {
              constexpr int i0 = 4 * (k < 3 ? k : 3) + 2 * u;
              addr(std::integral_constant<int, i0>{}); SB(); addr(std::integral_constant<int, i0 + 1>{});
            }
            if constexpr (hk && j >= 4 && j != 5) {
              constexpr int i0 = j == 4 ? 0 : 2 + 3 * (j - 6);
              mkhalf(KN{}, std::integral_constant<int, i0>{}); SB(); mkhalf(KN{}, std::integral_constant<int, i0 + 1>{});
              if constexpr (j >= 6 && i0 + 2 < 16) { SB(); mkhalf(KN{}, std::integral_constant<int, i0 + 2>{}); }
            }
            if constexpr (k < 10 && j == 5) { mx0 = mx0_fin(); PIN(mx0); SB(); bm = __ballot(HONE ? mx0 == 0 : mx0 != 0); PINS(bm); }
            if constexpr (u == 3) {
              constexpr int q = cum(n);
              if constexpr (cum(n + 1) == q) {}
              else if constexpr (q < LB) hseq(std::integral_constant<int, NEARLY + q>{});
              else if constexpr (q < LB + 4) hload(std::false_type{}, std::integral_constant<int, 10>{}, std::integral_constant<int, q - LB>{});
              else hload(std::false_type{}, std::integral_constant<int, 11 + (q - LB - 4) % 3>{}, std::integral_constant<int, ((q - LB - 4) / 3 + 1) & 3>{});
              if constexpr (n == 23) {
                bar_nw();
              }
              if constexpr (L == 1 && n == XG) { ld4<0>(x3a, eo, x3s); ld4<64>(x3b, eo, x3s); }
              if constexpr (c == 3) {
                if constexpr (k < 10) fixk(KN{}, bm); else qchk(std::integral_constant<int, 0>{}, std::integral_constant<int, 5>{});
              }
            }
          });
        });
        static_for<16>([&](auto TC) {
          constexpr int t = decltype(TC)::value, u = ((t >> 2) + 1) & 3, c = t & 3;
          using QN = std::integral_constant<int, (t >> 2) + 1>;
          SB();
          mmw<false>(acc[c][u], w2a[11][c], hl[4 + u]);
          SB();
          if constexpr (t < 12) {
            if constexpr (c == 0) { vmwait<young(12 + (t >> 2), 44)>(); qchk(QN{}, std::integral_constant<int, 0>{}); }
            else qchk(QN{}, std::integral_constant<int, c>{});
          }
          SB();
          mmw<false>(acc[c][u], w2a[12][c], hl[8 + u]);
          SB();
          if constexpr (t >= 3 && t < 15) *(f32x4*)(lds + rw[(t - 3) >> 2] + ((t - 3) & 3) * 1024) = acc[(t - 3) & 3][((t - 3) >> 2) + 1];
          SB();
          mmw<false>(acc[c][u], w2a[13][c], hl[12 + u]);
          SB();
          if constexpr (t < 12 && c == 3) { qchk(QN{}, std::integral_constant<int, 4>{}); qchk(QN{}, std::integral_constant<int, 5>{}); }
        });
        SB();
        bar();
        f32x4 s[3][4];
#pragma unroll
        for (int c = 0; c < 4; ++c)
#pragma unroll
          for (int p = 0; p < 3; ++p) s[p][c] = *(const f32x4*)(lds + w * 12288 + p * 4096 + c * 1024 + l16);
        SB();
        if constexpr (L == 1) { vmwait<0>(); PIN(x3a); PIN(x3b); }
        u32x4 y[2];
#pragma unroll
        for (int a = 0; a < 2; ++a) {
          const f32x4 s0 = (acc[2 * a][0] + s[0][2 * a]) + (s[1][2 * a] + s[2][2 * a]);
          const f32x4 s1 = (acc[2 * a + 1][0] + s[0][2 * a + 1]) + (s[1][2 * a + 1] + s[2][2 * a + 1]);
          y[a] = u32x4{pkb(s0[0], s0[1]), pkb(s0[2], s0[3]), pkb(s1[0], s1[1]), pkb(s1[2], s1[3])};
          if constexpr (L == 1) {
            const u32x4 xr = a ? x3b : x3a;
#pragma unroll
            for (int p = 0; p < 4; ++p) y[a][p] = pkb(blo(xr[p]) + blo(y[a][p]), bhi(xr[p]) + bhi(y[a][p]));
          }
        }
        {
          const bool odd = m & 1;
          u32x4 z0, z1;
#pragma unroll
          for (int p = 0; p < 4; ++p) {
            const u32 t = (u32)__builtin_amdgcn_mov_dpp((int)(odd ? y[0][p] : y[1][p]), 0xB1, 0xf, 0xf, true);
            z0[p] = odd ? t : y[0][p]; z1[p] = odd ? y[1][p] : t;
          }
          if constexpr (L == 0) { st4(mark4<0x10001u, 0u>(z0), OFF_A + eo); st4(mark4<0x10001u, 0u>(z1), OFF_A + eo + 1024); }
          else {
            const __amdgpu_buffer_rsrc_t ro = __builtin_amdgcn_make_buffer_rsrc(P.out, 0, 2048 * 1024, 0x00020000);
            __builtin_amdgcn_raw_buffer_store_b128(z0, ro, oo, 0, 16); __builtin_amdgcn_raw_buffer_store_b128(z1, ro, oo + 1024 * 1024, 0, 16);
          }
        }
      }
    }
    if constexpr (L == 0) {
      FRESH();
      SB();
      row_consts<false, 0>(rc, m, g);
      SB();
      u32x2 d0, d1, d2, d3;
      const u8* db = ws + OFF_A + (8 * pos + 2 * w) * 1024;
      const u32x4 sl = *(const u32x4*)(lds + LDS_STASH + t16), sh = *(const u32x4*)(lds + LDS_STASH + 4096 + t16);
#pragma unroll
      for (int i = 0; i < 4; ++i) {
        xv[2 * i] = f32x2{blo(sl[i]), blo(sh[i])}; xv[2 * i + 1] = f32x2{bhi(sl[i]), bhi(sh[i])};
      }
      xc = f32x2{first_lane(xv[0].x), first_lane(xv[0].y)};
      xch = lane < 32 ? xc.x : xc.y;
      PIN(xch);
#pragma unroll
      for (int i = 0; i < 8; ++i) { xv[i] -= xc; PIN(xv[i]); }
      SB();
      bar_nw();
      ld2<0>(d0, xo, db); ld2<512>(d1, xo, db); ld2<1024>(d2, xo, db); ld2<1536>(d3, xo, db);
      ring4(0);
      SB();
      row_consts<false, 1>(rc, m, g);
      SB();
      fwd_pre(fp, tid, w, pos);
      SB();
      vmwait<4>(); PIN(d0); PIN(d1); PIN(d2); PIN(d3);
      RETRY((stale<false, 1u>(red8<false>(u32x4{d0.x, d0.y, d1.x, d1.y}, u32x4{d2.x, d2.y, d3.x, d3.y}))), {
        if (bad_) { ld2r<0>(d0, xo, db); ld2r<512>(d1, xo, db); ld2r<1024>(d2, xo, db); ld2r<1536>(d3, xo, db); }
        vmwait<0>(); PIN(d0); PIN(d1); PIN(d2); PIN(d3);
      });
      xv[0] += f32x2{blo(d0.x), blo(d2.x)}; xv[1] += f32x2{bhi(d0.x), bhi(d2.x)}; xv[2] += f32x2{blo(d0.y), blo(d2.y)}; xv[3] += f32x2{bhi(d0.y), bhi(d2.y)};
      xv[4] += f32x2{blo(d1.x), blo(d3.x)}; xv[5] += f32x2{bhi(d1.x), bhi(d3.x)}; xv[6] += f32x2{blo(d1.y), blo(d3.y)}; xv[7] += f32x2{bhi(d1.y), bhi(d3.y)};
    }
  };
  layer(std::integral_constant<int, 0>{});
  layer(std::integral_constant<int, 1>{});
  if (blockIdx.x == 0 && threadIdx.x == 0) st2(ep, OFF_E);
  asm volatile(".Lgf_end:");
}

extern "C" void kernel_launch(void* const* d_in, const int* in_sizes, int n_in, void* d_out, int out_size, void* d_ws, size_t ws_size,
                              hipStream_t stream) {
  Params p;
  p.x = (const u16*)d_in[0]; p.wr = (const u16*)d_in[1]; p.wi = (const u16*)d_in[2];
  p.wg = (const u16*)d_in[4]; p.wu = (const u16*)d_in[5]; p.wd = (const u16*)d_in[6];
  p.out = (u16*)d_out; p.ws = (u8*)d_ws;
  (void)hipFuncSetAttribute((const void*)k_all, hipFuncAttributeMaxDynamicSharedMemorySize, LDS_TOTAL);
  k_all<<<256, 256, LDS_TOTAL, stream>>>(p);
}
```
